```python
import torch

def _softmax(x, axis=-1):
    return torch.softmax(x, dim=axis)
def _gelu(x):
    return torch.nn.functional.gelu(x, approximate="tanh")
def _silu(x):
    return torch.nn.functional.silu(x)
def _sigmoid(x):
    return torch.sigmoid(x)
def _layernorm(x, axis=-1, eps=1e-5):
    m = x.mean(dim=axis, keepdim=True)
    v = x.var(dim=axis, keepdim=True, unbiased=False)
    return (x - m) / torch.sqrt(v + eps)
def _scatter_add(tgt, idx, src, axis=0):
    return tgt.clone().index_add_(axis, idx, src)
def _segment_sum(src, seg, n):
    out = torch.zeros((n,) + src.shape[1:], dtype=src.dtype)
    return out.index_add_(0, seg, src)
def _pad(x, pw):
    flat = tuple(v for pair in reversed(pw) for v in pair)
    return torch.nn.functional.pad(x, flat)

kernel_name = 'recurrent_scan__compressed_kv_cla__moe_arch_mod__prompt'

def setup_inputs():

    _w = (lambda a: (lambda t: t.to(torch.bfloat16) if t.is_floating_point() else t)(torch.as_tensor(a)))
    def _A_setup():
        B, H, Sq, Sk, D, M = 1, 32, 2048, 2048, 128, 512
        Dc = 4 * D
        return {
            "x": _w((lambda *s: torch.randn(*s, dtype=torch.bfloat16) * 0.05)(B, Sq, M)),
            "c_kv": _w((lambda *s: torch.randn(*s, dtype=torch.bfloat16) * 0.2)(B, Sk, Dc)),
            "w_q": _w((lambda *s: torch.randn(*s, dtype=torch.bfloat16) * 3.2)(M, H, D) * (M ** -0.5)),
            "w_uk": _w((lambda *s: torch.randn(*s, dtype=torch.bfloat16) * 3.2)(Dc, H, D) * (Dc ** -0.5)),
            "w_uv": _w((lambda *s: torch.randn(*s, dtype=torch.bfloat16) * 0.2)(Dc, H, D) * (Dc ** -0.5)),
            "w_o": _w((lambda *s: torch.randn(*s, dtype=torch.bfloat16) * 0.2)(H, D, M) * ((H * D) ** -0.5)),
        }
    _A_in = _A_setup()

    def _B_setup():
        B, S, D, F, C = 1, 2048, 512, 1792, 1
        return {
            "x": _w((lambda *s: torch.randn(*s, dtype=torch.bfloat16) * 0.05)(B, S, D)),
            "w_r": _w((lambda *s: torch.randn(*s, dtype=torch.bfloat16) * 1.6)(D)),
            "w_gate": _w((lambda *s: torch.randn(*s, dtype=torch.bfloat16) * 0.2)(D, F)),
            "w_up": _w((lambda *s: torch.randn(*s, dtype=torch.bfloat16) * 0.2)(D, F)),
            "w_down": _w((lambda *s: torch.randn(*s, dtype=torch.bfloat16) * 0.2)(F, D)),
        }
    _B_in = _B_setup()

    return {
        "A_x": _A_in["x"],
        "A_c_kv": _A_in["c_kv"],
        "A_w_q": _A_in["w_q"],
        "A_w_uk": _A_in["w_uk"],
        "A_w_uv": _A_in["w_uv"],
        "A_w_o": _A_in["w_o"],
        "B_x": _B_in["x"],
        "B_w_r": _B_in["w_r"],
        "B_w_gate": _B_in["w_gate"],
        "B_w_up": _B_in["w_up"],
        "B_w_down": _B_in["w_down"],
    }

def reference(A_x, A_c_kv, A_w_q, A_w_uk, A_w_uv, A_w_o, B_x, B_w_r, B_w_gate, B_w_up, B_w_down):
    def _A_ref(x, c_kv, w_q, w_uk, w_uv, w_o):
        B, Sq, M = x.shape[0], x.shape[1], x.shape[2]
        Sk = c_kv.shape[1]
        H, D = w_q.shape[1], w_q.shape[2]
        q = torch.einsum("bqm,mhd->bhqd", x, w_q)
        k = torch.einsum("bkc,chd->bhkd", c_kv, w_uk)
        v = torch.einsum("bkc,chd->bhkd", c_kv, w_uv)
        scores = torch.einsum("bhqd,bhkd->bhqk", q, k) * (D ** -0.5)
        off = Sk - Sq
        qi = torch.arange(Sq)[:, None]
        kj = torch.arange(Sk)[None, :]
        cmask = kj <= (qi + off)
        scores = torch.where(cmask[None, None], scores, -1e9)
        p = _softmax(scores, axis=-1)
        ctx = torch.einsum("bhqk,bhkd->bhqd", p, v)
        return torch.einsum("bhqd,hdm->bqm", ctx, w_o)

    def _B_ref(x, w_r, w_gate, w_up, w_down):
        B, S, D = x.shape
        C = 1
        r = x @ w_r
        idx = torch.argsort(-r, axis=-1)[:, :C]
        idx_e = (lambda x, n, axis: torch.repeat_interleave(x, n, dim=axis))(idx.reshape(B, C, 1), D, axis=2)
        x_sel = (lambda x, i, axis: torch.take_along_dim(x, i, dim=axis))(x, idx_e, axis=1)
        g = _silu(x_sel @ w_gate)
        u = x_sel @ w_up
        y = (g * u) @ w_down
        r_sel = _sigmoid((lambda x, i, axis: torch.take_along_dim(x, i, dim=axis))(r, idx, axis=1))
        y = y * r_sel.reshape(B, C, 1)
        b_off = (torch.arange(B) * S).reshape(B, 1)
        flat_idx = (idx + b_off).reshape(B * C)
        zero = (x * 0.0).reshape(B * S, D)
        upd = _scatter_add(zero, flat_idx, y.reshape(B * C, D), axis=0)
        return x + upd.reshape(B, S, D)

    x = A_x
    xn = _layernorm(x, axis=-1)
    s = _A_ref(xn, A_c_kv, A_w_q, A_w_uk, A_w_uv, A_w_o)
    g = _silu(_B_ref(xn, B_w_r, B_w_gate, B_w_up, B_w_down))
    return x + s * g
```

```cpp
#include <hip/hip_runtime.h>
#include <hip/hip_bf16.h>
#include <stdint.h>

typedef short bf16x8 __attribute__((ext_vector_type(8)));
typedef float f32x16 __attribute__((ext_vector_type(16)));
typedef unsigned short u16;

constexpr int S = 2048, M = 512, H = 32, D = 128, F = 1792, HD = H * D;

__device__ __forceinline__ float bf2f(u16 v) { return __uint_as_float(((unsigned)v) << 16); }
__device__ __forceinline__ u16 f2bf(float f) { __hip_bfloat16 b = __float2bfloat16(f); return *reinterpret_cast<u16*>(&b); }
__device__ __forceinline__ float rbf(float f) { return bf2f(f2bf(f)); }

__global__ void ln_kernel(const u16* __restrict__ x, const u16* __restrict__ wr, u16* __restrict__ xn, float* __restrict__ r) {
  int row = blockIdx.x * 4 + (threadIdx.x >> 6), lane = threadIdx.x & 63;
  const u16* xr = x + (size_t)row * M;
  float v[8]; float s = 0;
  bf16x8 t = *reinterpret_cast<const bf16x8*>(xr + lane * 8);
  for (int i = 0; i < 8; ++i) { v[i] = bf2f((u16)t[i]); s += v[i]; }
  for (int o = 32; o; o >>= 1) s += __shfl_xor(s, o);
  float mean = s * (1.f / M);
  float q = 0;
  for (int i = 0; i < 8; ++i) { float d = v[i] - mean; q += d * d; }
  for (int o = 32; o; o >>= 1) q += __shfl_xor(q, o);
  float var = rbf(q * (1.f / M));
  float mb = rbf(mean);
  float sd = rbf(sqrtf(rbf(var + 1e-5f)));
  bf16x8 w = *reinterpret_cast<const bf16x8*>(wr + lane * 8);
  bf16x8 o; float acc = 0;
  for (int i = 0; i < 8; ++i) { u16 b = f2bf(rbf(v[i] - mb) / sd); o[i] = (short)b; acc += bf2f(b) * bf2f((u16)w[i]); }
  for (int of = 32; of; of >>= 1) acc += __shfl_xor(acc, of);
  *reinterpret_cast<bf16x8*>(xn + (size_t)row * M + lane * 8) = o;
  if (lane == 0) r[row] = rbf(acc);
}

template <int EPI>
__global__ __launch_bounds__(256) void gemm_kernel(const u16* __restrict__ A, const u16* __restrict__ B, u16* __restrict__ C, int K, int N,
                            const u16* __restrict__ x, const u16* __restrict__ xn, const float* __restrict__ y, const int* __restrict__ sel) {
  __shared__ u16 As[64][40];
  __shared__ u16 Bs[64][40];
  int bm = blockIdx.y * 64, bn = blockIdx.x * 64;
  int tid = threadIdx.x, lane = tid & 63, wid = tid >> 6, wm = wid >> 1, wn = wid & 1;
  f32x16 acc = {};
  for (int k0 = 0; k0 < K; k0 += 32) {
    { int r = tid >> 2, c = (tid & 3) * 8;
      *reinterpret_cast<bf16x8*>(&As[r][c]) = *reinterpret_cast<const bf16x8*>(A + (size_t)(bm + r) * K + k0 + c); }
    { int kk = tid >> 3, c = (tid & 7) * 8;
      bf16x8 t = *reinterpret_cast<const bf16x8*>(B + (size_t)(k0 + kk) * N + bn + c);
      for (int i = 0; i < 8; ++i) Bs[c + i][kk] = (u16)t[i]; }
    __syncthreads();
    for (int ks = 0; ks < 2; ++ks) {
      bf16x8 a = *reinterpret_cast<const bf16x8*>(&As[wm * 32 + (lane & 31)][ks * 16 + (lane >> 5) * 8]);
      bf16x8 b = *reinterpret_cast<const bf16x8*>(&Bs[wn * 32 + (lane & 31)][ks * 16 + (lane >> 5) * 8]);
      acc = __builtin_amdgcn_mfma_f32_32x32x16_bf16(a, b, acc, 0, 0, 0);
    }
    __syncthreads();
  }
  for (int r = 0; r < 16; ++r) {
    int row = bm + wm * 32 + (r & 3) + 8 * (r >> 2) + 4 * (lane >> 5), col = bn + wn * 32 + (lane & 31);
    if (EPI == 0) C[(size_t)row * N + col] = f2bf(acc[r]);
    else {
      float s = rbf(acc[r]);
      float xv = bf2f(x[(size_t)row * N + col]), xnv = bf2f(xn[(size_t)row * N + col]);
      float b = xnv;
      if (row == *sel) b = rbf(xnv + y[col]);
      float g = rbf(b / (1.f + __expf(-b)));
      C[(size_t)row * N + col] = f2bf(xv + rbf(s * g));
    }
  }
}

__global__ __launch_bounds__(256) void attn_kernel(const u16* __restrict__ Q, const u16* __restrict__ Kt, const u16* __restrict__ V, u16* __restrict__ O) {
  __shared__ u16 Ks[32][136];
  __shared__ u16 Vs[128][40];
  int h = blockIdx.y, qb = gridDim.x - 1 - blockIdx.x;
  int tid = threadIdx.x, lane = tid & 63, wid = tid >> 6, l32 = lane & 31, hi = lane >> 5;
  int q0 = qb * 128 + wid * 32;
  bf16x8 qf[8];
  for (int d0 = 0; d0 < 8; ++d0) qf[d0] = *reinterpret_cast<const bf16x8*>(Q + (size_t)(q0 + l32) * HD + h * D + d0 * 16 + hi * 8);
  f32x16 o[4] = {};
  float m = -1e30f, l = 0;
  const float sc = 0.08838834764831845f * 1.4426950408889634f;
  int nk = qb * 128 + 128;
  for (int k0 = 0; k0 < nk; k0 += 32) {
    for (int i = 0; i < 2; ++i) {
      int idx = tid + i * 256, r = idx >> 4, c = (idx & 15) * 8;
      *reinterpret_cast<bf16x8*>(&Ks[r][c]) = *reinterpret_cast<const bf16x8*>(Kt + (size_t)(k0 + r) * HD + h * D + c);
      bf16x8 t = *reinterpret_cast<const bf16x8*>(V + (size_t)(k0 + r) * HD + h * D + c);
      int s = r >> 4, rr = r & 15, hh = (rr >> 2) & 1, j = (rr & 3) + 4 * (rr >> 3);
      int pos = 16 * s + 8 * hh + j;
      for (int e = 0; e < 8; ++e) Vs[c + e][pos] = (u16)t[e];
    }
    __syncthreads();
    f32x16 s = {};
    for (int d0 = 0; d0 < 8; ++d0) {
      bf16x8 a = *reinterpret_cast<const bf16x8*>(&Ks[l32][d0 * 16 + hi * 8]);
      s = __builtin_amdgcn_mfma_f32_32x32x16_bf16(a, qf[d0], s, 0, 0, 0);
    }
    float mx = -1e30f;
    for (int r = 0; r < 16; ++r) {
      int key = k0 + (r & 3) + 8 * (r >> 2) + 4 * hi;
      s[r] = (key <= q0 + l32) ? s[r] * sc : -__builtin_inff();
      mx = fmaxf(mx, s[r]);
    }
    mx = fmaxf(mx, __shfl_xor(mx, 32));
    float mn = fmaxf(m, mx), al = exp2f(m - mn);
    m = mn;
    float ps = 0;
    for (int r = 0; r < 16; ++r) { s[r] = exp2f(s[r] - mn); ps += s[r]; }
    l = l * al + ps;
    for (int db = 0; db < 4; ++db) for (int r = 0; r < 16; ++r) o[db][r] *= al;
    for (int ks = 0; ks < 2; ++ks) {
      bf16x8 p;
      for (int j = 0; j < 8; ++j) p[j] = (short)f2bf(s[8 * ks + j]);
      for (int db = 0; db < 4; ++db) {
        bf16x8 a = *reinterpret_cast<const bf16x8*>(&Vs[db * 32 + l32][ks * 16 + hi * 8]);
        o[db] = __builtin_amdgcn_mfma_f32_32x32x16_bf16(a, p, o[db], 0, 0, 0);
      }
    }
    __syncthreads();
  }
  l += __shfl_xor(l, 32);
  float il = 1.f / l;
  for (int db = 0; db < 4; ++db) for (int r = 0; r < 16; ++r) {
    int d = db * 32 + (r & 3) + 8 * (r >> 2) + 4 * hi;
    O[(size_t)(q0 + l32) * HD + h * D + d] = f2bf(o[db][r] * il);
  }
}

__global__ void mod1_kernel(const float* __restrict__ r, const u16* __restrict__ xn, const u16* __restrict__ wg, const u16* __restrict__ wu,
                            float* __restrict__ hbuf, int* __restrict__ sel) {
  __shared__ float bv[256]; __shared__ int bi[256]; __shared__ float pg[4][64], pu[4][64];
  int tid = threadIdx.x;
  float best = -__builtin_inff(); int idx = 0;
  for (int i = tid; i < S; i += 256) { float v = r[i]; if (v > best) { best = v; idx = i; } }
  bv[tid] = best; bi[tid] = idx; __syncthreads();
  for (int o = 128; o; o >>= 1) {
    if (tid < o) { if (bv[tid + o] > bv[tid] || (bv[tid + o] == bv[tid] && bi[tid + o] < bi[tid])) { bv[tid] = bv[tid + o]; bi[tid] = bi[tid + o]; } }
    __syncthreads();
  }
  int s = bi[0];
  if (blockIdx.x == 0 && tid == 0) *sel = s;
  int c = blockIdx.x * 64 + (tid & 63), kq = tid >> 6;
  float g = 0, u = 0;
  for (int k = kq * 128; k < kq * 128 + 128; ++k) { float xv = bf2f(xn[(size_t)s * M + k]); g += xv * bf2f(wg[(size_t)k * F + c]); u += xv * bf2f(wu[(size_t)k * F + c]); }
  pg[kq][tid & 63] = g; pu[kq][tid & 63] = u; __syncthreads();
  if (tid < 64) {
    float gg = rbf(pg[0][tid] + pg[1][tid] + pg[2][tid] + pg[3][tid]), uu = rbf(pu[0][tid] + pu[1][tid] + pu[2][tid] + pu[3][tid]);
    float sg = rbf(gg / (1.f + __expf(-gg)));
    hbuf[c] = rbf(sg * uu);
  }
}
__global__ void mod2_kernel(const float* __restrict__ hbuf, const u16* __restrict__ wd, const float* __restrict__ r, const int* __restrict__ sel, float* __restrict__ y) {
  __shared__ float p[4][64];
  int tid = threadIdx.x, c = blockIdx.x * 64 + (tid & 63), kq = tid >> 6;
  float a = 0;
  for (int k = kq * (F / 4); k < (kq + 1) * (F / 4); ++k) a += hbuf[k] * bf2f(wd[(size_t)k * M + c]);
  p[kq][tid & 63] = a; __syncthreads();
  if (tid < 64) {
    float yy = rbf(p[0][tid] + p[1][tid] + p[2][tid] + p[3][tid]);
    float rs = rbf(1.f / (1.f + __expf(-r[*sel])));
    y[c] = rbf(yy * rs);
  }
}

extern "C" void kernel_launch(void* const* d_in, const int* in_sizes, int n_in, void* d_out, int out_size, void* d_ws, size_t ws_size, hipStream_t stream) {
  const u16* x = (const u16*)d_in[0]; const u16* ckv = (const u16*)d_in[1];
  const u16* wq = (const u16*)d_in[2]; const u16* wuk = (const u16*)d_in[3]; const u16* wuv = (const u16*)d_in[4]; const u16* wo = (const u16*)d_in[5];
  const u16* wr = (const u16*)d_in[7]; const u16* wg = (const u16*)d_in[8]; const u16* wu = (const u16*)d_in[9]; const u16* wd = (const u16*)d_in[10];
  char* ws = (char*)d_ws;
  u16* xn = (u16*)ws; ws += (size_t)S * M * 2;
  u16* q = (u16*)ws; ws += (size_t)S * HD * 2;
  u16* k = (u16*)ws; ws += (size_t)S * HD * 2;
  u16* v = (u16*)ws; ws += (size_t)S * HD * 2;
  u16* ctx = (u16*)ws; ws += (size_t)S * HD * 2;
  float* r = (float*)ws; ws += S * 4;
  float* hbuf = (float*)ws; ws += 8192;
  float* y = (float*)ws; ws += 4096;
  int* sel = (int*)ws;
  ln_kernel<<<S / 4, 256, 0, stream>>>(x, wr, xn, r);
  gemm_kernel<0><<<dim3(HD / 64, S / 64), 256, 0, stream>>>(xn, wq, q, M, HD, nullptr, nullptr, nullptr, nullptr);
  gemm_kernel<0><<<dim3(HD / 64, S / 64), 256, 0, stream>>>(ckv, wuk, k, M, HD, nullptr, nullptr, nullptr, nullptr);
  gemm_kernel<0><<<dim3(HD / 64, S / 64), 256, 0, stream>>>(ckv, wuv, v, M, HD, nullptr, nullptr, nullptr, nullptr);
  mod1_kernel<<<F / 64, 256, 0, stream>>>(r, xn, wg, wu, hbuf, sel);
  mod2_kernel<<<M / 64, 256, 0, stream>>>(hbuf, wd, r, sel, y);
  attn_kernel<<<dim3(S / 128, H), 256, 0, stream>>>(q, k, v, ctx);
  gemm_kernel<1><<<dim3(M / 64, S / 64), 256, 0, stream>>>(ctx, wo, (u16*)d_out, HD, M, x, xn, y, sel);
}
```

```cpp
#include <hip/hip_runtime.h>
#include <hip/hip_bf16.h>
#include <stdint.h>

typedef unsigned short u16;
typedef short bf16x8 __attribute__((ext_vector_type(8)));
typedef float f32x4 __attribute__((ext_vector_type(4)));
typedef unsigned u32x4 __attribute__((ext_vector_type(4)));

__device__ __forceinline__ unsigned pk2(float a, float b) {
  typedef float f2 __attribute__((ext_vector_type(2))); typedef __bf16 b2 __attribute__((ext_vector_type(2)));
  f2 v = {a, b}; return __builtin_bit_cast(unsigned, __builtin_convertvector(v, b2));
}
__device__ __forceinline__ bf16x8 pk8(f32x4 a, f32x4 b) {
  u32x4 w = {pk2(a[0], a[1]), pk2(a[2], a[3]), pk2(b[0], b[1]), pk2(b[2], b[3])}; return __builtin_bit_cast(bf16x8, w);
}
__device__ __forceinline__ float xorl(float v, unsigned lane, unsigned m) {
  return __uint_as_float((unsigned)__builtin_amdgcn_ds_bpermute((int)((lane ^ m) << 2), (int)__float_as_uint(v)));
}
__device__ __forceinline__ void slow_rows(const u16* wq, char* ws, unsigned bid, unsigned w, unsigned qb0) {
  unsigned lane;
  asm volatile("v_mbcnt_lo_u32_b32 %0, -1, 0\n\tv_mbcnt_hi_u32_b32 %0, -1, %0" : "=v"(lane));
  const unsigned c = lane & 15, g = lane >> 4;
  const unsigned x = (bid >> 3) & 7, h = (bid & 7) + ((bid >> 6) << 3), hp = ((h & 6) << 2) + (h & 1) + ((h >> 3) << 1);
  const auto rs = __builtin_amdgcn_make_buffer_rsrc(ws, 0, 0x4a00000, 0x20000);
#pragma unroll 1
  for (unsigned qb = qb0; qb < 4; ++qb) {
    const unsigned rb = (qb < 2 ? x : 15 - x) * 8 + 2 * w + (qb & 1);
    f32x4 q[8];
#pragma unroll
    for (int db = 0; db < 8; ++db) q[db] = f32x4{0, 0, 0, 0};
#pragma unroll 1
    for (unsigned ks = 0; ks < 16; ++ks) {
      const bf16x8 b = __builtin_bit_cast(bf16x8, __builtin_amdgcn_raw_buffer_load_b128(rs, lane * 16, 0x200000 + rb * 16384 + ks * 1024, 16));
#pragma unroll
      for (int db = 0; db < 8; ++db) {
        bf16x8 a;
#pragma unroll
        for (int j = 0; j < 8; ++j) a[j] = (short)wq[(size_t)(32 * ks + 8 * g + j) * 4096 + h * 128 + 16 * db + c];
        q[db] = __builtin_amdgcn_mfma_f32_16x16x32_bf16(a, b, q[db], 0, 0, 0);
      }
    }
    bf16x8 qf[4];
#pragma unroll
    for (int ds = 0; ds < 4; ++ds) qf[ds] = pk8(q[2 * ds], q[2 * ds + 1]);
    f32x4 o[8];
#pragma unroll
    for (int db = 0; db < 8; ++db) o[db] = f32x4{0, 0, 0, 0};
    float m = -1e30f, l = 0.f;
    const unsigned row = rb * 16 + c, nu = (rb >> 1) + 1;
#pragma unroll 1
    for (unsigned u = 0; u < nu; ++u) {
      const unsigned base = 0x400000 + h * 0x100000 + (u >> 1) * 32768 + (u & 1) * 8192;
      f32x4 s[2];
      float mx = -__builtin_inff();
#pragma unroll
      for (int kb = 0; kb < 2; ++kb) {
        s[kb] = f32x4{0, 0, 0, 0};
#pragma unroll
        for (int ds = 0; ds < 4; ++ds)
          s[kb] = __builtin_amdgcn_mfma_f32_16x16x32_bf16(
              __builtin_bit_cast(bf16x8, __builtin_amdgcn_raw_buffer_load_b128(rs, lane * 16, base + (kb * 4 + ds) * 1024, 16)), qf[ds], s[kb], 0, 0, 0);
#pragma unroll
        for (int i = 0; i < 4; ++i) {
          if (32 * u + 16 * kb + 4 * g + i > row) s[kb][i] = -__builtin_inff();
          mx = fmaxf(mx, s[kb][i]);
        }
      }
      mx = fmaxf(mx, xorl(mx, lane, 16)); mx = fmaxf(mx, xorl(mx, lane, 32));
      const float mn = fmaxf(m, mx), al = __builtin_amdgcn_exp2f(m - mn);
      m = mn;
      float ps = 0.f;
#pragma unroll
      for (int kb = 0; kb < 2; ++kb)
#pragma unroll
        for (int i = 0; i < 4; ++i) { s[kb][i] = __builtin_amdgcn_exp2f(s[kb][i] - mn); ps += s[kb][i]; }
      l = l * al + ps;
      const bf16x8 p = pk8(s[0], s[1]);
#pragma unroll
      for (int db = 0; db < 8; ++db) {
        o[db] *= al;
        o[db] = __builtin_amdgcn_mfma_f32_16x16x32_bf16(
            __builtin_bit_cast(bf16x8, __builtin_amdgcn_raw_buffer_load_b128(rs, lane * 16, base + 16384 + db * 1024, 16)), p, o[db], 0, 0, 0);
      }
    }
    l += xorl(l, lane, 16); l += xorl(l, lane, 32);
    const float il = 1.f / l;
#pragma unroll
    for (int ds = 0; ds < 4; ++ds)
      __builtin_amdgcn_raw_buffer_store_b128(__builtin_bit_cast(u32x4, pk8(o[2 * ds] * il, o[2 * ds + 1] * il)), rs, lane * 16,
                                             0x2400000 + rb * 131072 + (hp * 4 + ds) * 1024, 16);
  }
  asm volatile("s_waitcnt vmcnt(0)" ::: "memory");
}

#define CLOBBERS "v0", "v1", "v2", "v3", "v4", "v5", "v6", "v7", "v8", "v9", "v10", "v11", "v12", "v13", "v14", "v15", "v16", "v17", "v18", "v19", "v20", "v21", "v22", "v23", "v24", "v25", "v26", "v27", "v28", "v29", "v30", "v31", "v32", "v33", "v34", "v35", "v36", "v37", "v38", "v39", "v40", "v41", "v42", "v43", "v44", "v45", "v46", "v47", "v48", "v49", "v50", "v51", "v52", "v53", "v54", "v55", "v56", "v57", "v58", "v59", "v60", "v61", "v62", "v63", "v64", "v65", "v66", "v67", "v68", "v69", "v70", "v71", "v72", "v73", "v74", "v75", "v76", "v77", "v78", "v79", "v80", "v81", "v82", "v83", "v84", "v85", "v86", "v87", "v88", "v89", "v90", "v91", "v92", "v93", "v94", "v95", "v96", "v97", "v98", "v99", "v100", "v101", "v102", "v103", "v104", "v105", "v106", "v107", "v108", "v109", "v110", "v111", "v112", "v113", "v114", "v115", "v116", "v117", "v118", "v119", "v120", "v121", "v122", "v123", "v124", "v125", "v126", "v127", "v128", "v129", "v130", "v131", "v132", "v133", "v134", "v135", "v136", "v137", "v138", "v139", "v140", "v141", "v142", "v143", "v144", "v145", "v146", "v147", "v148", "v149", "v150", "v151", "v152", "v153", "v154", "v155", "v156", "v157", "v158", "v159", "v160", "v161", "v162", "v163", "v164", "v165", "v166", "v167", "v168", "v169", "v170", "v171", "v172", "v173", "v174", "v175", "v176", "v177", "v178", "v179", "v180", "v181", "v182", "v183", "v184", "v185", "v186", "v187", "v188", "v189", "v190", "v191", "v192", "v193", "v194", "v195", "v196", "v197", "v198", "v199", "v200", "v201", "v202", "v203", "v204", "v205", "v206", "v207", "v208", "v209", "v210", "v211", "v212", "v213", "v214", "v215", "v216", "v217", "v218", "v219", "v220", "v221", "v222", "v223", "v224", "v225", "v226", "v227", "v228", "v229", "v230", "v231", "v232", "v233", "v234", "v235", "v236", "v237", "v238", "v239", "v240", "v241", "v242", "v243", "v244", "v245", "v246", "v247", "v248", "v249", "v250", "v251", "v252", "v253", "v254", "v255", "a0", "a1", "a2", "a3", "a4", "a5", "a6", "a7", "a8", "a9", "a10", "a11", "a12", "a13", "a14", "a15", "a16", "a17", "a18", "a19", "a20", "a21", "a22", "a23", "a24", "a25", "a26", "a27", "a28", "a29", "a30", "a31", "a32", "a33", "a34", "a35", "a36", "a37", "a38", "a39", "a40", "a41", "a42", "a43", "a44", "a45", "a46", "a47", "a48", "a49", "a50", "a51", "a52", "a53", "a54", "a55", "a56", "a57", "a58", "a59", "a60", "a61", "a62", "a63", "a64", "a65", "a66", "a67", "a68", "a69", "a70", "a71", "a72", "a73", "a74", "a75", "a76", "a77", "a78", "a79", "a80", "a81", "a82", "a83", "a84", "a85", "a86", "a87", "a88", "a89", "a90", "a91", "a92", "a93", "a94", "a95", "a96", "a97", "a98", "a99", "a100", "a101", "a102", "a103", "a104", "a105", "a106", "a107", "a108", "a109", "a110", "a111", "a112", "a113", "a114", "a115", "a116", "a117", "a118", "a119", "a120", "a121", "a122", "a123", "a124", "a125", "a126", "a127", "a128", "a129", "a130", "a131", "a132", "a133", "a134", "a135", "a136", "a137", "a138", "a139", "a140", "a141", "a142", "a143", "a144", "a145", "a146", "a147", "a148", "a149", "a150", "a151", "a152", "a153", "a154", "a155", "a156", "a157", "a158", "a159", "a160", "a161", "a162", "a163", "a164", "a165", "a166", "a167", "a168", "a169", "a170", "a171", "a172", "a173", "a174", "a175", "a176", "a177", "a178", "a179", "a180", "a181", "a182", "a183", "a184", "a185", "a186", "a187", "a188", "a189", "a190", "a191", "a192", "a193", "a194", "a195", "a196", "a197", "a198", "a199", "a200", "a201", "a202", "a203", "a204", "a205", "a206", "a207", "a208", "a209", "a210", "a211", "a212", "a213", "a214", "a215", "a216", "a217", "a218", "a219", "a220", "a221", "a222", "a223", "a224", "a225", "a226", "a227", "a228", "a229", "a230", "a231", "a232", "a233", "a234", "a235", "a236", "a237", "a238", "a239", "a240", "a241", "a242", "a243", "a244", "a245", "a246", "a247", "a248", "a249", "a250", "a251", "a252", "a253", "a254", "a255", "s8", "s9", "s10", "s11", "s12", "s13", "s14", "s15", "s16", "s17", "s18", "s19", "s20", "s21", "s22", "s23", "s24", "s25", "s26", "s27", "s28", "s29", "s30", "s31", "s34", "s35", "s36", "s37", "s38", "s39", "s40", "s41", "s42", "s43", "s44", "s45", "s46", "s47", "s48", "s49", "s50", "s51", "s52", "s53", "s54", "s55", "s56", "s57", "s58", "s59", "s60", "s61", "s62", "s63", "s64", "s65", "s66", "s67", "s68", "s69", "s70", "s71", "s72", "s73", "s74", "s75", "s76", "s77", "s78", "s79", "s80", "s81", "s82", "s83", "s84", "s85", "s86", "s87", "s88", "s89", "s90", "s91", "s92", "s93", "s94", "s95", "vcc", "scc", "memory"
__global__ __launch_bounds__(256, 1) void fused_kernel(const u16* x, const u16* ckv, const u16* wq, const u16* wuk, const u16* wuv, const u16* wo,
                                                       const u16* wr, const u16* wg, const u16* wu, const u16* wd, u16* out, char* ws) {
  extern __shared__ __attribute__((aligned(16))) char lds[];
  const unsigned bw = blockIdx.x * 4 + __builtin_amdgcn_readfirstlane(threadIdx.x >> 6);
  unsigned code, tag, key;
  asm volatile(
R"A(.macro MF d, a, b, c
v_mfma_f32_16x16x32_bf16 \d, \a, \b, \c
.endm
.macro PK d, a, b
v_cvt_pk_bf16_f32 \d, \a, \b
.endm
.macro R16 d, a, o
ds_read_b128 \d, \a offset:\o
.endm
.macro RT d, a, o
ds_read_b64_tr_b16 \d, \a offset:\o
.endm
s_getpc_b64 s[40:41]
v_mbcnt_lo_u32_b32 v0, -1, 0
v_mbcnt_hi_u32_b32 v0, -1, v0
s_lshr_b32 s48, %[bw], 5
s_and_b32 s49, %[bw], 3
s_lshl2_add_u32 s48, s48, s49
s_lshl_b32 s48, s48, 10
s_and_b32 s41, s41, 0xffff
s_mov_b32 s42, 0x1a000
s_mov_b32 s43, 0x20000
v_lshlrev_b32 v242, 4, v0
buffer_load_dwordx4 v[8:11], v242, s[40:43], s48 offen
s_load_dwordx2 s[8:9], %[ka], 24
s_load_dwordx2 s[12:13], %[ka], 32
s_load_dwordx2 s[16:17], %[ka], 8
s_load_dwordx2 s[56:57], %[ka], 0
s_load_dwordx2 s[60:61], %[ka], 48
s_load_dwordx2 s[20:21], %[ka], 88
s_load_dwordx2 s[24:25], %[ka], 16
s_load_dwordx2 s[72:73], %[ka], 40
s_load_dwordx2 s[68:69], %[ka], 56
s_load_dwordx2 s[64:65], %[ka], 64
s_load_dwordx2 s[70:71], %[ka], 72
s_load_dwordx2 s[74:75], %[ka], 80
s_lshr_b32 s76, %[bw], 2
s_and_b32 s28, %[bw], 3
s_lshr_b32 s29, s76, 3
s_and_b32 s30, s76, 7
s_lshr_b32 s31, s29, 3
s_lshl_b32 s31, s31, 3
s_add_u32 s30, s30, s31
s_and_b32 s29, s29, 7
v_mov_b32 v245, 0
v_lshlrev_b32 v243, 5, v0
v_and_b32 v244, 0x3ff, v243
v_lshrrev_b32 v1, 4, v0
v_and_b32 v2, 15, v0
v_lshlrev_b32 v3, 1, v1
v_xor_b32 v3, v3, v2
v_lshlrev_b32 v3, 4, v3
v_lshl_add_u32 v232, v1, 13, v3
v_add_u32 v233, 31744, v232
v_xor_b32 v234, 0x80, v232
v_add_u32 v235, 95232, v234
v_add_u32 v234, 63488, v234
v_lshrrev_b32 v3, 3, v0
v_and_b32 v4, 7, v0
v_lshrrev_b32 v5, 1, v3
v_xor_b32 v4, v4, v5
v_lshlrev_b32 v4, 4, v4
v_lshl_add_u32 v228, v3, 10, v4
v_xor_b32 v229, 0x40, v228
v_add_u32 v230, 14336, v228
v_add_u32 v231, 21504, v229
v_add_u32 v229, 7168, v229
s_lshl_b32 s34, s28, 12
s_lshl_b32 s85, s28, 13
s_lshl_b32 s80, s28, 17
s_mov_b32 s86, s80
s_lshl_b32 s81, s28, 18
s_lshl_b32 s48, s29, 15
s_add_u32 s81, s81, s48
s_lshr_b32 s48, s30, 3
s_add_u32 s48, s48, s29
s_and_b32 s48, s48, 1
s_lshl_b32 s49, s48, 19
s_add_u32 s80, s80, s49
s_lshl_b32 s51, s48, 7
s_add_u32 s81, s81, s51
s_mov_b32 s78, s80
s_mov_b32 s84, s81
s_lshl_b32 s79, s76, 13
s_lshl_b32 s48, s28, 11
s_add_u32 s79, s79, s48
s_mov_b32 s11, 0x20000
s_mov_b32 s15, 0x20000
s_mov_b32 s19, 0x20000
s_mov_b32 s18, 0x200000
s_mov_b32 s58, 0x200000
s_mov_b32 s59, 0x20000
s_mov_b32 s62, 0x400
s_mov_b32 s63, 0x20000
s_lshl_b32 s31, s30, 8
s_sub_u32 s10, 0x400000, s31
s_mov_b32 s14, s10
s_waitcnt lgkmcnt(0)
s_and_b32 s57, s57, 0xffff
s_and_b32 s61, s61, 0xffff
s_and_b32 s17, s17, 0xffff
s_add_u32 s8, s8, s31
s_addc_u32 s9, s9, 0
s_and_b32 s9, s9, 0xffff
s_add_u32 s12, s12, s31
s_addc_u32 s13, s13, 0
s_and_b32 s13, s13, 0xffff
buffer_load_dwordx4 v[192:195], v243, s[56:59], s79 offen offset:0
buffer_load_dwordx4 v[196:199], v243, s[56:59], s79 offen offset:16
buffer_load_dwordx4 v[248:251], v244, s[60:63], 0 offen offset:0
buffer_load_dwordx4 v[252:255], v244, s[60:63], 0 offen offset:16
s_add_u32 m0, s34, 0x8000
s_nop 0
buffer_load_dwordx4 v228, s[16:19], s81 offen offset:0 lds
buffer_load_dwordx4 v229, s[16:19], s81 offen offset:1024 lds
buffer_load_dwordx4 v230, s[16:19], s81 offen offset:2048 lds
buffer_load_dwordx4 v231, s[16:19], s81 offen offset:3072 lds
s_add_u32 m0, s34, 0x0
s_nop 0
buffer_load_dwordx4 v232, s[8:11], s80 offen offset:0 lds
buffer_load_dwordx4 v233, s[8:11], s80 offen offset:1024 lds
buffer_load_dwordx4 v234, s[8:11], s80 offen offset:2048 lds
s_add_u32 s88, s20, 0x4900000
s_addc_u32 s89, s21, 0
s_load_dword s37, s[88:89], 0
v_lshrrev_b32 v3, 1, v2
v_xor_b32 v3, v3, v1
v_lshlrev_b32 v3, 4, v3
v_lshl_add_u32 v224, v2, 7, v3
v_xor_b32 v225, 0x40, v224
v_lshrrev_b32 v3, 2, v2
v_lshl_add_u32 v4, v1, 3, v3
v_lshlrev_b32 v4, 8, v4
v_and_b32 v5, 1, v1
v_lshl_add_u32 v5, v5, 2, v3
v_lshl_add_u32 v4, v5, 5, v4
v_and_b32 v5, 3, v0
v_lshl_add_u32 v4, v5, 3, v4
s_lshl_b32 s48, s28, 6
v_xor_b32 v226, s48, v4
v_xor_b32 v227, 32, v226
v_mov_b32 v240, v242
v_add_u32 v241, 0x18000, v242
s_lshl_b32 s82, s28, 8
s_xor_b32 s82, s82, s51
s_lshl_b32 s48, s29, 15
s_add_u32 s82, s82, s48
s_add_u32 s82, s82, 0x100000
s_lshr_b32 s49, s30, 3
s_mul_i32 s48, s49, 1
s_add_u32 s48, s48, 0
s_and_b32 s48, s48, 3
s_lshl_b32 s44, s48, 18
s_add_u32 s44, s44, s82
s_lshl_b32 s90, s48, 17
s_add_u32 s90, s90, 0x80000
s_mul_i32 s48, s49, 1
s_add_u32 s48, s48, 1
s_and_b32 s48, s48, 3
s_lshl_b32 s45, s48, 18
s_add_u32 s45, s45, s82
s_lshl_b32 s91, s48, 17
s_add_u32 s91, s91, 0x80000
s_mul_i32 s48, s49, 1
s_add_u32 s48, s48, 2
s_and_b32 s48, s48, 3
s_lshl_b32 s46, s48, 18
s_add_u32 s46, s46, s82
s_lshl_b32 s92, s48, 17
s_add_u32 s92, s92, 0x80000
s_mul_i32 s48, s49, 1
s_add_u32 s48, s48, 3
s_and_b32 s48, s48, 3
s_lshl_b32 s47, s48, 18
s_add_u32 s47, s47, s82
s_lshl_b32 s93, s48, 17
s_add_u32 s93, s93, 0x80000
s_lshl_b32 s49, s49, 1
s_add_u32 s49, s49, s29
s_and_b32 s49, s49, 7
s_mul_i32 s49, s49, 1
s_lshl_b32 s82, s49, 11
s_lshl_b32 s49, s49, 19
s_add_u32 s86, s86, s49
s_add_u32 s24, s24, s31
s_addc_u32 s25, s25, 0
s_and_b32 s25, s25, 0xffff
s_mov_b32 s26, s10
s_mov_b32 s27, 0x20000
s_lshl_b32 s48, s30, 20
s_add_u32 s48, s48, 0x400000
s_lshr_b32 s49, s29, 1
s_lshl_b32 s49, s49, 15
s_add_u32 s49, s49, s48
s_and_b32 s83, s29, 1
s_lshl_b32 s83, s83, 13
s_add_u32 s49, s49, s83
s_lshl_b32 s83, s28, 10
s_add_u32 s83, s83, s49
s_lshl_b32 s95, s28, 11
s_add_u32 s95, s95, s49
s_add_u32 s95, s95, 0x4000
s_add_u32 s94, s83, 0x1000
s_cmp_ge_u32 %[bw], 448
s_cselect_b32 s68, s64, s68
s_cselect_b32 s69, s65, s69
s_cselect_b32 s49, 448, 0
s_and_b32 s69, s69, 0xffff
s_sub_u32 s49, %[bw], s49
s_lshr_b32 s65, s49, 4
s_lshl_b32 s65, s65, 7
s_and_b32 s49, s49, 15
s_mul_i32 s49, s49, 114688
s_add_u32 s65, s65, s49
s_cmp_lt_u32 %[bw], 896
s_cselect_b32 s67, 1835008, 0
s_mov_b32 s66, %[bw]
s_waitcnt lgkmcnt(0)
s_add_u32 s37, s37, 1
s_add_u32 s88, s20, s48
s_addc_u32 s89, s21, 0
s_sub_u32 s40, s88, 0xc000
s_subb_u32 s41, s89, 0
s_and_b32 s89, s89, 0xffff
s_and_b32 s41, s41, 0xffff
s_lshl_b32 s42, s29, 16
s_sub_u32 s42, 0x100000, s42
s_add_u32 s43, s42, 0xc000
s_and_b32 s21, s21, 0xffff
s_mov_b32 s22, 0x4c00000
s_mov_b32 s23, 0x20000
s_add_u32 s50, s34, 0x20000
s_mov_b64 s[38:39], 0
s_mov_b32 s87, 0x40000
s_lshl_b32 s48, s29, 3
s_lshl_b32 s49, s28, 1
s_add_u32 s48, s48, s49
s_lshl_b32 s52, s48, 14
s_add_u32 s52, s52, 0x200000
s_add_u32 s53, s52, 0x4000
s_lshl_b32 s35, s48, 17
s_sub_u32 s48, 15, s29
s_lshl_b32 s48, s48, 3
s_add_u32 s48, s48, s49
s_lshl_b32 s54, s48, 14
s_add_u32 s54, s54, 0x200000
s_add_u32 s55, s54, 0x4000
s_lshl_b32 s36, s48, 17
s_and_b32 s48, s30, 6
s_lshl_b32 s48, s48, 2
s_and_b32 s49, s30, 1
s_add_u32 s48, s48, s49
s_lshr_b32 s49, s30, 3
s_lshl_b32 s49, s49, 1
s_add_u32 s48, s48, s49
s_lshl_b32 s48, s48, 12
s_add_u32 s48, s48, 0x2400000
s_add_u32 s35, s35, s48
s_add_u32 s36, s36, s48
buffer_load_dwordx4 v235, s[8:11], s80 offen offset:3072 lds
s_add_u32 m0, s34, 0x4000
s_nop 0
buffer_load_dwordx4 v232, s[12:15], s80 offen offset:0 lds
buffer_load_dwordx4 v233, s[12:15], s80 offen offset:1024 lds
buffer_load_dwordx4 v234, s[12:15], s80 offen offset:2048 lds
buffer_load_dwordx4 v235, s[12:15], s80 offen offset:3072 lds
s_waitcnt vmcnt(12)
v_lshlrev_b32 v200, 16, v192
v_and_b32 v201, 0xffff0000, v192
v_lshlrev_b32 v202, 16, v193
v_and_b32 v203, 0xffff0000, v193
v_lshlrev_b32 v204, 16, v194
v_and_b32 v205, 0xffff0000, v194
v_lshlrev_b32 v206, 16, v195
v_and_b32 v207, 0xffff0000, v195
v_lshlrev_b32 v208, 16, v196
v_and_b32 v209, 0xffff0000, v196
v_lshlrev_b32 v210, 16, v197
s_xor_b32 s80, s78, 0x80000
s_xor_b32 s81, s84, 0x80
s_add_u32 m0, s34, 0x14000
s_nop 0
buffer_load_dwordx4 v228, s[16:19], s81 offen offset:0 lds
v_and_b32 v211, 0xffff0000, v197
v_lshlrev_b32 v212, 16, v198
v_and_b32 v213, 0xffff0000, v198
v_lshlrev_b32 v214, 16, v199
v_and_b32 v215, 0xffff0000, v199
v_add_f32 v236, v200, v201
v_add_f32 v237, v202, v203
v_add_f32 v236, v236, v204
v_add_f32 v237, v237, v205
v_add_f32 v236, v236, v206
buffer_load_dwordx4 v229, s[16:19], s81 offen offset:1024 lds
v_add_f32 v237, v237, v207
v_add_f32 v236, v236, v208
v_add_f32 v237, v237, v209
v_add_f32 v236, v236, v210
v_add_f32 v237, v237, v211
v_add_f32 v236, v236, v212
v_add_f32 v237, v237, v213
v_add_f32 v236, v236, v214
v_add_f32 v237, v237, v215
v_add_f32 v236, v236, v237
s_nop 1
v_add_f32_dpp v236, v236, v236 quad_perm:[1,0,3,2] row_mask:0xf bank_mask:0xf
buffer_load_dwordx4 v230, s[16:19], s81 offen offset:2048 lds
s_nop 0
v_add_f32_dpp v236, v236, v236 quad_perm:[2,3,0,1] row_mask:0xf bank_mask:0xf
s_nop 1
v_add_f32_dpp v236, v236, v236 row_half_mirror row_mask:0xf bank_mask:0xf
s_nop 1
v_add_f32_dpp v236, v236, v236 row_mirror row_mask:0xf bank_mask:0xf
v_mov_b32 v237, v236
s_nop 1
v_permlane16_swap_b32 v236, v237
v_add_f32 v236, v236, v237
v_mul_f32 v236, 0x3b000000, v236
v_sub_f32 v216, v200, v236
v_mul_f32 v237, v216, v216
v_sub_f32 v217, v201, v236
buffer_load_dwordx4 v231, s[16:19], s81 offen offset:3072 lds
v_mul_f32 v238, v217, v217
v_sub_f32 v218, v202, v236
v_fmac_f32 v237, v218, v218
v_sub_f32 v219, v203, v236
v_fmac_f32 v238, v219, v219
v_sub_f32 v216, v204, v236
v_fmac_f32 v237, v216, v216
v_sub_f32 v217, v205, v236
v_fmac_f32 v238, v217, v217
v_sub_f32 v218, v206, v236
s_add_u32 m0, s34, 0xc000
s_nop 0
buffer_load_dwordx4 v232, s[8:11], s80 offen offset:0 lds
v_fmac_f32 v237, v218, v218
v_sub_f32 v219, v207, v236
v_fmac_f32 v238, v219, v219
v_sub_f32 v216, v208, v236
v_fmac_f32 v237, v216, v216
v_sub_f32 v217, v209, v236
v_fmac_f32 v238, v217, v217
v_sub_f32 v218, v210, v236
v_fmac_f32 v237, v218, v218
v_sub_f32 v219, v211, v236
v_fmac_f32 v238, v219, v219
buffer_load_dwordx4 v233, s[8:11], s80 offen offset:1024 lds
v_sub_f32 v216, v212, v236
v_fmac_f32 v237, v216, v216
v_sub_f32 v217, v213, v236
v_fmac_f32 v238, v217, v217
v_sub_f32 v218, v214, v236
v_fmac_f32 v237, v218, v218
v_sub_f32 v219, v215, v236
v_fmac_f32 v238, v219, v219
v_add_f32 v237, v237, v238
s_nop 1
v_add_f32_dpp v237, v237, v237 quad_perm:[1,0,3,2] row_mask:0xf bank_mask:0xf
buffer_load_dwordx4 v234, s[8:11], s80 offen offset:2048 lds
s_nop 0
v_add_f32_dpp v237, v237, v237 quad_perm:[2,3,0,1] row_mask:0xf bank_mask:0xf
s_nop 1
v_add_f32_dpp v237, v237, v237 row_half_mirror row_mask:0xf bank_mask:0xf
s_nop 1
v_add_f32_dpp v237, v237, v237 row_mirror row_mask:0xf bank_mask:0xf
v_mov_b32 v238, v237
s_nop 1
v_permlane16_swap_b32 v237, v238
v_add_f32 v237, v237, v238
v_mul_f32 v237, 0x3b000000, v237
PK v237,v237,v237
v_lshlrev_b32 v237, 16, v237
v_add_f32 v237, 0x3727c5ac, v237
buffer_load_dwordx4 v235, s[8:11], s80 offen offset:3072 lds
PK v237,v237,v237
v_lshlrev_b32 v237, 16, v237
v_sqrt_f32 v237, v237
PK v236,v236,v236
v_lshlrev_b32 v236, 16, v236
PK v237,v237,v237
v_lshlrev_b32 v237, 16, v237
v_rcp_f32 v238, v237
v_sub_f32 v200, v200, v236
v_sub_f32 v201, v201, v236
PK v192,v200,v201
s_add_u32 m0, s34, 0x10000
s_nop 0
buffer_load_dwordx4 v232, s[12:15], s80 offen offset:0 lds
v_sub_f32 v202, v202, v236
v_sub_f32 v203, v203, v236
PK v193,v202,v203
v_sub_f32 v204, v204, v236
v_sub_f32 v205, v205, v236
PK v194,v204,v205
v_sub_f32 v206, v206, v236
v_sub_f32 v207, v207, v236
PK v195,v206,v207
v_sub_f32 v208, v208, v236
buffer_load_dwordx4 v233, s[12:15], s80 offen offset:1024 lds
v_sub_f32 v209, v209, v236
)A"
R"A(PK v196,v208,v209
v_sub_f32 v210, v210, v236
v_sub_f32 v211, v211, v236
PK v197,v210,v211
v_sub_f32 v212, v212, v236
v_sub_f32 v213, v213, v236
PK v198,v212,v213
v_sub_f32 v214, v214, v236
v_sub_f32 v215, v215, v236
PK v199,v214,v215
buffer_load_dwordx4 v234, s[12:15], s80 offen offset:2048 lds
v_lshlrev_b32 v200, 16, v192
v_and_b32 v201, 0xffff0000, v192
v_lshlrev_b32 v202, 16, v193
v_and_b32 v203, 0xffff0000, v193
v_lshlrev_b32 v204, 16, v194
v_and_b32 v205, 0xffff0000, v194
v_lshlrev_b32 v206, 16, v195
v_and_b32 v207, 0xffff0000, v195
v_lshlrev_b32 v208, 16, v196
v_and_b32 v209, 0xffff0000, v196
buffer_load_dwordx4 v235, s[12:15], s80 offen offset:3072 lds
v_lshlrev_b32 v210, 16, v197
v_and_b32 v211, 0xffff0000, v197
v_lshlrev_b32 v212, 16, v198
v_and_b32 v213, 0xffff0000, v198
v_lshlrev_b32 v214, 16, v199
v_and_b32 v215, 0xffff0000, v199
v_mul_f32 v216, v200, v238
v_fma_f32 v200, -v216, v237, v200
v_fma_f32 v200, v200, v238, v216
v_mul_f32 v217, v201, v238
s_xor_b32 s80, s78, 0x100000
s_xor_b32 s81, s84, 0x100
s_add_u32 m0, s34, 0x20000
s_nop 0
buffer_load_dwordx4 v228, s[16:19], s81 offen offset:0 lds
v_fma_f32 v201, -v217, v237, v201
v_fma_f32 v201, v201, v238, v217
v_mul_f32 v218, v202, v238
v_fma_f32 v202, -v218, v237, v202
v_fma_f32 v202, v202, v238, v218
v_mul_f32 v219, v203, v238
v_fma_f32 v203, -v219, v237, v203
v_fma_f32 v203, v203, v238, v219
v_mul_f32 v216, v204, v238
v_fma_f32 v204, -v216, v237, v204
v_fma_f32 v204, v204, v238, v216
buffer_load_dwordx4 v229, s[16:19], s81 offen offset:1024 lds
v_mul_f32 v217, v205, v238
v_fma_f32 v205, -v217, v237, v205
v_fma_f32 v205, v205, v238, v217
v_mul_f32 v218, v206, v238
v_fma_f32 v206, -v218, v237, v206
v_fma_f32 v206, v206, v238, v218
v_mul_f32 v219, v207, v238
v_fma_f32 v207, -v219, v237, v207
v_fma_f32 v207, v207, v238, v219
v_mul_f32 v216, v208, v238
buffer_load_dwordx4 v230, s[16:19], s81 offen offset:2048 lds
v_fma_f32 v208, -v216, v237, v208
v_fma_f32 v208, v208, v238, v216
v_mul_f32 v217, v209, v238
v_fma_f32 v209, -v217, v237, v209
v_fma_f32 v209, v209, v238, v217
v_mul_f32 v218, v210, v238
v_fma_f32 v210, -v218, v237, v210
v_fma_f32 v210, v210, v238, v218
v_mul_f32 v219, v211, v238
v_fma_f32 v211, -v219, v237, v211
buffer_load_dwordx4 v231, s[16:19], s81 offen offset:3072 lds
v_fma_f32 v211, v211, v238, v219
v_mul_f32 v216, v212, v238
v_fma_f32 v212, -v216, v237, v212
v_fma_f32 v212, v212, v238, v216
v_mul_f32 v217, v213, v238
v_fma_f32 v213, -v217, v237, v213
v_fma_f32 v213, v213, v238, v217
v_mul_f32 v218, v214, v238
v_fma_f32 v214, -v218, v237, v214
v_fma_f32 v214, v214, v238, v218
v_mul_f32 v219, v215, v238
s_add_u32 m0, s34, 0x18000
s_nop 0
buffer_load_dwordx4 v232, s[8:11], s80 offen offset:0 lds
v_fma_f32 v215, -v219, v237, v215
v_fma_f32 v215, v215, v238, v219
PK v192,v200,v201
PK v193,v202,v203
PK v194,v204,v205
PK v195,v206,v207
PK v196,v208,v209
PK v197,v210,v211
PK v198,v212,v213
PK v199,v214,v215
buffer_load_dwordx4 v233, s[8:11], s80 offen offset:1024 lds
v_dot2_f32_bf16 v239, v192, v248, 0
v_dot2_f32_bf16 v239, v193, v249, v239
v_dot2_f32_bf16 v239, v194, v250, v239
v_dot2_f32_bf16 v239, v195, v251, v239
v_dot2_f32_bf16 v239, v196, v252, v239
v_dot2_f32_bf16 v239, v197, v253, v239
v_dot2_f32_bf16 v239, v198, v254, v239
v_dot2_f32_bf16 v239, v199, v255, v239
v_mul_f32 v200, 0x3e0293ee, v200
v_mul_f32 v201, 0x3e0293ee, v201
buffer_load_dwordx4 v234, s[8:11], s80 offen offset:2048 lds
v_mul_f32 v202, 0x3e0293ee, v202
v_mul_f32 v203, 0x3e0293ee, v203
v_mul_f32 v204, 0x3e0293ee, v204
v_mul_f32 v205, 0x3e0293ee, v205
v_mul_f32 v206, 0x3e0293ee, v206
v_mul_f32 v207, 0x3e0293ee, v207
v_mul_f32 v208, 0x3e0293ee, v208
v_mul_f32 v209, 0x3e0293ee, v209
v_mul_f32 v210, 0x3e0293ee, v210
v_mul_f32 v211, 0x3e0293ee, v211
v_mul_f32 v212, 0x3e0293ee, v212
buffer_load_dwordx4 v235, s[8:11], s80 offen offset:3072 lds
v_mul_f32 v213, 0x3e0293ee, v213
v_mul_f32 v214, 0x3e0293ee, v214
v_mul_f32 v215, 0x3e0293ee, v215
PK v216,v200,v201
PK v217,v202,v203
PK v218,v204,v205
PK v219,v206,v207
PK v220,v208,v209
PK v221,v210,v211
PK v222,v212,v213
s_add_u32 m0, s34, 0x1c000
s_nop 0
buffer_load_dwordx4 v232, s[12:15], s80 offen offset:0 lds
PK v223,v214,v215
v_lshlrev_b32 v236, 4, v242
v_and_b32 v236, 0x1f00, v236
v_lshrrev_b32 v237, 5, v242
v_and_b32 v237, 16, v237
s_lshl_b32 s51, s28, 5
s_add_u32 s51, s51, 0x24000
v_add3_u32 v244, v236, v237, s51
ds_write_b128 v244, v[192:195] offset:0
ds_write_b128 v244, v[196:199] offset:128
ds_write_b128 v244, v[216:219] offset:8192
buffer_load_dwordx4 v233, s[12:15], s80 offen offset:1024 lds
ds_write_b128 v244, v[220:223] offset:8320
v_add_f32_dpp v239, v239, v239 quad_perm:[1,0,3,2] row_mask:0xf bank_mask:0xf
s_nop 1
v_add_f32_dpp v239, v239, v239 quad_perm:[2,3,0,1] row_mask:0xf bank_mask:0xf
s_nop 1
v_add_f32_dpp v239, v239, v239 row_half_mirror row_mask:0xf bank_mask:0xf
s_nop 1
v_add_f32_dpp v239, v239, v239 row_mirror row_mask:0xf bank_mask:0xf
v_mov_b32 v236, v239
s_nop 1
v_permlane16_swap_b32 v239, v236
v_add_f32 v239, v239, v236
PK v239,v239,v239
v_lshlrev_b32 v239, 16, v239
buffer_load_dwordx4 v234, s[12:15], s80 offen offset:2048 lds
v_ashrrev_i32 v236, 31, v239
v_or_b32 v236, 0x80000000, v236
v_xor_b32 v239, v239, v236
v_and_b32 v239, 0xffff0000, v239
s_lshl_b32 s51, s76, 3
s_lshl_b32 s79, s28, 1
s_add_u32 s51, s51, s79
s_sub_u32 s51, 0xffff, s51
v_lshrrev_b32 v236, 9, v242
v_sub_u32 v236, s51, v236
buffer_load_dwordx4 v235, s[12:15], s80 offen offset:3072 lds
v_or_b32 v238, v239, v236
v_mov_b32 v239, v238
s_nop 1
v_permlane32_swap_b32 v238, v239
v_max_u32 v238, v238, v239
v_mov_b32 v239, s37
s_xor_b32 s80, s78, 0x180000
s_xor_b32 s81, s84, 0x180
s_waitcnt vmcnt(24) lgkmcnt(0)
s_barrier
RT a[0:1],v226,0
RT a[2:3],v226,1024
RT a[4:5],v227,0
RT a[6:7],v227,1024
RT a[8:9],v226,16384
RT a[10:11],v226,17408
RT a[12:13],v227,16384
RT a[14:15],v227,17408
R16 v[128:131],v224,32768
R16 v[132:135],v225,32768
R16 v[136:139],v224,34816
R16 v[140:143],v225,34816
R16 v[144:147],v224,36864
R16 v[148:151],v225,36864
R16 v[152:155],v224,38912
R16 v[156:159],v225,38912
RT a[16:17],v226,8192
RT a[18:19],v226,9216
RT a[20:21],v227,8192
RT a[22:23],v227,9216
RT a[24:25],v226,24576
RT a[26:27],v226,25600
RT a[28:29],v227,24576
RT a[30:31],v227,25600
R16 v[160:163],v224,40960
R16 v[164:167],v225,40960
R16 v[168:171],v224,43008
R16 v[172:175],v225,43008
R16 v[176:179],v224,45056
R16 v[180:183],v225,45056
s_waitcnt lgkmcnt(0)
MF v[0:3],a[0:3],v[128:131],0
R16 v[184:187],v224,47104
R16 v[188:191],v225,47104
MF v[0:3],a[16:19],v[132:135],v[0:3]
MF v[4:7],a[4:7],v[128:131],0
v_add_u32 v224, 0xc000, v224
v_add_u32 v225, 0xc000, v225
MF v[4:7],a[20:23],v[132:135],v[4:7]
v_add_u32 v226, 0xc000, v226
v_add_u32 v227, 0xc000, v227
MF v[64:67],v[128:131],a[8:11],0
MF v[64:67],v[132:135],a[24:27],v[64:67]
MF v[72:75],v[128:131],a[12:15],0
MF v[72:75],v[132:135],a[28:31],v[72:75]
s_waitcnt vmcnt(12) lgkmcnt(0)
s_barrier
MF v[8:11],a[0:3],v[136:139],0
R16 v[128:131],v224,32768
s_add_u32 m0, s34, 0x8000
MF v[8:11],a[16:19],v[140:143],v[8:11]
R16 v[132:135],v225,32768
buffer_load_dwordx4 v228, s[16:19], s81 offen offset:0 lds
MF v[12:15],a[4:7],v[136:139],0
RT a[32:33],v226,0
s_lshr_b32 s79, s34, 1
MF v[12:15],a[20:23],v[140:143],v[12:15]
s_add_u32 s79, s79, 0x24000
v_add_u32 v243, s79, v242
MF v[68:71],v[136:139],a[8:11],0
RT a[34:35],v226,1024
buffer_load_dwordx4 v229, s[16:19], s81 offen offset:1024 lds
MF v[68:71],v[140:143],a[24:27],v[68:71]
R16 v[192:195],v243,0
R16 v[196:199],v243,1024
MF v[76:79],v[136:139],a[12:15],0
RT a[36:37],v227,0
R16 v[216:219],v243,8192
MF v[76:79],v[140:143],a[28:31],v[76:79]
R16 v[220:223],v243,9216
v_lshlrev_b32 v236, 1, v242
MF v[16:19],a[0:3],v[144:147],0
R16 v[136:139],v224,34816
v_and_b32 v236, 0x700, v236
MF v[16:19],a[16:19],v[148:151],v[16:19]
R16 v[140:143],v225,34816
buffer_load_dwordx4 v230, s[16:19], s81 offen offset:2048 lds
MF v[20:23],a[4:7],v[144:147],0
RT a[38:39],v227,1024
v_and_b32 v237, 0x70, v242
MF v[20:23],a[20:23],v[148:151],v[20:23]
v_or_b32 v244, v236, v237
s_lshr_b32 s79, s76, 1
MF v[80:83],v[144:147],a[8:11],0
RT a[40:41],v226,16384
s_lshl_b32 s79, s79, 14
MF v[80:83],v[148:151],a[24:27],v[80:83]
buffer_load_dwordx4 v231, s[16:19], s81 offen offset:3072 lds
s_and_b32 s51, s76, 1
MF v[88:91],v[144:147],a[12:15],0
RT a[42:43],v226,17408
s_lshl_b32 s51, s51, 7
MF v[88:91],v[148:151],a[28:31],v[88:91]
s_add_u32 s79, s79, s51
s_add_u32 s79, s79, s34
MF v[24:27],a[0:3],v[152:155],0
R16 v[144:147],v224,36864
s_waitcnt lgkmcnt(6)
MF v[24:27],a[16:19],v[156:159],v[24:27]
R16 v[148:151],v225,36864
buffer_store_dwordx4 v[192:195], v244, s[20:23], s79 offen offset:0 sc1
MF v[28:31],a[4:7],v[152:155],0
RT a[44:45],v227,16384
s_add_u32 m0, s34, 0x0
MF v[28:31],a[20:23],v[156:159],v[28:31]
buffer_load_dwordx4 v232, s[8:11], s80 offen offset:0 lds
buffer_store_dwordx4 v[196:199], v244, s[20:23], s79 offen offset:2048 sc1
MF v[84:87],v[152:155],a[8:11],0
RT a[46:47],v227,17408
s_add_u32 s79, s79, 0x200000
MF v[84:87],v[156:159],a[24:27],v[84:87]
buffer_store_dwordx4 v[216:219], v244, s[20:23], s79 offen offset:0 sc1
buffer_store_dwordx4 v[220:223], v244, s[20:23], s79 offen offset:2048 sc1
MF v[92:95],v[152:155],a[12:15],0
RT a[48:49],v226,8192
buffer_load_dwordx4 v233, s[8:11], s80 offen offset:1024 lds
MF v[92:95],v[156:159],a[28:31],v[92:95]
MF v[32:35],a[0:3],v[160:163],0
R16 v[152:155],v224,38912
MF v[32:35],a[16:19],v[164:167],v[32:35]
R16 v[156:159],v225,38912
MF v[36:39],a[4:7],v[160:163],0
RT a[50:51],v226,9216
v_mov_b32 v236, 0
MF v[36:39],a[20:23],v[164:167],v[36:39]
buffer_load_dwordx4 v234, s[8:11], s80 offen offset:2048 lds
s_lshl_b32 s51, s76, 2
MF v[96:99],v[160:163],a[8:11],0
RT a[52:53],v227,8192
s_add_u32 s51, s51, s28
MF v[96:99],v[164:167],a[24:27],v[96:99]
s_lshl_b32 s51, s51, 3
s_add_u32 s51, s51, 0x4902000
MF v[104:107],v[160:163],a[12:15],0
RT a[54:55],v227,9216
MF v[104:107],v[164:167],a[28:31],v[104:107]
buffer_load_dwordx4 v235, s[8:11], s80 offen offset:3072 lds
MF v[40:43],a[0:3],v[168:171],0
R16 v[160:163],v224,40960
MF v[40:43],a[16:19],v[172:175],v[40:43]
R16 v[164:167],v225,40960
MF v[44:47],a[4:7],v[168:171],0
RT a[56:57],v226,24576
MF v[44:47],a[20:23],v[172:175],v[44:47]
MF v[100:103],v[168:171],a[8:11],0
RT a[58:59],v226,25600
s_add_u32 m0, s34, 0x4000
MF v[100:103],v[172:175],a[24:27],v[100:103]
buffer_load_dwordx4 v232, s[12:15], s80 offen offset:0 lds
MF v[108:111],v[168:171],a[12:15],0
RT a[60:61],v227,24576
MF v[108:111],v[172:175],a[28:31],v[108:111]
MF v[48:51],a[0:3],v[176:179],0
R16 v[168:171],v224,43008
buffer_load_dwordx4 v233, s[12:15], s80 offen offset:1024 lds
MF v[48:51],a[16:19],v[180:183],v[48:51]
R16 v[172:175],v225,43008
MF v[52:55],a[4:7],v[176:179],0
RT a[62:63],v227,25600
MF v[52:55],a[20:23],v[180:183],v[52:55]
MF v[112:115],v[176:179],a[8:11],0
MF v[112:115],v[180:183],a[24:27],v[112:115]
buffer_load_dwordx4 v234, s[12:15], s80 offen offset:2048 lds
MF v[120:123],v[176:179],a[12:15],0
MF v[120:123],v[180:183],a[28:31],v[120:123]
MF v[56:59],a[0:3],v[184:187],0
R16 v[176:179],v224,45056
MF v[56:59],a[16:19],v[188:191],v[56:59]
R16 v[180:183],v225,45056
buffer_load_dwordx4 v235, s[12:15], s80 offen offset:3072 lds
MF v[60:63],a[4:7],v[184:187],0
MF v[60:63],a[20:23],v[188:191],v[60:63]
MF v[116:119],v[184:187],a[8:11],0
MF v[116:119],v[188:191],a[24:27],v[116:119]
MF v[124:127],v[184:187],a[12:15],0
s_xor_b32 s80, s78, 0x200000
s_xor_b32 s81, s84, 0x200
MF v[124:127],v[188:191],a[28:31],v[124:127]
s_waitcnt lgkmcnt(2)
MF v[0:3],a[32:35],v[128:131],v[0:3]
R16 v[184:187],v224,47104
R16 v[188:191],v225,47104
MF v[0:3],a[48:51],v[132:135],v[0:3]
MF v[4:7],a[36:39],v[128:131],v[4:7]
v_add_u32 v224, 0xc000, v224
v_add_u32 v225, 0xc000, v225
MF v[4:7],a[52:55],v[132:135],v[4:7]
v_add_u32 v226, 0xc000, v226
v_add_u32 v227, 0xc000, v227
MF v[64:67],v[128:131],a[40:43],v[64:67]
MF v[64:67],v[132:135],a[56:59],v[64:67]
MF v[72:75],v[128:131],a[44:47],v[72:75]
MF v[72:75],v[132:135],a[60:63],v[72:75]
s_waitcnt vmcnt(16) lgkmcnt(0)
s_barrier
MF v[8:11],a[32:35],v[136:139],v[8:11]
R16 v[128:131],v224,32768
s_add_u32 m0, s34, 0x14000
MF v[8:11],a[48:51],v[140:143],v[8:11]
R16 v[132:135],v225,32768
buffer_load_dwordx4 v228, s[16:19], s81 offen offset:0 lds
MF v[12:15],a[36:39],v[136:139],v[12:15]
RT a[64:65],v226,0
MF v[12:15],a[52:55],v[140:143],v[12:15]
MF v[68:71],v[136:139],a[40:43],v[68:71]
RT a[66:67],v226,1024
buffer_load_dwordx4 v229, s[16:19], s81 offen offset:1024 lds
MF v[68:71],v[140:143],a[56:59],v[68:71]
v_lshrrev_b32 v254, 4, v242
v_lshrrev_b32 v253, 1, v254
MF v[76:79],v[136:139],a[44:47],v[76:79]
RT a[68:69],v227,0
v_lshlrev_b32 v253, 16, v253
MF v[76:79],v[140:143],a[60:63],v[76:79]
v_and_b32 v252, 1, v254
v_lshl_add_u32 v253, v252, 7, v253
MF v[16:19],a[32:35],v[144:147],v[16:19]
R16 v[136:139],v224,34816
s_lshl_b32 s49, s28, 20
MF v[16:19],a[48:51],v[148:151],v[16:19]
)A"
R"A(R16 v[140:143],v225,34816
buffer_load_dwordx4 v230, s[16:19], s81 offen offset:2048 lds
MF v[20:23],a[36:39],v[144:147],v[20:23]
RT a[70:71],v227,1024
s_lshl_b32 s31, s29, 13
MF v[20:23],a[52:55],v[148:151],v[20:23]
s_add_u32 s49, s49, s31
MF v[80:83],v[144:147],a[40:43],v[80:83]
RT a[72:73],v226,16384
s_mov_b32 exec_hi, 0
buffer_load_dword v252, v253, s[24:27], s49 offen offset:0
s_mov_b64 exec, -1
MF v[80:83],v[148:151],a[56:59],v[80:83]
buffer_load_dwordx4 v231, s[16:19], s81 offen offset:3072 lds
MF v[88:91],v[144:147],a[44:47],v[88:91]
RT a[74:75],v226,17408
MF v[88:91],v[148:151],a[60:63],v[88:91]
MF v[24:27],a[32:35],v[152:155],v[24:27]
R16 v[144:147],v224,36864
MF v[24:27],a[48:51],v[156:159],v[24:27]
R16 v[148:151],v225,36864
MF v[28:31],a[36:39],v[152:155],v[28:31]
RT a[76:77],v227,16384
s_add_u32 m0, s34, 0xc000
MF v[28:31],a[52:55],v[156:159],v[28:31]
buffer_load_dwordx4 v232, s[8:11], s80 offen offset:0 lds
MF v[84:87],v[152:155],a[40:43],v[84:87]
RT a[78:79],v227,17408
MF v[84:87],v[156:159],a[56:59],v[84:87]
MF v[92:95],v[152:155],a[44:47],v[92:95]
RT a[80:81],v226,8192
buffer_load_dwordx4 v233, s[8:11], s80 offen offset:1024 lds
MF v[92:95],v[156:159],a[60:63],v[92:95]
MF v[32:35],a[32:35],v[160:163],v[32:35]
R16 v[152:155],v224,38912
MF v[32:35],a[48:51],v[164:167],v[32:35]
R16 v[156:159],v225,38912
MF v[36:39],a[36:39],v[160:163],v[36:39]
RT a[82:83],v226,9216
MF v[36:39],a[52:55],v[164:167],v[36:39]
buffer_load_dwordx4 v234, s[8:11], s80 offen offset:2048 lds
MF v[96:99],v[160:163],a[40:43],v[96:99]
RT a[84:85],v227,8192
MF v[96:99],v[164:167],a[56:59],v[96:99]
MF v[104:107],v[160:163],a[44:47],v[104:107]
RT a[86:87],v227,9216
MF v[104:107],v[164:167],a[60:63],v[104:107]
buffer_load_dwordx4 v235, s[8:11], s80 offen offset:3072 lds
MF v[40:43],a[32:35],v[168:171],v[40:43]
R16 v[160:163],v224,40960
MF v[40:43],a[48:51],v[172:175],v[40:43]
R16 v[164:167],v225,40960
MF v[44:47],a[36:39],v[168:171],v[44:47]
RT a[88:89],v226,24576
MF v[44:47],a[52:55],v[172:175],v[44:47]
MF v[100:103],v[168:171],a[40:43],v[100:103]
RT a[90:91],v226,25600
s_add_u32 m0, s34, 0x10000
MF v[100:103],v[172:175],a[56:59],v[100:103]
buffer_load_dwordx4 v232, s[12:15], s80 offen offset:0 lds
MF v[108:111],v[168:171],a[44:47],v[108:111]
RT a[92:93],v227,24576
MF v[108:111],v[172:175],a[60:63],v[108:111]
MF v[48:51],a[32:35],v[176:179],v[48:51]
R16 v[168:171],v224,43008
buffer_load_dwordx4 v233, s[12:15], s80 offen offset:1024 lds
MF v[48:51],a[48:51],v[180:183],v[48:51]
R16 v[172:175],v225,43008
MF v[52:55],a[36:39],v[176:179],v[52:55]
RT a[94:95],v227,25600
MF v[52:55],a[52:55],v[180:183],v[52:55]
MF v[112:115],v[176:179],a[40:43],v[112:115]
MF v[112:115],v[180:183],a[56:59],v[112:115]
buffer_load_dwordx4 v234, s[12:15], s80 offen offset:2048 lds
MF v[120:123],v[176:179],a[44:47],v[120:123]
MF v[120:123],v[180:183],a[60:63],v[120:123]
MF v[56:59],a[32:35],v[184:187],v[56:59]
R16 v[176:179],v224,45056
MF v[56:59],a[48:51],v[188:191],v[56:59]
R16 v[180:183],v225,45056
buffer_load_dwordx4 v235, s[12:15], s80 offen offset:3072 lds
MF v[60:63],a[36:39],v[184:187],v[60:63]
MF v[60:63],a[52:55],v[188:191],v[60:63]
MF v[116:119],v[184:187],a[40:43],v[116:119]
MF v[116:119],v[188:191],a[56:59],v[116:119]
MF v[124:127],v[184:187],a[44:47],v[124:127]
s_xor_b32 s80, s78, 0x280000
s_xor_b32 s81, s84, 0x280
MF v[124:127],v[188:191],a[60:63],v[124:127]
s_waitcnt lgkmcnt(2)
MF v[0:3],a[64:67],v[128:131],v[0:3]
R16 v[184:187],v224,47104
R16 v[188:191],v225,47104
MF v[0:3],a[80:83],v[132:135],v[0:3]
MF v[4:7],a[68:71],v[128:131],v[4:7]
v_subrev_u32 v224, 0x18000, v224
v_subrev_u32 v225, 0x18000, v225
MF v[4:7],a[84:87],v[132:135],v[4:7]
v_subrev_u32 v226, 0x18000, v226
v_subrev_u32 v227, 0x18000, v227
MF v[64:67],v[128:131],a[72:75],v[64:67]
MF v[64:67],v[132:135],a[88:91],v[64:67]
MF v[72:75],v[128:131],a[76:79],v[72:75]
MF v[72:75],v[132:135],a[92:95],v[72:75]
s_waitcnt vmcnt(13) lgkmcnt(0)
s_barrier
MF v[8:11],a[64:67],v[136:139],v[8:11]
R16 v[128:131],v224,32768
s_add_u32 m0, s34, 0x20000
MF v[8:11],a[80:83],v[140:143],v[8:11]
R16 v[132:135],v225,32768
buffer_load_dwordx4 v228, s[16:19], s81 offen offset:0 lds
MF v[12:15],a[68:71],v[136:139],v[12:15]
RT a[96:97],v226,0
buffer_store_dwordx2 v[238:239], v236, s[20:23], s51 offen offset:0 sc1
MF v[12:15],a[84:87],v[140:143],v[12:15]
MF v[68:71],v[136:139],a[72:75],v[68:71]
RT a[98:99],v226,1024
buffer_load_dwordx4 v229, s[16:19], s81 offen offset:1024 lds
MF v[68:71],v[140:143],a[88:91],v[68:71]
MF v[76:79],v[136:139],a[76:79],v[76:79]
RT a[100:101],v227,0
MF v[76:79],v[140:143],a[92:95],v[76:79]
MF v[16:19],a[64:67],v[144:147],v[16:19]
R16 v[136:139],v224,34816
MF v[16:19],a[80:83],v[148:151],v[16:19]
R16 v[140:143],v225,34816
buffer_load_dwordx4 v230, s[16:19], s81 offen offset:2048 lds
MF v[20:23],a[68:71],v[144:147],v[20:23]
RT a[102:103],v227,1024
MF v[20:23],a[84:87],v[148:151],v[20:23]
MF v[80:83],v[144:147],a[72:75],v[80:83]
RT a[104:105],v226,16384
MF v[80:83],v[148:151],a[88:91],v[80:83]
buffer_load_dwordx4 v231, s[16:19], s81 offen offset:3072 lds
MF v[88:91],v[144:147],a[76:79],v[88:91]
RT a[106:107],v226,17408
MF v[88:91],v[148:151],a[92:95],v[88:91]
MF v[24:27],a[64:67],v[152:155],v[24:27]
R16 v[144:147],v224,36864
MF v[24:27],a[80:83],v[156:159],v[24:27]
R16 v[148:151],v225,36864
MF v[28:31],a[68:71],v[152:155],v[28:31]
RT a[108:109],v227,16384
s_add_u32 m0, s34, 0x18000
MF v[28:31],a[84:87],v[156:159],v[28:31]
buffer_load_dwordx4 v232, s[8:11], s80 offen offset:0 lds
MF v[84:87],v[152:155],a[72:75],v[84:87]
RT a[110:111],v227,17408
MF v[84:87],v[156:159],a[88:91],v[84:87]
MF v[92:95],v[152:155],a[76:79],v[92:95]
RT a[112:113],v226,8192
buffer_load_dwordx4 v233, s[8:11], s80 offen offset:1024 lds
MF v[92:95],v[156:159],a[92:95],v[92:95]
MF v[32:35],a[64:67],v[160:163],v[32:35]
R16 v[152:155],v224,38912
MF v[32:35],a[80:83],v[164:167],v[32:35]
R16 v[156:159],v225,38912
MF v[36:39],a[68:71],v[160:163],v[36:39]
RT a[114:115],v226,9216
MF v[36:39],a[84:87],v[164:167],v[36:39]
buffer_load_dwordx4 v234, s[8:11], s80 offen offset:2048 lds
MF v[96:99],v[160:163],a[72:75],v[96:99]
RT a[116:117],v227,8192
MF v[96:99],v[164:167],a[88:91],v[96:99]
MF v[104:107],v[160:163],a[76:79],v[104:107]
RT a[118:119],v227,9216
MF v[104:107],v[164:167],a[92:95],v[104:107]
buffer_load_dwordx4 v235, s[8:11], s80 offen offset:3072 lds
MF v[40:43],a[64:67],v[168:171],v[40:43]
R16 v[160:163],v224,40960
MF v[40:43],a[80:83],v[172:175],v[40:43]
R16 v[164:167],v225,40960
MF v[44:47],a[68:71],v[168:171],v[44:47]
RT a[120:121],v226,24576
MF v[44:47],a[84:87],v[172:175],v[44:47]
MF v[100:103],v[168:171],a[72:75],v[100:103]
RT a[122:123],v226,25600
s_add_u32 m0, s34, 0x1c000
MF v[100:103],v[172:175],a[88:91],v[100:103]
buffer_load_dwordx4 v232, s[12:15], s80 offen offset:0 lds
MF v[108:111],v[168:171],a[76:79],v[108:111]
RT a[124:125],v227,24576
MF v[108:111],v[172:175],a[92:95],v[108:111]
MF v[48:51],a[64:67],v[176:179],v[48:51]
R16 v[168:171],v224,43008
buffer_load_dwordx4 v233, s[12:15], s80 offen offset:1024 lds
MF v[48:51],a[80:83],v[180:183],v[48:51]
R16 v[172:175],v225,43008
MF v[52:55],a[68:71],v[176:179],v[52:55]
RT a[126:127],v227,25600
MF v[52:55],a[84:87],v[180:183],v[52:55]
MF v[112:115],v[176:179],a[72:75],v[112:115]
MF v[112:115],v[180:183],a[88:91],v[112:115]
buffer_load_dwordx4 v234, s[12:15], s80 offen offset:2048 lds
MF v[120:123],v[176:179],a[76:79],v[120:123]
MF v[120:123],v[180:183],a[92:95],v[120:123]
MF v[56:59],a[64:67],v[184:187],v[56:59]
R16 v[176:179],v224,45056
MF v[56:59],a[80:83],v[188:191],v[56:59]
R16 v[180:183],v225,45056
buffer_load_dwordx4 v235, s[12:15], s80 offen offset:3072 lds
MF v[60:63],a[68:71],v[184:187],v[60:63]
MF v[60:63],a[84:87],v[188:191],v[60:63]
MF v[116:119],v[184:187],a[72:75],v[116:119]
MF v[116:119],v[188:191],a[88:91],v[116:119]
MF v[124:127],v[184:187],a[76:79],v[124:127]
s_xor_b32 s80, s78, 0x300000
s_xor_b32 s81, s84, 0x300
MF v[124:127],v[188:191],a[92:95],v[124:127]
s_waitcnt lgkmcnt(2)
MF v[0:3],a[96:99],v[128:131],v[0:3]
R16 v[184:187],v224,47104
R16 v[188:191],v225,47104
MF v[0:3],a[112:115],v[132:135],v[0:3]
MF v[4:7],a[100:103],v[128:131],v[4:7]
v_add_u32 v224, 0xc000, v224
v_add_u32 v225, 0xc000, v225
MF v[4:7],a[116:119],v[132:135],v[4:7]
v_add_u32 v226, 0xc000, v226
v_add_u32 v227, 0xc000, v227
MF v[64:67],v[128:131],a[104:107],v[64:67]
MF v[64:67],v[132:135],a[120:123],v[64:67]
MF v[72:75],v[128:131],a[108:111],v[72:75]
MF v[72:75],v[132:135],a[124:127],v[72:75]
s_waitcnt vmcnt(13) lgkmcnt(0)
s_barrier
MF v[8:11],a[96:99],v[136:139],v[8:11]
R16 v[128:131],v224,32768
s_add_u32 m0, s34, 0x8000
MF v[8:11],a[112:115],v[140:143],v[8:11]
R16 v[132:135],v225,32768
buffer_load_dwordx4 v228, s[16:19], s81 offen offset:0 lds
MF v[12:15],a[100:103],v[136:139],v[12:15]
RT a[128:129],v226,0
MF v[12:15],a[116:119],v[140:143],v[12:15]
buffer_load_dwordx4 v229, s[16:19], s81 offen offset:1024 lds
MF v[68:71],v[136:139],a[104:107],v[68:71]
RT a[130:131],v226,1024
MF v[68:71],v[140:143],a[120:123],v[68:71]
MF v[76:79],v[136:139],a[108:111],v[76:79]
RT a[132:133],v227,0
buffer_load_dwordx4 v230, s[16:19], s81 offen offset:2048 lds
MF v[76:79],v[140:143],a[124:127],v[76:79]
MF v[16:19],a[96:99],v[144:147],v[16:19]
R16 v[136:139],v224,34816
MF v[16:19],a[112:115],v[148:151],v[16:19]
R16 v[140:143],v225,34816
buffer_load_dwordx4 v231, s[16:19], s81 offen offset:3072 lds
MF v[20:23],a[100:103],v[144:147],v[20:23]
RT a[134:135],v227,1024
MF v[20:23],a[116:119],v[148:151],v[20:23]
MF v[80:83],v[144:147],a[104:107],v[80:83]
RT a[136:137],v226,16384
MF v[80:83],v[148:151],a[120:123],v[80:83]
s_add_u32 m0, s34, 0x0
MF v[88:91],v[144:147],a[108:111],v[88:91]
RT a[138:139],v226,17408
buffer_load_dwordx4 v232, s[8:11], s80 offen offset:0 lds
MF v[88:91],v[148:151],a[124:127],v[88:91]
MF v[24:27],a[96:99],v[152:155],v[24:27]
R16 v[144:147],v224,36864
buffer_load_dwordx4 v233, s[8:11], s80 offen offset:1024 lds
MF v[24:27],a[112:115],v[156:159],v[24:27]
R16 v[148:151],v225,36864
MF v[28:31],a[100:103],v[152:155],v[28:31]
RT a[140:141],v227,16384
MF v[28:31],a[116:119],v[156:159],v[28:31]
buffer_load_dwordx4 v234, s[8:11], s80 offen offset:2048 lds
MF v[84:87],v[152:155],a[104:107],v[84:87]
RT a[142:143],v227,17408
MF v[84:87],v[156:159],a[120:123],v[84:87]
MF v[92:95],v[152:155],a[108:111],v[92:95]
RT a[144:145],v226,8192
MF v[92:95],v[156:159],a[124:127],v[92:95]
buffer_load_dwordx4 v235, s[8:11], s80 offen offset:3072 lds
MF v[32:35],a[96:99],v[160:163],v[32:35]
R16 v[152:155],v224,38912
MF v[32:35],a[112:115],v[164:167],v[32:35]
R16 v[156:159],v225,38912
MF v[36:39],a[100:103],v[160:163],v[36:39]
RT a[146:147],v226,9216
s_add_u32 m0, s34, 0x4000
MF v[36:39],a[116:119],v[164:167],v[36:39]
buffer_load_dwordx4 v232, s[12:15], s80 offen offset:0 lds
MF v[96:99],v[160:163],a[104:107],v[96:99]
RT a[148:149],v227,8192
MF v[96:99],v[164:167],a[120:123],v[96:99]
buffer_load_dwordx4 v233, s[12:15], s80 offen offset:1024 lds
MF v[104:107],v[160:163],a[108:111],v[104:107]
RT a[150:151],v227,9216
MF v[104:107],v[164:167],a[124:127],v[104:107]
MF v[40:43],a[96:99],v[168:171],v[40:43]
R16 v[160:163],v224,40960
MF v[40:43],a[112:115],v[172:175],v[40:43]
R16 v[164:167],v225,40960
buffer_load_dwordx4 v234, s[12:15], s80 offen offset:2048 lds
MF v[44:47],a[100:103],v[168:171],v[44:47]
RT a[152:153],v226,24576
MF v[44:47],a[116:119],v[172:175],v[44:47]
MF v[100:103],v[168:171],a[104:107],v[100:103]
RT a[154:155],v226,25600
buffer_load_dwordx4 v235, s[12:15], s80 offen offset:3072 lds
MF v[100:103],v[172:175],a[120:123],v[100:103]
MF v[108:111],v[168:171],a[108:111],v[108:111]
RT a[156:157],v227,24576
MF v[108:111],v[172:175],a[124:127],v[108:111]
s_xor_b32 s80, s78, 0x380000
s_xor_b32 s81, s84, 0x380
MF v[48:51],a[96:99],v[176:179],v[48:51]
R16 v[168:171],v224,43008
s_mov_b32 s60, s68
MF v[48:51],a[112:115],v[180:183],v[48:51]
R16 v[172:175],v225,43008
s_mov_b32 s61, s69
MF v[52:55],a[100:103],v[176:179],v[52:55]
RT a[158:159],v227,25600
s_mov_b32 s62, s67
MF v[52:55],a[116:119],v[180:183],v[52:55]
v_lshrrev_b32 v245, 7, v242
v_mul_u32_u24 v245, 3456, v245
MF v[112:115],v[176:179],a[104:107],v[112:115]
v_add_u32 v245, v245, v242
s_add_u32 m0, s34, 0x24000
MF v[112:115],v[180:183],a[120:123],v[112:115]
s_mov_b32 s79, s65
MF v[120:123],v[176:179],a[108:111],v[120:123]
buffer_load_dwordx4 v245, s[60:63], s79 offen offset:0 lds
s_add_u32 s79, s79, 0x6c00
MF v[120:123],v[180:183],a[124:127],v[120:123]
buffer_load_dwordx4 v245, s[60:63], s79 offen offset:1024 lds
s_add_u32 s79, s79, 0x6c00
MF v[56:59],a[96:99],v[184:187],v[56:59]
R16 v[176:179],v224,45056
buffer_load_dwordx4 v245, s[60:63], s79 offen offset:2048 lds
MF v[56:59],a[112:115],v[188:191],v[56:59]
R16 v[180:183],v225,45056
s_add_u32 s79, s79, 0x6c00
MF v[60:63],a[100:103],v[184:187],v[60:63]
buffer_load_dwordx4 v245, s[60:63], s79 offen offset:3072 lds
MF v[60:63],a[116:119],v[188:191],v[60:63]
MF v[116:119],v[184:187],a[104:107],v[116:119]
MF v[116:119],v[188:191],a[120:123],v[116:119]
MF v[124:127],v[184:187],a[108:111],v[124:127]
MF v[124:127],v[188:191],a[124:127],v[124:127]
s_waitcnt lgkmcnt(2)
MF v[0:3],a[128:131],v[128:131],v[0:3]
R16 v[184:187],v224,47104
R16 v[188:191],v225,47104
MF v[0:3],a[144:147],v[132:135],v[0:3]
MF v[4:7],a[132:135],v[128:131],v[4:7]
v_add_u32 v224, 0xc000, v224
v_add_u32 v225, 0xc000, v225
MF v[4:7],a[148:151],v[132:135],v[4:7]
v_add_u32 v226, 0xc000, v226
v_add_u32 v227, 0xc000, v227
MF v[64:67],v[128:131],a[136:139],v[64:67]
MF v[64:67],v[132:135],a[152:155],v[64:67]
MF v[72:75],v[128:131],a[140:143],v[72:75]
MF v[72:75],v[132:135],a[156:159],v[72:75]
s_waitcnt vmcnt(16) lgkmcnt(0)
s_barrier
MF v[8:11],a[128:131],v[136:139],v[8:11]
R16 v[128:131],v224,32768
s_add_u32 m0, s34, 0x14000
MF v[8:11],a[144:147],v[140:143],v[8:11]
R16 v[132:135],v225,32768
buffer_load_dwordx4 v228, s[16:19], s81 offen offset:0 lds
MF v[12:15],a[132:135],v[136:139],v[12:15]
RT a[160:161],v226,0
MF v[12:15],a[148:151],v[140:143],v[12:15]
MF v[68:71],v[136:139],a[136:139],v[68:71]
RT a[162:163],v226,1024
buffer_load_dwordx4 v229, s[16:19], s81 offen offset:1024 lds
MF v[68:71],v[140:143],a[152:155],v[68:71]
MF v[76:79],v[136:139],a[140:143],v[76:79]
RT a[164:165],v227,0
MF v[76:79],v[140:143],a[156:159],v[76:79]
MF v[16:19],a[128:131],v[144:147],v[16:19]
R16 v[136:139],v224,34816
MF v[16:19],a[144:147],v[148:151],v[16:19]
R16 v[140:143],v225,34816
buffer_load_dwordx4 v230, s[16:19], s81 offen offset:2048 lds
MF v[20:23],a[132:135],v[144:147],v[20:23]
RT a[166:167],v227,1024
MF v[20:23],a[148:151],v[148:151],v[20:23]
MF v[80:83],v[144:147],a[136:139],v[80:83]
RT a[168:169],v226,16384
MF v[80:83],v[148:151],a[152:155],v[80:83]
buffer_load_dwordx4 v231, s[16:19], s81 offen offset:3072 lds
MF v[88:91],v[144:147],a[140:143],v[88:91]
RT a[170:171],v226,17408
MF v[88:91],v[148:151],a[156:159],v[88:91]
MF v[24:27],a[128:131],v[152:155],v[24:27]
R16 v[144:147],v224,36864
MF v[24:27],a[144:147],v[156:159],v[24:27]
R16 v[148:151],v225,36864
MF v[28:31],a[132:135],v[152:155],v[28:31]
RT a[172:173],v227,16384
s_add_u32 m0, s34, 0xc000
)A"
R"A(MF v[28:31],a[148:151],v[156:159],v[28:31]
buffer_load_dwordx4 v232, s[8:11], s80 offen offset:0 lds
MF v[84:87],v[152:155],a[136:139],v[84:87]
RT a[174:175],v227,17408
MF v[84:87],v[156:159],a[152:155],v[84:87]
MF v[92:95],v[152:155],a[140:143],v[92:95]
RT a[176:177],v226,8192
buffer_load_dwordx4 v233, s[8:11], s80 offen offset:1024 lds
MF v[92:95],v[156:159],a[156:159],v[92:95]
MF v[32:35],a[128:131],v[160:163],v[32:35]
R16 v[152:155],v224,38912
MF v[32:35],a[144:147],v[164:167],v[32:35]
R16 v[156:159],v225,38912
MF v[36:39],a[132:135],v[160:163],v[36:39]
RT a[178:179],v226,9216
MF v[36:39],a[148:151],v[164:167],v[36:39]
buffer_load_dwordx4 v234, s[8:11], s80 offen offset:2048 lds
MF v[96:99],v[160:163],a[136:139],v[96:99]
RT a[180:181],v227,8192
MF v[96:99],v[164:167],a[152:155],v[96:99]
MF v[104:107],v[160:163],a[140:143],v[104:107]
RT a[182:183],v227,9216
MF v[104:107],v[164:167],a[156:159],v[104:107]
buffer_load_dwordx4 v235, s[8:11], s80 offen offset:3072 lds
MF v[40:43],a[128:131],v[168:171],v[40:43]
R16 v[160:163],v224,40960
MF v[40:43],a[144:147],v[172:175],v[40:43]
R16 v[164:167],v225,40960
MF v[44:47],a[132:135],v[168:171],v[44:47]
RT a[184:185],v226,24576
MF v[44:47],a[148:151],v[172:175],v[44:47]
MF v[100:103],v[168:171],a[136:139],v[100:103]
RT a[186:187],v226,25600
s_add_u32 m0, s34, 0x10000
MF v[100:103],v[172:175],a[152:155],v[100:103]
buffer_load_dwordx4 v232, s[12:15], s80 offen offset:0 lds
MF v[108:111],v[168:171],a[140:143],v[108:111]
RT a[188:189],v227,24576
MF v[108:111],v[172:175],a[156:159],v[108:111]
MF v[48:51],a[128:131],v[176:179],v[48:51]
R16 v[168:171],v224,43008
buffer_load_dwordx4 v233, s[12:15], s80 offen offset:1024 lds
MF v[48:51],a[144:147],v[180:183],v[48:51]
R16 v[172:175],v225,43008
MF v[52:55],a[132:135],v[176:179],v[52:55]
RT a[190:191],v227,25600
MF v[52:55],a[148:151],v[180:183],v[52:55]
MF v[112:115],v[176:179],a[136:139],v[112:115]
MF v[112:115],v[180:183],a[152:155],v[112:115]
buffer_load_dwordx4 v234, s[12:15], s80 offen offset:2048 lds
MF v[120:123],v[176:179],a[140:143],v[120:123]
MF v[120:123],v[180:183],a[156:159],v[120:123]
MF v[56:59],a[128:131],v[184:187],v[56:59]
R16 v[176:179],v224,45056
MF v[56:59],a[144:147],v[188:191],v[56:59]
R16 v[180:183],v225,45056
buffer_load_dwordx4 v235, s[12:15], s80 offen offset:3072 lds
MF v[60:63],a[132:135],v[184:187],v[60:63]
MF v[60:63],a[148:151],v[188:191],v[60:63]
MF v[116:119],v[184:187],a[136:139],v[116:119]
MF v[116:119],v[188:191],a[152:155],v[116:119]
MF v[124:127],v[184:187],a[140:143],v[124:127]
MF v[124:127],v[188:191],a[156:159],v[124:127]
s_waitcnt lgkmcnt(2)
MF v[0:3],a[160:163],v[128:131],v[0:3]
R16 v[184:187],v224,47104
R16 v[188:191],v225,47104
MF v[0:3],a[176:179],v[132:135],v[0:3]
MF v[4:7],a[164:167],v[128:131],v[4:7]
v_subrev_u32 v224, 0x18000, v224
v_subrev_u32 v225, 0x18000, v225
MF v[4:7],a[180:183],v[132:135],v[4:7]
v_subrev_u32 v226, 0x18000, v226
v_subrev_u32 v227, 0x18000, v227
MF v[64:67],v[128:131],a[168:171],v[64:67]
MF v[64:67],v[132:135],a[184:187],v[64:67]
MF v[72:75],v[128:131],a[172:175],v[72:75]
MF v[72:75],v[132:135],a[188:191],v[72:75]
s_waitcnt vmcnt(16) lgkmcnt(0)
s_barrier
MF v[8:11],a[160:163],v[136:139],v[8:11]
R16 v[128:131],v224,32768
s_add_u32 m0, s85, 0x18000
MF v[8:11],a[176:179],v[140:143],v[8:11]
R16 v[132:135],v225,32768
s_xor_b32 s48, s44, 0x0
MF v[12:15],a[164:167],v[136:139],v[12:15]
RT a[192:193],v226,0
buffer_load_dwordx4 v228, s[16:19], s48 offen offset:0 lds
MF v[12:15],a[180:183],v[140:143],v[12:15]
MF v[68:71],v[136:139],a[168:171],v[68:71]
RT a[194:195],v226,1024
MF v[68:71],v[140:143],a[184:187],v[68:71]
MF v[76:79],v[136:139],a[172:175],v[76:79]
RT a[196:197],v227,0
buffer_load_dwordx4 v229, s[16:19], s48 offen offset:1024 lds
MF v[76:79],v[140:143],a[188:191],v[76:79]
MF v[16:19],a[160:163],v[144:147],v[16:19]
R16 v[136:139],v224,34816
MF v[16:19],a[176:179],v[148:151],v[16:19]
R16 v[140:143],v225,34816
MF v[20:23],a[164:167],v[144:147],v[20:23]
RT a[198:199],v227,1024
MF v[20:23],a[180:183],v[148:151],v[20:23]
MF v[80:83],v[144:147],a[168:171],v[80:83]
RT a[200:201],v226,16384
buffer_load_dwordx4 v230, s[16:19], s48 offen offset:2048 lds
MF v[80:83],v[148:151],a[184:187],v[80:83]
MF v[88:91],v[144:147],a[172:175],v[88:91]
RT a[202:203],v226,17408
MF v[88:91],v[148:151],a[188:191],v[88:91]
MF v[24:27],a[160:163],v[152:155],v[24:27]
R16 v[144:147],v224,36864
MF v[24:27],a[176:179],v[156:159],v[24:27]
R16 v[148:151],v225,36864
MF v[28:31],a[164:167],v[152:155],v[28:31]
RT a[204:205],v227,16384
buffer_load_dwordx4 v231, s[16:19], s48 offen offset:3072 lds
MF v[28:31],a[180:183],v[156:159],v[28:31]
MF v[84:87],v[152:155],a[168:171],v[84:87]
RT a[206:207],v227,17408
MF v[84:87],v[156:159],a[184:187],v[84:87]
MF v[92:95],v[152:155],a[172:175],v[92:95]
RT a[208:209],v226,8192
MF v[92:95],v[156:159],a[188:191],v[92:95]
MF v[32:35],a[160:163],v[160:163],v[32:35]
R16 v[152:155],v224,38912
s_add_u32 m0, s85, 0x19000
MF v[32:35],a[176:179],v[164:167],v[32:35]
R16 v[156:159],v225,38912
s_xor_b32 s48, s44, 0x80
MF v[36:39],a[164:167],v[160:163],v[36:39]
RT a[210:211],v226,9216
buffer_load_dwordx4 v228, s[16:19], s48 offen offset:0 lds
MF v[36:39],a[180:183],v[164:167],v[36:39]
MF v[96:99],v[160:163],a[168:171],v[96:99]
RT a[212:213],v227,8192
MF v[96:99],v[164:167],a[184:187],v[96:99]
MF v[104:107],v[160:163],a[172:175],v[104:107]
RT a[214:215],v227,9216
buffer_load_dwordx4 v229, s[16:19], s48 offen offset:1024 lds
MF v[104:107],v[164:167],a[188:191],v[104:107]
MF v[40:43],a[160:163],v[168:171],v[40:43]
R16 v[160:163],v224,40960
MF v[40:43],a[176:179],v[172:175],v[40:43]
R16 v[164:167],v225,40960
MF v[44:47],a[164:167],v[168:171],v[44:47]
RT a[216:217],v226,24576
MF v[44:47],a[180:183],v[172:175],v[44:47]
MF v[100:103],v[168:171],a[168:171],v[100:103]
RT a[218:219],v226,25600
buffer_load_dwordx4 v230, s[16:19], s48 offen offset:2048 lds
MF v[100:103],v[172:175],a[184:187],v[100:103]
MF v[108:111],v[168:171],a[172:175],v[108:111]
RT a[220:221],v227,24576
MF v[108:111],v[172:175],a[188:191],v[108:111]
MF v[48:51],a[160:163],v[176:179],v[48:51]
R16 v[168:171],v224,43008
MF v[48:51],a[176:179],v[180:183],v[48:51]
R16 v[172:175],v225,43008
MF v[52:55],a[164:167],v[176:179],v[52:55]
RT a[222:223],v227,25600
buffer_load_dwordx4 v231, s[16:19], s48 offen offset:3072 lds
MF v[52:55],a[180:183],v[180:183],v[52:55]
MF v[112:115],v[176:179],a[168:171],v[112:115]
MF v[112:115],v[180:183],a[184:187],v[112:115]
MF v[120:123],v[176:179],a[172:175],v[120:123]
MF v[120:123],v[180:183],a[188:191],v[120:123]
MF v[56:59],a[160:163],v[184:187],v[56:59]
R16 v[176:179],v224,45056
MF v[56:59],a[176:179],v[188:191],v[56:59]
R16 v[180:183],v225,45056
MF v[60:63],a[164:167],v[184:187],v[60:63]
MF v[60:63],a[180:183],v[188:191],v[60:63]
MF v[116:119],v[184:187],a[168:171],v[116:119]
MF v[116:119],v[188:191],a[184:187],v[116:119]
MF v[124:127],v[184:187],a[172:175],v[124:127]
MF v[124:127],v[188:191],a[188:191],v[124:127]
s_waitcnt lgkmcnt(2)
MF v[0:3],a[192:195],v[128:131],v[0:3]
R16 v[184:187],v224,47104
R16 v[188:191],v225,47104
MF v[0:3],a[208:211],v[132:135],v[0:3]
MF v[4:7],a[196:199],v[128:131],v[4:7]
v_add_u32 v224, 0xc000, v224
v_add_u32 v225, 0xc000, v225
MF v[4:7],a[212:215],v[132:135],v[4:7]
v_add_u32 v226, 0xc000, v226
v_add_u32 v227, 0xc000, v227
MF v[64:67],v[128:131],a[200:203],v[64:67]
MF v[64:67],v[132:135],a[216:219],v[64:67]
MF v[72:75],v[128:131],a[204:207],v[72:75]
MF v[72:75],v[132:135],a[220:223],v[72:75]
s_waitcnt vmcnt(8) lgkmcnt(0)
s_barrier
MF v[8:11],a[192:195],v[136:139],v[8:11]
R16 v[128:131],v224,32768
s_add_u32 m0, s85, 0x0
MF v[8:11],a[208:211],v[140:143],v[8:11]
R16 v[132:135],v225,32768
s_xor_b32 s48, s45, 0x0
MF v[12:15],a[196:199],v[136:139],v[12:15]
RT a[224:225],v226,0
buffer_load_dwordx4 v228, s[16:19], s48 offen offset:0 lds
MF v[12:15],a[212:215],v[140:143],v[12:15]
MF v[68:71],v[136:139],a[200:203],v[68:71]
RT a[226:227],v226,1024
MF v[68:71],v[140:143],a[216:219],v[68:71]
MF v[76:79],v[136:139],a[204:207],v[76:79]
RT a[228:229],v227,0
buffer_load_dwordx4 v229, s[16:19], s48 offen offset:1024 lds
MF v[76:79],v[140:143],a[220:223],v[76:79]
MF v[16:19],a[192:195],v[144:147],v[16:19]
R16 v[136:139],v224,34816
MF v[16:19],a[208:211],v[148:151],v[16:19]
R16 v[140:143],v225,34816
MF v[20:23],a[196:199],v[144:147],v[20:23]
RT a[230:231],v227,1024
MF v[20:23],a[212:215],v[148:151],v[20:23]
MF v[80:83],v[144:147],a[200:203],v[80:83]
RT a[232:233],v226,16384
buffer_load_dwordx4 v230, s[16:19], s48 offen offset:2048 lds
MF v[80:83],v[148:151],a[216:219],v[80:83]
MF v[88:91],v[144:147],a[204:207],v[88:91]
RT a[234:235],v226,17408
MF v[88:91],v[148:151],a[220:223],v[88:91]
MF v[24:27],a[192:195],v[152:155],v[24:27]
R16 v[144:147],v224,36864
MF v[24:27],a[208:211],v[156:159],v[24:27]
R16 v[148:151],v225,36864
MF v[28:31],a[196:199],v[152:155],v[28:31]
RT a[236:237],v227,16384
buffer_load_dwordx4 v231, s[16:19], s48 offen offset:3072 lds
MF v[28:31],a[212:215],v[156:159],v[28:31]
MF v[84:87],v[152:155],a[200:203],v[84:87]
RT a[238:239],v227,17408
MF v[84:87],v[156:159],a[216:219],v[84:87]
MF v[92:95],v[152:155],a[204:207],v[92:95]
RT a[240:241],v226,8192
s_add_u32 s51, 0, 0x4902000
MF v[92:95],v[156:159],a[220:223],v[92:95]
buffer_load_dwordx4 v[192:195], v242, s[20:23], s51 offen offset:0 sc1
MF v[32:35],a[192:195],v[160:163],v[32:35]
R16 v[152:155],v224,38912
s_add_u32 m0, s85, 0x1000
MF v[32:35],a[208:211],v[164:167],v[32:35]
R16 v[156:159],v225,38912
s_xor_b32 s48, s45, 0x80
MF v[36:39],a[196:199],v[160:163],v[36:39]
RT a[242:243],v226,9216
buffer_load_dwordx4 v228, s[16:19], s48 offen offset:0 lds
MF v[36:39],a[212:215],v[164:167],v[36:39]
buffer_load_dwordx4 v[196:199], v242, s[20:23], s51 offen offset:1024 sc1
MF v[96:99],v[160:163],a[200:203],v[96:99]
RT a[244:245],v227,8192
MF v[96:99],v[164:167],a[216:219],v[96:99]
MF v[104:107],v[160:163],a[204:207],v[104:107]
RT a[246:247],v227,9216
buffer_load_dwordx4 v229, s[16:19], s48 offen offset:1024 lds
MF v[104:107],v[164:167],a[220:223],v[104:107]
buffer_load_dwordx4 v[200:203], v242, s[20:23], s51 offen offset:2048 sc1
MF v[40:43],a[192:195],v[168:171],v[40:43]
R16 v[160:163],v224,40960
MF v[40:43],a[208:211],v[172:175],v[40:43]
R16 v[164:167],v225,40960
MF v[44:47],a[196:199],v[168:171],v[44:47]
RT a[248:249],v226,24576
buffer_load_dwordx4 v[204:207], v242, s[20:23], s51 offen offset:3072 sc1
MF v[44:47],a[212:215],v[172:175],v[44:47]
MF v[100:103],v[168:171],a[200:203],v[100:103]
RT a[250:251],v226,25600
buffer_load_dwordx4 v230, s[16:19], s48 offen offset:2048 lds
MF v[100:103],v[172:175],a[216:219],v[100:103]
MF v[108:111],v[168:171],a[204:207],v[108:111]
RT a[252:253],v227,24576
s_add_u32 s51, s51, 0x1000
MF v[108:111],v[172:175],a[220:223],v[108:111]
buffer_load_dwordx4 v[208:211], v242, s[20:23], s51 offen offset:0 sc1
MF v[48:51],a[192:195],v[176:179],v[48:51]
R16 v[168:171],v224,43008
MF v[48:51],a[208:211],v[180:183],v[48:51]
R16 v[172:175],v225,43008
MF v[52:55],a[196:199],v[176:179],v[52:55]
RT a[254:255],v227,25600
buffer_load_dwordx4 v231, s[16:19], s48 offen offset:3072 lds
MF v[52:55],a[212:215],v[180:183],v[52:55]
buffer_load_dwordx4 v[212:215], v242, s[20:23], s51 offen offset:1024 sc1
MF v[112:115],v[176:179],a[200:203],v[112:115]
MF v[112:115],v[180:183],a[216:219],v[112:115]
MF v[120:123],v[176:179],a[204:207],v[120:123]
buffer_load_dwordx4 v[216:219], v242, s[20:23], s51 offen offset:2048 sc1
MF v[120:123],v[180:183],a[220:223],v[120:123]
MF v[56:59],a[192:195],v[184:187],v[56:59]
R16 v[176:179],v224,45056
MF v[56:59],a[208:211],v[188:191],v[56:59]
R16 v[180:183],v225,45056
MF v[60:63],a[196:199],v[184:187],v[60:63]
buffer_load_dwordx4 v[220:223], v242, s[20:23], s51 offen offset:3072 sc1
MF v[60:63],a[212:215],v[188:191],v[60:63]
MF v[116:119],v[184:187],a[200:203],v[116:119]
MF v[116:119],v[188:191],a[216:219],v[116:119]
MF v[124:127],v[184:187],a[204:207],v[124:127]
MF v[124:127],v[188:191],a[220:223],v[124:127]
s_waitcnt lgkmcnt(2)
MF v[0:3],a[224:227],v[128:131],v[0:3]
R16 v[184:187],v224,47104
R16 v[188:191],v225,47104
MF v[0:3],a[240:243],v[132:135],v[0:3]
MF v[4:7],a[228:231],v[128:131],v[4:7]
v_add_u32 v224, 0xc000, v224
v_add_u32 v225, 0xc000, v225
MF v[4:7],a[244:247],v[132:135],v[4:7]
MF v[64:67],v[128:131],a[232:235],v[64:67]
MF v[64:67],v[132:135],a[248:251],v[64:67]
MF v[72:75],v[128:131],a[236:239],v[72:75]
MF v[72:75],v[132:135],a[252:255],v[72:75]
s_waitcnt vmcnt(16) lgkmcnt(0)
s_barrier
MF v[8:11],a[224:227],v[136:139],v[8:11]
s_add_u32 m0, s85, 0x8000
s_xor_b32 s48, s46, 0x0
MF v[8:11],a[240:243],v[140:143],v[8:11]
buffer_load_dwordx4 v228, s[16:19], s48 offen offset:0 lds
MF v[12:15],a[228:231],v[136:139],v[12:15]
s_waitcnt vmcnt(1)
v_max_u32 v236, v192, v194
MF v[12:15],a[244:247],v[140:143],v[12:15]
buffer_load_dwordx4 v229, s[16:19], s48 offen offset:1024 lds
v_xor_b32 v193, s37, v193
MF v[68:71],v[136:139],a[232:235],v[68:71]
v_xor_b32 v195, s37, v195
v_or_b32 v237, v193, v195
MF v[68:71],v[140:143],a[248:251],v[68:71]
v_max3_u32 v236, v236, v196, v198
v_xor_b32 v197, s37, v197
MF v[76:79],v[136:139],a[236:239],v[76:79]
buffer_load_dwordx4 v230, s[16:19], s48 offen offset:2048 lds
v_xor_b32 v199, s37, v199
MF v[76:79],v[140:143],a[252:255],v[76:79]
v_or3_b32 v237, v237, v197, v199
v_max3_u32 v236, v236, v200, v202
MF v[16:19],a[224:227],v[144:147],v[16:19]
v_xor_b32 v201, s37, v201
v_xor_b32 v203, s37, v203
MF v[16:19],a[240:243],v[148:151],v[16:19]
buffer_load_dwordx4 v231, s[16:19], s48 offen offset:3072 lds
v_or3_b32 v237, v237, v201, v203
MF v[20:23],a[228:231],v[144:147],v[20:23]
v_max3_u32 v236, v236, v204, v206
v_xor_b32 v205, s37, v205
MF v[20:23],a[244:247],v[148:151],v[20:23]
v_xor_b32 v207, s37, v207
v_or3_b32 v237, v237, v205, v207
MF v[80:83],v[144:147],a[232:235],v[80:83]
v_max3_u32 v236, v236, v208, v210
v_xor_b32 v209, s37, v209
MF v[80:83],v[148:151],a[248:251],v[80:83]
s_add_u32 m0, s85, 0x9000
s_xor_b32 s48, s46, 0x80
MF v[88:91],v[144:147],a[236:239],v[88:91]
buffer_load_dwordx4 v228, s[16:19], s48 offen offset:0 lds
v_xor_b32 v211, s37, v211
MF v[88:91],v[148:151],a[252:255],v[88:91]
v_or3_b32 v237, v237, v209, v211
v_max3_u32 v236, v236, v212, v214
MF v[24:27],a[224:227],v[152:155],v[24:27]
buffer_load_dwordx4 v229, s[16:19], s48 offen offset:1024 lds
v_xor_b32 v213, s37, v213
MF v[24:27],a[240:243],v[156:159],v[24:27]
v_xor_b32 v215, s37, v215
v_or3_b32 v237, v237, v213, v215
MF v[28:31],a[228:231],v[152:155],v[28:31]
v_max3_u32 v236, v236, v216, v218
v_xor_b32 v217, s37, v217
MF v[28:31],a[244:247],v[156:159],v[28:31]
buffer_load_dwordx4 v230, s[16:19], s48 offen offset:2048 lds
v_xor_b32 v219, s37, v219
MF v[84:87],v[152:155],a[232:235],v[84:87]
v_or3_b32 v237, v237, v217, v219
v_max3_u32 v236, v236, v220, v222
MF v[84:87],v[156:159],a[248:251],v[84:87]
v_xor_b32 v221, s37, v221
v_xor_b32 v223, s37, v223
MF v[92:95],v[152:155],a[236:239],v[92:95]
v_or3_b32 v237, v237, v221, v223
v_cmp_ne_u32 vcc, 0, v237
MF v[92:95],v[156:159],a[252:255],v[92:95]
buffer_load_dwordx4 v231, s[16:19], s48 offen offset:3072 lds
s_nop 0
MF v[32:35],a[224:227],v[160:163],v[32:35]
s_cbranch_vccnz L_SP_KEY_%=
L_RT_KEY_%=:
v_max_u32_dpp v236, v236, v236 quad_perm:[1,0,3,2] row_mask:0xf bank_mask:0xf
MF v[32:35],a[240:243],v[164:167],v[32:35]
MF v[36:39],a[228:231],v[160:163],v[36:39]
R16 v[128:131],v224,0
s_add_u32 s48, s86, 0x0
MF v[36:39],a[244:247],v[164:167],v[36:39]
s_and_b32 s48, s48, 0x3fffff
s_add_u32 m0, s34, 0x10000
)A"
R"A(MF v[96:99],v[160:163],a[232:235],v[96:99]
R16 v[132:135],v225,0
buffer_load_dwordx4 v232, s[24:27], s48 offen offset:0 lds
MF v[96:99],v[164:167],a[248:251],v[96:99]
buffer_load_dwordx4 v233, s[24:27], s48 offen offset:1024 lds
v_max_u32_dpp v236, v236, v236 quad_perm:[2,3,0,1] row_mask:0xf bank_mask:0xf
MF v[104:107],v[160:163],a[236:239],v[104:107]
R16 v[136:139],v224,4096
MF v[104:107],v[164:167],a[252:255],v[104:107]
v_max_u32_dpp v236, v236, v236 row_half_mirror row_mask:0xf bank_mask:0xf
MF v[40:43],a[224:227],v[168:171],v[40:43]
R16 v[140:143],v225,4096
MF v[40:43],a[240:243],v[172:175],v[40:43]
buffer_load_dwordx4 v234, s[24:27], s48 offen offset:2048 lds
v_max_u32_dpp v236, v236, v236 row_mirror row_mask:0xf bank_mask:0xf
MF v[44:47],a[228:231],v[168:171],v[44:47]
R16 v[144:147],v224,2048
v_mov_b32 v237, v236
MF v[44:47],a[244:247],v[172:175],v[44:47]
MF v[100:103],v[168:171],a[232:235],v[100:103]
R16 v[148:151],v225,2048
buffer_load_dwordx4 v235, s[24:27], s48 offen offset:3072 lds
MF v[100:103],v[172:175],a[248:251],v[100:103]
v_permlane16_swap_b32 v236, v237
v_max_u32 v236, v236, v237
MF v[108:111],v[168:171],a[236:239],v[108:111]
R16 v[152:155],v224,6144
v_mov_b32 v237, v236
MF v[108:111],v[172:175],a[252:255],v[108:111]
s_add_u32 s48, s86, 0x80000
s_and_b32 s48, s48, 0x3fffff
MF v[48:51],a[224:227],v[176:179],v[48:51]
R16 v[156:159],v225,6144
s_add_u32 m0, s34, 0x14000
MF v[48:51],a[240:243],v[180:183],v[48:51]
buffer_load_dwordx4 v232, s[24:27], s48 offen offset:0 lds
v_permlane32_swap_b32 v236, v237
MF v[52:55],a[228:231],v[176:179],v[52:55]
buffer_load_dwordx4 v233, s[24:27], s48 offen offset:1024 lds
v_max_u32 v236, v236, v237
MF v[52:55],a[244:247],v[180:183],v[52:55]
v_readfirstlane_b32 s78, v236
s_nop 0
MF v[112:115],v[176:179],a[232:235],v[112:115]
s_and_b32 s77, s78, 0xffff
s_sub_u32 s77, 0xffff, s77
MF v[112:115],v[180:183],a[248:251],v[112:115]
s_min_u32 s77, s77, 2047
MF v[120:123],v[176:179],a[236:239],v[120:123]
buffer_load_dwordx4 v234, s[24:27], s48 offen offset:2048 lds
MF v[120:123],v[180:183],a[252:255],v[120:123]
MF v[56:59],a[224:227],v[184:187],v[56:59]
MF v[56:59],a[240:243],v[188:191],v[56:59]
buffer_load_dwordx4 v235, s[24:27], s48 offen offset:3072 lds
MF v[60:63],a[228:231],v[184:187],v[60:63]
MF v[60:63],a[244:247],v[188:191],v[60:63]
MF v[116:119],v[184:187],a[232:235],v[116:119]
MF v[116:119],v[188:191],a[248:251],v[116:119]
MF v[124:127],v[184:187],a[236:239],v[124:127]
MF v[124:127],v[188:191],a[252:255],v[124:127]
s_waitcnt lgkmcnt(0)
MF v[192:195],a[0:3],v[128:131],0
s_lshr_b32 s51, s77, 4
s_lshl_b32 s51, s51, 14
MF v[192:195],a[16:19],v[132:135],v[192:195]
s_and_b32 s79, s77, 15
s_lshl_b32 s79, s79, 4
MF v[192:195],a[32:35],v[136:139],v[192:195]
R16 v[160:163],v224,8192
s_add_u32 s51, s51, s79
MF v[192:195],a[48:51],v[140:143],v[192:195]
s_and_b32 s79, s66, 15
s_lshl_b32 s79, s79, 10
MF v[196:199],a[4:7],v[128:131],0
R16 v[164:167],v225,8192
PK v0,v0,v1
MF v[196:199],a[20:23],v[132:135],v[196:199]
PK v1,v2,v3
PK v2,v4,v5
MF v[196:199],a[36:39],v[136:139],v[196:199]
R16 v[168:171],v224,12288
PK v3,v6,v7
MF v[196:199],a[52:55],v[140:143],v[196:199]
PK v8,v8,v9
PK v9,v10,v11
MF v[208:211],v[128:131],a[8:11],0
R16 v[172:175],v225,12288
PK v10,v12,v13
MF v[208:211],v[132:135],a[24:27],v[208:211]
PK v11,v14,v15
PK v16,v16,v17
MF v[208:211],v[136:139],a[40:43],v[208:211]
R16 v[176:179],v224,10240
PK v17,v18,v19
MF v[208:211],v[140:143],a[56:59],v[208:211]
PK v18,v20,v21
PK v19,v22,v23
MF v[216:219],v[128:131],a[12:15],0
R16 v[180:183],v225,10240
PK v24,v24,v25
MF v[216:219],v[132:135],a[28:31],v[216:219]
PK v25,v26,v27
PK v26,v28,v29
MF v[216:219],v[136:139],a[44:47],v[216:219]
R16 v[184:187],v224,14336
PK v27,v30,v31
MF v[216:219],v[140:143],a[60:63],v[216:219]
PK v32,v32,v33
PK v33,v34,v35
MF v[200:203],a[0:3],v[144:147],0
R16 v[188:191],v225,14336
PK v34,v36,v37
MF v[200:203],a[16:19],v[148:151],v[200:203]
PK v35,v38,v39
PK v40,v40,v41
MF v[200:203],a[32:35],v[152:155],v[200:203]
PK v41,v42,v43
PK v42,v44,v45
MF v[200:203],a[48:51],v[156:159],v[200:203]
PK v43,v46,v47
PK v48,v48,v49
MF v[204:207],a[4:7],v[144:147],0
PK v49,v50,v51
PK v50,v52,v53
MF v[204:207],a[20:23],v[148:151],v[204:207]
PK v51,v54,v55
PK v56,v56,v57
MF v[204:207],a[36:39],v[152:155],v[204:207]
PK v57,v58,v59
PK v58,v60,v61
MF v[204:207],a[52:55],v[156:159],v[204:207]
PK v59,v62,v63
PK v64,v64,v65
MF v[212:215],v[144:147],a[8:11],0
PK v65,v66,v67
PK v66,v68,v69
MF v[212:215],v[148:151],a[24:27],v[212:215]
PK v67,v70,v71
PK v72,v72,v73
MF v[212:215],v[152:155],a[40:43],v[212:215]
PK v73,v74,v75
PK v74,v76,v77
MF v[212:215],v[156:159],a[56:59],v[212:215]
PK v75,v78,v79
PK v80,v80,v81
MF v[220:223],v[144:147],a[12:15],0
PK v81,v82,v83
PK v82,v84,v85
MF v[220:223],v[148:151],a[28:31],v[220:223]
PK v83,v86,v87
PK v88,v88,v89
MF v[220:223],v[152:155],a[44:47],v[220:223]
PK v89,v90,v91
PK v90,v92,v93
MF v[220:223],v[156:159],a[60:63],v[220:223]
s_waitcnt lgkmcnt(0)
PK v91,v94,v95
MF v[192:195],a[64:67],v[160:163],v[192:195]
PK v96,v96,v97
PK v97,v98,v99
MF v[192:195],a[80:83],v[164:167],v[192:195]
PK v98,v100,v101
PK v99,v102,v103
MF v[192:195],a[96:99],v[168:171],v[192:195]
R16 v[128:131],v224,16384
PK v104,v104,v105
MF v[192:195],a[112:115],v[172:175],v[192:195]
PK v105,v106,v107
PK v106,v108,v109
MF v[196:199],a[68:71],v[160:163],v[196:199]
R16 v[132:135],v225,16384
PK v107,v110,v111
MF v[196:199],a[84:87],v[164:167],v[196:199]
PK v112,v112,v113
PK v113,v114,v115
MF v[196:199],a[100:103],v[168:171],v[196:199]
R16 v[136:139],v224,20480
PK v114,v116,v117
MF v[196:199],a[116:119],v[172:175],v[196:199]
PK v115,v118,v119
PK v120,v120,v121
MF v[208:211],v[160:163],a[72:75],v[208:211]
R16 v[140:143],v225,20480
PK v121,v122,v123
MF v[208:211],v[164:167],a[88:91],v[208:211]
PK v122,v124,v125
PK v123,v126,v127
MF v[208:211],v[168:171],a[104:107],v[208:211]
R16 v[144:147],v224,18432
s_add_u32 s49, s83, 0x0
MF v[208:211],v[172:175],a[120:123],v[208:211]
buffer_store_dwordx4 v[0:3], v242, s[20:23], s49 offen offset:0 sc1
s_add_u32 s49, s83, 0x1000
MF v[216:219],v[160:163],a[76:79],v[216:219]
R16 v[148:151],v225,18432
buffer_store_dwordx4 v[8:11], v242, s[20:23], s49 offen offset:0 sc1
MF v[216:219],v[164:167],a[92:95],v[216:219]
s_add_u32 s49, s83, 0x20000
s_add_u32 s51, s51, s79
MF v[216:219],v[168:171],a[108:111],v[216:219]
R16 v[152:155],v224,22528
buffer_store_dwordx4 v[16:19], v242, s[20:23], s49 offen offset:0 sc1
MF v[216:219],v[172:175],a[124:127],v[216:219]
v_and_b32 v236, 0x300, v242
MF v[200:203],a[64:67],v[176:179],v[200:203]
R16 v[156:159],v225,22528
buffer_load_dwordx4 v[248:251], v236, s[20:23], s51 offen offset:0 sc1
MF v[200:203],a[80:83],v[180:183],v[200:203]
v_lshlrev_b32 v245, 2, v236
v_and_b32 v237, 0xc0, v242
MF v[200:203],a[96:99],v[184:187],v[200:203]
v_lshl_add_u32 v245, v237, 1, v245
v_and_b32 v237, 48, v242
MF v[200:203],a[112:115],v[188:191],v[200:203]
s_add_u32 s49, s83, 0x21000
v_lshrrev_b32 v237, 1, v237
MF v[204:207],a[68:71],v[176:179],v[204:207]
buffer_store_dwordx4 v[24:27], v242, s[20:23], s49 offen offset:0 sc1
s_add_u32 s79, s34, 0x24000
MF v[204:207],a[84:87],v[180:183],v[204:207]
v_add3_u32 v245, v245, v237, s79
s_waitcnt vmcnt(57)
MF v[204:207],a[100:103],v[184:187],v[204:207]
RT v[36:37],v245,0
RT v[38:39],v245,512
MF v[204:207],a[116:119],v[188:191],v[204:207]
RT v[44:45],v245,32
RT v[46:47],v245,544
MF v[212:215],v[176:179],a[72:75],v[212:215]
RT v[52:53],v245,64
RT v[54:55],v245,576
MF v[212:215],v[180:183],a[88:91],v[212:215]
RT v[60:61],v245,96
RT v[62:63],v245,608
MF v[212:215],v[184:187],a[104:107],v[212:215]
MF v[212:215],v[188:191],a[120:123],v[212:215]
MF v[220:223],v[176:179],a[76:79],v[220:223]
MF v[220:223],v[180:183],a[92:95],v[220:223]
MF v[220:223],v[184:187],a[108:111],v[220:223]
MF v[220:223],v[188:191],a[124:127],v[220:223]
s_waitcnt lgkmcnt(0)
MF v[192:195],a[128:131],v[128:131],v[192:195]
s_add_u32 s49, s95, 0x0
MF v[192:195],a[144:147],v[132:135],v[192:195]
buffer_store_dwordx4 v[64:67], v242, s[20:23], s49 offen offset:0 sc1
MF v[192:195],a[160:163],v[136:139],v[192:195]
R16 v[160:163],v224,24576
MF v[192:195],a[176:179],v[140:143],v[192:195]
MF v[196:199],a[132:135],v[128:131],v[196:199]
R16 v[164:167],v225,24576
MF v[196:199],a[148:151],v[132:135],v[196:199]
MF v[196:199],a[164:167],v[136:139],v[196:199]
R16 v[168:171],v224,28672
MF v[196:199],a[180:183],v[140:143],v[196:199]
MF v[208:211],v[128:131],a[136:139],v[208:211]
R16 v[172:175],v225,28672
MF v[208:211],v[132:135],a[152:155],v[208:211]
MF v[208:211],v[136:139],a[168:171],v[208:211]
R16 v[176:179],v224,26624
MF v[208:211],v[140:143],a[184:187],v[208:211]
MF v[216:219],v[128:131],a[140:143],v[216:219]
R16 v[180:183],v225,26624
MF v[216:219],v[132:135],a[156:159],v[216:219]
s_add_u32 s49, s95, 0x0
MF v[216:219],v[136:139],a[172:175],v[216:219]
R16 v[184:187],v224,30720
buffer_store_dwordx4 v[72:75], v242, s[20:23], s49 offen offset:1024 sc1
MF v[216:219],v[140:143],a[188:191],v[216:219]
MF v[200:203],a[128:131],v[144:147],v[200:203]
R16 v[188:191],v225,30720
MF v[200:203],a[144:147],v[148:151],v[200:203]
MF v[200:203],a[160:163],v[152:155],v[200:203]
MF v[200:203],a[176:179],v[156:159],v[200:203]
s_waitcnt vmcnt(3) lgkmcnt(8)
MF v[4:7],v[248:251],v[36:39],0
MF v[12:15],v[248:251],v[44:47],0
MF v[20:23],v[248:251],v[52:55],0
MF v[28:31],v[248:251],v[60:63],0
MF v[204:207],a[132:135],v[144:147],v[204:207]
MF v[204:207],a[148:151],v[148:151],v[204:207]
MF v[204:207],a[164:167],v[152:155],v[204:207]
s_add_u32 s49, s95, 0x20000
MF v[204:207],a[180:183],v[156:159],v[204:207]
buffer_store_dwordx4 v[80:83], v242, s[20:23], s49 offen offset:0 sc1
MF v[212:215],v[144:147],a[136:139],v[212:215]
MF v[212:215],v[148:151],a[152:155],v[212:215]
MF v[212:215],v[152:155],a[168:171],v[212:215]
v_subrev_u32 v224, 0x18000, v224
v_subrev_u32 v225, 0x18000, v225
MF v[212:215],v[156:159],a[184:187],v[212:215]
MF v[220:223],v[144:147],a[140:143],v[220:223]
v_cmp_le_u32 vcc, 0x100, v242
MF v[220:223],v[148:151],a[156:159],v[220:223]
MF v[220:223],v[152:155],a[172:175],v[220:223]
v_cndmask_b32 v237, v4, v12, vcc
v_cmp_le_u32 vcc, 0x200, v242
MF v[220:223],v[156:159],a[188:191],v[220:223]
s_waitcnt lgkmcnt(0)
MF v[192:195],a[192:195],v[160:163],v[192:195]
s_waitcnt vmcnt(27)
s_barrier
MF v[192:195],a[208:211],v[164:167],v[192:195]
v_cndmask_b32 v237, v237, v20, vcc
v_cmp_le_u32 vcc, 0x300, v242
MF v[192:195],a[224:227],v[168:171],v[192:195]
R16 v[128:131],v224,0
s_add_u32 m0, s85, 0x18000
MF v[192:195],a[240:243],v[172:175],v[192:195]
s_xor_b32 s48, s47, 0x0
s_add_u32 s49, s95, 0x20000
MF v[196:199],a[196:199],v[160:163],v[196:199]
R16 v[132:135],v225,0
buffer_load_dwordx4 v228, s[16:19], s48 offen offset:0 lds
MF v[196:199],a[212:215],v[164:167],v[196:199]
buffer_load_dwordx4 v229, s[16:19], s48 offen offset:1024 lds
buffer_store_dwordx4 v[88:91], v242, s[20:23], s49 offen offset:1024 sc1
MF v[196:199],a[228:231],v[168:171],v[196:199]
R16 v[136:139],v224,4096
v_cndmask_b32 v238, v237, v28, vcc
MF v[196:199],a[244:247],v[172:175],v[196:199]
v_mov_b32 v239, s37
v_lshrrev_b32 v236, 1, v242
MF v[208:211],v[160:163],a[200:203],v[208:211]
R16 v[140:143],v225,4096
s_lshl_b32 s79, s66, 9
MF v[208:211],v[164:167],a[216:219],v[208:211]
buffer_load_dwordx4 v230, s[16:19], s48 offen offset:2048 lds
s_add_u32 s79, s79, 0x4910000
MF v[208:211],v[168:171],a[232:235],v[208:211]
R16 v[144:147],v224,2048
buffer_store_dwordx2 v[238:239], v236, s[20:23], s79 offen offset:0 sc1
MF v[208:211],v[172:175],a[248:251],v[208:211]
MF v[216:219],v[160:163],a[204:207],v[216:219]
R16 v[148:151],v225,2048
buffer_load_dwordx4 v231, s[16:19], s48 offen offset:3072 lds
MF v[216:219],v[164:167],a[220:223],v[216:219]
MF v[216:219],v[168:171],a[236:239],v[216:219]
R16 v[152:155],v224,6144
MF v[216:219],v[172:175],a[252:255],v[216:219]
MF v[200:203],a[192:195],v[176:179],v[200:203]
R16 v[156:159],v225,6144
s_add_u32 m0, s85, 0x19000
MF v[200:203],a[208:211],v[180:183],v[200:203]
s_xor_b32 s48, s47, 0x80
MF v[200:203],a[224:227],v[184:187],v[200:203]
buffer_load_dwordx4 v228, s[16:19], s48 offen offset:0 lds
MF v[200:203],a[240:243],v[188:191],v[200:203]
buffer_load_dwordx4 v229, s[16:19], s48 offen offset:1024 lds
MF v[204:207],a[196:199],v[176:179],v[204:207]
MF v[204:207],a[212:215],v[180:183],v[204:207]
MF v[204:207],a[228:231],v[184:187],v[204:207]
MF v[204:207],a[244:247],v[188:191],v[204:207]
buffer_load_dwordx4 v230, s[16:19], s48 offen offset:2048 lds
MF v[212:215],v[176:179],a[200:203],v[212:215]
MF v[212:215],v[180:183],a[216:219],v[212:215]
MF v[212:215],v[184:187],a[232:235],v[212:215]
buffer_load_dwordx4 v231, s[16:19], s48 offen offset:3072 lds
MF v[212:215],v[188:191],a[248:251],v[212:215]
MF v[220:223],v[176:179],a[204:207],v[220:223]
MF v[220:223],v[180:183],a[220:223],v[220:223]
MF v[220:223],v[184:187],a[236:239],v[220:223]
MF v[220:223],v[188:191],a[252:255],v[220:223]
s_waitcnt lgkmcnt(0)
MF v[64:67],a[0:3],v[128:131],0
s_mov_b32 s16, s70
s_and_b32 s17, s71, 0xffff
MF v[64:67],a[16:19],v[132:135],v[64:67]
s_mov_b32 s18, 1835008
MF v[64:67],a[32:35],v[136:139],v[64:67]
R16 v[160:163],v224,8192
s_add_u32 s49, s83, 0x40000
MF v[64:67],a[48:51],v[140:143],v[64:67]
buffer_store_dwordx4 v[32:35], v242, s[20:23], s49 offen offset:0 sc1
MF v[68:71],a[4:7],v[128:131],0
R16 v[164:167],v225,8192
MF v[68:71],a[20:23],v[132:135],v[68:71]
MF v[68:71],a[36:39],v[136:139],v[68:71]
R16 v[168:171],v224,12288
PK v4,v192,v193
MF v[68:71],a[52:55],v[140:143],v[68:71]
PK v5,v194,v195
PK v6,v196,v197
MF v[80:83],v[128:131],a[8:11],0
R16 v[172:175],v225,12288
PK v7,v198,v199
MF v[80:83],v[132:135],a[24:27],v[80:83]
PK v12,v200,v201
PK v13,v202,v203
MF v[80:83],v[136:139],a[40:43],v[80:83]
R16 v[176:179],v224,10240
PK v14,v204,v205
MF v[80:83],v[140:143],a[56:59],v[80:83]
PK v15,v206,v207
PK v20,v208,v209
MF v[88:91],v[128:131],a[12:15],0
R16 v[180:183],v225,10240
PK v21,v210,v211
MF v[88:91],v[132:135],a[28:31],v[88:91]
PK v22,v212,v213
PK v23,v214,v215
)A"
R"A(MF v[88:91],v[136:139],a[44:47],v[88:91]
R16 v[184:187],v224,14336
PK v28,v216,v217
MF v[88:91],v[140:143],a[60:63],v[88:91]
PK v29,v218,v219
PK v30,v220,v221
MF v[72:75],a[0:3],v[144:147],0
R16 v[188:191],v225,14336
PK v31,v222,v223
MF v[72:75],a[16:19],v[148:151],v[72:75]
s_add_u32 s49, s83, 0x41000
MF v[72:75],a[32:35],v[152:155],v[72:75]
buffer_store_dwordx4 v[40:43], v242, s[20:23], s49 offen offset:0 sc1
MF v[72:75],a[48:51],v[156:159],v[72:75]
MF v[76:79],a[4:7],v[144:147],0
MF v[76:79],a[20:23],v[148:151],v[76:79]
MF v[76:79],a[36:39],v[152:155],v[76:79]
MF v[76:79],a[52:55],v[156:159],v[76:79]
MF v[84:87],v[144:147],a[8:11],0
s_add_u32 s49, s83, 0x60000
MF v[84:87],v[148:151],a[24:27],v[84:87]
buffer_store_dwordx4 v[48:51], v242, s[20:23], s49 offen offset:0 sc1
MF v[84:87],v[152:155],a[40:43],v[84:87]
MF v[84:87],v[156:159],a[56:59],v[84:87]
MF v[92:95],v[144:147],a[12:15],0
MF v[92:95],v[148:151],a[28:31],v[92:95]
MF v[92:95],v[152:155],a[44:47],v[92:95]
MF v[92:95],v[156:159],a[60:63],v[92:95]
s_waitcnt lgkmcnt(0)
MF v[64:67],a[64:67],v[160:163],v[64:67]
MF v[64:67],a[80:83],v[164:167],v[64:67]
MF v[64:67],a[96:99],v[168:171],v[64:67]
R16 v[128:131],v224,16384
MF v[64:67],a[112:115],v[172:175],v[64:67]
s_add_u32 s49, s83, 0x61000
MF v[68:71],a[68:71],v[160:163],v[68:71]
R16 v[132:135],v225,16384
buffer_store_dwordx4 v[56:59], v242, s[20:23], s49 offen offset:0 sc1
MF v[68:71],a[84:87],v[164:167],v[68:71]
MF v[68:71],a[100:103],v[168:171],v[68:71]
R16 v[136:139],v224,20480
MF v[68:71],a[116:119],v[172:175],v[68:71]
MF v[80:83],v[160:163],a[72:75],v[80:83]
R16 v[140:143],v225,20480
MF v[80:83],v[164:167],a[88:91],v[80:83]
MF v[80:83],v[168:171],a[104:107],v[80:83]
R16 v[144:147],v224,18432
MF v[80:83],v[172:175],a[120:123],v[80:83]
MF v[88:91],v[160:163],a[76:79],v[88:91]
R16 v[148:151],v225,18432
MF v[88:91],v[164:167],a[92:95],v[88:91]
MF v[88:91],v[168:171],a[108:111],v[88:91]
R16 v[152:155],v224,22528
s_add_u32 s49, s95, 0x40000
MF v[88:91],v[172:175],a[124:127],v[88:91]
buffer_store_dwordx4 v[96:99], v242, s[20:23], s49 offen offset:0 sc1
MF v[72:75],a[64:67],v[176:179],v[72:75]
R16 v[156:159],v225,22528
MF v[72:75],a[80:83],v[180:183],v[72:75]
MF v[72:75],a[96:99],v[184:187],v[72:75]
MF v[72:75],a[112:115],v[188:191],v[72:75]
MF v[76:79],a[68:71],v[176:179],v[76:79]
MF v[76:79],a[84:87],v[180:183],v[76:79]
MF v[76:79],a[100:103],v[184:187],v[76:79]
MF v[76:79],a[116:119],v[188:191],v[76:79]
MF v[84:87],v[176:179],a[72:75],v[84:87]
MF v[84:87],v[180:183],a[88:91],v[84:87]
s_add_u32 s49, s95, 0x40000
MF v[84:87],v[184:187],a[104:107],v[84:87]
buffer_store_dwordx4 v[104:107], v242, s[20:23], s49 offen offset:1024 sc1
MF v[84:87],v[188:191],a[120:123],v[84:87]
MF v[92:95],v[176:179],a[76:79],v[92:95]
s_and_b32 s79, s76, 7
s_lshl_b32 s79, s79, 5
MF v[92:95],v[180:183],a[92:95],v[92:95]
s_lshr_b32 s51, s76, 3
s_add_u32 s79, s79, s51
MF v[92:95],v[184:187],a[108:111],v[92:95]
s_lshl_b32 s79, s79, 13
s_lshl_b32 s51, s28, 8
MF v[92:95],v[188:191],a[124:127],v[92:95]
s_waitcnt lgkmcnt(0)
s_add_u32 s79, s79, s51
MF v[64:67],a[128:131],v[128:131],v[64:67]
v_lshrrev_b32 v238, 2, v242
MF v[64:67],a[144:147],v[132:135],v[64:67]
buffer_load_dword v40, v238, s[16:19], s79 offen offset:0
MF v[64:67],a[160:163],v[136:139],v[64:67]
R16 v[160:163],v224,24576
MF v[64:67],a[176:179],v[140:143],v[64:67]
buffer_load_dword v41, v238, s[16:19], s79 offen offset:1024
MF v[68:71],a[132:135],v[128:131],v[68:71]
R16 v[164:167],v225,24576
s_add_u32 s49, s95, 0x60000
MF v[68:71],a[148:151],v[132:135],v[68:71]
buffer_store_dwordx4 v[112:115], v242, s[20:23], s49 offen offset:0 sc1
MF v[68:71],a[164:167],v[136:139],v[68:71]
R16 v[168:171],v224,28672
MF v[68:71],a[180:183],v[140:143],v[68:71]
MF v[80:83],v[128:131],a[136:139],v[80:83]
R16 v[172:175],v225,28672
MF v[80:83],v[132:135],a[152:155],v[80:83]
MF v[80:83],v[136:139],a[168:171],v[80:83]
R16 v[176:179],v224,26624
buffer_load_dword v42, v238, s[16:19], s79 offen offset:2048
MF v[80:83],v[140:143],a[184:187],v[80:83]
MF v[88:91],v[128:131],a[140:143],v[88:91]
R16 v[180:183],v225,26624
MF v[88:91],v[132:135],a[156:159],v[88:91]
MF v[88:91],v[136:139],a[172:175],v[88:91]
R16 v[184:187],v224,30720
MF v[88:91],v[140:143],a[188:191],v[88:91]
s_add_u32 s49, s95, 0x60000
MF v[72:75],a[128:131],v[144:147],v[72:75]
R16 v[188:191],v225,30720
buffer_store_dwordx4 v[120:123], v242, s[20:23], s49 offen offset:1024 sc1
MF v[72:75],a[144:147],v[148:151],v[72:75]
buffer_load_dword v43, v238, s[16:19], s79 offen offset:3072
MF v[72:75],a[160:163],v[152:155],v[72:75]
MF v[72:75],a[176:179],v[156:159],v[72:75]
MF v[76:79],a[132:135],v[144:147],v[76:79]
MF v[76:79],a[148:151],v[148:151],v[76:79]
MF v[76:79],a[164:167],v[152:155],v[76:79]
MF v[76:79],a[180:183],v[156:159],v[76:79]
MF v[84:87],v[144:147],a[136:139],v[84:87]
s_add_u32 s79, s79, 0x1000
MF v[84:87],v[148:151],a[152:155],v[84:87]
buffer_load_dword v48, v238, s[16:19], s79 offen offset:0
MF v[84:87],v[152:155],a[168:171],v[84:87]
MF v[84:87],v[156:159],a[184:187],v[84:87]
MF v[92:95],v[144:147],a[140:143],v[92:95]
MF v[92:95],v[148:151],a[156:159],v[92:95]
MF v[92:95],v[152:155],a[172:175],v[92:95]
MF v[92:95],v[156:159],a[188:191],v[92:95]
buffer_load_dword v49, v238, s[16:19], s79 offen offset:1024
s_waitcnt lgkmcnt(0)
MF v[64:67],a[192:195],v[160:163],v[64:67]
s_waitcnt vmcnt(32)
s_barrier
MF v[64:67],a[208:211],v[164:167],v[64:67]
MF v[64:67],a[224:227],v[168:171],v[64:67]
R16 v[128:131],v224,32768
MF v[64:67],a[240:243],v[172:175],v[64:67]
MF v[68:71],a[196:199],v[160:163],v[68:71]
R16 v[132:135],v225,32768
MF v[68:71],a[212:215],v[164:167],v[68:71]
MF v[68:71],a[228:231],v[168:171],v[68:71]
R16 v[136:139],v224,36864
buffer_load_dword v50, v238, s[16:19], s79 offen offset:2048
MF v[68:71],a[244:247],v[172:175],v[68:71]
MF v[80:83],v[160:163],a[200:203],v[80:83]
R16 v[140:143],v225,36864
MF v[80:83],v[164:167],a[216:219],v[80:83]
MF v[80:83],v[168:171],a[232:235],v[80:83]
R16 v[144:147],v224,34816
MF v[80:83],v[172:175],a[248:251],v[80:83]
MF v[88:91],v[160:163],a[204:207],v[88:91]
R16 v[148:151],v225,34816
MF v[88:91],v[164:167],a[220:223],v[88:91]
buffer_load_dword v51, v238, s[16:19], s79 offen offset:3072
MF v[88:91],v[168:171],a[236:239],v[88:91]
R16 v[152:155],v224,38912
MF v[88:91],v[172:175],a[252:255],v[88:91]
MF v[72:75],a[192:195],v[176:179],v[72:75]
R16 v[156:159],v225,38912
MF v[72:75],a[208:211],v[180:183],v[72:75]
MF v[72:75],a[224:227],v[184:187],v[72:75]
MF v[72:75],a[240:243],v[188:191],v[72:75]
MF v[76:79],a[196:199],v[176:179],v[76:79]
MF v[76:79],a[212:215],v[180:183],v[76:79]
MF v[76:79],a[228:231],v[184:187],v[76:79]
MF v[76:79],a[244:247],v[188:191],v[76:79]
MF v[84:87],v[176:179],a[200:203],v[84:87]
MF v[84:87],v[180:183],a[216:219],v[84:87]
MF v[84:87],v[184:187],a[232:235],v[84:87]
MF v[84:87],v[188:191],a[248:251],v[84:87]
MF v[92:95],v[176:179],a[204:207],v[92:95]
MF v[92:95],v[180:183],a[220:223],v[92:95]
MF v[92:95],v[184:187],a[236:239],v[92:95]
MF v[92:95],v[188:191],a[252:255],v[92:95]
s_waitcnt lgkmcnt(0)
MF v[96:99],a[0:3],v[128:131],0
s_and_b32 s79, s76, 7
s_lshl_b32 s79, s79, 5
MF v[96:99],a[16:19],v[132:135],v[96:99]
s_lshr_b32 s51, s76, 3
s_add_u32 s79, s79, s51
MF v[96:99],a[32:35],v[136:139],v[96:99]
R16 v[160:163],v224,40960
s_min_u32 s79, s79, 223
MF v[96:99],a[48:51],v[140:143],v[96:99]
s_lshr_b32 s51, s79, 3
s_lshl_b32 s51, s51, 13
MF v[100:103],a[4:7],v[128:131],0
R16 v[164:167],v225,40960
PK v36,v64,v65
MF v[100:103],a[20:23],v[132:135],v[100:103]
PK v37,v66,v67
PK v38,v68,v69
MF v[100:103],a[36:39],v[136:139],v[100:103]
R16 v[168:171],v224,45056
s_add_u32 s48, s86, 0x100000
MF v[100:103],a[52:55],v[140:143],v[100:103]
s_and_b32 s48, s48, 0x3fffff
s_add_u32 m0, s34, 0x20000
MF v[112:115],v[128:131],a[8:11],0
R16 v[172:175],v225,45056
buffer_load_dwordx4 v232, s[24:27], s48 offen offset:0 lds
MF v[112:115],v[132:135],a[24:27],v[112:115]
PK v39,v70,v71
PK v44,v72,v73
MF v[112:115],v[136:139],a[40:43],v[112:115]
R16 v[176:179],v224,43008
PK v45,v74,v75
MF v[112:115],v[140:143],a[56:59],v[112:115]
buffer_load_dwordx4 v233, s[24:27], s48 offen offset:1024 lds
PK v46,v76,v77
MF v[120:123],v[128:131],a[12:15],0
R16 v[180:183],v225,43008
PK v47,v78,v79
MF v[120:123],v[132:135],a[28:31],v[120:123]
PK v52,v80,v81
PK v53,v82,v83
MF v[120:123],v[136:139],a[44:47],v[120:123]
R16 v[184:187],v224,47104
PK v54,v84,v85
MF v[120:123],v[140:143],a[60:63],v[120:123]
PK v55,v86,v87
PK v60,v88,v89
MF v[104:107],a[0:3],v[144:147],0
R16 v[188:191],v225,47104
buffer_load_dwordx4 v234, s[24:27], s48 offen offset:2048 lds
MF v[104:107],a[16:19],v[148:151],v[104:107]
PK v61,v90,v91
PK v62,v92,v93
MF v[104:107],a[32:35],v[152:155],v[104:107]
PK v63,v94,v95
s_and_b32 s79, s79, 7
MF v[104:107],a[48:51],v[156:159],v[104:107]
s_lshl_b32 s79, s79, 6
s_add_u32 s51, s51, s79
MF v[108:111],a[4:7],v[144:147],0
s_add_u32 s51, s51, 0x4910000
s_add_u32 s79, s51, 0x38000
MF v[108:111],a[20:23],v[148:151],v[108:111]
buffer_load_dwordx4 v235, s[24:27], s48 offen offset:3072 lds
v_lshrrev_b32 v236, 6, v242
MF v[108:111],a[36:39],v[152:155],v[108:111]
v_and_b32 v237, 48, v242
v_lshl_add_u32 v236, v236, 9, v237
MF v[108:111],a[52:55],v[156:159],v[108:111]
buffer_load_dwordx4 v[56:59], v236, s[20:23], s51 offen offset:0 sc1
buffer_load_dwordx4 v[32:35], v236, s[20:23], s79 offen offset:0 sc1
MF v[116:119],v[144:147],a[8:11],0
MF v[116:119],v[148:151],a[24:27],v[116:119]
MF v[116:119],v[152:155],a[40:43],v[116:119]
s_add_u32 s48, s86, 0x180000
s_and_b32 s48, s48, 0x3fffff
MF v[116:119],v[156:159],a[56:59],v[116:119]
s_add_u32 m0, s34, 0x24000
MF v[124:127],v[144:147],a[12:15],0
buffer_load_dwordx4 v232, s[24:27], s48 offen offset:0 lds
MF v[124:127],v[148:151],a[28:31],v[124:127]
MF v[124:127],v[152:155],a[44:47],v[124:127]
MF v[124:127],v[156:159],a[60:63],v[124:127]
buffer_load_dwordx4 v233, s[24:27], s48 offen offset:1024 lds
s_waitcnt lgkmcnt(0)
MF v[96:99],a[64:67],v[160:163],v[96:99]
MF v[96:99],a[80:83],v[164:167],v[96:99]
MF v[96:99],a[96:99],v[168:171],v[96:99]
R16 v[128:131],v224,49152
MF v[96:99],a[112:115],v[172:175],v[96:99]
MF v[100:103],a[68:71],v[160:163],v[100:103]
R16 v[132:135],v225,49152
buffer_load_dwordx4 v234, s[24:27], s48 offen offset:2048 lds
MF v[100:103],a[84:87],v[164:167],v[100:103]
MF v[100:103],a[100:103],v[168:171],v[100:103]
R16 v[136:139],v224,53248
MF v[100:103],a[116:119],v[172:175],v[100:103]
MF v[112:115],v[160:163],a[72:75],v[112:115]
R16 v[140:143],v225,53248
s_lshl_b32 s49, s30, 3
MF v[112:115],v[164:167],a[88:91],v[112:115]
buffer_load_dwordx4 v235, s[24:27], s48 offen offset:3072 lds
s_add_u32 s49, s49, s29
MF v[112:115],v[168:171],a[104:107],v[112:115]
R16 v[144:147],v224,51200
s_lshl_b32 s49, s49, 2
MF v[112:115],v[172:175],a[120:123],v[112:115]
s_add_u32 s49, s49, s28
s_lshl_b32 s49, s49, 2
MF v[120:123],v[160:163],a[76:79],v[120:123]
R16 v[148:151],v225,51200
s_add_u32 s49, s49, 0x4901000
MF v[120:123],v[164:167],a[92:95],v[120:123]
v_mov_b32 v243, s37
v_mov_b32 v244, 0
MF v[120:123],v[168:171],a[108:111],v[120:123]
R16 v[152:155],v224,55296
s_waitcnt vmcnt(15)
MF v[120:123],v[172:175],a[124:127],v[120:123]
buffer_store_dword v243, v244, s[20:23], s49 offen offset:0 sc1
MF v[104:107],a[64:67],v[176:179],v[104:107]
R16 v[156:159],v225,55296
MF v[104:107],a[80:83],v[180:183],v[104:107]
MF v[104:107],a[96:99],v[184:187],v[104:107]
MF v[104:107],a[112:115],v[188:191],v[104:107]
MF v[108:111],a[68:71],v[176:179],v[108:111]
v_add_u32 v246, s82, v242
s_add_u32 s82, s82, 0x800
MF v[108:111],a[84:87],v[180:183],v[108:111]
s_and_b32 s82, s82, 0x3fff
MF v[108:111],a[100:103],v[184:187],v[108:111]
buffer_load_dwordx4 v[64:67], v246, s[20:23], s52 offen offset:0 sc1
MF v[108:111],a[116:119],v[188:191],v[108:111]
MF v[116:119],v[176:179],a[72:75],v[116:119]
MF v[116:119],v[180:183],a[88:91],v[116:119]
MF v[116:119],v[184:187],a[104:107],v[116:119]
MF v[116:119],v[188:191],a[120:123],v[116:119]
MF v[124:127],v[176:179],a[76:79],v[124:127]
buffer_load_dwordx4 v[68:71], v246, s[20:23], s53 offen offset:0 sc1
s_lshl_b32 s84, s28, 6
MF v[124:127],v[180:183],a[92:95],v[124:127]
v_xor_b32 v248, s84, v226
v_add_u32 v248, 0x4000, v248
MF v[124:127],v[184:187],a[108:111],v[124:127]
v_xor_b32 v249, 32, v248
v_xor_b32 v250, 64, v248
MF v[124:127],v[188:191],a[124:127],v[124:127]
s_waitcnt lgkmcnt(0)
v_xor_b32 v251, 96, v248
MF v[96:99],a[128:131],v[128:131],v[96:99]
v_xor_b32 v252, 128, v248
v_xor_b32 v253, 160, v248
MF v[96:99],a[144:147],v[132:135],v[96:99]
v_xor_b32 v254, 192, v248
v_xor_b32 v255, 224, v248
MF v[96:99],a[160:163],v[136:139],v[96:99]
R16 v[160:163],v224,57344
MF v[96:99],a[176:179],v[140:143],v[96:99]
MF v[100:103],a[132:135],v[128:131],v[100:103]
R16 v[164:167],v225,57344
buffer_load_dwordx4 v[72:75], v246, s[20:23], s54 offen offset:0 sc1
MF v[100:103],a[148:151],v[132:135],v[100:103]
MF v[100:103],a[164:167],v[136:139],v[100:103]
R16 v[168:171],v224,61440
s_waitcnt vmcnt(8)
MF v[100:103],a[180:183],v[140:143],v[100:103]
v_xor_b32 v57, s37, v57
v_xor_b32 v59, s37, v59
MF v[112:115],v[128:131],a[136:139],v[112:115]
R16 v[172:175],v225,61440
v_xor_b32 v33, s37, v33
MF v[112:115],v[132:135],a[152:155],v[112:115]
v_xor_b32 v35, s37, v35
v_or3_b32 v237, v57, v59, v33
MF v[112:115],v[136:139],a[168:171],v[112:115]
R16 v[176:179],v224,59392
v_or_b32 v237, v237, v35
MF v[112:115],v[140:143],a[184:187],v[112:115]
v_cmp_ne_u32 vcc, 0, v237
s_nop 0
MF v[120:123],v[128:131],a[140:143],v[120:123]
R16 v[180:183],v225,59392
buffer_load_dwordx4 v[76:79], v246, s[20:23], s55 offen offset:0 sc1
MF v[120:123],v[132:135],a[156:159],v[120:123]
s_cbranch_vccnz L_SP_GP_%=
L_RT_GP_%=:
v_add_f32_dpp v56, v56, v56 row_ror:4 row_mask:0xf bank_mask:0xf
MF v[120:123],v[136:139],a[172:175],v[120:123]
R16 v[184:187],v224,63488
v_add_f32_dpp v58, v58, v58 row_ror:4 row_mask:0xf bank_mask:0xf
MF v[120:123],v[140:143],a[188:191],v[120:123]
v_add_f32_dpp v32, v32, v32 row_ror:4 row_mask:0xf bank_mask:0xf
v_add_f32_dpp v34, v34, v34 row_ror:4 row_mask:0xf bank_mask:0xf
MF v[104:107],a[128:131],v[144:147],v[104:107]
R16 v[188:191],v225,63488
v_add_f32_dpp v56, v56, v56 row_ror:8 row_mask:0xf bank_mask:0xf
MF v[104:107],a[144:147],v[148:151],v[104:107]
v_add_f32_dpp v58, v58, v58 row_ror:8 row_mask:0xf bank_mask:0xf
v_add_f32_dpp v32, v32, v32 row_ror:8 row_mask:0xf bank_mask:0xf
MF v[104:107],a[160:163],v[152:155],v[104:107]
v_add_f32_dpp v34, v34, v34 row_ror:8 row_mask:0xf bank_mask:0xf
v_mov_b32 v57, v56
MF v[104:107],a[176:179],v[156:159],v[104:107]
v_mov_b32 v59, v58
v_mov_b32 v33, v32
MF v[108:111],a[132:135],v[144:147],v[108:111]
buffer_load_dwordx4 v[80:83], v246, s[20:23], s52 offen offset:1024 sc1
v_mov_b32 v35, v34
MF v[108:111],a[148:151],v[148:151],v[108:111]
v_permlane16_swap_b32 v56, v57
v_permlane16_swap_b32 v58, v59
MF v[108:111],a[164:167],v[152:155],v[108:111]
v_permlane16_swap_b32 v32, v33
v_permlane16_swap_b32 v34, v35
MF v[108:111],a[180:183],v[156:159],v[108:111]
v_add_f32 v56, v56, v57
v_add_f32 v58, v58, v59
MF v[116:119],v[144:147],a[136:139],v[116:119]
)A"
R"A(v_add_f32 v32, v32, v33
v_add_f32 v34, v34, v35
MF v[116:119],v[148:151],a[152:155],v[116:119]
v_mov_b32 v57, v56
v_mov_b32 v59, v58
MF v[116:119],v[152:155],a[168:171],v[116:119]
v_add_u32 v224, 0x18000, v224
v_add_u32 v225, 0x18000, v225
MF v[116:119],v[156:159],a[184:187],v[116:119]
v_mov_b32 v33, v32
v_mov_b32 v35, v34
MF v[124:127],v[144:147],a[140:143],v[124:127]
buffer_load_dwordx4 v[84:87], v246, s[20:23], s53 offen offset:1024 sc1
v_permlane32_swap_b32 v56, v57
MF v[124:127],v[148:151],a[156:159],v[124:127]
v_permlane32_swap_b32 v58, v59
v_permlane32_swap_b32 v32, v33
MF v[124:127],v[152:155],a[172:175],v[124:127]
v_permlane32_swap_b32 v34, v35
v_add_f32 v56, v56, v57
MF v[124:127],v[156:159],a[188:191],v[124:127]
s_waitcnt lgkmcnt(0)
v_add_f32 v58, v58, v59
MF v[96:99],a[192:195],v[160:163],v[96:99]
s_waitcnt vmcnt(33)
s_barrier
MF v[96:99],a[208:211],v[164:167],v[96:99]
v_add_f32 v32, v32, v33
v_add_f32 v34, v34, v35
MF v[96:99],a[224:227],v[168:171],v[96:99]
R16 v[128:131],v224,0
PK v56,v56,v58
MF v[96:99],a[240:243],v[172:175],v[96:99]
v_and_b32 v58, 0xffff0000, v56
v_lshlrev_b32 v56, 16, v56
MF v[100:103],a[196:199],v[160:163],v[100:103]
R16 v[132:135],v225,0
buffer_load_dwordx4 v[88:91], v246, s[20:23], s54 offen offset:1024 sc1
MF v[100:103],a[212:215],v[164:167],v[100:103]
PK v32,v32,v34
v_and_b32 v34, 0xffff0000, v32
MF v[100:103],a[228:231],v[168:171],v[100:103]
R16 v[136:139],v224,4096
v_lshlrev_b32 v32, 16, v32
MF v[100:103],a[244:247],v[172:175],v[100:103]
v_mul_f32 v57, 0xbfb8aa3b, v56
v_mul_f32 v59, 0xbfb8aa3b, v58
MF v[112:115],v[160:163],a[200:203],v[112:115]
R16 v[140:143],v225,4096
MF v[112:115],v[164:167],a[216:219],v[112:115]
v_exp_f32 v57, v57
MF v[112:115],v[168:171],a[232:235],v[112:115]
R16 v[144:147],v224,2048
MF v[112:115],v[172:175],a[248:251],v[112:115]
v_exp_f32 v59, v59
MF v[120:123],v[160:163],a[204:207],v[120:123]
R16 v[148:151],v225,2048
buffer_load_dwordx4 v[92:95], v246, s[20:23], s55 offen offset:1024 sc1
MF v[120:123],v[164:167],a[220:223],v[120:123]
v_add_f32 v57, 1.0, v57
v_add_f32 v59, 1.0, v59
MF v[120:123],v[168:171],a[236:239],v[120:123]
R16 v[152:155],v224,6144
MF v[120:123],v[172:175],a[252:255],v[120:123]
v_rcp_f32 v57, v57
MF v[104:107],a[192:195],v[176:179],v[104:107]
R16 v[156:159],v225,6144
MF v[104:107],a[208:211],v[180:183],v[104:107]
v_rcp_f32 v59, v59
MF v[104:107],a[224:227],v[184:187],v[104:107]
v_mul_f32 v56, v56, v57
v_mul_f32 v58, v58, v59
MF v[104:107],a[240:243],v[188:191],v[104:107]
PK v56,v56,v58
v_and_b32 v58, 0xffff0000, v56
MF v[108:111],a[196:199],v[176:179],v[108:111]
v_lshlrev_b32 v56, 16, v56
v_mul_f32 v56, v56, v32
MF v[108:111],a[212:215],v[180:183],v[108:111]
v_mul_f32 v58, v58, v34
PK v56,v56,v58
MF v[108:111],a[228:231],v[184:187],v[108:111]
v_and_b32 v58, 0xffff0000, v56
v_lshlrev_b32 v56, 16, v56
MF v[108:111],a[244:247],v[188:191],v[108:111]
v_readlane_b32 s80, v56, 0
v_readlane_b32 s81, v58, 0
MF v[116:119],v[176:179],a[200:203],v[116:119]
v_readlane_b32 s85, v56, 1
v_readlane_b32 s44, v58, 1
MF v[116:119],v[180:183],a[216:219],v[116:119]
v_readlane_b32 s45, v56, 2
v_readlane_b32 s46, v58, 2
MF v[116:119],v[184:187],a[232:235],v[116:119]
v_readlane_b32 s47, v56, 3
v_readlane_b32 s79, v58, 3
MF v[116:119],v[188:191],a[248:251],v[116:119]
v_lshlrev_b32 v32, 16, v40
v_and_b32 v33, 0xffff0000, v40
MF v[124:127],v[176:179],a[204:207],v[124:127]
v_mul_f32 v24, s80, v32
v_mul_f32 v26, s80, v33
MF v[124:127],v[180:183],a[220:223],v[124:127]
v_lshlrev_b32 v32, 16, v41
v_and_b32 v33, 0xffff0000, v41
MF v[124:127],v[184:187],a[236:239],v[124:127]
MF v[124:127],v[188:191],a[252:255],v[124:127]
s_waitcnt lgkmcnt(0)
MF v[192:195],a[0:3],v[128:131],0
v_fmac_f32 v24, s81, v32
v_fmac_f32 v26, s81, v33
MF v[192:195],a[16:19],v[132:135],v[192:195]
v_lshlrev_b32 v32, 16, v42
v_and_b32 v33, 0xffff0000, v42
MF v[192:195],a[32:35],v[136:139],v[192:195]
R16 v[160:163],v224,8192
v_fmac_f32 v24, s85, v32
MF v[192:195],a[48:51],v[140:143],v[192:195]
v_fmac_f32 v26, s85, v33
v_lshlrev_b32 v32, 16, v43
MF v[196:199],a[4:7],v[128:131],0
R16 v[164:167],v225,8192
v_and_b32 v33, 0xffff0000, v43
MF v[196:199],a[20:23],v[132:135],v[196:199]
v_fmac_f32 v24, s44, v32
v_fmac_f32 v26, s44, v33
MF v[196:199],a[36:39],v[136:139],v[196:199]
R16 v[168:171],v224,12288
v_lshlrev_b32 v32, 16, v48
MF v[196:199],a[52:55],v[140:143],v[196:199]
v_and_b32 v33, 0xffff0000, v48
v_fmac_f32 v24, s45, v32
MF v[208:211],v[128:131],a[8:11],0
R16 v[172:175],v225,12288
v_fmac_f32 v26, s45, v33
MF v[208:211],v[132:135],a[24:27],v[208:211]
v_lshlrev_b32 v32, 16, v49
v_and_b32 v33, 0xffff0000, v49
MF v[208:211],v[136:139],a[40:43],v[208:211]
R16 v[176:179],v224,10240
s_add_u32 s49, s83, s90
MF v[208:211],v[140:143],a[56:59],v[208:211]
buffer_store_dwordx4 v[4:7], v242, s[20:23], s49 offen offset:0 sc1
v_fmac_f32 v24, s46, v32
MF v[216:219],v[128:131],a[12:15],0
R16 v[180:183],v225,10240
v_fmac_f32 v26, s46, v33
MF v[216:219],v[132:135],a[28:31],v[216:219]
v_lshlrev_b32 v32, 16, v50
v_and_b32 v33, 0xffff0000, v50
MF v[216:219],v[136:139],a[44:47],v[216:219]
R16 v[184:187],v224,14336
v_fmac_f32 v24, s47, v32
MF v[216:219],v[140:143],a[60:63],v[216:219]
v_fmac_f32 v26, s47, v33
v_lshlrev_b32 v32, 16, v51
MF v[200:203],a[0:3],v[144:147],0
R16 v[188:191],v225,14336
v_and_b32 v33, 0xffff0000, v51
MF v[200:203],a[16:19],v[148:151],v[200:203]
v_fmac_f32 v24, s79, v32
v_fmac_f32 v26, s79, v33
MF v[200:203],a[32:35],v[152:155],v[200:203]
v_mov_b32 v25, s37
v_mov_b32 v27, s37
MF v[200:203],a[48:51],v[156:159],v[200:203]
v_lshrrev_b32 v236, 7, v242
v_and_b32 v237, 0x70, v242
MF v[204:207],a[4:7],v[144:147],0
v_lshl_add_u32 v236, v236, 15, v237
s_and_b32 s79, s76, 7
MF v[204:207],a[20:23],v[148:151],v[204:207]
s_lshl_b32 s79, s79, 5
s_lshr_b32 s51, s76, 3
MF v[204:207],a[36:39],v[152:155],v[204:207]
s_add_u32 s79, s79, s51
s_lshl_b32 s79, s79, 7
MF v[204:207],a[52:55],v[156:159],v[204:207]
s_lshl_b32 s51, s28, 18
s_add_u32 s79, s79, s51
MF v[212:215],v[144:147],a[8:11],0
s_add_u32 s79, s79, 0x4800000
MF v[212:215],v[148:151],a[24:27],v[212:215]
buffer_store_dwordx4 v[24:27], v236, s[20:23], s79 offen offset:0 sc1
MF v[212:215],v[152:155],a[40:43],v[212:215]
MF v[212:215],v[156:159],a[56:59],v[212:215]
MF v[220:223],v[144:147],a[12:15],0
MF v[220:223],v[148:151],a[28:31],v[220:223]
MF v[220:223],v[152:155],a[44:47],v[220:223]
MF v[220:223],v[156:159],a[60:63],v[220:223]
s_waitcnt lgkmcnt(0)
MF v[192:195],a[64:67],v[160:163],v[192:195]
MF v[192:195],a[80:83],v[164:167],v[192:195]
MF v[192:195],a[96:99],v[168:171],v[192:195]
R16 v[128:131],v224,16384
v_add_u32 v246, s82, v242
MF v[192:195],a[112:115],v[172:175],v[192:195]
s_add_u32 s82, s82, 0x800
s_and_b32 s82, s82, 0x3fff
MF v[196:199],a[68:71],v[160:163],v[196:199]
R16 v[132:135],v225,16384
buffer_load_dwordx4 a[0:3], v246, s[20:23], s52 offen offset:0 sc1
MF v[196:199],a[84:87],v[164:167],v[196:199]
MF v[196:199],a[100:103],v[168:171],v[196:199]
R16 v[136:139],v224,20480
MF v[196:199],a[116:119],v[172:175],v[196:199]
buffer_load_dwordx4 a[4:7], v246, s[20:23], s53 offen offset:0 sc1
MF v[208:211],v[160:163],a[72:75],v[208:211]
R16 v[140:143],v225,20480
MF v[208:211],v[164:167],a[88:91],v[208:211]
MF v[208:211],v[168:171],a[104:107],v[208:211]
R16 v[144:147],v224,18432
MF v[208:211],v[172:175],a[120:123],v[208:211]
MF v[216:219],v[160:163],a[76:79],v[216:219]
R16 v[148:151],v225,18432
MF v[216:219],v[164:167],a[92:95],v[216:219]
buffer_load_dwordx4 a[8:11], v246, s[20:23], s54 offen offset:0 sc1
MF v[216:219],v[168:171],a[108:111],v[216:219]
R16 v[152:155],v224,22528
MF v[216:219],v[172:175],a[124:127],v[216:219]
MF v[200:203],a[64:67],v[176:179],v[200:203]
R16 v[156:159],v225,22528
MF v[200:203],a[80:83],v[180:183],v[200:203]
MF v[200:203],a[96:99],v[184:187],v[200:203]
buffer_load_dwordx4 a[12:15], v246, s[20:23], s55 offen offset:0 sc1
MF v[200:203],a[112:115],v[188:191],v[200:203]
MF v[204:207],a[68:71],v[176:179],v[204:207]
MF v[204:207],a[84:87],v[180:183],v[204:207]
MF v[204:207],a[100:103],v[184:187],v[204:207]
MF v[204:207],a[116:119],v[188:191],v[204:207]
MF v[212:215],v[176:179],a[72:75],v[212:215]
buffer_load_dwordx4 a[16:19], v246, s[20:23], s52 offen offset:1024 sc1
MF v[212:215],v[180:183],a[88:91],v[212:215]
MF v[212:215],v[184:187],a[104:107],v[212:215]
MF v[212:215],v[188:191],a[120:123],v[212:215]
MF v[220:223],v[176:179],a[76:79],v[220:223]
MF v[220:223],v[180:183],a[92:95],v[220:223]
buffer_load_dwordx4 a[20:23], v246, s[20:23], s53 offen offset:1024 sc1
MF v[220:223],v[184:187],a[108:111],v[220:223]
MF v[220:223],v[188:191],a[124:127],v[220:223]
s_waitcnt lgkmcnt(0)
MF v[192:195],a[128:131],v[128:131],v[192:195]
MF v[192:195],a[144:147],v[132:135],v[192:195]
MF v[192:195],a[160:163],v[136:139],v[192:195]
R16 v[160:163],v224,24576
MF v[192:195],a[176:179],v[140:143],v[192:195]
buffer_load_dwordx4 a[24:27], v246, s[20:23], s54 offen offset:1024 sc1
MF v[196:199],a[132:135],v[128:131],v[196:199]
R16 v[164:167],v225,24576
MF v[196:199],a[148:151],v[132:135],v[196:199]
MF v[196:199],a[164:167],v[136:139],v[196:199]
R16 v[168:171],v224,28672
MF v[196:199],a[180:183],v[140:143],v[196:199]
MF v[208:211],v[128:131],a[136:139],v[208:211]
R16 v[172:175],v225,28672
buffer_load_dwordx4 a[28:31], v246, s[20:23], s55 offen offset:1024 sc1
MF v[208:211],v[132:135],a[152:155],v[208:211]
MF v[208:211],v[136:139],a[168:171],v[208:211]
R16 v[176:179],v224,26624
MF v[208:211],v[140:143],a[184:187],v[208:211]
MF v[216:219],v[128:131],a[140:143],v[216:219]
R16 v[180:183],v225,26624
MF v[216:219],v[132:135],a[156:159],v[216:219]
MF v[216:219],v[136:139],a[172:175],v[216:219]
R16 v[184:187],v224,30720
v_add_u32 v246, s82, v242
MF v[216:219],v[140:143],a[188:191],v[216:219]
s_add_u32 s82, s82, 0x800
s_and_b32 s82, s82, 0x3fff
MF v[200:203],a[128:131],v[144:147],v[200:203]
R16 v[188:191],v225,30720
buffer_load_dwordx4 a[32:35], v246, s[20:23], s52 offen offset:0 sc1
MF v[200:203],a[144:147],v[148:151],v[200:203]
MF v[200:203],a[160:163],v[152:155],v[200:203]
MF v[200:203],a[176:179],v[156:159],v[200:203]
buffer_load_dwordx4 a[36:39], v246, s[20:23], s53 offen offset:0 sc1
MF v[204:207],a[132:135],v[144:147],v[204:207]
RT a[64:65],v248,0
MF v[204:207],a[148:151],v[148:151],v[204:207]
RT a[66:67],v248,1024
MF v[204:207],a[164:167],v[152:155],v[204:207]
MF v[204:207],a[180:183],v[156:159],v[204:207]
RT a[68:69],v248,8192
MF v[212:215],v[144:147],a[136:139],v[212:215]
RT a[70:71],v248,9216
MF v[212:215],v[148:151],a[152:155],v[212:215]
buffer_load_dwordx4 a[40:43], v246, s[20:23], s54 offen offset:0 sc1
MF v[212:215],v[152:155],a[168:171],v[212:215]
RT a[72:73],v249,0
MF v[212:215],v[156:159],a[184:187],v[212:215]
RT a[74:75],v249,1024
MF v[220:223],v[144:147],a[140:143],v[220:223]
MF v[220:223],v[148:151],a[156:159],v[220:223]
RT a[76:77],v249,8192
MF v[220:223],v[152:155],a[172:175],v[220:223]
RT a[78:79],v249,9216
buffer_load_dwordx4 a[44:47], v246, s[20:23], s55 offen offset:0 sc1
MF v[220:223],v[156:159],a[188:191],v[220:223]
s_waitcnt lgkmcnt(0)
MF v[192:195],a[192:195],v[160:163],v[192:195]
RT a[80:81],v250,0
MF v[192:195],a[208:211],v[164:167],v[192:195]
RT a[82:83],v250,1024
MF v[192:195],a[224:227],v[168:171],v[192:195]
MF v[192:195],a[240:243],v[172:175],v[192:195]
RT a[84:85],v250,8192
MF v[196:199],a[196:199],v[160:163],v[196:199]
RT a[86:87],v250,9216
buffer_load_dwordx4 a[48:51], v246, s[20:23], s52 offen offset:1024 sc1
MF v[196:199],a[212:215],v[164:167],v[196:199]
MF v[196:199],a[228:231],v[168:171],v[196:199]
RT a[88:89],v251,0
MF v[196:199],a[244:247],v[172:175],v[196:199]
RT a[90:91],v251,1024
MF v[208:211],v[160:163],a[200:203],v[208:211]
MF v[208:211],v[164:167],a[216:219],v[208:211]
RT a[92:93],v251,8192
buffer_load_dwordx4 a[52:55], v246, s[20:23], s53 offen offset:1024 sc1
MF v[208:211],v[168:171],a[232:235],v[208:211]
RT a[94:95],v251,9216
MF v[208:211],v[172:175],a[248:251],v[208:211]
MF v[216:219],v[160:163],a[204:207],v[216:219]
RT a[96:97],v252,0
MF v[216:219],v[164:167],a[220:223],v[216:219]
RT a[98:99],v252,1024
MF v[216:219],v[168:171],a[236:239],v[216:219]
PK v0,v96,v97
PK v1,v98,v99
MF v[216:219],v[172:175],a[252:255],v[216:219]
RT a[100:101],v252,8192
buffer_load_dwordx4 a[56:59], v246, s[20:23], s54 offen offset:1024 sc1
MF v[200:203],a[192:195],v[176:179],v[200:203]
RT a[102:103],v252,9216
PK v2,v100,v101
MF v[200:203],a[208:211],v[180:183],v[200:203]
PK v3,v102,v103
PK v8,v104,v105
MF v[200:203],a[224:227],v[184:187],v[200:203]
RT a[104:105],v253,0
PK v9,v106,v107
MF v[200:203],a[240:243],v[188:191],v[200:203]
RT a[106:107],v253,1024
PK v10,v108,v109
MF v[204:207],a[196:199],v[176:179],v[204:207]
buffer_load_dwordx4 a[60:63], v246, s[20:23], s55 offen offset:1024 sc1
PK v11,v110,v111
MF v[204:207],a[212:215],v[180:183],v[204:207]
RT a[108:109],v253,8192
PK v16,v112,v113
MF v[204:207],a[228:231],v[184:187],v[204:207]
RT a[110:111],v253,9216
PK v17,v114,v115
MF v[204:207],a[244:247],v[188:191],v[204:207]
PK v18,v116,v117
PK v19,v118,v119
MF v[212:215],v[176:179],a[200:203],v[212:215]
RT a[112:113],v254,0
PK v24,v120,v121
MF v[212:215],v[180:183],a[216:219],v[212:215]
RT a[114:115],v254,1024
PK v25,v122,v123
MF v[212:215],v[184:187],a[232:235],v[212:215]
PK v26,v124,v125
PK v27,v126,v127
MF v[212:215],v[188:191],a[248:251],v[212:215]
RT a[116:117],v254,8192
MF v[220:223],v[176:179],a[204:207],v[220:223]
RT a[118:119],v254,9216
MF v[220:223],v[180:183],a[220:223],v[220:223]
MF v[220:223],v[184:187],a[236:239],v[220:223]
MF v[220:223],v[188:191],a[252:255],v[220:223]
s_waitcnt vmcnt(18) lgkmcnt(0)
MF v[96:99],a[64:67],v[64:67],0
RT a[120:121],v255,0
RT a[122:123],v255,1024
MF v[96:99],a[68:71],v[80:83],v[96:99]
RT a[124:125],v255,8192
RT a[126:127],v255,9216
MF v[120:123],a[64:67],v[68:71],0
MF v[120:123],a[68:71],v[84:87],v[120:123]
MF v[144:147],a[64:67],v[72:75],0
MF v[144:147],a[68:71],v[88:91],v[144:147]
MF v[168:171],a[64:67],v[76:79],0
PK v32,v192,v193
PK v33,v194,v195
MF v[168:171],a[68:71],v[92:95],v[168:171]
s_waitcnt vmcnt(8) lgkmcnt(0)
s_barrier
MF v[100:103],a[72:75],v[64:67],0
RT a[64:65],v248,16384
s_add_u32 s48, s86, 0x200000
MF v[100:103],a[76:79],v[80:83],v[100:103]
RT a[66:67],v248,17408
s_and_b32 s48, s48, 0x3fffff
MF v[124:127],a[72:75],v[68:71],0
RT a[68:69],v248,24576
s_add_u32 m0, s34, 0x18000
MF v[124:127],a[76:79],v[84:87],v[124:127]
RT a[70:71],v248,25600
buffer_load_dwordx4 v232, s[24:27], s48 offen offset:0 lds
)A"
R"A(MF v[148:151],a[72:75],v[72:75],0
buffer_load_dwordx4 v233, s[24:27], s48 offen offset:1024 lds
PK v34,v196,v197
MF v[148:151],a[76:79],v[88:91],v[148:151]
PK v35,v198,v199
PK v40,v200,v201
MF v[172:175],a[72:75],v[76:79],0
buffer_load_dwordx4 v234, s[24:27], s48 offen offset:2048 lds
PK v41,v202,v203
MF v[172:175],a[76:79],v[92:95],v[172:175]
PK v42,v204,v205
PK v43,v206,v207
MF v[104:107],a[80:83],v[64:67],0
RT a[72:73],v249,16384
PK v48,v208,v209
MF v[104:107],a[84:87],v[80:83],v[104:107]
RT a[74:75],v249,17408
buffer_load_dwordx4 v235, s[24:27], s48 offen offset:3072 lds
MF v[128:131],a[80:83],v[68:71],0
RT a[76:77],v249,24576
PK v49,v210,v211
MF v[128:131],a[84:87],v[84:87],v[128:131]
RT a[78:79],v249,25600
PK v50,v212,v213
MF v[152:155],a[80:83],v[72:75],0
PK v51,v214,v215
PK v56,v216,v217
MF v[152:155],a[84:87],v[88:91],v[152:155]
s_add_u32 s48, s86, 0x280000
s_and_b32 s48, s48, 0x3fffff
MF v[176:179],a[80:83],v[76:79],0
s_add_u32 m0, s34, 0x1c000
PK v57,v218,v219
MF v[176:179],a[84:87],v[92:95],v[176:179]
buffer_load_dwordx4 v232, s[24:27], s48 offen offset:0 lds
PK v58,v220,v221
MF v[108:111],a[88:91],v[64:67],0
RT a[80:81],v250,16384
buffer_load_dwordx4 v233, s[24:27], s48 offen offset:1024 lds
MF v[108:111],a[92:95],v[80:83],v[108:111]
RT a[82:83],v250,17408
PK v59,v222,v223
MF v[132:135],a[88:91],v[68:71],0
RT a[84:85],v250,24576
s_mov_b32 s8, s88
MF v[132:135],a[92:95],v[84:87],v[132:135]
RT a[86:87],v250,25600
buffer_load_dwordx4 v234, s[24:27], s48 offen offset:2048 lds
MF v[156:159],a[88:91],v[72:75],0
s_mov_b32 s9, s89
s_mov_b32 s10, s42
MF v[156:159],a[92:95],v[88:91],v[156:159]
s_mov_b32 s12, s40
s_mov_b32 s13, s41
MF v[180:183],a[88:91],v[76:79],0
buffer_load_dwordx4 v235, s[24:27], s48 offen offset:3072 lds
s_mov_b32 s14, s43
MF v[180:183],a[92:95],v[92:95],v[180:183]
v_lshrrev_b32 v244, 2, v242
v_and_b32 v244, 0x7c, v244
MF v[112:115],a[96:99],v[64:67],0
RT a[88:89],v251,16384
s_lshl_b32 s51, s30, 7
MF v[112:115],a[100:103],v[80:83],v[112:115]
RT a[90:91],v251,17408
s_add_u32 s51, s51, 0x4901000
MF v[136:139],a[96:99],v[68:71],0
RT a[92:93],v251,24576
v_add_u32 v246, s82, v242
MF v[136:139],a[100:103],v[84:87],v[136:139]
RT a[94:95],v251,25600
s_add_u32 s82, s82, 0x800
MF v[160:163],a[96:99],v[72:75],0
s_and_b32 s82, s82, 0x3fff
s_add_u32 s49, s94, s90
MF v[160:163],a[100:103],v[88:91],v[160:163]
buffer_load_dwordx4 a[128:131], v246, s[20:23], s52 offen offset:0 sc1
buffer_load_dwordx4 a[132:135], v246, s[20:23], s53 offen offset:0 sc1
MF v[184:187],a[96:99],v[76:79],0
buffer_store_dwordx4 v[12:15], v242, s[20:23], s49 offen offset:0 sc1
MF v[184:187],a[100:103],v[92:95],v[184:187]
MF v[116:119],a[104:107],v[64:67],0
RT a[96:97],v252,16384
buffer_load_dwordx4 a[136:139], v246, s[20:23], s54 offen offset:0 sc1
MF v[116:119],a[108:111],v[80:83],v[116:119]
RT a[98:99],v252,17408
MF v[140:143],a[104:107],v[68:71],0
RT a[100:101],v252,24576
MF v[140:143],a[108:111],v[84:87],v[140:143]
RT a[102:103],v252,25600
buffer_load_dwordx4 a[140:143], v246, s[20:23], s55 offen offset:0 sc1
MF v[164:167],a[104:107],v[72:75],0
MF v[164:167],a[108:111],v[88:91],v[164:167]
MF v[188:191],a[104:107],v[76:79],0
MF v[188:191],a[108:111],v[92:95],v[188:191]
buffer_load_dwordx4 a[144:147], v246, s[20:23], s52 offen offset:1024 sc1
MF v[192:195],a[112:115],v[64:67],0
RT a[104:105],v253,16384
MF v[192:195],a[116:119],v[80:83],v[192:195]
RT a[106:107],v253,17408
MF v[200:203],a[112:115],v[68:71],0
RT a[108:109],v253,24576
buffer_load_dwordx4 a[148:151], v246, s[20:23], s53 offen offset:1024 sc1
MF v[200:203],a[116:119],v[84:87],v[200:203]
RT a[110:111],v253,25600
MF v[208:211],a[112:115],v[72:75],0
MF v[208:211],a[116:119],v[88:91],v[208:211]
buffer_load_dwordx4 a[152:155], v246, s[20:23], s54 offen offset:1024 sc1
MF v[216:219],a[112:115],v[76:79],0
MF v[216:219],a[116:119],v[92:95],v[216:219]
MF v[196:199],a[120:123],v[64:67],0
RT a[112:113],v254,16384
buffer_load_dwordx4 a[156:159], v246, s[20:23], s55 offen offset:1024 sc1
MF v[196:199],a[124:127],v[80:83],v[196:199]
RT a[114:115],v254,17408
MF v[204:207],a[120:123],v[68:71],0
RT a[116:117],v254,24576
MF v[204:207],a[124:127],v[84:87],v[204:207]
RT a[118:119],v254,25600
MF v[212:215],a[120:123],v[72:75],0
MF v[212:215],a[124:127],v[88:91],v[212:215]
MF v[220:223],a[120:123],v[76:79],0
MF v[220:223],a[124:127],v[92:95],v[220:223]
s_waitcnt lgkmcnt(15)
MF v[96:99],a[64:67],a[0:3],v[96:99]
RT a[120:121],v255,16384
RT a[122:123],v255,17408
MF v[96:99],a[68:71],a[16:19],v[96:99]
RT a[124:125],v255,24576
RT a[126:127],v255,25600
MF v[120:123],a[64:67],a[4:7],v[120:123]
MF v[120:123],a[68:71],a[20:23],v[120:123]
v_add_u32 v248, 0x10000, v248
v_add_u32 v249, 0x10000, v249
MF v[144:147],a[64:67],a[8:11],v[144:147]
v_add_u32 v250, 0x10000, v250
v_add_u32 v251, 0x10000, v251
MF v[144:147],a[68:71],a[24:27],v[144:147]
v_add_u32 v252, 0x10000, v252
v_add_u32 v253, 0x10000, v253
MF v[168:171],a[64:67],a[12:15],v[168:171]
v_add_u32 v254, 0x10000, v254
v_add_u32 v255, 0x10000, v255
MF v[168:171],a[68:71],a[28:31],v[168:171]
s_waitcnt vmcnt(17) lgkmcnt(0)
s_barrier
MF v[100:103],a[72:75],a[0:3],v[100:103]
RT a[64:65],v248,0
s_add_u32 s48, s86, 0x300000
MF v[100:103],a[76:79],a[16:19],v[100:103]
RT a[66:67],v248,1024
s_and_b32 s48, s48, 0x3fffff
MF v[124:127],a[72:75],a[4:7],v[124:127]
RT a[68:69],v248,8192
s_add_u32 m0, s34, 0x10000
MF v[124:127],a[76:79],a[20:23],v[124:127]
RT a[70:71],v248,9216
buffer_load_dwordx4 v232, s[24:27], s48 offen offset:0 lds
MF v[148:151],a[72:75],a[8:11],v[148:151]
buffer_load_dwordx4 v233, s[24:27], s48 offen offset:1024 lds
s_add_u32 s49, s95, s90
MF v[148:151],a[76:79],a[24:27],v[148:151]
buffer_store_dwordx4 v[20:23], v242, s[20:23], s49 offen offset:0 sc1
MF v[172:175],a[72:75],a[12:15],v[172:175]
MF v[172:175],a[76:79],a[28:31],v[172:175]
MF v[104:107],a[80:83],a[0:3],v[104:107]
RT a[72:73],v249,0
buffer_load_dwordx4 v234, s[24:27], s48 offen offset:2048 lds
MF v[104:107],a[84:87],a[16:19],v[104:107]
RT a[74:75],v249,1024
MF v[128:131],a[80:83],a[4:7],v[128:131]
RT a[76:77],v249,8192
MF v[128:131],a[84:87],a[20:23],v[128:131]
RT a[78:79],v249,9216
MF v[152:155],a[80:83],a[8:11],v[152:155]
buffer_load_dwordx4 v235, s[24:27], s48 offen offset:3072 lds
MF v[152:155],a[84:87],a[24:27],v[152:155]
MF v[176:179],a[80:83],a[12:15],v[176:179]
MF v[176:179],a[84:87],a[28:31],v[176:179]
MF v[108:111],a[88:91],a[0:3],v[108:111]
RT a[80:81],v250,0
v_add_u32 v246, s82, v242
MF v[108:111],a[92:95],a[16:19],v[108:111]
RT a[82:83],v250,1024
s_add_u32 s82, s82, 0x800
MF v[132:135],a[88:91],a[4:7],v[132:135]
RT a[84:85],v250,8192
s_and_b32 s82, s82, 0x3fff
MF v[132:135],a[92:95],a[20:23],v[132:135]
RT a[86:87],v250,9216
buffer_load_dwordx4 a[160:163], v246, s[20:23], s52 offen offset:0 sc1
MF v[156:159],a[88:91],a[8:11],v[156:159]
buffer_load_dwordx4 a[164:167], v246, s[20:23], s53 offen offset:0 sc1
MF v[156:159],a[92:95],a[24:27],v[156:159]
MF v[180:183],a[88:91],a[12:15],v[180:183]
MF v[180:183],a[92:95],a[28:31],v[180:183]
MF v[112:115],a[96:99],a[0:3],v[112:115]
RT a[88:89],v251,0
buffer_load_dwordx4 a[168:171], v246, s[20:23], s54 offen offset:0 sc1
MF v[112:115],a[100:103],a[16:19],v[112:115]
RT a[90:91],v251,1024
MF v[136:139],a[96:99],a[4:7],v[136:139]
RT a[92:93],v251,8192
s_add_u32 s49, s95, s90
MF v[136:139],a[100:103],a[20:23],v[136:139]
RT a[94:95],v251,9216
buffer_store_dwordx4 v[28:31], v242, s[20:23], s49 offen offset:1024 sc1
MF v[160:163],a[96:99],a[8:11],v[160:163]
buffer_load_dwordx4 a[172:175], v246, s[20:23], s55 offen offset:0 sc1
MF v[160:163],a[100:103],a[24:27],v[160:163]
MF v[184:187],a[96:99],a[12:15],v[184:187]
MF v[184:187],a[100:103],a[28:31],v[184:187]
MF v[116:119],a[104:107],a[0:3],v[116:119]
RT a[96:97],v252,0
buffer_load_dwordx4 a[176:179], v246, s[20:23], s52 offen offset:1024 sc1
MF v[116:119],a[108:111],a[16:19],v[116:119]
RT a[98:99],v252,1024
MF v[140:143],a[104:107],a[4:7],v[140:143]
RT a[100:101],v252,8192
MF v[140:143],a[108:111],a[20:23],v[140:143]
RT a[102:103],v252,9216
MF v[164:167],a[104:107],a[8:11],v[164:167]
buffer_load_dwordx4 a[180:183], v246, s[20:23], s53 offen offset:1024 sc1
MF v[164:167],a[108:111],a[24:27],v[164:167]
MF v[188:191],a[104:107],a[12:15],v[188:191]
MF v[188:191],a[108:111],a[28:31],v[188:191]
MF v[192:195],a[112:115],a[0:3],v[192:195]
RT a[104:105],v253,0
buffer_load_dwordx4 a[184:187], v246, s[20:23], s54 offen offset:1024 sc1
MF v[192:195],a[116:119],a[16:19],v[192:195]
RT a[106:107],v253,1024
MF v[200:203],a[112:115],a[4:7],v[200:203]
RT a[108:109],v253,8192
MF v[200:203],a[116:119],a[20:23],v[200:203]
RT a[110:111],v253,9216
MF v[208:211],a[112:115],a[8:11],v[208:211]
buffer_load_dwordx4 a[188:191], v246, s[20:23], s55 offen offset:1024 sc1
MF v[208:211],a[116:119],a[24:27],v[208:211]
MF v[216:219],a[112:115],a[12:15],v[216:219]
MF v[216:219],a[116:119],a[28:31],v[216:219]
MF v[196:199],a[120:123],a[0:3],v[196:199]
RT a[112:113],v254,0
buffer_load_dword v243, v244, s[20:23], s51 offen offset:0 sc1
MF v[196:199],a[124:127],a[16:19],v[196:199]
RT a[114:115],v254,1024
MF v[204:207],a[120:123],a[4:7],v[204:207]
RT a[116:117],v254,8192
MF v[204:207],a[124:127],a[20:23],v[204:207]
RT a[118:119],v254,9216
MF v[212:215],a[120:123],a[8:11],v[212:215]
MF v[212:215],a[124:127],a[24:27],v[212:215]
MF v[220:223],a[120:123],a[12:15],v[220:223]
MF v[220:223],a[124:127],a[28:31],v[220:223]
s_waitcnt lgkmcnt(15)
MF v[96:99],a[64:67],a[32:35],v[96:99]
RT a[120:121],v255,0
RT a[122:123],v255,1024
MF v[96:99],a[68:71],a[48:51],v[96:99]
RT a[124:125],v255,8192
RT a[126:127],v255,9216
MF v[120:123],a[64:67],a[36:39],v[120:123]
MF v[120:123],a[68:71],a[52:55],v[120:123]
MF v[144:147],a[64:67],a[40:43],v[144:147]
MF v[144:147],a[68:71],a[56:59],v[144:147]
MF v[168:171],a[64:67],a[44:47],v[168:171]
s_add_u32 s49, s83, s91
MF v[168:171],a[68:71],a[60:63],v[168:171]
s_waitcnt vmcnt(15) lgkmcnt(0)
s_barrier
MF v[100:103],a[72:75],a[32:35],v[100:103]
RT a[64:65],v248,16384
s_add_u32 s48, s86, 0x380000
MF v[100:103],a[76:79],a[48:51],v[100:103]
RT a[66:67],v248,17408
s_and_b32 s48, s48, 0x3fffff
MF v[124:127],a[72:75],a[36:39],v[124:127]
RT a[68:69],v248,24576
s_add_u32 m0, s34, 0x14000
MF v[124:127],a[76:79],a[52:55],v[124:127]
RT a[70:71],v248,25600
buffer_load_dwordx4 v232, s[24:27], s48 offen offset:0 lds
MF v[148:151],a[72:75],a[40:43],v[148:151]
buffer_load_dwordx4 v233, s[24:27], s48 offen offset:1024 lds
buffer_store_dwordx4 v[36:39], v242, s[20:23], s49 offen offset:0 sc1
MF v[148:151],a[76:79],a[56:59],v[148:151]
MF v[172:175],a[72:75],a[44:47],v[172:175]
buffer_load_dwordx4 v234, s[24:27], s48 offen offset:2048 lds
MF v[172:175],a[76:79],a[60:63],v[172:175]
MF v[104:107],a[80:83],a[32:35],v[104:107]
RT a[72:73],v249,16384
MF v[104:107],a[84:87],a[48:51],v[104:107]
RT a[74:75],v249,17408
buffer_load_dwordx4 v235, s[24:27], s48 offen offset:3072 lds
MF v[128:131],a[80:83],a[36:39],v[128:131]
RT a[76:77],v249,24576
MF v[128:131],a[84:87],a[52:55],v[128:131]
RT a[78:79],v249,25600
MF v[152:155],a[80:83],a[40:43],v[152:155]
MF v[152:155],a[84:87],a[56:59],v[152:155]
v_add_u32 v246, s82, v242
s_add_u32 s82, s82, 0x800
MF v[176:179],a[80:83],a[44:47],v[176:179]
s_and_b32 s82, s82, 0x3fff
MF v[176:179],a[84:87],a[60:63],v[176:179]
buffer_load_dwordx4 a[192:195], v246, s[20:23], s52 offen offset:0 sc1
MF v[108:111],a[88:91],a[32:35],v[108:111]
RT a[80:81],v250,16384
buffer_load_dwordx4 a[196:199], v246, s[20:23], s53 offen offset:0 sc1
MF v[108:111],a[92:95],a[48:51],v[108:111]
RT a[82:83],v250,17408
MF v[132:135],a[88:91],a[36:39],v[132:135]
RT a[84:85],v250,24576
MF v[132:135],a[92:95],a[52:55],v[132:135]
RT a[86:87],v250,25600
buffer_load_dwordx4 a[200:203], v246, s[20:23], s54 offen offset:0 sc1
MF v[156:159],a[88:91],a[40:43],v[156:159]
MF v[156:159],a[92:95],a[56:59],v[156:159]
MF v[180:183],a[88:91],a[44:47],v[180:183]
buffer_load_dwordx4 a[204:207], v246, s[20:23], s55 offen offset:0 sc1
MF v[180:183],a[92:95],a[60:63],v[180:183]
MF v[112:115],a[96:99],a[32:35],v[112:115]
RT a[88:89],v251,16384
MF v[112:115],a[100:103],a[48:51],v[112:115]
RT a[90:91],v251,17408
MF v[136:139],a[96:99],a[36:39],v[136:139]
RT a[92:93],v251,24576
buffer_load_dwordx4 a[208:211], v246, s[20:23], s52 offen offset:1024 sc1
MF v[136:139],a[100:103],a[52:55],v[136:139]
RT a[94:95],v251,25600
s_add_u32 s49, s94, s91
MF v[160:163],a[96:99],a[40:43],v[160:163]
buffer_store_dwordx4 v[44:47], v242, s[20:23], s49 offen offset:0 sc1
MF v[160:163],a[100:103],a[56:59],v[160:163]
buffer_load_dwordx4 a[212:215], v246, s[20:23], s53 offen offset:1024 sc1
MF v[184:187],a[96:99],a[44:47],v[184:187]
MF v[184:187],a[100:103],a[60:63],v[184:187]
MF v[116:119],a[104:107],a[32:35],v[116:119]
RT a[96:97],v252,16384
buffer_load_dwordx4 a[216:219], v246, s[20:23], s54 offen offset:1024 sc1
MF v[116:119],a[108:111],a[48:51],v[116:119]
RT a[98:99],v252,17408
MF v[140:143],a[104:107],a[36:39],v[140:143]
RT a[100:101],v252,24576
MF v[140:143],a[108:111],a[52:55],v[140:143]
RT a[102:103],v252,25600
buffer_load_dwordx4 a[220:223], v246, s[20:23], s55 offen offset:1024 sc1
MF v[164:167],a[104:107],a[40:43],v[164:167]
MF v[164:167],a[108:111],a[56:59],v[164:167]
MF v[188:191],a[104:107],a[44:47],v[188:191]
MF v[188:191],a[108:111],a[60:63],v[188:191]
s_waitcnt vmcnt(14)
v_cmp_ne_u32 vcc, s37, v243
MF v[192:195],a[112:115],a[32:35],v[192:195]
RT a[104:105],v253,16384
s_nop 0
MF v[192:195],a[116:119],a[48:51],v[192:195]
RT a[106:107],v253,17408
s_cbranch_vccnz L_SP_K1_%=
L_RT_K1_%=:
MF v[200:203],a[112:115],a[36:39],v[200:203]
RT a[108:109],v253,24576
s_add_u32 m0, s34, 0x0
MF v[200:203],a[116:119],a[52:55],v[200:203]
RT a[110:111],v253,25600
s_add_u32 s48, s34, 0x0
MF v[208:211],a[112:115],a[40:43],v[208:211]
buffer_load_dwordx4 v242, s[8:11], s48 offen offset:0 sc1 lds
buffer_load_dwordx4 v242, s[8:11], s48 offen offset:1024 sc1 lds
MF v[208:211],a[116:119],a[56:59],v[208:211]
buffer_load_dwordx4 v242, s[8:11], s48 offen offset:2048 sc1 lds
MF v[216:219],a[112:115],a[44:47],v[216:219]
MF v[216:219],a[116:119],a[60:63],v[216:219]
MF v[196:199],a[120:123],a[32:35],v[196:199]
RT a[112:113],v254,16384
buffer_load_dwordx4 v242, s[8:11], s48 offen offset:3072 sc1 lds
MF v[196:199],a[124:127],a[48:51],v[196:199]
RT a[114:115],v254,17408
MF v[204:207],a[120:123],a[36:39],v[204:207]
RT a[116:117],v254,24576
MF v[204:207],a[124:127],a[52:55],v[204:207]
RT a[118:119],v254,25600
MF v[212:215],a[120:123],a[40:43],v[212:215]
MF v[212:215],a[124:127],a[56:59],v[212:215]
MF v[220:223],a[120:123],a[44:47],v[220:223]
MF v[220:223],a[124:127],a[60:63],v[220:223]
s_waitcnt lgkmcnt(15)
MF v[96:99],a[64:67],a[128:131],v[96:99]
RT a[120:121],v255,16384
RT a[122:123],v255,17408
MF v[96:99],a[68:71],a[144:147],v[96:99]
RT a[124:125],v255,24576
RT a[126:127],v255,25600
MF v[120:123],a[64:67],a[132:135],v[120:123]
MF v[120:123],a[68:71],a[148:151],v[120:123]
v_subrev_u32 v248, 0x10000, v248
v_subrev_u32 v249, 0x10000, v249
MF v[144:147],a[64:67],a[136:139],v[144:147]
v_subrev_u32 v250, 0x10000, v250
v_subrev_u32 v251, 0x10000, v251
MF v[144:147],a[68:71],a[152:155],v[144:147]
)A"
R"A(v_subrev_u32 v252, 0x10000, v252
v_subrev_u32 v253, 0x10000, v253
MF v[168:171],a[64:67],a[140:143],v[168:171]
v_subrev_u32 v254, 0x10000, v254
v_subrev_u32 v255, 0x10000, v255
MF v[168:171],a[68:71],a[156:159],v[168:171]
s_waitcnt vmcnt(19) lgkmcnt(0)
s_barrier
MF v[100:103],a[72:75],a[128:131],v[100:103]
RT a[64:65],v248,32768
v_add_u32 v246, s82, v242
MF v[100:103],a[76:79],a[144:147],v[100:103]
RT a[66:67],v248,33792
s_add_u32 s82, s82, 0x800
MF v[124:127],a[72:75],a[132:135],v[124:127]
RT a[68:69],v248,40960
s_and_b32 s82, s82, 0x3fff
MF v[124:127],a[76:79],a[148:151],v[124:127]
RT a[70:71],v248,41984
buffer_load_dwordx4 a[224:227], v246, s[20:23], s52 offen offset:0 sc1
MF v[148:151],a[72:75],a[136:139],v[148:151]
s_add_u32 s49, s95, s91
MF v[148:151],a[76:79],a[152:155],v[148:151]
buffer_store_dwordx4 v[52:55], v242, s[20:23], s49 offen offset:0 sc1
MF v[172:175],a[72:75],a[140:143],v[172:175]
buffer_load_dwordx4 a[228:231], v246, s[20:23], s53 offen offset:0 sc1
MF v[172:175],a[76:79],a[156:159],v[172:175]
MF v[104:107],a[80:83],a[128:131],v[104:107]
RT a[72:73],v249,32768
MF v[104:107],a[84:87],a[144:147],v[104:107]
RT a[74:75],v249,33792
MF v[128:131],a[80:83],a[132:135],v[128:131]
RT a[76:77],v249,40960
MF v[128:131],a[84:87],a[148:151],v[128:131]
RT a[78:79],v249,41984
MF v[152:155],a[80:83],a[136:139],v[152:155]
s_lshl_b32 s79, s76, 12
s_add_u32 s79, s79, 0x4800000
MF v[152:155],a[84:87],a[152:155],v[152:155]
buffer_load_dwordx4 a[232:235], v246, s[20:23], s54 offen offset:0 sc1
buffer_load_dwordx4 v[224:227], v242, s[20:23], s79 offen offset:0 sc1
MF v[176:179],a[80:83],a[140:143],v[176:179]
MF v[176:179],a[84:87],a[156:159],v[176:179]
MF v[108:111],a[88:91],a[128:131],v[108:111]
RT a[80:81],v250,32768
MF v[108:111],a[92:95],a[144:147],v[108:111]
RT a[82:83],v250,33792
MF v[132:135],a[88:91],a[132:135],v[132:135]
RT a[84:85],v250,40960
buffer_load_dwordx4 v[228:231], v242, s[20:23], s79 offen offset:1024 sc1
MF v[132:135],a[92:95],a[148:151],v[132:135]
RT a[86:87],v250,41984
buffer_load_dwordx4 a[236:239], v246, s[20:23], s55 offen offset:0 sc1
MF v[156:159],a[88:91],a[136:139],v[156:159]
MF v[156:159],a[92:95],a[152:155],v[156:159]
MF v[180:183],a[88:91],a[140:143],v[180:183]
MF v[180:183],a[92:95],a[156:159],v[180:183]
MF v[112:115],a[96:99],a[128:131],v[112:115]
RT a[88:89],v251,32768
buffer_load_dwordx4 v[232:235], v242, s[20:23], s79 offen offset:2048 sc1
MF v[112:115],a[100:103],a[144:147],v[112:115]
RT a[90:91],v251,33792
MF v[136:139],a[96:99],a[132:135],v[136:139]
RT a[92:93],v251,40960
buffer_load_dwordx4 a[240:243], v246, s[20:23], s52 offen offset:1024 sc1
MF v[136:139],a[100:103],a[148:151],v[136:139]
RT a[94:95],v251,41984
s_add_u32 s49, s95, s91
MF v[160:163],a[96:99],a[136:139],v[160:163]
buffer_store_dwordx4 v[60:63], v242, s[20:23], s49 offen offset:1024 sc1
MF v[160:163],a[100:103],a[152:155],v[160:163]
MF v[184:187],a[96:99],a[140:143],v[184:187]
buffer_load_dwordx4 v[236:239], v242, s[20:23], s79 offen offset:3072 sc1
MF v[184:187],a[100:103],a[156:159],v[184:187]
MF v[116:119],a[104:107],a[128:131],v[116:119]
RT a[96:97],v252,32768
buffer_load_dwordx4 a[244:247], v246, s[20:23], s53 offen offset:1024 sc1
MF v[116:119],a[108:111],a[144:147],v[116:119]
RT a[98:99],v252,33792
MF v[140:143],a[104:107],a[132:135],v[140:143]
RT a[100:101],v252,40960
MF v[140:143],a[108:111],a[148:151],v[140:143]
RT a[102:103],v252,41984
MF v[164:167],a[104:107],a[136:139],v[164:167]
MF v[164:167],a[108:111],a[152:155],v[164:167]
MF v[188:191],a[104:107],a[140:143],v[188:191]
MF v[188:191],a[108:111],a[156:159],v[188:191]
buffer_load_dwordx4 a[248:251], v246, s[20:23], s54 offen offset:1024 sc1
MF v[192:195],a[112:115],a[128:131],v[192:195]
RT a[104:105],v253,32768
MF v[192:195],a[116:119],a[144:147],v[192:195]
RT a[106:107],v253,33792
MF v[200:203],a[112:115],a[132:135],v[200:203]
RT a[108:109],v253,40960
MF v[200:203],a[116:119],a[148:151],v[200:203]
RT a[110:111],v253,41984
MF v[208:211],a[112:115],a[136:139],v[208:211]
MF v[208:211],a[116:119],a[152:155],v[208:211]
buffer_load_dwordx4 a[252:255], v246, s[20:23], s55 offen offset:1024 sc1
MF v[216:219],a[112:115],a[140:143],v[216:219]
MF v[216:219],a[116:119],a[156:159],v[216:219]
MF v[196:199],a[120:123],a[128:131],v[196:199]
RT a[112:113],v254,32768
MF v[196:199],a[124:127],a[144:147],v[196:199]
RT a[114:115],v254,33792
MF v[204:207],a[120:123],a[132:135],v[204:207]
RT a[116:117],v254,40960
MF v[204:207],a[124:127],a[148:151],v[204:207]
RT a[118:119],v254,41984
MF v[212:215],a[120:123],a[136:139],v[212:215]
MF v[212:215],a[124:127],a[152:155],v[212:215]
MF v[220:223],a[120:123],a[140:143],v[220:223]
MF v[220:223],a[124:127],a[156:159],v[220:223]
s_waitcnt lgkmcnt(15)
MF v[96:99],a[64:67],a[160:163],v[96:99]
RT a[120:121],v255,32768
RT a[122:123],v255,33792
MF v[96:99],a[68:71],a[176:179],v[96:99]
RT a[124:125],v255,40960
RT a[126:127],v255,41984
MF v[120:123],a[64:67],a[164:167],v[120:123]
MF v[120:123],a[68:71],a[180:183],v[120:123]
MF v[144:147],a[64:67],a[168:171],v[144:147]
MF v[144:147],a[68:71],a[184:187],v[144:147]
MF v[168:171],a[64:67],a[172:175],v[168:171]
s_add_u32 s49, s83, s93
MF v[168:171],a[68:71],a[188:191],v[168:171]
s_waitcnt vmcnt(18) lgkmcnt(0)
s_barrier
MF v[100:103],a[72:75],a[160:163],v[100:103]
RT a[64:65],v248,49152
v_add_u32 v246, s82, v242
MF v[100:103],a[76:79],a[176:179],v[100:103]
RT a[66:67],v248,50176
s_add_u32 s82, s82, 0x800
MF v[124:127],a[72:75],a[164:167],v[124:127]
RT a[68:69],v248,57344
s_and_b32 s82, s82, 0x3fff
MF v[124:127],a[76:79],a[180:183],v[124:127]
RT a[70:71],v248,58368
buffer_load_dwordx4 v[64:67], v246, s[20:23], s52 offen offset:0 sc1
MF v[148:151],a[72:75],a[168:171],v[148:151]
buffer_load_dwordx4 v[68:71], v246, s[20:23], s53 offen offset:0 sc1
buffer_store_dwordx4 v[32:35], v242, s[20:23], s49 offen offset:0 sc1
MF v[148:151],a[76:79],a[184:187],v[148:151]
s_waitcnt vmcnt(6)
v_xor_b32 v225, s37, v225
MF v[172:175],a[72:75],a[172:175],v[172:175]
v_xor_b32 v227, s37, v227
v_xor_b32 v229, s37, v229
MF v[172:175],a[76:79],a[188:191],v[172:175]
v_xor_b32 v231, s37, v231
v_xor_b32 v233, s37, v233
MF v[104:107],a[80:83],a[160:163],v[104:107]
RT a[72:73],v249,49152
buffer_load_dwordx4 v[72:75], v246, s[20:23], s54 offen offset:0 sc1
MF v[104:107],a[84:87],a[176:179],v[104:107]
RT a[74:75],v249,50176
v_xor_b32 v235, s37, v235
MF v[128:131],a[80:83],a[164:167],v[128:131]
RT a[76:77],v249,57344
v_xor_b32 v237, s37, v237
MF v[128:131],a[84:87],a[180:183],v[128:131]
RT a[78:79],v249,58368
v_xor_b32 v239, s37, v239
MF v[152:155],a[80:83],a[168:171],v[152:155]
buffer_load_dwordx4 v[76:79], v246, s[20:23], s55 offen offset:0 sc1
v_or3_b32 v243, v225, v227, v229
MF v[152:155],a[84:87],a[184:187],v[152:155]
v_or3_b32 v243, v243, v231, v233
v_or3_b32 v243, v243, v235, v237
MF v[176:179],a[80:83],a[172:175],v[176:179]
v_or_b32 v243, v243, v239
v_cmp_ne_u32 vcc, 0, v243
MF v[176:179],a[84:87],a[188:191],v[176:179]
s_nop 0
s_cbranch_vccnz L_SP_YB_%=
L_RT_YB_%=:
MF v[108:111],a[88:91],a[160:163],v[108:111]
RT a[80:81],v250,49152
v_add_f32 v224, v224, v228
MF v[108:111],a[92:95],a[176:179],v[108:111]
RT a[82:83],v250,50176
buffer_load_dwordx4 v[80:83], v246, s[20:23], s52 offen offset:1024 sc1
MF v[132:135],a[88:91],a[164:167],v[132:135]
RT a[84:85],v250,57344
v_add_f32 v232, v232, v236
MF v[132:135],a[92:95],a[180:183],v[132:135]
RT a[86:87],v250,58368
v_add_f32 v224, v224, v232
MF v[156:159],a[88:91],a[168:171],v[156:159]
v_add_f32 v226, v226, v230
v_add_f32 v234, v234, v238
MF v[156:159],a[92:95],a[184:187],v[156:159]
buffer_load_dwordx4 v[84:87], v246, s[20:23], s53 offen offset:1024 sc1
v_add_f32 v226, v226, v234
MF v[180:183],a[88:91],a[172:175],v[180:183]
v_add_f32_dpp v224, v224, v224 row_ror:8 row_mask:0xf bank_mask:0xf
MF v[180:183],a[92:95],a[188:191],v[180:183]
v_add_f32_dpp v226, v226, v226 row_ror:8 row_mask:0xf bank_mask:0xf
v_mov_b32 v225, v224
MF v[112:115],a[96:99],a[160:163],v[112:115]
RT a[88:89],v251,49152
v_mov_b32 v227, v226
MF v[112:115],a[100:103],a[176:179],v[112:115]
RT a[90:91],v251,50176
buffer_load_dwordx4 v[88:91], v246, s[20:23], s54 offen offset:1024 sc1
MF v[136:139],a[96:99],a[164:167],v[136:139]
RT a[92:93],v251,57344
s_add_u32 s49, s94, s93
MF v[136:139],a[100:103],a[180:183],v[136:139]
RT a[94:95],v251,58368
buffer_store_dwordx4 v[40:43], v242, s[20:23], s49 offen offset:0 sc1
MF v[160:163],a[96:99],a[168:171],v[160:163]
v_permlane16_swap_b32 v224, v225
v_permlane16_swap_b32 v226, v227
MF v[160:163],a[100:103],a[184:187],v[160:163]
v_add_f32 v224, v224, v225
v_add_f32 v226, v226, v227
MF v[184:187],a[96:99],a[172:175],v[184:187]
buffer_load_dwordx4 v[92:95], v246, s[20:23], s55 offen offset:1024 sc1
v_mov_b32 v225, v224
MF v[184:187],a[100:103],a[188:191],v[184:187]
v_mov_b32 v227, v226
MF v[116:119],a[104:107],a[160:163],v[116:119]
RT a[96:97],v252,49152
v_permlane32_swap_b32 v224, v225
MF v[116:119],a[108:111],a[176:179],v[116:119]
RT a[98:99],v252,50176
v_permlane32_swap_b32 v226, v227
MF v[140:143],a[104:107],a[164:167],v[140:143]
RT a[100:101],v252,57344
s_add_u32 m0, s34, 0x8000
MF v[140:143],a[108:111],a[180:183],v[140:143]
RT a[102:103],v252,58368
s_add_u32 s48, s34, 0x8000
MF v[164:167],a[104:107],a[168:171],v[164:167]
buffer_load_dwordx4 v242, s[8:11], s48 offen offset:0 sc1 lds
v_add_f32 v224, v224, v225
MF v[164:167],a[108:111],a[184:187],v[164:167]
v_add_f32 v226, v226, v227
v_mov_b32 v225, s37
MF v[188:191],a[104:107],a[172:175],v[188:191]
v_mov_b32 v227, s37
v_and_b32 v228, 0x70, v242
MF v[188:191],a[108:111],a[188:191],v[188:191]
buffer_load_dwordx4 v242, s[8:11], s48 offen offset:1024 sc1 lds
s_lshl_b32 s49, s76, 7
MF v[192:195],a[112:115],a[160:163],v[192:195]
RT a[104:105],v253,49152
s_add_u32 s49, s49, 0x4908000
MF v[192:195],a[116:119],a[176:179],v[192:195]
RT a[106:107],v253,50176
buffer_store_dwordx4 v[224:227], v228, s[20:23], s49 offen offset:0 sc1
MF v[200:203],a[112:115],a[164:167],v[200:203]
RT a[108:109],v253,57344
MF v[200:203],a[116:119],a[180:183],v[200:203]
RT a[110:111],v253,58368
buffer_load_dwordx4 v242, s[8:11], s48 offen offset:2048 sc1 lds
MF v[208:211],a[112:115],a[168:171],v[208:211]
MF v[208:211],a[116:119],a[184:187],v[208:211]
MF v[216:219],a[112:115],a[172:175],v[216:219]
MF v[216:219],a[116:119],a[188:191],v[216:219]
buffer_load_dwordx4 v242, s[8:11], s48 offen offset:3072 sc1 lds
MF v[196:199],a[120:123],a[160:163],v[196:199]
RT a[112:113],v254,49152
MF v[196:199],a[124:127],a[176:179],v[196:199]
RT a[114:115],v254,50176
MF v[204:207],a[120:123],a[164:167],v[204:207]
RT a[116:117],v254,57344
MF v[204:207],a[124:127],a[180:183],v[204:207]
RT a[118:119],v254,58368
MF v[212:215],a[120:123],a[168:171],v[212:215]
MF v[212:215],a[124:127],a[184:187],v[212:215]
MF v[220:223],a[120:123],a[172:175],v[220:223]
MF v[220:223],a[124:127],a[188:191],v[220:223]
s_waitcnt lgkmcnt(15)
MF v[96:99],a[64:67],a[192:195],v[96:99]
RT a[120:121],v255,49152
RT a[122:123],v255,50176
MF v[96:99],a[68:71],a[208:211],v[96:99]
RT a[124:125],v255,57344
RT a[126:127],v255,58368
MF v[120:123],a[64:67],a[196:199],v[120:123]
MF v[120:123],a[68:71],a[212:215],v[120:123]
MF v[144:147],a[64:67],a[200:203],v[144:147]
MF v[144:147],a[68:71],a[216:219],v[144:147]
MF v[168:171],a[64:67],a[204:207],v[168:171]
s_add_u32 s49, s95, s93
MF v[168:171],a[68:71],a[220:223],v[168:171]
s_waitcnt vmcnt(15) lgkmcnt(0)
s_barrier
MF v[100:103],a[72:75],a[192:195],v[100:103]
RT a[64:65],v248,0
s_add_u32 m0, s34, 0x4000
MF v[100:103],a[76:79],a[208:211],v[100:103]
RT a[66:67],v248,1024
s_add_u32 s48, s34, 0x10000
MF v[124:127],a[72:75],a[196:199],v[124:127]
RT a[68:69],v248,8192
buffer_load_dwordx4 v242, s[12:15], s48 offen offset:0 sc1 lds
MF v[124:127],a[76:79],a[212:215],v[124:127]
RT a[70:71],v248,9216
buffer_store_dwordx4 v[48:51], v242, s[20:23], s49 offen offset:0 sc1
MF v[148:151],a[72:75],a[200:203],v[148:151]
MF v[148:151],a[76:79],a[216:219],v[148:151]
MF v[172:175],a[72:75],a[204:207],v[172:175]
buffer_load_dwordx4 v242, s[12:15], s48 offen offset:1024 sc1 lds
MF v[172:175],a[76:79],a[220:223],v[172:175]
MF v[104:107],a[80:83],a[192:195],v[104:107]
RT a[72:73],v249,0
MF v[104:107],a[84:87],a[208:211],v[104:107]
RT a[74:75],v249,1024
MF v[128:131],a[80:83],a[196:199],v[128:131]
RT a[76:77],v249,8192
MF v[128:131],a[84:87],a[212:215],v[128:131]
RT a[78:79],v249,9216
MF v[152:155],a[80:83],a[200:203],v[152:155]
MF v[152:155],a[84:87],a[216:219],v[152:155]
buffer_load_dwordx4 v242, s[12:15], s48 offen offset:2048 sc1 lds
MF v[176:179],a[80:83],a[204:207],v[176:179]
MF v[176:179],a[84:87],a[220:223],v[176:179]
MF v[108:111],a[88:91],a[192:195],v[108:111]
RT a[80:81],v250,0
MF v[108:111],a[92:95],a[208:211],v[108:111]
RT a[82:83],v250,1024
MF v[132:135],a[88:91],a[196:199],v[132:135]
RT a[84:85],v250,8192
MF v[132:135],a[92:95],a[212:215],v[132:135]
RT a[86:87],v250,9216
buffer_load_dwordx4 v242, s[12:15], s48 offen offset:3072 sc1 lds
MF v[156:159],a[88:91],a[200:203],v[156:159]
MF v[156:159],a[92:95],a[216:219],v[156:159]
MF v[180:183],a[88:91],a[204:207],v[180:183]
MF v[180:183],a[92:95],a[220:223],v[180:183]
MF v[112:115],a[96:99],a[192:195],v[112:115]
RT a[88:89],v251,0
MF v[112:115],a[100:103],a[208:211],v[112:115]
RT a[90:91],v251,1024
MF v[136:139],a[96:99],a[196:199],v[136:139]
RT a[92:93],v251,8192
s_add_u32 m0, s34, 0xc000
MF v[136:139],a[100:103],a[212:215],v[136:139]
RT a[94:95],v251,9216
s_add_u32 s48, s34, 0x18000
MF v[160:163],a[96:99],a[200:203],v[160:163]
buffer_load_dwordx4 v242, s[12:15], s48 offen offset:0 sc1 lds
s_add_u32 s49, s95, s93
MF v[160:163],a[100:103],a[216:219],v[160:163]
buffer_store_dwordx4 v[56:59], v242, s[20:23], s49 offen offset:1024 sc1
MF v[184:187],a[96:99],a[204:207],v[184:187]
MF v[184:187],a[100:103],a[220:223],v[184:187]
MF v[116:119],a[104:107],a[192:195],v[116:119]
RT a[96:97],v252,0
buffer_load_dwordx4 v242, s[12:15], s48 offen offset:1024 sc1 lds
MF v[116:119],a[108:111],a[208:211],v[116:119]
RT a[98:99],v252,1024
MF v[140:143],a[104:107],a[196:199],v[140:143]
RT a[100:101],v252,8192
MF v[140:143],a[108:111],a[212:215],v[140:143]
RT a[102:103],v252,9216
MF v[164:167],a[104:107],a[200:203],v[164:167]
MF v[164:167],a[108:111],a[216:219],v[164:167]
MF v[188:191],a[104:107],a[204:207],v[188:191]
MF v[188:191],a[108:111],a[220:223],v[188:191]
buffer_load_dwordx4 v242, s[12:15], s48 offen offset:2048 sc1 lds
MF v[192:195],a[112:115],a[192:195],v[192:195]
RT a[104:105],v253,0
MF v[192:195],a[116:119],a[208:211],v[192:195]
RT a[106:107],v253,1024
MF v[200:203],a[112:115],a[196:199],v[200:203]
RT a[108:109],v253,8192
MF v[200:203],a[116:119],a[212:215],v[200:203]
RT a[110:111],v253,9216
MF v[208:211],a[112:115],a[200:203],v[208:211]
MF v[208:211],a[116:119],a[216:219],v[208:211]
buffer_load_dwordx4 v242, s[12:15], s48 offen offset:3072 sc1 lds
MF v[216:219],a[112:115],a[204:207],v[216:219]
MF v[216:219],a[116:119],a[220:223],v[216:219]
MF v[196:199],a[120:123],a[192:195],v[196:199]
RT a[112:113],v254,0
MF v[196:199],a[124:127],a[208:211],v[196:199]
RT a[114:115],v254,1024
MF v[204:207],a[120:123],a[196:199],v[204:207]
RT a[116:117],v254,8192
MF v[204:207],a[124:127],a[212:215],v[204:207]
RT a[118:119],v254,9216
MF v[212:215],a[120:123],a[200:203],v[212:215]
MF v[212:215],a[124:127],a[216:219],v[212:215]
MF v[220:223],a[120:123],a[204:207],v[220:223]
MF v[220:223],a[124:127],a[220:223],v[220:223]
s_waitcnt lgkmcnt(15)
MF v[96:99],a[64:67],a[224:227],v[96:99]
RT a[120:121],v255,0
RT a[122:123],v255,1024
MF v[96:99],a[68:71],a[240:243],v[96:99]
RT a[124:125],v255,8192
RT a[126:127],v255,9216
MF v[120:123],a[64:67],a[228:231],v[120:123]
s_and_b32 s80, s76, 7
)A"
R"A(s_and_b32 s81, s80, 1
MF v[120:123],a[68:71],a[244:247],v[120:123]
s_lshr_b32 s80, s80, 1
s_lshl_b32 s81, s81, 3
MF v[144:147],a[64:67],a[232:235],v[144:147]
s_lshr_b32 s82, s76, 5
s_add_u32 s81, s81, s82
MF v[144:147],a[68:71],a[248:251],v[144:147]
s_lshr_b32 s82, s76, 3
s_and_b32 s82, s82, 3
MF v[168:171],a[64:67],a[236:239],v[168:171]
s_add_u32 s49, s83, s92
s_lshl_b32 s53, s80, 2
MF v[168:171],a[68:71],a[252:255],v[168:171]
s_waitcnt vmcnt(15) lgkmcnt(0)
s_barrier
MF v[100:103],a[72:75],a[224:227],v[100:103]
RT a[64:65],v248,16384
s_add_u32 m0, s34, 0x18000
MF v[100:103],a[76:79],a[240:243],v[100:103]
RT a[66:67],v248,17408
s_add_u32 s48, s34, 0x18000
MF v[124:127],a[72:75],a[228:231],v[124:127]
RT a[68:69],v248,24576
buffer_load_dwordx4 v242, s[8:11], s48 offen offset:0 sc1 lds
MF v[124:127],a[76:79],a[244:247],v[124:127]
RT a[70:71],v248,25600
buffer_store_dwordx4 v[0:3], v242, s[20:23], s49 offen offset:0 sc1
MF v[148:151],a[72:75],a[232:235],v[148:151]
s_add_u32 s53, s53, s82
s_lshl_b32 s53, s53, 2
MF v[148:151],a[76:79],a[248:251],v[148:151]
s_add_u32 s53, s53, s28
s_lshl_b32 s53, s53, 16
MF v[172:175],a[72:75],a[236:239],v[172:175]
s_add_u32 s53, s53, 0x3400000
s_lshl_b32 s47, s80, 15
MF v[172:175],a[76:79],a[252:255],v[172:175]
s_lshl_b32 s55, s28, 13
s_add_u32 s47, s47, s55
MF v[104:107],a[80:83],a[224:227],v[104:107]
RT a[72:73],v249,16384
s_add_u32 s47, s47, 0x2400000
MF v[104:107],a[84:87],a[240:243],v[104:107]
RT a[74:75],v249,17408
s_mul_i32 s55, s81, 3
MF v[128:131],a[80:83],a[228:231],v[128:131]
RT a[76:77],v249,24576
s_add_u32 s84, s55, s80
MF v[128:131],a[84:87],a[244:247],v[128:131]
RT a[78:79],v249,25600
s_lshl_b32 s55, s55, 17
MF v[152:155],a[80:83],a[232:235],v[152:155]
s_add_u32 s60, s55, s47
s_add_u32 s61, s60, 0x20000
MF v[152:155],a[84:87],a[248:251],v[152:155]
buffer_load_dwordx4 v242, s[8:11], s48 offen offset:1024 sc1 lds
s_add_u32 s62, s60, 0x40000
MF v[176:179],a[80:83],a[236:239],v[176:179]
s_add_u32 s55, s81, 0x30
MF v[176:179],a[84:87],a[252:255],v[176:179]
s_cmp_eq_u32 s80, 3
s_cselect_b32 s84, s55, s84
MF v[108:111],a[88:91],a[224:227],v[108:111]
RT a[80:81],v250,16384
s_lshl_b32 s55, s55, 17
MF v[108:111],a[92:95],a[240:243],v[108:111]
RT a[82:83],v250,17408
s_add_u32 s63, s55, s47
MF v[132:135],a[88:91],a[228:231],v[132:135]
RT a[84:85],v250,24576
s_lshl_b32 s85, s81, 2
MF v[132:135],a[92:95],a[244:247],v[132:135]
RT a[86:87],v250,25600
s_add_u32 s85, s85, 0x40
MF v[156:159],a[88:91],a[232:235],v[156:159]
s_add_u32 s55, s80, 0x1
s_and_b32 s55, s55, 3
MF v[156:159],a[92:95],a[248:251],v[156:159]
s_add_u32 s55, s55, s85
s_lshl_b32 s55, s55, 17
MF v[180:183],a[88:91],a[236:239],v[180:183]
s_add_u32 s64, s55, s47
s_add_u32 s55, s80, 0x2
MF v[180:183],a[92:95],a[252:255],v[180:183]
s_and_b32 s55, s55, 3
s_add_u32 s55, s55, s85
MF v[112:115],a[96:99],a[224:227],v[112:115]
RT a[88:89],v251,16384
s_lshl_b32 s55, s55, 17
MF v[112:115],a[100:103],a[240:243],v[112:115]
RT a[90:91],v251,17408
s_add_u32 s65, s55, s47
MF v[136:139],a[96:99],a[228:231],v[136:139]
RT a[92:93],v251,24576
buffer_load_dwordx4 v242, s[8:11], s48 offen offset:2048 sc1 lds
MF v[136:139],a[100:103],a[244:247],v[136:139]
RT a[94:95],v251,25600
s_add_u32 s49, s94, s92
MF v[160:163],a[96:99],a[232:235],v[160:163]
buffer_store_dwordx4 v[8:11], v242, s[20:23], s49 offen offset:0 sc1
s_add_u32 s55, s80, 0x3
MF v[160:163],a[100:103],a[248:251],v[160:163]
s_and_b32 s55, s55, 3
s_add_u32 s55, s55, s85
MF v[184:187],a[96:99],a[236:239],v[184:187]
s_lshl_b32 s55, s55, 17
s_add_u32 s66, s55, s47
MF v[184:187],a[100:103],a[252:255],v[184:187]
s_add_u32 s55, s80, 0x4
s_and_b32 s55, s55, 3
MF v[116:119],a[104:107],a[224:227],v[116:119]
RT a[96:97],v252,16384
s_add_u32 s55, s55, s85
MF v[116:119],a[108:111],a[240:243],v[116:119]
RT a[98:99],v252,17408
s_lshl_b32 s55, s55, 17
MF v[140:143],a[104:107],a[228:231],v[140:143]
RT a[100:101],v252,24576
s_add_u32 s67, s55, s47
MF v[140:143],a[108:111],a[244:247],v[140:143]
RT a[102:103],v252,25600
s_add_u32 s85, s85, s80
MF v[164:167],a[104:107],a[232:235],v[164:167]
s_and_b32 s54, s29, 1
s_lshl_b32 s54, s54, 3
MF v[164:167],a[108:111],a[248:251],v[164:167]
s_and_b32 s55, s28, 1
s_add_u32 s54, s54, s55
MF v[188:191],a[104:107],a[236:239],v[188:191]
s_lshl_b32 s54, s54, 13
s_lshl_b32 s86, s30, 6
MF v[188:191],a[108:111],a[252:255],v[188:191]
buffer_load_dwordx4 v242, s[8:11], s48 offen offset:3072 sc1 lds
s_lshl_b32 s55, s29, 2
MF v[192:195],a[112:115],a[224:227],v[192:195]
RT a[104:105],v253,16384
s_add_u32 s88, s86, s28
MF v[192:195],a[116:119],a[240:243],v[192:195]
RT a[106:107],v253,17408
s_add_u32 s86, s88, s55
MF v[200:203],a[112:115],a[228:231],v[200:203]
RT a[108:109],v253,24576
s_sub_u32 s88, s88, s55
MF v[200:203],a[116:119],a[244:247],v[200:203]
RT a[110:111],v253,25600
s_add_u32 s88, s88, 0x3c
MF v[208:211],a[112:115],a[232:235],v[208:211]
s_lshl_b32 s86, s86, 7
s_lshl_b32 s88, s88, 7
MF v[208:211],a[116:119],a[248:251],v[208:211]
s_add_u32 s86, s86, 0x4990000
s_add_u32 s88, s88, 0x4990000
MF v[216:219],a[112:115],a[236:239],v[216:219]
s_lshl_b32 s47, s82, 2
s_add_u32 s47, s47, s28
MF v[216:219],a[116:119],a[252:255],v[216:219]
s_lshl_b32 s94, s47, 11
s_add_u32 s94, s94, 0x4908000
MF v[196:199],a[120:123],a[224:227],v[196:199]
RT a[112:113],v254,16384
s_lshr_b32 s55, s77, 4
MF v[196:199],a[124:127],a[240:243],v[196:199]
RT a[114:115],v254,17408
s_sub_u32 s70, s55, s84
MF v[204:207],a[120:123],a[228:231],v[204:207]
RT a[116:117],v254,24576
s_lshl_b32 s84, s84, 14
MF v[204:207],a[124:127],a[244:247],v[204:207]
RT a[118:119],v254,25600
s_lshl_b32 s55, s47, 10
MF v[212:215],a[120:123],a[232:235],v[212:215]
s_add_u32 s68, s55, s84
s_lshl_b32 s55, s47, 6
MF v[212:215],a[124:127],a[248:251],v[212:215]
s_add_u32 s84, s84, s55
s_lshr_b32 s55, s77, 4
MF v[220:223],a[120:123],a[236:239],v[220:223]
s_sub_u32 s71, s55, s85
s_lshl_b32 s85, s85, 14
MF v[220:223],a[124:127],a[252:255],v[220:223]
s_lshl_b32 s55, s47, 10
s_add_u32 s69, s55, s85
s_lshl_b32 s55, s47, 6
s_add_u32 s85, s85, s55
s_waitcnt lgkmcnt(15)
MF v[96:99],a[64:67],v[64:67],v[96:99]
RT a[120:121],v255,16384
RT a[122:123],v255,17408
MF v[96:99],a[68:71],v[80:83],v[96:99]
RT a[124:125],v255,24576
RT a[126:127],v255,25600
MF v[120:123],a[64:67],v[68:71],v[120:123]
MF v[120:123],a[68:71],v[84:87],v[120:123]
MF v[144:147],a[64:67],v[72:75],v[144:147]
MF v[144:147],a[68:71],v[88:91],v[144:147]
MF v[168:171],a[64:67],v[76:79],v[168:171]
s_add_u32 s49, s95, s92
MF v[168:171],a[68:71],v[92:95],v[168:171]
s_waitcnt vmcnt(45) lgkmcnt(0)
s_barrier
MF v[100:103],a[72:75],v[64:67],v[100:103]
s_add_u32 m0, s34, 0x10000
s_add_u32 s48, s34, 0x10000
MF v[100:103],a[76:79],v[80:83],v[100:103]
buffer_load_dwordx4 v242, s[8:11], s48 offen offset:0 sc1 lds
buffer_store_dwordx4 v[16:19], v242, s[20:23], s49 offen offset:0 sc1
MF v[124:127],a[72:75],v[68:71],v[124:127]
MF v[124:127],a[76:79],v[84:87],v[124:127]
MF v[148:151],a[72:75],v[72:75],v[148:151]
MF v[148:151],a[76:79],v[88:91],v[148:151]
MF v[172:175],a[72:75],v[76:79],v[172:175]
MF v[172:175],a[76:79],v[92:95],v[172:175]
MF v[104:107],a[80:83],v[64:67],v[104:107]
MF v[104:107],a[84:87],v[80:83],v[104:107]
MF v[128:131],a[80:83],v[68:71],v[128:131]
MF v[128:131],a[84:87],v[84:87],v[128:131]
MF v[152:155],a[80:83],v[72:75],v[152:155]
PK v96,v96,v97
PK v97,v98,v99
MF v[152:155],a[84:87],v[88:91],v[152:155]
buffer_load_dwordx4 v242, s[8:11], s48 offen offset:1024 sc1 lds
PK v98,v100,v101
MF v[176:179],a[80:83],v[76:79],v[176:179]
PK v99,v102,v103
v_accvgpr_write_b32 a0, v96
MF v[176:179],a[84:87],v[92:95],v[176:179]
v_accvgpr_write_b32 a1, v97
v_accvgpr_write_b32 a2, v98
MF v[108:111],a[88:91],v[64:67],v[108:111]
v_accvgpr_write_b32 a3, v99
PK v120,v120,v121
MF v[108:111],a[92:95],v[80:83],v[108:111]
PK v121,v122,v123
PK v122,v124,v125
MF v[132:135],a[88:91],v[68:71],v[132:135]
PK v123,v126,v127
v_accvgpr_write_b32 a16, v120
MF v[132:135],a[92:95],v[84:87],v[132:135]
v_accvgpr_write_b32 a17, v121
v_accvgpr_write_b32 a18, v122
MF v[156:159],a[88:91],v[72:75],v[156:159]
v_accvgpr_write_b32 a19, v123
PK v144,v144,v145
MF v[156:159],a[92:95],v[88:91],v[156:159]
PK v145,v146,v147
PK v146,v148,v149
MF v[180:183],a[88:91],v[76:79],v[180:183]
PK v147,v150,v151
v_accvgpr_write_b32 a32, v144
MF v[180:183],a[92:95],v[92:95],v[180:183]
v_accvgpr_write_b32 a33, v145
v_accvgpr_write_b32 a34, v146
MF v[112:115],a[96:99],v[64:67],v[112:115]
v_accvgpr_write_b32 a35, v147
PK v168,v168,v169
MF v[112:115],a[100:103],v[80:83],v[112:115]
PK v169,v170,v171
PK v170,v172,v173
MF v[136:139],a[96:99],v[68:71],v[136:139]
R16 a[64:67],v240,0
buffer_load_dwordx4 v242, s[8:11], s48 offen offset:2048 sc1 lds
MF v[136:139],a[100:103],v[84:87],v[136:139]
PK v171,v174,v175
v_accvgpr_write_b32 a48, v168
MF v[160:163],a[96:99],v[72:75],v[160:163]
R16 a[68:71],v240,1024
v_accvgpr_write_b32 a49, v169
MF v[160:163],a[100:103],v[88:91],v[160:163]
v_accvgpr_write_b32 a50, v170
v_accvgpr_write_b32 a51, v171
MF v[184:187],a[96:99],v[76:79],v[184:187]
R16 a[72:75],v240,2048
PK v104,v104,v105
MF v[184:187],a[100:103],v[92:95],v[184:187]
PK v105,v106,v107
PK v106,v108,v109
MF v[116:119],a[104:107],v[64:67],v[116:119]
R16 a[76:79],v240,3072
PK v107,v110,v111
MF v[116:119],a[108:111],v[80:83],v[116:119]
v_accvgpr_write_b32 a4, v104
v_accvgpr_write_b32 a5, v105
MF v[140:143],a[104:107],v[68:71],v[140:143]
R16 a[80:83],v240,4096
v_accvgpr_write_b32 a6, v106
MF v[140:143],a[108:111],v[84:87],v[140:143]
v_accvgpr_write_b32 a7, v107
PK v128,v128,v129
MF v[164:167],a[104:107],v[72:75],v[164:167]
R16 a[84:87],v240,5120
PK v129,v130,v131
MF v[164:167],a[108:111],v[88:91],v[164:167]
PK v130,v132,v133
PK v131,v134,v135
MF v[188:191],a[104:107],v[76:79],v[188:191]
R16 a[88:91],v240,6144
v_accvgpr_write_b32 a20, v128
MF v[188:191],a[108:111],v[92:95],v[188:191]
buffer_load_dwordx4 v242, s[8:11], s48 offen offset:3072 sc1 lds
v_accvgpr_write_b32 a21, v129
MF v[192:195],a[112:115],v[64:67],v[192:195]
R16 a[92:95],v240,7168
v_accvgpr_write_b32 a22, v130
MF v[192:195],a[116:119],v[80:83],v[192:195]
v_accvgpr_write_b32 a23, v131
PK v152,v152,v153
MF v[200:203],a[112:115],v[68:71],v[200:203]
PK v153,v154,v155
PK v154,v156,v157
MF v[200:203],a[116:119],v[84:87],v[200:203]
PK v155,v158,v159
v_accvgpr_write_b32 a36, v152
MF v[208:211],a[112:115],v[72:75],v[208:211]
v_accvgpr_write_b32 a37, v153
v_accvgpr_write_b32 a38, v154
MF v[208:211],a[116:119],v[88:91],v[208:211]
v_accvgpr_write_b32 a39, v155
PK v176,v176,v177
MF v[216:219],a[112:115],v[76:79],v[216:219]
PK v177,v178,v179
PK v178,v180,v181
MF v[216:219],a[116:119],v[92:95],v[216:219]
PK v179,v182,v183
v_accvgpr_write_b32 a52, v176
MF v[196:199],a[120:123],v[64:67],v[196:199]
v_accvgpr_write_b32 a53, v177
v_accvgpr_write_b32 a54, v178
MF v[196:199],a[124:127],v[80:83],v[196:199]
v_accvgpr_write_b32 a55, v179
PK v112,v112,v113
MF v[204:207],a[120:123],v[68:71],v[204:207]
PK v113,v114,v115
PK v114,v116,v117
MF v[204:207],a[124:127],v[84:87],v[204:207]
PK v115,v118,v119
v_accvgpr_write_b32 a8, v112
MF v[212:215],a[120:123],v[72:75],v[212:215]
v_accvgpr_write_b32 a9, v113
v_accvgpr_write_b32 a10, v114
MF v[212:215],a[124:127],v[88:91],v[212:215]
v_accvgpr_write_b32 a11, v115
PK v136,v136,v137
MF v[220:223],a[120:123],v[76:79],v[220:223]
MF v[220:223],a[124:127],v[92:95],v[220:223]
PK v137,v138,v139
PK v138,v140,v141
PK v139,v142,v143
v_accvgpr_write_b32 a24, v136
v_accvgpr_write_b32 a25, v137
v_accvgpr_write_b32 a26, v138
v_accvgpr_write_b32 a27, v139
PK v160,v160,v161
PK v161,v162,v163
PK v162,v164,v165
PK v163,v166,v167
v_accvgpr_write_b32 a40, v160
v_accvgpr_write_b32 a41, v161
v_accvgpr_write_b32 a42, v162
v_accvgpr_write_b32 a43, v163
PK v184,v184,v185
PK v185,v186,v187
PK v186,v188,v189
PK v187,v190,v191
v_accvgpr_write_b32 a56, v184
v_accvgpr_write_b32 a57, v185
v_accvgpr_write_b32 a58, v186
v_accvgpr_write_b32 a59, v187
s_waitcnt lgkmcnt(0)
s_add_u32 s49, s95, s92
buffer_store_dwordx4 v[24:27], v242, s[20:23], s49 offen offset:1024 sc1
MF v[32:35],a[64:67],a[0:3],0
v_mov_b32 v245, 0x3f803f80
v_accvgpr_write_b32 a192, v245
v_accvgpr_write_b32 a193, v245
v_accvgpr_write_b32 a194, v245
MF v[40:43],a[64:67],a[16:19],0
v_accvgpr_write_b32 a195, v245
v_mov_b64 v[64:65], 0
v_mov_b64 v[66:67], 0
v_mov_b64 v[68:69], 0
MF v[48:51],a[64:67],a[32:35],0
MF v[56:59],a[64:67],a[48:51],0
MF v[32:35],a[68:71],a[4:7],v[32:35]
PK v192,v192,v193
PK v193,v194,v195
PK v194,v196,v197
PK v195,v198,v199
MF v[40:43],a[68:71],a[20:23],v[40:43]
v_accvgpr_write_b32 a12, v192
v_accvgpr_write_b32 a13, v193
v_accvgpr_write_b32 a14, v194
v_accvgpr_write_b32 a15, v195
MF v[48:51],a[68:71],a[36:39],v[48:51]
PK v200,v200,v201
PK v201,v202,v203
PK v202,v204,v205
PK v203,v206,v207
MF v[56:59],a[68:71],a[52:55],v[56:59]
v_accvgpr_write_b32 a28, v200
)A"
R"A(v_accvgpr_write_b32 a29, v201
v_accvgpr_write_b32 a30, v202
v_accvgpr_write_b32 a31, v203
MF v[32:35],a[72:75],a[8:11],v[32:35]
PK v208,v208,v209
PK v209,v210,v211
PK v210,v212,v213
PK v211,v214,v215
MF v[40:43],a[72:75],a[24:27],v[40:43]
v_accvgpr_write_b32 a44, v208
v_accvgpr_write_b32 a45, v209
v_accvgpr_write_b32 a46, v210
v_accvgpr_write_b32 a47, v211
MF v[48:51],a[72:75],a[40:43],v[48:51]
PK v216,v216,v217
PK v217,v218,v219
PK v218,v220,v221
PK v219,v222,v223
MF v[56:59],a[72:75],a[56:59],v[56:59]
v_accvgpr_write_b32 a60, v216
v_accvgpr_write_b32 a61, v217
v_accvgpr_write_b32 a62, v218
v_accvgpr_write_b32 a63, v219
s_nop 1
MF v[32:35],a[76:79],a[12:15],v[32:35]
v_mov_b64 v[208:209], 0
v_mov_b64 v[210:211], 0
v_mov_b64 v[212:213], 0
v_mov_b64 v[214:215], 0
MF v[40:43],a[76:79],a[28:31],v[40:43]
v_mov_b64 v[216:217], 0
v_mov_b64 v[218:219], 0
v_mov_b64 v[220:221], 0
v_mov_b64 v[222:223], 0
MF v[48:51],a[76:79],a[44:47],v[48:51]
v_mov_b64 v[70:71], 0
v_mov_b64 v[72:73], 0
v_mov_b64 v[74:75], 0
v_mov_b64 v[76:77], 0
MF v[56:59],a[76:79],a[60:63],v[56:59]
v_mov_b64 v[78:79], 0
v_mov_b64 v[80:81], 0
v_mov_b64 v[82:83], 0
v_mov_b64 v[84:85], 0
v_mov_b64 v[86:87], 0
v_mov_b64 v[88:89], 0
v_mov_b64 v[90:91], 0
v_mov_b64 v[92:93], 0
v_mov_b64 v[94:95], 0
v_bfe_u32 v245, v242, 4, 4
v_lshrrev_b32 v243, 8, v242
v_lshlrev_b32 v243, 2, v243
v_sub_u32 v243, v245, v243
v_mov_b32 v244, 0xff800000
v_cmp_le_i32_e64 s[40:41], 0, v243
v_cmp_le_i32_e64 s[42:43], 1, v243
v_cmp_le_i32_e64 s[44:45], 2, v243
v_cmp_le_i32_e64 s[46:47], 3, v243
v_max3_f32 v32, v32, v33, v34
v_max_f32 v32, v32, v35
v_mov_b32 v33, v32
v_max3_f32 v40, v40, v41, v42
v_max_f32 v40, v40, v43
v_mov_b32 v41, v40
v_max3_f32 v48, v48, v49, v50
v_max_f32 v48, v48, v51
v_mov_b32 v49, v48
v_max3_f32 v56, v56, v57, v58
v_max_f32 v56, v56, v59
v_mov_b32 v57, v56
s_nop 0
v_permlane32_swap_b32 v32, v33
v_permlane32_swap_b32 v40, v41
v_permlane32_swap_b32 v48, v49
v_permlane32_swap_b32 v56, v57
v_max_f32 v32, v32, v33
v_mov_b32 v33, v32
v_max_f32 v40, v40, v41
v_mov_b32 v41, v40
v_max_f32 v48, v48, v49
v_mov_b32 v49, v48
v_max_f32 v56, v56, v57
v_mov_b32 v57, v56
s_nop 0
v_permlane16_swap_b32 v32, v33
v_permlane16_swap_b32 v40, v41
v_permlane16_swap_b32 v48, v49
v_permlane16_swap_b32 v56, v57
v_min_f32_e64 v192, -v32, -v33
v_min_f32_e64 v193, -v32, -v33
v_min_f32_e64 v194, -v32, -v33
v_min_f32_e64 v195, -v32, -v33
v_min_f32_e64 v196, -v40, -v41
v_min_f32_e64 v197, -v40, -v41
v_min_f32_e64 v198, -v40, -v41
v_min_f32_e64 v199, -v40, -v41
v_min_f32_e64 v200, -v48, -v49
v_min_f32_e64 v201, -v48, -v49
v_min_f32_e64 v202, -v48, -v49
v_min_f32_e64 v203, -v48, -v49
v_min_f32_e64 v204, -v56, -v57
v_min_f32_e64 v205, -v56, -v57
v_min_f32_e64 v206, -v56, -v57
v_min_f32_e64 v207, -v56, -v57
v_cndmask_b32 v224, v244, v192, s[40:41]
v_cndmask_b32 v225, v244, v193, s[42:43]
v_cndmask_b32 v226, v244, v194, s[44:45]
v_cndmask_b32 v227, v244, v195, s[46:47]
v_cndmask_b32 v228, v244, v196, s[40:41]
v_cndmask_b32 v229, v244, v197, s[42:43]
v_cndmask_b32 v230, v244, v198, s[44:45]
v_cndmask_b32 v231, v244, v199, s[46:47]
v_cndmask_b32 v232, v244, v200, s[40:41]
v_cndmask_b32 v233, v244, v201, s[42:43]
v_cndmask_b32 v234, v244, v202, s[44:45]
v_cndmask_b32 v235, v244, v203, s[46:47]
v_cndmask_b32 v236, v244, v204, s[40:41]
v_cndmask_b32 v237, v244, v205, s[42:43]
v_cndmask_b32 v238, v244, v206, s[44:45]
v_cndmask_b32 v239, v244, v207, s[46:47]
s_nop 0
s_mov_b64 s[46:47], 0
s_mov_b32 s90, 2
s_getpc_b64 s[16:17]
L_PC_%=:
s_add_u32 s16, s16, L_STUB0_%=-L_PC_%=
s_addc_u32 s17, s17, 0
s_or_b32 s49, s29, s28
s_cmp_eq_u32 s49, 0
s_cbranch_scc1 L_HD_%=
MF v[0:3],a[64:67],a[0:3],v[192:195]
s_mov_b32 s24, s72
s_and_b32 s25, s73, 0xffff
MF v[0:3],a[68:71],a[4:7],v[0:3]
R16 a[96:99],v240,8192
s_mov_b32 s26, 0x400000
MF v[0:3],a[72:75],a[8:11],v[0:3]
v_lshrrev_b32 v252, 9, v242
v_lshlrev_b32 v252, 9, v252
MF v[0:3],a[76:79],a[12:15],v[0:3]
R16 a[100:103],v240,9216
v_add_u32 v252, v252, v242
MF v[8:11],a[64:67],a[16:19],v[196:199]
s_lshl_b32 s48, s30, 2
s_lshr_b32 s31, s29, 1
MF v[8:11],a[68:71],a[20:23],v[8:11]
R16 a[104:107],v240,10240
s_add_u32 s48, s48, s31
MF v[8:11],a[72:75],a[24:27],v[8:11]
s_lshl_b32 s48, s48, 2
s_add_u32 s48, s48, s28
MF v[8:11],a[76:79],a[28:31],v[8:11]
R16 a[108:111],v240,11264
s_lshl_b32 s48, s48, 13
MF v[16:19],a[64:67],a[32:35],v[200:203]
s_and_b32 s31, s29, 1
s_lshl_b32 s31, s31, 9
MF v[16:19],a[68:71],a[36:39],v[16:19]
R16 a[112:115],v240,12288
s_add_u32 s48, s48, s31
MF v[16:19],a[72:75],a[40:43],v[16:19]
s_add_u32 m0, s34, 0x20000
v_mov_b64 v[96:97], 0
MF v[16:19],a[76:79],a[44:47],v[16:19]
R16 a[116:119],v240,13312
buffer_load_dwordx4 v252, s[24:27], s48 offen offset:0 lds
MF v[24:27],a[64:67],a[48:51],v[204:207]
s_add_u32 s48, s48, 0x400
v_mov_b64 v[98:99], 0
MF v[24:27],a[68:71],a[52:55],v[24:27]
R16 a[120:123],v240,14336
buffer_load_dwordx4 v252, s[24:27], s48 offen offset:1024 lds
MF v[24:27],a[72:75],a[56:59],v[24:27]
s_add_u32 s48, s48, 0x400
v_mov_b64 v[100:101], 0
MF v[24:27],a[76:79],a[60:63],v[24:27]
R16 a[124:127],v240,15360
buffer_load_dwordx4 v252, s[24:27], s48 offen offset:2048 lds
MF v[4:7],a[80:83],a[0:3],v[192:195]
s_add_u32 s48, s48, 0x400
v_mov_b64 v[102:103], 0
MF v[4:7],a[84:87],a[4:7],v[4:7]
buffer_load_dwordx4 v252, s[24:27], s48 offen offset:3072 lds
v_mov_b64 v[104:105], 0
MF v[4:7],a[88:91],a[8:11],v[4:7]
v_mov_b64 v[106:107], 0
v_mov_b64 v[108:109], 0
MF v[4:7],a[92:95],a[12:15],v[4:7]
v_mov_b64 v[110:111], 0
v_mov_b64 v[112:113], 0
MF v[12:15],a[80:83],a[16:19],v[196:199]
v_exp_f32 v0, v0
MF v[12:15],a[84:87],a[20:23],v[12:15]
v_mov_b64 v[114:115], 0
v_mov_b64 v[116:117], 0
MF v[12:15],a[88:91],a[24:27],v[12:15]
v_exp_f32 v1, v1
MF v[12:15],a[92:95],a[28:31],v[12:15]
PK v0,v0,v1
v_mov_b64 v[118:119], 0
MF v[20:23],a[80:83],a[32:35],v[200:203]
v_exp_f32 v2, v2
MF v[20:23],a[84:87],a[36:39],v[20:23]
v_mov_b64 v[120:121], 0
v_mov_b64 v[122:123], 0
MF v[20:23],a[88:91],a[40:43],v[20:23]
v_exp_f32 v3, v3
MF v[20:23],a[92:95],a[44:47],v[20:23]
PK v1,v2,v3
v_mov_b64 v[124:125], 0
MF v[28:31],a[80:83],a[48:51],v[204:207]
v_exp_f32 v8, v8
MF v[28:31],a[84:87],a[52:55],v[28:31]
v_mov_b64 v[126:127], 0
v_mov_b64 v[128:129], 0
MF v[28:31],a[88:91],a[56:59],v[28:31]
v_exp_f32 v9, v9
MF v[28:31],a[92:95],a[60:63],v[28:31]
PK v8,v8,v9
v_mov_b64 v[130:131], 0
v_mov_b64 v[132:133], 0
v_mov_b64 v[134:135], 0
v_mov_b64 v[136:137], 0
v_mov_b64 v[138:139], 0
v_mov_b64 v[140:141], 0
v_mov_b64 v[142:143], 0
v_mov_b64 v[144:145], 0
v_mov_b64 v[146:147], 0
v_mov_b64 v[148:149], 0
v_mov_b64 v[150:151], 0
v_mov_b64 v[152:153], 0
v_mov_b64 v[154:155], 0
v_mov_b64 v[156:157], 0
v_mov_b64 v[158:159], 0
v_mov_b64 v[160:161], 0
v_mov_b64 v[162:163], 0
v_mov_b64 v[164:165], 0
v_mov_b64 v[166:167], 0
v_exp_f32 v10, v10
v_exp_f32 v11, v11
s_nop 0
PK v9,v10,v11
v_exp_f32 v16, v16
v_exp_f32 v17, v17
s_nop 0
PK v16,v16,v17
v_exp_f32 v18, v18
v_exp_f32 v19, v19
s_nop 0
PK v17,v18,v19
v_exp_f32 v24, v24
v_exp_f32 v25, v25
s_nop 0
PK v24,v24,v25
v_exp_f32 v26, v26
v_exp_f32 v27, v27
s_nop 0
PK v25,v26,v27
v_exp_f32 v4, v4
v_exp_f32 v5, v5
s_nop 0
PK v2,v4,v5
v_exp_f32 v6, v6
v_exp_f32 v7, v7
s_nop 0
PK v3,v6,v7
v_exp_f32 v12, v12
v_exp_f32 v13, v13
s_nop 0
PK v10,v12,v13
v_exp_f32 v14, v14
v_exp_f32 v15, v15
s_nop 0
PK v11,v14,v15
s_waitcnt lgkmcnt(0)
v_mov_b64 v[168:169], 0
v_mov_b64 v[170:171], 0
v_mov_b64 v[172:173], 0
v_mov_b64 v[174:175], 0
v_mov_b64 v[176:177], 0
v_mov_b64 v[178:179], 0
v_mov_b64 v[180:181], 0
v_mov_b64 v[182:183], 0
v_mov_b64 v[184:185], 0
v_mov_b64 v[186:187], 0
v_mov_b64 v[188:189], 0
v_mov_b64 v[190:191], 0
s_cmp_eq_u32 s29, 0
s_cbranch_scc1 L_K1_%=
s_lshl_b32 s31, s29, 1
s_cmp_ge_u32 s29, 6
s_cselect_b32 s49, 3, 2
s_sub_u32 s31, s31, s49
MF v[32:35],a[96:99],a[0:3],v[192:195]
v_exp_f32 v20, v20
MF v[32:35],a[100:103],a[4:7],v[32:35]
s_waitcnt vmcnt(21)
s_barrier
MF v[32:35],a[104:107],a[8:11],v[32:35]
v_exp_f32 v21, v21
MF v[32:35],a[108:111],a[12:15],v[32:35]
v_add_u32 v241, 0x8000, v241
v_and_b32 v241, 0x1ffff, v241
s_lshl_b32 s49, s30, 3
MF v[40:43],a[96:99],a[16:19],v[196:199]
v_exp_f32 v22, v22
s_add_u32 s49, s49, s29
MF v[40:43],a[100:103],a[20:23],v[40:43]
R16 a[128:131],v241,16384
s_and_b32 m0, s50, 0x1ffff
s_lshl_b32 s49, s49, 2
MF v[40:43],a[104:107],a[24:27],v[40:43]
v_exp_f32 v23, v23
s_add_u32 s49, s49, s28
s_lshl_b32 s49, s49, 2
s_add_u32 s49, s49, 0x4906000
MF v[40:43],a[108:111],a[28:31],v[40:43]
R16 a[132:135],v241,17408
s_xor_b32 m0, m0, 0x14000
MF v[48:51],a[96:99],a[32:35],v[200:203]
v_exp_f32 v28, v28
MF v[48:51],a[100:103],a[36:39],v[48:51]
R16 a[136:139],v241,18432
buffer_load_dwordx4 v242, s[12:15], s50 offen offset:0 sc1 lds
MF v[48:51],a[104:107],a[40:43],v[48:51]
v_exp_f32 v29, v29
MF v[48:51],a[108:111],a[44:47],v[48:51]
R16 a[140:143],v241,19456
PK v18,v20,v21
MF v[56:59],a[96:99],a[48:51],v[204:207]
v_exp_f32 v30, v30
MF v[56:59],a[100:103],a[52:55],v[56:59]
R16 a[144:147],v241,20480
PK v19,v22,v23
MF v[56:59],a[104:107],a[56:59],v[56:59]
v_exp_f32 v31, v31
MF v[56:59],a[108:111],a[60:63],v[56:59]
R16 a[148:151],v241,21504
buffer_load_dwordx4 v242, s[12:15], s50 offen offset:1024 sc1 lds
MF v[36:39],a[112:115],a[0:3],v[192:195]
PK v26,v28,v29
PK v27,v30,v31
MF v[36:39],a[116:119],a[4:7],v[36:39]
R16 a[152:155],v241,22528
MF v[36:39],a[120:123],a[8:11],v[36:39]
MF v[36:39],a[124:127],a[12:15],v[36:39]
R16 a[156:159],v241,23552
v_add_u32 v240, 0x8000, v240
MF v[44:47],a[112:115],a[16:19],v[196:199]
v_exp_f32 v32, v32
MF v[44:47],a[116:119],a[20:23],v[44:47]
buffer_load_dwordx4 v242, s[12:15], s50 offen offset:2048 sc1 lds
v_and_b32 v240, 0x1ffff, v240
MF v[44:47],a[120:123],a[24:27],v[44:47]
v_exp_f32 v33, v33
MF v[44:47],a[124:127],a[28:31],v[44:47]
PK v32,v32,v33
MF v[52:55],a[112:115],a[32:35],v[200:203]
v_exp_f32 v34, v34
MF v[52:55],a[116:119],a[36:39],v[52:55]
MF v[52:55],a[120:123],a[40:43],v[52:55]
v_exp_f32 v35, v35
MF v[52:55],a[124:127],a[44:47],v[52:55]
R16 a[64:67],v240,0
PK v33,v34,v35
MF v[60:63],a[112:115],a[48:51],v[204:207]
v_exp_f32 v40, v40
MF v[60:63],a[116:119],a[52:55],v[60:63]
buffer_load_dwordx4 v242, s[12:15], s50 offen offset:3072 sc1 lds
MF v[60:63],a[120:123],a[56:59],v[60:63]
v_exp_f32 v41, v41
MF v[60:63],a[124:127],a[60:63],v[60:63]
R16 a[68:71],v240,1024
s_waitcnt lgkmcnt(2)
MF v[64:67],a[128:131],v[0:3],v[64:67]
v_exp_f32 v42, v42
MF v[96:99],a[128:131],v[8:11],v[96:99]
s_xor_b32 m0, m0, 0x14000
PK v40,v40,v41
MF v[128:131],a[128:131],v[16:19],v[128:131]
v_exp_f32 v43, v43
MF v[160:163],a[128:131],v[24:27],v[160:163]
R16 a[72:75],v240,2048
PK v41,v42,v43
MF v[68:71],a[132:135],v[0:3],v[68:71]
v_exp_f32 v48, v48
MF v[100:103],a[132:135],v[8:11],v[100:103]
buffer_load_dwordx4 v242, s[8:11], s50 offen offset:0 sc1 lds
MF v[132:135],a[132:135],v[16:19],v[132:135]
v_exp_f32 v49, v49
MF v[164:167],a[132:135],v[24:27],v[164:167]
R16 a[76:79],v240,3072
PK v48,v48,v49
MF v[72:75],a[136:139],v[0:3],v[72:75]
v_exp_f32 v50, v50
MF v[104:107],a[136:139],v[8:11],v[104:107]
buffer_load_dwordx4 v242, s[8:11], s50 offen offset:1024 sc1 lds
MF v[136:139],a[136:139],v[16:19],v[136:139]
v_exp_f32 v51, v51
MF v[168:171],a[136:139],v[24:27],v[168:171]
)A"
R"A(R16 a[80:83],v240,4096
PK v49,v50,v51
MF v[76:79],a[140:143],v[0:3],v[76:79]
v_exp_f32 v56, v56
MF v[108:111],a[140:143],v[8:11],v[108:111]
v_mov_b32 v254, s37
v_mov_b32 v255, 0
s_waitcnt vmcnt(10)
MF v[140:143],a[140:143],v[16:19],v[140:143]
buffer_store_dword v254, v255, s[20:23], s49 offen offset:0 sc1
v_exp_f32 v57, v57
MF v[172:175],a[140:143],v[24:27],v[172:175]
R16 a[84:87],v240,5120
PK v56,v56,v57
MF v[80:83],a[144:147],v[0:3],v[80:83]
v_exp_f32 v58, v58
MF v[112:115],a[144:147],v[8:11],v[112:115]
buffer_load_dwordx4 v242, s[8:11], s50 offen offset:2048 sc1 lds
MF v[144:147],a[144:147],v[16:19],v[144:147]
v_exp_f32 v59, v59
MF v[176:179],a[144:147],v[24:27],v[176:179]
R16 a[88:91],v240,6144
PK v57,v58,v59
MF v[84:87],a[148:151],v[0:3],v[84:87]
v_exp_f32 v36, v36
MF v[116:119],a[148:151],v[8:11],v[116:119]
buffer_load_dwordx4 v242, s[8:11], s50 offen offset:3072 sc1 lds
MF v[148:151],a[148:151],v[16:19],v[148:151]
v_exp_f32 v37, v37
MF v[180:183],a[148:151],v[24:27],v[180:183]
R16 a[92:95],v240,7168
s_add_u32 s50, s50, 0x8000
MF v[88:91],a[152:155],v[0:3],v[88:91]
v_exp_f32 v38, v38
MF v[120:123],a[152:155],v[8:11],v[120:123]
PK v34,v36,v37
MF v[152:155],a[152:155],v[16:19],v[152:155]
v_exp_f32 v39, v39
MF v[184:187],a[152:155],v[24:27],v[184:187]
PK v35,v38,v39
MF v[92:95],a[156:159],v[0:3],v[92:95]
v_exp_f32 v44, v44
MF v[124:127],a[156:159],v[8:11],v[124:127]
MF v[156:159],a[156:159],v[16:19],v[156:159]
v_exp_f32 v45, v45
MF v[188:191],a[156:159],v[24:27],v[188:191]
PK v42,v44,v45
MF v[208:211],a[192:195],v[0:3],v[208:211]
v_exp_f32 v46, v46
MF v[212:215],a[192:195],v[8:11],v[212:215]
MF v[216:219],a[192:195],v[16:19],v[216:219]
v_exp_f32 v47, v47
MF v[220:223],a[192:195],v[24:27],v[220:223]
PK v43,v46,v47
s_waitcnt lgkmcnt(0)
s_sub_u32 s90, s90, 1
s_cbranch_scc0 L_SVA_%=
s_swappc_b64 s[18:19], s[16:17]
L_SVA_%=:
L_L1_%=:
MF v[0:3],a[64:67],a[0:3],v[192:195]
v_exp_f32 v52, v52
MF v[0:3],a[68:71],a[4:7],v[0:3]
MF v[0:3],a[72:75],a[8:11],v[0:3]
v_exp_f32 v53, v53
MF v[0:3],a[76:79],a[12:15],v[0:3]
PK v50,v52,v53
MF v[8:11],a[64:67],a[16:19],v[196:199]
v_exp_f32 v54, v54
MF v[8:11],a[68:71],a[20:23],v[8:11]
R16 a[160:163],v241,24576
MF v[8:11],a[72:75],a[24:27],v[8:11]
v_exp_f32 v55, v55
MF v[8:11],a[76:79],a[28:31],v[8:11]
R16 a[164:167],v241,25600
PK v51,v54,v55
MF v[16:19],a[64:67],a[32:35],v[200:203]
v_exp_f32 v60, v60
MF v[16:19],a[68:71],a[36:39],v[16:19]
R16 a[168:171],v241,26624
MF v[16:19],a[72:75],a[40:43],v[16:19]
v_exp_f32 v61, v61
MF v[16:19],a[76:79],a[44:47],v[16:19]
R16 a[172:175],v241,27648
PK v58,v60,v61
MF v[24:27],a[64:67],a[48:51],v[204:207]
v_exp_f32 v62, v62
MF v[24:27],a[68:71],a[52:55],v[24:27]
R16 a[176:179],v241,28672
MF v[24:27],a[72:75],a[56:59],v[24:27]
v_exp_f32 v63, v63
MF v[24:27],a[76:79],a[60:63],v[24:27]
R16 a[180:183],v241,29696
PK v59,v62,v63
MF v[4:7],a[80:83],a[0:3],v[192:195]
MF v[4:7],a[84:87],a[4:7],v[4:7]
R16 a[184:187],v241,30720
MF v[4:7],a[88:91],a[8:11],v[4:7]
MF v[4:7],a[92:95],a[12:15],v[4:7]
R16 a[188:191],v241,31744
MF v[12:15],a[80:83],a[16:19],v[196:199]
v_exp_f32 v0, v0
MF v[12:15],a[84:87],a[20:23],v[12:15]
MF v[12:15],a[88:91],a[24:27],v[12:15]
v_exp_f32 v1, v1
MF v[12:15],a[92:95],a[28:31],v[12:15]
PK v0,v0,v1
MF v[20:23],a[80:83],a[32:35],v[200:203]
v_exp_f32 v2, v2
MF v[20:23],a[84:87],a[36:39],v[20:23]
MF v[20:23],a[88:91],a[40:43],v[20:23]
v_exp_f32 v3, v3
MF v[20:23],a[92:95],a[44:47],v[20:23]
R16 a[96:99],v240,8192
PK v1,v2,v3
MF v[28:31],a[80:83],a[48:51],v[204:207]
v_exp_f32 v8, v8
MF v[28:31],a[84:87],a[52:55],v[28:31]
MF v[28:31],a[88:91],a[56:59],v[28:31]
v_exp_f32 v9, v9
MF v[28:31],a[92:95],a[60:63],v[28:31]
R16 a[100:103],v240,9216
s_waitcnt lgkmcnt(2)
MF v[64:67],a[160:163],v[32:35],v[64:67]
v_exp_f32 v10, v10
MF v[96:99],a[160:163],v[40:43],v[96:99]
PK v8,v8,v9
MF v[128:131],a[160:163],v[48:51],v[128:131]
v_exp_f32 v11, v11
MF v[160:163],a[160:163],v[56:59],v[160:163]
R16 a[104:107],v240,10240
PK v9,v10,v11
MF v[68:71],a[164:167],v[32:35],v[68:71]
v_exp_f32 v16, v16
MF v[100:103],a[164:167],v[40:43],v[100:103]
MF v[132:135],a[164:167],v[48:51],v[132:135]
v_exp_f32 v17, v17
MF v[164:167],a[164:167],v[56:59],v[164:167]
R16 a[108:111],v240,11264
PK v16,v16,v17
MF v[72:75],a[168:171],v[32:35],v[72:75]
v_exp_f32 v18, v18
MF v[104:107],a[168:171],v[40:43],v[104:107]
MF v[136:139],a[168:171],v[48:51],v[136:139]
v_exp_f32 v19, v19
MF v[168:171],a[168:171],v[56:59],v[168:171]
R16 a[112:115],v240,12288
PK v17,v18,v19
MF v[76:79],a[172:175],v[32:35],v[76:79]
v_exp_f32 v24, v24
MF v[108:111],a[172:175],v[40:43],v[108:111]
MF v[140:143],a[172:175],v[48:51],v[140:143]
v_exp_f32 v25, v25
MF v[172:175],a[172:175],v[56:59],v[172:175]
R16 a[116:119],v240,13312
PK v24,v24,v25
MF v[80:83],a[176:179],v[32:35],v[80:83]
v_exp_f32 v26, v26
MF v[112:115],a[176:179],v[40:43],v[112:115]
MF v[144:147],a[176:179],v[48:51],v[144:147]
v_exp_f32 v27, v27
MF v[176:179],a[176:179],v[56:59],v[176:179]
R16 a[120:123],v240,14336
PK v25,v26,v27
MF v[84:87],a[180:183],v[32:35],v[84:87]
v_exp_f32 v4, v4
MF v[116:119],a[180:183],v[40:43],v[116:119]
MF v[148:151],a[180:183],v[48:51],v[148:151]
v_exp_f32 v5, v5
MF v[180:183],a[180:183],v[56:59],v[180:183]
R16 a[124:127],v240,15360
PK v2,v4,v5
MF v[88:91],a[184:187],v[32:35],v[88:91]
v_exp_f32 v6, v6
MF v[120:123],a[184:187],v[40:43],v[120:123]
MF v[152:155],a[184:187],v[48:51],v[152:155]
v_exp_f32 v7, v7
MF v[184:187],a[184:187],v[56:59],v[184:187]
PK v3,v6,v7
MF v[92:95],a[188:191],v[32:35],v[92:95]
v_exp_f32 v12, v12
MF v[124:127],a[188:191],v[40:43],v[124:127]
MF v[156:159],a[188:191],v[48:51],v[156:159]
v_exp_f32 v13, v13
MF v[188:191],a[188:191],v[56:59],v[188:191]
PK v10,v12,v13
MF v[208:211],a[192:195],v[32:35],v[208:211]
v_exp_f32 v14, v14
MF v[212:215],a[192:195],v[40:43],v[212:215]
MF v[216:219],a[192:195],v[48:51],v[216:219]
v_exp_f32 v15, v15
MF v[220:223],a[192:195],v[56:59],v[220:223]
PK v11,v14,v15
s_waitcnt lgkmcnt(0)
MF v[32:35],a[96:99],a[0:3],v[192:195]
v_exp_f32 v20, v20
MF v[32:35],a[100:103],a[4:7],v[32:35]
s_waitcnt vmcnt(12)
s_barrier
MF v[32:35],a[104:107],a[8:11],v[32:35]
v_exp_f32 v21, v21
MF v[32:35],a[108:111],a[12:15],v[32:35]
v_add_u32 v241, 0x8000, v241
v_and_b32 v241, 0x1ffff, v241
MF v[40:43],a[96:99],a[16:19],v[196:199]
v_exp_f32 v22, v22
MF v[40:43],a[100:103],a[20:23],v[40:43]
R16 a[128:131],v241,16384
s_and_b32 m0, s50, 0x1ffff
MF v[40:43],a[104:107],a[24:27],v[40:43]
v_exp_f32 v23, v23
MF v[40:43],a[108:111],a[28:31],v[40:43]
R16 a[132:135],v241,17408
s_xor_b32 m0, m0, 0x14000
MF v[48:51],a[96:99],a[32:35],v[200:203]
v_exp_f32 v28, v28
MF v[48:51],a[100:103],a[36:39],v[48:51]
R16 a[136:139],v241,18432
buffer_load_dwordx4 v242, s[12:15], s50 offen offset:0 sc1 lds
MF v[48:51],a[104:107],a[40:43],v[48:51]
v_exp_f32 v29, v29
MF v[48:51],a[108:111],a[44:47],v[48:51]
R16 a[140:143],v241,19456
PK v18,v20,v21
MF v[56:59],a[96:99],a[48:51],v[204:207]
v_exp_f32 v30, v30
MF v[56:59],a[100:103],a[52:55],v[56:59]
R16 a[144:147],v241,20480
PK v19,v22,v23
MF v[56:59],a[104:107],a[56:59],v[56:59]
v_exp_f32 v31, v31
MF v[56:59],a[108:111],a[60:63],v[56:59]
R16 a[148:151],v241,21504
buffer_load_dwordx4 v242, s[12:15], s50 offen offset:1024 sc1 lds
MF v[36:39],a[112:115],a[0:3],v[192:195]
PK v26,v28,v29
PK v27,v30,v31
MF v[36:39],a[116:119],a[4:7],v[36:39]
R16 a[152:155],v241,22528
MF v[36:39],a[120:123],a[8:11],v[36:39]
MF v[36:39],a[124:127],a[12:15],v[36:39]
R16 a[156:159],v241,23552
v_add_u32 v240, 0x8000, v240
MF v[44:47],a[112:115],a[16:19],v[196:199]
v_exp_f32 v32, v32
MF v[44:47],a[116:119],a[20:23],v[44:47]
buffer_load_dwordx4 v242, s[12:15], s50 offen offset:2048 sc1 lds
v_and_b32 v240, 0x1ffff, v240
MF v[44:47],a[120:123],a[24:27],v[44:47]
v_exp_f32 v33, v33
MF v[44:47],a[124:127],a[28:31],v[44:47]
PK v32,v32,v33
MF v[52:55],a[112:115],a[32:35],v[200:203]
v_exp_f32 v34, v34
MF v[52:55],a[116:119],a[36:39],v[52:55]
MF v[52:55],a[120:123],a[40:43],v[52:55]
v_exp_f32 v35, v35
MF v[52:55],a[124:127],a[44:47],v[52:55]
R16 a[64:67],v240,0
PK v33,v34,v35
MF v[60:63],a[112:115],a[48:51],v[204:207]
v_exp_f32 v40, v40
MF v[60:63],a[116:119],a[52:55],v[60:63]
buffer_load_dwordx4 v242, s[12:15], s50 offen offset:3072 sc1 lds
MF v[60:63],a[120:123],a[56:59],v[60:63]
v_exp_f32 v41, v41
MF v[60:63],a[124:127],a[60:63],v[60:63]
R16 a[68:71],v240,1024
s_waitcnt lgkmcnt(2)
MF v[64:67],a[128:131],v[0:3],v[64:67]
v_exp_f32 v42, v42
MF v[96:99],a[128:131],v[8:11],v[96:99]
s_xor_b32 m0, m0, 0x14000
PK v40,v40,v41
MF v[128:131],a[128:131],v[16:19],v[128:131]
v_exp_f32 v43, v43
MF v[160:163],a[128:131],v[24:27],v[160:163]
R16 a[72:75],v240,2048
PK v41,v42,v43
MF v[68:71],a[132:135],v[0:3],v[68:71]
v_exp_f32 v48, v48
MF v[100:103],a[132:135],v[8:11],v[100:103]
buffer_load_dwordx4 v242, s[8:11], s50 offen offset:0 sc1 lds
MF v[132:135],a[132:135],v[16:19],v[132:135]
v_exp_f32 v49, v49
MF v[164:167],a[132:135],v[24:27],v[164:167]
R16 a[76:79],v240,3072
PK v48,v48,v49
MF v[72:75],a[136:139],v[0:3],v[72:75]
v_exp_f32 v50, v50
MF v[104:107],a[136:139],v[8:11],v[104:107]
buffer_load_dwordx4 v242, s[8:11], s50 offen offset:1024 sc1 lds
MF v[136:139],a[136:139],v[16:19],v[136:139]
v_exp_f32 v51, v51
MF v[168:171],a[136:139],v[24:27],v[168:171]
R16 a[80:83],v240,4096
PK v49,v50,v51
MF v[76:79],a[140:143],v[0:3],v[76:79]
v_exp_f32 v56, v56
MF v[108:111],a[140:143],v[8:11],v[108:111]
MF v[140:143],a[140:143],v[16:19],v[140:143]
v_exp_f32 v57, v57
MF v[172:175],a[140:143],v[24:27],v[172:175]
R16 a[84:87],v240,5120
PK v56,v56,v57
MF v[80:83],a[144:147],v[0:3],v[80:83]
v_exp_f32 v58, v58
MF v[112:115],a[144:147],v[8:11],v[112:115]
buffer_load_dwordx4 v242, s[8:11], s50 offen offset:2048 sc1 lds
MF v[144:147],a[144:147],v[16:19],v[144:147]
v_exp_f32 v59, v59
MF v[176:179],a[144:147],v[24:27],v[176:179]
R16 a[88:91],v240,6144
PK v57,v58,v59
MF v[84:87],a[148:151],v[0:3],v[84:87]
v_exp_f32 v36, v36
MF v[116:119],a[148:151],v[8:11],v[116:119]
buffer_load_dwordx4 v242, s[8:11], s50 offen offset:3072 sc1 lds
MF v[148:151],a[148:151],v[16:19],v[148:151]
v_exp_f32 v37, v37
MF v[180:183],a[148:151],v[24:27],v[180:183]
R16 a[92:95],v240,7168
s_add_u32 s50, s50, 0x8000
MF v[88:91],a[152:155],v[0:3],v[88:91]
v_exp_f32 v38, v38
MF v[120:123],a[152:155],v[8:11],v[120:123]
PK v34,v36,v37
MF v[152:155],a[152:155],v[16:19],v[152:155]
v_exp_f32 v39, v39
MF v[184:187],a[152:155],v[24:27],v[184:187]
PK v35,v38,v39
MF v[92:95],a[156:159],v[0:3],v[92:95]
v_exp_f32 v44, v44
MF v[124:127],a[156:159],v[8:11],v[124:127]
MF v[156:159],a[156:159],v[16:19],v[156:159]
v_exp_f32 v45, v45
MF v[188:191],a[156:159],v[24:27],v[188:191]
PK v42,v44,v45
MF v[208:211],a[192:195],v[0:3],v[208:211]
v_exp_f32 v46, v46
MF v[212:215],a[192:195],v[8:11],v[212:215]
MF v[216:219],a[192:195],v[16:19],v[216:219]
v_exp_f32 v47, v47
MF v[220:223],a[192:195],v[24:27],v[220:223]
PK v43,v46,v47
s_waitcnt lgkmcnt(0)
s_sub_u32 s90, s90, 1
s_cbranch_scc0 L_SVB_%=
s_swappc_b64 s[18:19], s[16:17]
L_SVB_%=:
s_sub_u32 s31, s31, 1
s_cbranch_scc0 L_L1_%=
s_cmp_lt_u32 s29, 6
s_cbranch_scc1 L_K0_%=
MF v[0:3],a[64:67],a[0:3],v[192:195]
v_exp_f32 v52, v52
MF v[0:3],a[68:71],a[4:7],v[0:3]
v_bfe_u32 v250, v242, 4, 1
v_bfe_u32 v251, v242, 5, 2
MF v[0:3],a[72:75],a[8:11],v[0:3]
v_exp_f32 v53, v53
MF v[0:3],a[76:79],a[12:15],v[0:3]
PK v50,v52,v53
v_lshl_add_u32 v250, v251, 3, v250
MF v[8:11],a[64:67],a[16:19],v[196:199]
v_exp_f32 v54, v54
MF v[8:11],a[68:71],a[20:23],v[8:11]
R16 a[160:163],v241,24576
v_lshlrev_b32 v250, 7, v250
v_bfe_u32 v251, v242, 7, 2
MF v[8:11],a[72:75],a[24:27],v[8:11]
v_exp_f32 v55, v55
v_lshl_add_u32 v250, v251, 5, v250
MF v[8:11],a[76:79],a[28:31],v[8:11]
R16 a[164:167],v241,25600
PK v51,v54,v55
MF v[16:19],a[64:67],a[32:35],v[200:203]
v_exp_f32 v60, v60
MF v[16:19],a[68:71],a[36:39],v[16:19]
R16 a[168:171],v241,26624
s_lshl_b32 s79, s80, 8
s_lshl_b32 s49, s82, 3
MF v[16:19],a[72:75],a[40:43],v[16:19]
v_exp_f32 v61, v61
s_add_u32 s79, s79, s49
MF v[16:19],a[76:79],a[44:47],v[16:19]
R16 a[172:175],v241,27648
PK v58,v60,v61
s_lshr_b32 s49, s28, 1
MF v[24:27],a[64:67],a[48:51],v[204:207]
v_exp_f32 v62, v62
MF v[24:27],a[68:71],a[52:55],v[24:27]
R16 a[176:179],v241,28672
s_lshl_b32 s49, s49, 2
s_add_u32 s79, s79, s49
MF v[24:27],a[72:75],a[56:59],v[24:27]
v_exp_f32 v63, v63
s_add_u32 s52, s79, 0x4905000
MF v[24:27],a[76:79],a[60:63],v[24:27]
R16 a[180:183],v241,29696
)A"
R"A(PK v59,v62,v63
MF v[4:7],a[80:83],a[0:3],v[192:195]
buffer_load_dword v251, v250, s[20:23], s52 offen offset:0 sc1
MF v[4:7],a[84:87],a[4:7],v[4:7]
R16 a[184:187],v241,30720
MF v[4:7],a[88:91],a[8:11],v[4:7]
MF v[4:7],a[92:95],a[12:15],v[4:7]
R16 a[188:191],v241,31744
MF v[12:15],a[80:83],a[16:19],v[196:199]
v_exp_f32 v0, v0
MF v[12:15],a[84:87],a[20:23],v[12:15]
MF v[12:15],a[88:91],a[24:27],v[12:15]
v_exp_f32 v1, v1
MF v[12:15],a[92:95],a[28:31],v[12:15]
PK v0,v0,v1
MF v[20:23],a[80:83],a[32:35],v[200:203]
v_exp_f32 v2, v2
MF v[20:23],a[84:87],a[36:39],v[20:23]
MF v[20:23],a[88:91],a[40:43],v[20:23]
v_exp_f32 v3, v3
MF v[20:23],a[92:95],a[44:47],v[20:23]
R16 a[96:99],v240,8192
PK v1,v2,v3
MF v[28:31],a[80:83],a[48:51],v[204:207]
v_exp_f32 v8, v8
MF v[28:31],a[84:87],a[52:55],v[28:31]
MF v[28:31],a[88:91],a[56:59],v[28:31]
v_exp_f32 v9, v9
MF v[28:31],a[92:95],a[60:63],v[28:31]
R16 a[100:103],v240,9216
s_waitcnt lgkmcnt(2)
MF v[64:67],a[160:163],v[32:35],v[64:67]
v_exp_f32 v10, v10
MF v[96:99],a[160:163],v[40:43],v[96:99]
PK v8,v8,v9
MF v[128:131],a[160:163],v[48:51],v[128:131]
v_exp_f32 v11, v11
MF v[160:163],a[160:163],v[56:59],v[160:163]
R16 a[104:107],v240,10240
PK v9,v10,v11
MF v[68:71],a[164:167],v[32:35],v[68:71]
v_exp_f32 v16, v16
MF v[100:103],a[164:167],v[40:43],v[100:103]
MF v[132:135],a[164:167],v[48:51],v[132:135]
v_exp_f32 v17, v17
MF v[164:167],a[164:167],v[56:59],v[164:167]
R16 a[108:111],v240,11264
PK v16,v16,v17
MF v[72:75],a[168:171],v[32:35],v[72:75]
v_exp_f32 v18, v18
MF v[104:107],a[168:171],v[40:43],v[104:107]
MF v[136:139],a[168:171],v[48:51],v[136:139]
v_exp_f32 v19, v19
MF v[168:171],a[168:171],v[56:59],v[168:171]
R16 a[112:115],v240,12288
PK v17,v18,v19
MF v[76:79],a[172:175],v[32:35],v[76:79]
v_exp_f32 v24, v24
MF v[108:111],a[172:175],v[40:43],v[108:111]
MF v[140:143],a[172:175],v[48:51],v[140:143]
v_exp_f32 v25, v25
MF v[172:175],a[172:175],v[56:59],v[172:175]
R16 a[116:119],v240,13312
PK v24,v24,v25
MF v[80:83],a[176:179],v[32:35],v[80:83]
v_exp_f32 v26, v26
MF v[112:115],a[176:179],v[40:43],v[112:115]
MF v[144:147],a[176:179],v[48:51],v[144:147]
v_exp_f32 v27, v27
MF v[176:179],a[176:179],v[56:59],v[176:179]
R16 a[120:123],v240,14336
PK v25,v26,v27
MF v[84:87],a[180:183],v[32:35],v[84:87]
v_exp_f32 v4, v4
MF v[116:119],a[180:183],v[40:43],v[116:119]
s_waitcnt vmcnt(0)
v_cmp_ne_u32 vcc, s37, v251
s_nop 0
MF v[148:151],a[180:183],v[48:51],v[148:151]
v_exp_f32 v5, v5
s_cbranch_vccnz L_SP_WF6_%=
L_RT_WF6_%=:
MF v[180:183],a[180:183],v[56:59],v[180:183]
R16 a[124:127],v240,15360
PK v2,v4,v5
MF v[88:91],a[184:187],v[32:35],v[88:91]
v_exp_f32 v6, v6
MF v[120:123],a[184:187],v[40:43],v[120:123]
s_add_u32 s79, s53, 0x0
MF v[152:155],a[184:187],v[48:51],v[152:155]
buffer_load_dwordx4 a[196:199], v242, s[20:23], s79 offen offset:0 sc1
v_exp_f32 v7, v7
MF v[184:187],a[184:187],v[56:59],v[184:187]
buffer_load_dwordx4 a[200:203], v242, s[20:23], s79 offen offset:1024 sc1
PK v3,v6,v7
MF v[92:95],a[188:191],v[32:35],v[92:95]
v_exp_f32 v12, v12
MF v[124:127],a[188:191],v[40:43],v[124:127]
MF v[156:159],a[188:191],v[48:51],v[156:159]
v_exp_f32 v13, v13
MF v[188:191],a[188:191],v[56:59],v[188:191]
buffer_load_dwordx4 a[204:207], v242, s[20:23], s79 offen offset:2048 sc1
PK v10,v12,v13
MF v[208:211],a[192:195],v[32:35],v[208:211]
v_exp_f32 v14, v14
MF v[212:215],a[192:195],v[40:43],v[212:215]
MF v[216:219],a[192:195],v[48:51],v[216:219]
v_exp_f32 v15, v15
MF v[220:223],a[192:195],v[56:59],v[220:223]
PK v11,v14,v15
s_waitcnt lgkmcnt(0)
MF v[32:35],a[96:99],a[0:3],v[192:195]
v_exp_f32 v20, v20
MF v[32:35],a[100:103],a[4:7],v[32:35]
s_waitcnt vmcnt(16)
s_barrier
MF v[32:35],a[104:107],a[8:11],v[32:35]
v_exp_f32 v21, v21
MF v[32:35],a[108:111],a[12:15],v[32:35]
v_add_u32 v241, 0x8000, v241
v_and_b32 v241, 0x1ffff, v241
s_add_u32 s79, s53, 0x0
MF v[40:43],a[96:99],a[16:19],v[196:199]
buffer_load_dwordx4 a[208:211], v242, s[20:23], s79 offen offset:3072 sc1
v_exp_f32 v22, v22
MF v[40:43],a[100:103],a[20:23],v[40:43]
R16 a[128:131],v241,16384
s_and_b32 m0, s50, 0x1ffff
MF v[40:43],a[104:107],a[24:27],v[40:43]
v_exp_f32 v23, v23
MF v[40:43],a[108:111],a[28:31],v[40:43]
R16 a[132:135],v241,17408
s_xor_b32 m0, m0, 0x14000
MF v[48:51],a[96:99],a[32:35],v[200:203]
v_exp_f32 v28, v28
s_add_u32 s79, s53, 0x1000
MF v[48:51],a[100:103],a[36:39],v[48:51]
R16 a[136:139],v241,18432
buffer_load_dwordx4 v242, s[12:15], s50 offen offset:0 sc1 lds
buffer_load_dwordx4 a[212:215], v242, s[20:23], s79 offen offset:0 sc1
MF v[48:51],a[104:107],a[40:43],v[48:51]
v_exp_f32 v29, v29
MF v[48:51],a[108:111],a[44:47],v[48:51]
R16 a[140:143],v241,19456
PK v18,v20,v21
MF v[56:59],a[96:99],a[48:51],v[204:207]
v_exp_f32 v30, v30
MF v[56:59],a[100:103],a[52:55],v[56:59]
R16 a[144:147],v241,20480
buffer_load_dwordx4 a[216:219], v242, s[20:23], s79 offen offset:1024 sc1
PK v19,v22,v23
MF v[56:59],a[104:107],a[56:59],v[56:59]
v_exp_f32 v31, v31
MF v[56:59],a[108:111],a[60:63],v[56:59]
R16 a[148:151],v241,21504
buffer_load_dwordx4 v242, s[12:15], s50 offen offset:1024 sc1 lds
MF v[36:39],a[112:115],a[0:3],v[192:195]
PK v26,v28,v29
PK v27,v30,v31
MF v[36:39],a[116:119],a[4:7],v[36:39]
R16 a[152:155],v241,22528
MF v[36:39],a[120:123],a[8:11],v[36:39]
buffer_load_dwordx4 a[220:223], v242, s[20:23], s79 offen offset:2048 sc1
MF v[36:39],a[124:127],a[12:15],v[36:39]
R16 a[156:159],v241,23552
v_add_u32 v240, 0x8000, v240
MF v[44:47],a[112:115],a[16:19],v[196:199]
v_exp_f32 v32, v32
MF v[44:47],a[116:119],a[20:23],v[44:47]
buffer_load_dwordx4 v242, s[12:15], s50 offen offset:2048 sc1 lds
v_and_b32 v240, 0x1ffff, v240
MF v[44:47],a[120:123],a[24:27],v[44:47]
v_exp_f32 v33, v33
MF v[44:47],a[124:127],a[28:31],v[44:47]
buffer_load_dwordx4 a[224:227], v242, s[20:23], s79 offen offset:3072 sc1
PK v32,v32,v33
MF v[52:55],a[112:115],a[32:35],v[200:203]
v_exp_f32 v34, v34
MF v[52:55],a[116:119],a[36:39],v[52:55]
MF v[52:55],a[120:123],a[40:43],v[52:55]
v_exp_f32 v35, v35
MF v[52:55],a[124:127],a[44:47],v[52:55]
R16 a[64:67],v240,0
PK v33,v34,v35
MF v[60:63],a[112:115],a[48:51],v[204:207]
v_exp_f32 v40, v40
MF v[60:63],a[116:119],a[52:55],v[60:63]
buffer_load_dwordx4 v242, s[12:15], s50 offen offset:3072 sc1 lds
s_add_u32 s79, s53, 0x2000
MF v[60:63],a[120:123],a[56:59],v[60:63]
buffer_load_dwordx4 a[228:231], v242, s[20:23], s79 offen offset:0 sc1
v_exp_f32 v41, v41
MF v[60:63],a[124:127],a[60:63],v[60:63]
R16 a[68:71],v240,1024
s_waitcnt lgkmcnt(2)
MF v[64:67],a[128:131],v[0:3],v[64:67]
v_exp_f32 v42, v42
MF v[96:99],a[128:131],v[8:11],v[96:99]
s_xor_b32 m0, m0, 0x14000
PK v40,v40,v41
MF v[128:131],a[128:131],v[16:19],v[128:131]
v_exp_f32 v43, v43
MF v[160:163],a[128:131],v[24:27],v[160:163]
R16 a[72:75],v240,2048
PK v41,v42,v43
MF v[68:71],a[132:135],v[0:3],v[68:71]
v_exp_f32 v48, v48
MF v[100:103],a[132:135],v[8:11],v[100:103]
buffer_load_dwordx4 v242, s[8:11], s50 offen offset:0 sc1 lds
buffer_load_dwordx4 a[232:235], v242, s[20:23], s79 offen offset:1024 sc1
MF v[132:135],a[132:135],v[16:19],v[132:135]
v_exp_f32 v49, v49
MF v[164:167],a[132:135],v[24:27],v[164:167]
R16 a[76:79],v240,3072
PK v48,v48,v49
MF v[72:75],a[136:139],v[0:3],v[72:75]
v_exp_f32 v50, v50
MF v[104:107],a[136:139],v[8:11],v[104:107]
buffer_load_dwordx4 v242, s[8:11], s50 offen offset:1024 sc1 lds
buffer_load_dwordx4 a[236:239], v242, s[20:23], s79 offen offset:2048 sc1
MF v[136:139],a[136:139],v[16:19],v[136:139]
v_exp_f32 v51, v51
MF v[168:171],a[136:139],v[24:27],v[168:171]
R16 a[80:83],v240,4096
PK v49,v50,v51
MF v[76:79],a[140:143],v[0:3],v[76:79]
v_exp_f32 v56, v56
MF v[108:111],a[140:143],v[8:11],v[108:111]
buffer_load_dwordx4 a[240:243], v242, s[20:23], s79 offen offset:3072 sc1
MF v[140:143],a[140:143],v[16:19],v[140:143]
v_exp_f32 v57, v57
MF v[172:175],a[140:143],v[24:27],v[172:175]
R16 a[84:87],v240,5120
PK v56,v56,v57
MF v[80:83],a[144:147],v[0:3],v[80:83]
v_exp_f32 v58, v58
MF v[112:115],a[144:147],v[8:11],v[112:115]
buffer_load_dwordx4 v242, s[8:11], s50 offen offset:2048 sc1 lds
s_add_u32 s79, s53, 0x3000
MF v[144:147],a[144:147],v[16:19],v[144:147]
buffer_load_dwordx4 a[244:247], v242, s[20:23], s79 offen offset:0 sc1
v_exp_f32 v59, v59
MF v[176:179],a[144:147],v[24:27],v[176:179]
R16 a[88:91],v240,6144
PK v57,v58,v59
MF v[84:87],a[148:151],v[0:3],v[84:87]
v_exp_f32 v36, v36
MF v[116:119],a[148:151],v[8:11],v[116:119]
buffer_load_dwordx4 v242, s[8:11], s50 offen offset:3072 sc1 lds
MF v[148:151],a[148:151],v[16:19],v[148:151]
v_exp_f32 v37, v37
MF v[180:183],a[148:151],v[24:27],v[180:183]
R16 a[92:95],v240,7168
s_add_u32 s50, s50, 0x8000
MF v[88:91],a[152:155],v[0:3],v[88:91]
v_exp_f32 v38, v38
MF v[120:123],a[152:155],v[8:11],v[120:123]
buffer_load_dwordx4 a[248:251], v242, s[20:23], s79 offen offset:1024 sc1
PK v34,v36,v37
MF v[152:155],a[152:155],v[16:19],v[152:155]
v_exp_f32 v39, v39
MF v[184:187],a[152:155],v[24:27],v[184:187]
PK v35,v38,v39
MF v[92:95],a[156:159],v[0:3],v[92:95]
v_exp_f32 v44, v44
MF v[124:127],a[156:159],v[8:11],v[124:127]
buffer_load_dwordx4 a[252:255], v242, s[20:23], s79 offen offset:2048 sc1
MF v[156:159],a[156:159],v[16:19],v[156:159]
v_exp_f32 v45, v45
MF v[188:191],a[156:159],v[24:27],v[188:191]
PK v42,v44,v45
MF v[208:211],a[192:195],v[0:3],v[208:211]
v_exp_f32 v46, v46
MF v[212:215],a[192:195],v[8:11],v[212:215]
MF v[216:219],a[192:195],v[16:19],v[216:219]
v_exp_f32 v47, v47
MF v[220:223],a[192:195],v[24:27],v[220:223]
PK v43,v46,v47
s_waitcnt lgkmcnt(0)
L_K0_%=:
s_cmp_eq_u32 s28, 0
s_cbranch_scc1 L_D0_%=
MF v[0:3],a[64:67],a[0:3],v[192:195]
v_exp_f32 v52, v52
MF v[0:3],a[68:71],a[4:7],v[0:3]
MF v[0:3],a[72:75],a[8:11],v[0:3]
v_exp_f32 v53, v53
MF v[0:3],a[76:79],a[12:15],v[0:3]
PK v50,v52,v53
MF v[8:11],a[64:67],a[16:19],v[196:199]
v_exp_f32 v54, v54
MF v[8:11],a[68:71],a[20:23],v[8:11]
R16 a[160:163],v241,24576
MF v[8:11],a[72:75],a[24:27],v[8:11]
v_exp_f32 v55, v55
MF v[8:11],a[76:79],a[28:31],v[8:11]
R16 a[164:167],v241,25600
PK v51,v54,v55
MF v[16:19],a[64:67],a[32:35],v[200:203]
v_exp_f32 v60, v60
MF v[16:19],a[68:71],a[36:39],v[16:19]
R16 a[168:171],v241,26624
MF v[16:19],a[72:75],a[40:43],v[16:19]
v_exp_f32 v61, v61
MF v[16:19],a[76:79],a[44:47],v[16:19]
R16 a[172:175],v241,27648
PK v58,v60,v61
MF v[24:27],a[64:67],a[48:51],v[204:207]
v_exp_f32 v62, v62
MF v[24:27],a[68:71],a[52:55],v[24:27]
R16 a[176:179],v241,28672
MF v[24:27],a[72:75],a[56:59],v[24:27]
v_exp_f32 v63, v63
MF v[24:27],a[76:79],a[60:63],v[24:27]
R16 a[180:183],v241,29696
PK v59,v62,v63
MF v[4:7],a[80:83],a[0:3],v[192:195]
MF v[4:7],a[84:87],a[4:7],v[4:7]
R16 a[184:187],v241,30720
MF v[4:7],a[88:91],a[8:11],v[4:7]
MF v[4:7],a[92:95],a[12:15],v[4:7]
R16 a[188:191],v241,31744
MF v[12:15],a[80:83],a[16:19],v[196:199]
v_exp_f32 v0, v0
MF v[12:15],a[84:87],a[20:23],v[12:15]
MF v[12:15],a[88:91],a[24:27],v[12:15]
v_exp_f32 v1, v1
MF v[12:15],a[92:95],a[28:31],v[12:15]
PK v0,v0,v1
MF v[20:23],a[80:83],a[32:35],v[200:203]
v_exp_f32 v2, v2
MF v[20:23],a[84:87],a[36:39],v[20:23]
MF v[20:23],a[88:91],a[40:43],v[20:23]
v_exp_f32 v3, v3
MF v[20:23],a[92:95],a[44:47],v[20:23]
R16 a[96:99],v240,8192
PK v1,v2,v3
MF v[28:31],a[80:83],a[48:51],v[204:207]
v_exp_f32 v8, v8
MF v[28:31],a[84:87],a[52:55],v[28:31]
MF v[28:31],a[88:91],a[56:59],v[28:31]
v_exp_f32 v9, v9
MF v[28:31],a[92:95],a[60:63],v[28:31]
R16 a[100:103],v240,9216
s_waitcnt lgkmcnt(2)
MF v[64:67],a[160:163],v[32:35],v[64:67]
v_exp_f32 v10, v10
MF v[96:99],a[160:163],v[40:43],v[96:99]
PK v8,v8,v9
MF v[128:131],a[160:163],v[48:51],v[128:131]
v_exp_f32 v11, v11
MF v[160:163],a[160:163],v[56:59],v[160:163]
R16 a[104:107],v240,10240
PK v9,v10,v11
MF v[68:71],a[164:167],v[32:35],v[68:71]
v_exp_f32 v16, v16
MF v[100:103],a[164:167],v[40:43],v[100:103]
MF v[132:135],a[164:167],v[48:51],v[132:135]
v_exp_f32 v17, v17
MF v[164:167],a[164:167],v[56:59],v[164:167]
R16 a[108:111],v240,11264
PK v16,v16,v17
MF v[72:75],a[168:171],v[32:35],v[72:75]
v_exp_f32 v18, v18
MF v[104:107],a[168:171],v[40:43],v[104:107]
MF v[136:139],a[168:171],v[48:51],v[136:139]
v_exp_f32 v19, v19
MF v[168:171],a[168:171],v[56:59],v[168:171]
R16 a[112:115],v240,12288
PK v17,v18,v19
MF v[76:79],a[172:175],v[32:35],v[76:79]
v_exp_f32 v24, v24
MF v[108:111],a[172:175],v[40:43],v[108:111]
MF v[140:143],a[172:175],v[48:51],v[140:143]
v_exp_f32 v25, v25
MF v[172:175],a[172:175],v[56:59],v[172:175]
R16 a[116:119],v240,13312
PK v24,v24,v25
MF v[80:83],a[176:179],v[32:35],v[80:83]
v_exp_f32 v26, v26
MF v[112:115],a[176:179],v[40:43],v[112:115]
MF v[144:147],a[176:179],v[48:51],v[144:147]
v_exp_f32 v27, v27
MF v[176:179],a[176:179],v[56:59],v[176:179]
R16 a[120:123],v240,14336
PK v25,v26,v27
MF v[84:87],a[180:183],v[32:35],v[84:87]
v_exp_f32 v4, v4
MF v[116:119],a[180:183],v[40:43],v[116:119]
MF v[148:151],a[180:183],v[48:51],v[148:151]
v_exp_f32 v5, v5
MF v[180:183],a[180:183],v[56:59],v[180:183]
R16 a[124:127],v240,15360
PK v2,v4,v5
MF v[88:91],a[184:187],v[32:35],v[88:91]
v_exp_f32 v6, v6
MF v[120:123],a[184:187],v[40:43],v[120:123]
MF v[152:155],a[184:187],v[48:51],v[152:155]
v_exp_f32 v7, v7
MF v[184:187],a[184:187],v[56:59],v[184:187]
)A"
R"A(PK v3,v6,v7
MF v[92:95],a[188:191],v[32:35],v[92:95]
v_exp_f32 v12, v12
MF v[124:127],a[188:191],v[40:43],v[124:127]
MF v[156:159],a[188:191],v[48:51],v[156:159]
v_exp_f32 v13, v13
MF v[188:191],a[188:191],v[56:59],v[188:191]
PK v10,v12,v13
MF v[208:211],a[192:195],v[32:35],v[208:211]
v_exp_f32 v14, v14
MF v[212:215],a[192:195],v[40:43],v[212:215]
MF v[216:219],a[192:195],v[48:51],v[216:219]
v_exp_f32 v15, v15
MF v[220:223],a[192:195],v[56:59],v[220:223]
PK v11,v14,v15
s_waitcnt lgkmcnt(0)
L_K1_%=:
s_cmp_eq_u32 s28, 1
s_cbranch_scc1 L_D1_%=
MF v[32:35],a[96:99],a[0:3],v[192:195]
v_exp_f32 v20, v20
MF v[32:35],a[100:103],a[4:7],v[32:35]
s_waitcnt vmcnt(12)
s_barrier
MF v[32:35],a[104:107],a[8:11],v[32:35]
v_exp_f32 v21, v21
MF v[32:35],a[108:111],a[12:15],v[32:35]
v_add_u32 v241, 0x8000, v241
v_and_b32 v241, 0x1ffff, v241
s_lshl_b32 s49, s30, 3
MF v[40:43],a[96:99],a[16:19],v[196:199]
v_exp_f32 v22, v22
s_add_u32 s49, s49, s29
MF v[40:43],a[100:103],a[20:23],v[40:43]
R16 a[128:131],v241,16384
s_and_b32 m0, s50, 0x1ffff
s_lshl_b32 s49, s49, 2
MF v[40:43],a[104:107],a[24:27],v[40:43]
v_exp_f32 v23, v23
s_add_u32 s49, s49, s28
s_lshl_b32 s49, s49, 2
s_add_u32 s49, s49, 0x4906000
MF v[40:43],a[108:111],a[28:31],v[40:43]
R16 a[132:135],v241,17408
s_xor_b32 m0, m0, 0x14000
MF v[48:51],a[96:99],a[32:35],v[200:203]
v_exp_f32 v28, v28
MF v[48:51],a[100:103],a[36:39],v[48:51]
R16 a[136:139],v241,18432
buffer_load_dwordx4 v242, s[12:15], s50 offen offset:0 sc1 lds
s_and_b32 s72, s76, 0xf9
MF v[48:51],a[104:107],a[40:43],v[48:51]
v_exp_f32 v29, v29
s_add_u32 s72, s72, 0x0
MF v[48:51],a[108:111],a[44:47],v[48:51]
R16 a[140:143],v241,19456
PK v18,v20,v21
s_lshl_b32 s72, s72, 3
MF v[56:59],a[96:99],a[48:51],v[204:207]
v_exp_f32 v30, v30
s_add_u32 s72, s72, s80
s_lshl_b32 s72, s72, 13
s_lshl_b32 s79, s28, 11
s_add_u32 s72, s72, s79
s_add_u32 s72, s72, 0x3800000
MF v[56:59],a[100:103],a[52:55],v[56:59]
R16 a[144:147],v241,20480
buffer_store_dwordx4 a[192:195], v242, s[20:23], s36 offen offset:0 sc1
PK v19,v22,v23
MF v[56:59],a[104:107],a[56:59],v[56:59]
v_exp_f32 v31, v31
MF v[56:59],a[108:111],a[60:63],v[56:59]
R16 a[148:151],v241,21504
buffer_load_dwordx4 v242, s[12:15], s50 offen offset:1024 sc1 lds
MF v[36:39],a[112:115],a[0:3],v[192:195]
PK v26,v28,v29
PK v27,v30,v31
MF v[36:39],a[116:119],a[4:7],v[36:39]
R16 a[152:155],v241,22528
MF v[36:39],a[120:123],a[8:11],v[36:39]
buffer_store_dwordx4 a[192:195], v242, s[20:23], s36 offen offset:1024 sc1
MF v[36:39],a[124:127],a[12:15],v[36:39]
R16 a[156:159],v241,23552
v_add_u32 v240, 0x8000, v240
MF v[44:47],a[112:115],a[16:19],v[196:199]
v_exp_f32 v32, v32
MF v[44:47],a[116:119],a[20:23],v[44:47]
buffer_load_dwordx4 v242, s[12:15], s50 offen offset:2048 sc1 lds
v_and_b32 v240, 0x1ffff, v240
MF v[44:47],a[120:123],a[24:27],v[44:47]
v_exp_f32 v33, v33
MF v[44:47],a[124:127],a[28:31],v[44:47]
PK v32,v32,v33
MF v[52:55],a[112:115],a[32:35],v[200:203]
v_exp_f32 v34, v34
MF v[52:55],a[116:119],a[36:39],v[52:55]
MF v[52:55],a[120:123],a[40:43],v[52:55]
v_exp_f32 v35, v35
MF v[52:55],a[124:127],a[44:47],v[52:55]
R16 a[64:67],v240,0
PK v33,v34,v35
MF v[60:63],a[112:115],a[48:51],v[204:207]
v_exp_f32 v40, v40
MF v[60:63],a[116:119],a[52:55],v[60:63]
buffer_load_dwordx4 v242, s[12:15], s50 offen offset:3072 sc1 lds
buffer_store_dwordx4 a[192:195], v242, s[20:23], s36 offen offset:2048 sc1
MF v[60:63],a[120:123],a[56:59],v[60:63]
v_exp_f32 v41, v41
MF v[60:63],a[124:127],a[60:63],v[60:63]
R16 a[68:71],v240,1024
s_waitcnt lgkmcnt(2)
MF v[64:67],a[128:131],v[0:3],v[64:67]
v_exp_f32 v42, v42
MF v[96:99],a[128:131],v[8:11],v[96:99]
s_xor_b32 m0, m0, 0x14000
PK v40,v40,v41
MF v[128:131],a[128:131],v[16:19],v[128:131]
v_exp_f32 v43, v43
MF v[160:163],a[128:131],v[24:27],v[160:163]
R16 a[72:75],v240,2048
PK v41,v42,v43
MF v[68:71],a[132:135],v[0:3],v[68:71]
v_exp_f32 v48, v48
MF v[100:103],a[132:135],v[8:11],v[100:103]
buffer_load_dwordx4 v242, s[8:11], s50 offen offset:0 sc1 lds
buffer_store_dwordx4 a[192:195], v242, s[20:23], s36 offen offset:3072 sc1
MF v[132:135],a[132:135],v[16:19],v[132:135]
v_exp_f32 v49, v49
MF v[164:167],a[132:135],v[24:27],v[164:167]
R16 a[76:79],v240,3072
PK v48,v48,v49
MF v[72:75],a[136:139],v[0:3],v[72:75]
v_exp_f32 v50, v50
MF v[104:107],a[136:139],v[8:11],v[104:107]
buffer_load_dwordx4 v242, s[8:11], s50 offen offset:1024 sc1 lds
MF v[136:139],a[136:139],v[16:19],v[136:139]
v_exp_f32 v51, v51
MF v[168:171],a[136:139],v[24:27],v[168:171]
R16 a[80:83],v240,4096
PK v49,v50,v51
MF v[76:79],a[140:143],v[0:3],v[76:79]
v_exp_f32 v56, v56
MF v[108:111],a[140:143],v[8:11],v[108:111]
v_mov_b32 v254, s37
v_mov_b32 v255, 0
s_waitcnt vmcnt(14)
MF v[140:143],a[140:143],v[16:19],v[140:143]
buffer_store_dword v254, v255, s[20:23], s49 offen offset:0 sc1
v_exp_f32 v57, v57
MF v[172:175],a[140:143],v[24:27],v[172:175]
R16 a[84:87],v240,5120
PK v56,v56,v57
s_add_u32 s79, s36, 0x20000
MF v[80:83],a[144:147],v[0:3],v[80:83]
buffer_store_dwordx4 a[192:195], v242, s[20:23], s79 offen offset:0 sc1
v_exp_f32 v58, v58
MF v[112:115],a[144:147],v[8:11],v[112:115]
buffer_load_dwordx4 v242, s[8:11], s50 offen offset:2048 sc1 lds
MF v[144:147],a[144:147],v[16:19],v[144:147]
v_exp_f32 v59, v59
MF v[176:179],a[144:147],v[24:27],v[176:179]
R16 a[88:91],v240,6144
PK v57,v58,v59
MF v[84:87],a[148:151],v[0:3],v[84:87]
v_exp_f32 v36, v36
MF v[116:119],a[148:151],v[8:11],v[116:119]
buffer_load_dwordx4 v242, s[8:11], s50 offen offset:3072 sc1 lds
MF v[148:151],a[148:151],v[16:19],v[148:151]
v_exp_f32 v37, v37
MF v[180:183],a[148:151],v[24:27],v[180:183]
R16 a[92:95],v240,7168
s_add_u32 s50, s50, 0x8000
MF v[88:91],a[152:155],v[0:3],v[88:91]
v_exp_f32 v38, v38
MF v[120:123],a[152:155],v[8:11],v[120:123]
PK v34,v36,v37
MF v[152:155],a[152:155],v[16:19],v[152:155]
v_exp_f32 v39, v39
MF v[184:187],a[152:155],v[24:27],v[184:187]
PK v35,v38,v39
MF v[92:95],a[156:159],v[0:3],v[92:95]
v_exp_f32 v44, v44
MF v[124:127],a[156:159],v[8:11],v[124:127]
MF v[156:159],a[156:159],v[16:19],v[156:159]
v_exp_f32 v45, v45
MF v[188:191],a[156:159],v[24:27],v[188:191]
PK v42,v44,v45
MF v[208:211],a[192:195],v[0:3],v[208:211]
v_exp_f32 v46, v46
MF v[212:215],a[192:195],v[8:11],v[212:215]
MF v[216:219],a[192:195],v[16:19],v[216:219]
v_exp_f32 v47, v47
MF v[220:223],a[192:195],v[24:27],v[220:223]
PK v43,v46,v47
s_waitcnt lgkmcnt(0)
s_sub_u32 s90, s90, 1
s_cbranch_scc0 L_SVC_%=
s_swappc_b64 s[18:19], s[16:17]
L_SVC_%=:
s_cmp_eq_u32 s28, 2
s_cbranch_scc1 L_D2_%=
MF v[0:3],a[64:67],a[0:3],v[192:195]
v_exp_f32 v52, v52
MF v[0:3],a[68:71],a[4:7],v[0:3]
MF v[0:3],a[72:75],a[8:11],v[0:3]
v_exp_f32 v53, v53
MF v[0:3],a[76:79],a[12:15],v[0:3]
PK v50,v52,v53
s_add_u32 s79, s36, 0x20000
MF v[8:11],a[64:67],a[16:19],v[196:199]
v_exp_f32 v54, v54
MF v[8:11],a[68:71],a[20:23],v[8:11]
R16 a[160:163],v241,24576
buffer_store_dwordx4 a[192:195], v242, s[20:23], s79 offen offset:1024 sc1
MF v[8:11],a[72:75],a[24:27],v[8:11]
v_exp_f32 v55, v55
MF v[8:11],a[76:79],a[28:31],v[8:11]
R16 a[164:167],v241,25600
PK v51,v54,v55
MF v[16:19],a[64:67],a[32:35],v[200:203]
v_exp_f32 v60, v60
MF v[16:19],a[68:71],a[36:39],v[16:19]
R16 a[168:171],v241,26624
MF v[16:19],a[72:75],a[40:43],v[16:19]
v_exp_f32 v61, v61
MF v[16:19],a[76:79],a[44:47],v[16:19]
R16 a[172:175],v241,27648
PK v58,v60,v61
MF v[24:27],a[64:67],a[48:51],v[204:207]
v_exp_f32 v62, v62
MF v[24:27],a[68:71],a[52:55],v[24:27]
R16 a[176:179],v241,28672
buffer_store_dwordx4 a[192:195], v242, s[20:23], s79 offen offset:2048 sc1
MF v[24:27],a[72:75],a[56:59],v[24:27]
v_exp_f32 v63, v63
MF v[24:27],a[76:79],a[60:63],v[24:27]
R16 a[180:183],v241,29696
PK v59,v62,v63
MF v[4:7],a[80:83],a[0:3],v[192:195]
MF v[4:7],a[84:87],a[4:7],v[4:7]
R16 a[184:187],v241,30720
MF v[4:7],a[88:91],a[8:11],v[4:7]
MF v[4:7],a[92:95],a[12:15],v[4:7]
R16 a[188:191],v241,31744
MF v[12:15],a[80:83],a[16:19],v[196:199]
v_exp_f32 v0, v0
MF v[12:15],a[84:87],a[20:23],v[12:15]
MF v[12:15],a[88:91],a[24:27],v[12:15]
v_exp_f32 v1, v1
MF v[12:15],a[92:95],a[28:31],v[12:15]
buffer_store_dwordx4 a[192:195], v242, s[20:23], s79 offen offset:3072 sc1
PK v0,v0,v1
MF v[20:23],a[80:83],a[32:35],v[200:203]
v_exp_f32 v2, v2
MF v[20:23],a[84:87],a[36:39],v[20:23]
MF v[20:23],a[88:91],a[40:43],v[20:23]
v_exp_f32 v3, v3
MF v[20:23],a[92:95],a[44:47],v[20:23]
R16 a[96:99],v240,8192
PK v1,v2,v3
MF v[28:31],a[80:83],a[48:51],v[204:207]
v_exp_f32 v8, v8
MF v[28:31],a[84:87],a[52:55],v[28:31]
MF v[28:31],a[88:91],a[56:59],v[28:31]
v_exp_f32 v9, v9
MF v[28:31],a[92:95],a[60:63],v[28:31]
R16 a[100:103],v240,9216
s_waitcnt lgkmcnt(2)
MF v[64:67],a[160:163],v[32:35],v[64:67]
v_exp_f32 v10, v10
MF v[96:99],a[160:163],v[40:43],v[96:99]
PK v8,v8,v9
s_add_u32 s79, s72, 0x0
MF v[128:131],a[160:163],v[48:51],v[128:131]
buffer_store_dwordx4 a[192:195], v242, s[20:23], s79 offen offset:0 sc1
v_exp_f32 v11, v11
MF v[160:163],a[160:163],v[56:59],v[160:163]
R16 a[104:107],v240,10240
PK v9,v10,v11
MF v[68:71],a[164:167],v[32:35],v[68:71]
v_exp_f32 v16, v16
MF v[100:103],a[164:167],v[40:43],v[100:103]
MF v[132:135],a[164:167],v[48:51],v[132:135]
v_exp_f32 v17, v17
MF v[164:167],a[164:167],v[56:59],v[164:167]
R16 a[108:111],v240,11264
PK v16,v16,v17
MF v[72:75],a[168:171],v[32:35],v[72:75]
v_exp_f32 v18, v18
MF v[104:107],a[168:171],v[40:43],v[104:107]
MF v[136:139],a[168:171],v[48:51],v[136:139]
v_exp_f32 v19, v19
MF v[168:171],a[168:171],v[56:59],v[168:171]
R16 a[112:115],v240,12288
PK v17,v18,v19
MF v[76:79],a[172:175],v[32:35],v[76:79]
v_exp_f32 v24, v24
MF v[108:111],a[172:175],v[40:43],v[108:111]
s_add_u32 s79, s72, 0x20000
MF v[140:143],a[172:175],v[48:51],v[140:143]
buffer_store_dwordx4 a[192:195], v242, s[20:23], s79 offen offset:0 sc1
v_exp_f32 v25, v25
MF v[172:175],a[172:175],v[56:59],v[172:175]
R16 a[116:119],v240,13312
PK v24,v24,v25
MF v[80:83],a[176:179],v[32:35],v[80:83]
v_exp_f32 v26, v26
MF v[112:115],a[176:179],v[40:43],v[112:115]
MF v[144:147],a[176:179],v[48:51],v[144:147]
v_exp_f32 v27, v27
MF v[176:179],a[176:179],v[56:59],v[176:179]
R16 a[120:123],v240,14336
PK v25,v26,v27
MF v[84:87],a[180:183],v[32:35],v[84:87]
v_exp_f32 v4, v4
MF v[116:119],a[180:183],v[40:43],v[116:119]
s_add_u32 s79, s72, 0x40000
MF v[148:151],a[180:183],v[48:51],v[148:151]
buffer_store_dwordx4 a[192:195], v242, s[20:23], s79 offen offset:0 sc1
v_exp_f32 v5, v5
MF v[180:183],a[180:183],v[56:59],v[180:183]
R16 a[124:127],v240,15360
PK v2,v4,v5
MF v[88:91],a[184:187],v[32:35],v[88:91]
v_exp_f32 v6, v6
MF v[120:123],a[184:187],v[40:43],v[120:123]
MF v[152:155],a[184:187],v[48:51],v[152:155]
v_exp_f32 v7, v7
MF v[184:187],a[184:187],v[56:59],v[184:187]
PK v3,v6,v7
MF v[92:95],a[188:191],v[32:35],v[92:95]
v_exp_f32 v12, v12
MF v[124:127],a[188:191],v[40:43],v[124:127]
MF v[156:159],a[188:191],v[48:51],v[156:159]
v_exp_f32 v13, v13
MF v[188:191],a[188:191],v[56:59],v[188:191]
PK v10,v12,v13
MF v[208:211],a[192:195],v[32:35],v[208:211]
v_exp_f32 v14, v14
MF v[212:215],a[192:195],v[40:43],v[212:215]
MF v[216:219],a[192:195],v[48:51],v[216:219]
v_exp_f32 v15, v15
MF v[220:223],a[192:195],v[56:59],v[220:223]
PK v11,v14,v15
s_waitcnt lgkmcnt(0)
MF v[32:35],a[96:99],a[0:3],v[224:227]
v_exp_f32 v20, v20
MF v[32:35],a[100:103],a[4:7],v[32:35]
s_waitcnt vmcnt(24)
s_barrier
MF v[32:35],a[104:107],a[8:11],v[32:35]
v_exp_f32 v21, v21
MF v[32:35],a[108:111],a[12:15],v[32:35]
v_add_u32 v241, 0x8000, v241
v_and_b32 v241, 0x1ffff, v241
s_add_u32 s79, s72, 0x60000
MF v[40:43],a[96:99],a[16:19],v[196:199]
buffer_store_dwordx4 a[192:195], v242, s[20:23], s79 offen offset:0 sc1
v_exp_f32 v22, v22
MF v[40:43],a[100:103],a[20:23],v[40:43]
R16 a[128:131],v241,16384
s_and_b32 m0, s50, 0x1ffff
MF v[40:43],a[104:107],a[24:27],v[40:43]
v_exp_f32 v23, v23
MF v[40:43],a[108:111],a[28:31],v[40:43]
R16 a[132:135],v241,17408
s_xor_b32 m0, m0, 0x14000
MF v[48:51],a[96:99],a[32:35],v[200:203]
v_exp_f32 v28, v28
MF v[48:51],a[100:103],a[36:39],v[48:51]
R16 a[136:139],v241,18432
buffer_load_dwordx4 v242, s[12:15], s50 offen offset:0 sc1 lds
MF v[48:51],a[104:107],a[40:43],v[48:51]
v_exp_f32 v29, v29
MF v[48:51],a[108:111],a[44:47],v[48:51]
R16 a[140:143],v241,19456
PK v18,v20,v21
MF v[56:59],a[96:99],a[48:51],v[204:207]
v_exp_f32 v30, v30
MF v[56:59],a[100:103],a[52:55],v[56:59]
R16 a[144:147],v241,20480
PK v19,v22,v23
s_add_u32 s79, s80, 0x1
MF v[56:59],a[104:107],a[56:59],v[56:59]
v_exp_f32 v31, v31
s_and_b32 s79, s79, 3
MF v[56:59],a[108:111],a[60:63],v[56:59]
R16 a[148:151],v241,21504
buffer_load_dwordx4 v242, s[12:15], s50 offen offset:1024 sc1 lds
s_lshl_b32 s79, s79, 17
PK v26,v28,v29
PK v27,v30,v31
s_add_u32 s79, s79, s72
s_add_u32 s79, s79, 0x8000
R16 a[152:155],v241,22528
buffer_store_dwordx4 a[192:195], v242, s[20:23], s79 offen offset:0 sc1
R16 a[156:159],v241,23552
v_add_u32 v240, 0x8000, v240
MF v[44:47],a[112:115],a[16:19],v[228:231]
v_exp_f32 v32, v32
)A"
R"A(MF v[44:47],a[116:119],a[20:23],v[44:47]
buffer_load_dwordx4 v242, s[12:15], s50 offen offset:2048 sc1 lds
v_and_b32 v240, 0x1ffff, v240
MF v[44:47],a[120:123],a[24:27],v[44:47]
v_exp_f32 v33, v33
MF v[44:47],a[124:127],a[28:31],v[44:47]
R16 a[64:67],v240,0
PK v32,v32,v33
MF v[52:55],a[112:115],a[32:35],v[200:203]
v_exp_f32 v34, v34
MF v[52:55],a[116:119],a[36:39],v[52:55]
R16 a[68:71],v240,1024
s_add_u32 s79, s80, 0x2
s_and_b32 s79, s79, 3
MF v[52:55],a[120:123],a[40:43],v[52:55]
v_exp_f32 v35, v35
s_lshl_b32 s79, s79, 17
s_add_u32 s79, s79, s72
s_add_u32 s79, s79, 0x8000
MF v[52:55],a[124:127],a[44:47],v[52:55]
R16 a[72:75],v240,2048
buffer_store_dwordx4 a[192:195], v242, s[20:23], s79 offen offset:0 sc1
PK v33,v34,v35
MF v[60:63],a[112:115],a[48:51],v[204:207]
v_exp_f32 v40, v40
MF v[60:63],a[116:119],a[52:55],v[60:63]
R16 a[76:79],v240,3072
buffer_load_dwordx4 v242, s[12:15], s50 offen offset:3072 sc1 lds
MF v[60:63],a[120:123],a[56:59],v[60:63]
v_exp_f32 v41, v41
MF v[60:63],a[124:127],a[60:63],v[60:63]
R16 a[80:83],v240,4096
s_waitcnt lgkmcnt(5)
MF v[64:67],a[128:131],v[0:3],v[64:67]
v_exp_f32 v42, v42
MF v[96:99],a[128:131],v[8:11],v[96:99]
R16 a[84:87],v240,5120
buffer_store_dwordx4 a[192:195], v242, s[20:23], s79 offen offset:1024 sc1
s_xor_b32 m0, m0, 0x14000
MF v[128:131],a[128:131],v[16:19],v[128:131]
v_exp_f32 v43, v43
MF v[160:163],a[128:131],v[24:27],v[160:163]
R16 a[88:91],v240,6144
PK v40,v40,v41
MF v[68:71],a[132:135],v[0:3],v[68:71]
v_exp_f32 v48, v48
MF v[100:103],a[132:135],v[8:11],v[100:103]
R16 a[92:95],v240,7168
buffer_load_dwordx4 v242, s[8:11], s50 offen offset:0 sc1 lds
MF v[132:135],a[132:135],v[16:19],v[132:135]
v_exp_f32 v49, v49
MF v[164:167],a[132:135],v[24:27],v[164:167]
R16 a[96:99],v240,8192
PK v41,v42,v43
MF v[72:75],a[136:139],v[0:3],v[72:75]
v_exp_f32 v50, v50
MF v[104:107],a[136:139],v[8:11],v[104:107]
R16 a[100:103],v240,9216
buffer_load_dwordx4 v242, s[8:11], s50 offen offset:1024 sc1 lds
MF v[136:139],a[136:139],v[16:19],v[136:139]
v_exp_f32 v51, v51
MF v[168:171],a[136:139],v[24:27],v[168:171]
R16 a[104:107],v240,10240
PK v48,v48,v49
s_add_u32 s79, s80, 0x3
MF v[76:79],a[140:143],v[0:3],v[76:79]
v_exp_f32 v56, v56
s_and_b32 s79, s79, 3
MF v[108:111],a[140:143],v[8:11],v[108:111]
R16 a[108:111],v240,11264
PK v49,v50,v51
s_lshl_b32 s79, s79, 17
MF v[140:143],a[140:143],v[16:19],v[140:143]
v_exp_f32 v57, v57
s_add_u32 s79, s79, s72
s_add_u32 s79, s79, 0x8000
MF v[172:175],a[140:143],v[24:27],v[172:175]
R16 a[112:115],v240,12288
buffer_store_dwordx4 a[192:195], v242, s[20:23], s79 offen offset:0 sc1
PK v56,v56,v57
MF v[80:83],a[144:147],v[0:3],v[80:83]
v_exp_f32 v58, v58
MF v[112:115],a[144:147],v[8:11],v[112:115]
R16 a[116:119],v240,13312
buffer_load_dwordx4 v242, s[8:11], s50 offen offset:2048 sc1 lds
MF v[144:147],a[144:147],v[16:19],v[144:147]
v_exp_f32 v59, v59
MF v[176:179],a[144:147],v[24:27],v[176:179]
R16 a[120:123],v240,14336
PK v57,v58,v59
MF v[84:87],a[148:151],v[0:3],v[84:87]
MF v[116:119],a[148:151],v[8:11],v[116:119]
R16 a[124:127],v240,15360
buffer_load_dwordx4 v242, s[8:11], s50 offen offset:3072 sc1 lds
MF v[148:151],a[148:151],v[16:19],v[148:151]
buffer_store_dwordx4 a[192:195], v242, s[20:23], s79 offen offset:1024 sc1
MF v[180:183],a[148:151],v[24:27],v[180:183]
s_add_u32 s50, s50, 0x8000
v_mov_b32 v34, 0
MF v[88:91],a[152:155],v[0:3],v[88:91]
MF v[120:123],a[152:155],v[8:11],v[120:123]
MF v[152:155],a[152:155],v[16:19],v[152:155]
MF v[184:187],a[152:155],v[24:27],v[184:187]
v_mov_b32 v35, 0
MF v[92:95],a[156:159],v[0:3],v[92:95]
v_exp_f32 v44, v44
MF v[124:127],a[156:159],v[8:11],v[124:127]
MF v[156:159],a[156:159],v[16:19],v[156:159]
v_exp_f32 v45, v45
MF v[188:191],a[156:159],v[24:27],v[188:191]
PK v42,v44,v45
MF v[208:211],a[192:195],v[0:3],v[208:211]
v_exp_f32 v46, v46
MF v[212:215],a[192:195],v[8:11],v[212:215]
MF v[216:219],a[192:195],v[16:19],v[216:219]
v_exp_f32 v47, v47
MF v[220:223],a[192:195],v[24:27],v[220:223]
PK v43,v46,v47
s_waitcnt lgkmcnt(0)
s_sub_u32 s90, s90, 1
s_cbranch_scc0 L_SVD_%=
s_swappc_b64 s[18:19], s[16:17]
L_SVD_%=:
s_cmp_eq_u32 s29, 7
s_cbranch_scc1 L_T73_%=
MF v[0:3],a[64:67],a[32:35],v[200:203]
v_exp_f32 v52, v52
MF v[0:3],a[68:71],a[36:39],v[0:3]
R16 a[160:163],v241,24576
MF v[0:3],a[72:75],a[40:43],v[0:3]
v_exp_f32 v53, v53
MF v[0:3],a[76:79],a[44:47],v[0:3]
R16 a[164:167],v241,25600
PK v50,v52,v53
MF v[8:11],a[64:67],a[48:51],v[204:207]
v_exp_f32 v54, v54
MF v[8:11],a[68:71],a[52:55],v[8:11]
R16 a[168:171],v241,26624
MF v[8:11],a[72:75],a[56:59],v[8:11]
v_exp_f32 v55, v55
MF v[8:11],a[76:79],a[60:63],v[8:11]
R16 a[172:175],v241,27648
PK v51,v54,v55
MF v[4:7],a[80:83],a[32:35],v[200:203]
v_exp_f32 v60, v60
MF v[4:7],a[84:87],a[36:39],v[4:7]
R16 a[176:179],v241,28672
MF v[4:7],a[88:91],a[40:43],v[4:7]
v_exp_f32 v61, v61
MF v[4:7],a[92:95],a[44:47],v[4:7]
R16 a[180:183],v241,29696
PK v58,v60,v61
MF v[12:15],a[80:83],a[48:51],v[204:207]
v_exp_f32 v62, v62
MF v[12:15],a[84:87],a[52:55],v[12:15]
R16 a[184:187],v241,30720
MF v[12:15],a[88:91],a[56:59],v[12:15]
v_exp_f32 v63, v63
MF v[12:15],a[92:95],a[60:63],v[12:15]
R16 a[188:191],v241,31744
PK v59,v62,v63
MF v[16:19],a[96:99],a[32:35],v[200:203]
v_add_u32 v240, 0x8000, v240
v_and_b32 v240, 0x1ffff, v240
MF v[16:19],a[100:103],a[36:39],v[16:19]
s_and_b32 m0, s50, 0x1ffff
MF v[16:19],a[104:107],a[40:43],v[16:19]
s_xor_b32 m0, m0, 0x14000
MF v[16:19],a[108:111],a[44:47],v[16:19]
MF v[24:27],a[96:99],a[48:51],v[204:207]
v_exp_f32 v0, v0
MF v[24:27],a[100:103],a[52:55],v[24:27]
s_waitcnt vmcnt(28)
s_barrier
MF v[24:27],a[104:107],a[56:59],v[24:27]
v_exp_f32 v1, v1
MF v[24:27],a[108:111],a[60:63],v[24:27]
R16 a[64:67],v240,0
PK v0,v0,v1
MF v[20:23],a[112:115],a[32:35],v[200:203]
v_exp_f32 v2, v2
MF v[20:23],a[116:119],a[36:39],v[20:23]
R16 a[68:71],v240,1024
buffer_load_dwordx4 v242, s[12:15], s50 offen offset:0 sc1 lds
MF v[20:23],a[120:123],a[40:43],v[20:23]
v_exp_f32 v3, v3
MF v[20:23],a[124:127],a[44:47],v[20:23]
R16 a[72:75],v240,2048
PK v1,v2,v3
MF v[28:31],a[112:115],a[48:51],v[204:207]
v_exp_f32 v8, v8
MF v[28:31],a[116:119],a[52:55],v[28:31]
R16 a[76:79],v240,3072
buffer_load_dwordx4 v242, s[12:15], s50 offen offset:1024 sc1 lds
MF v[28:31],a[120:123],a[56:59],v[28:31]
v_exp_f32 v9, v9
MF v[28:31],a[124:127],a[60:63],v[28:31]
R16 a[80:83],v240,4096
s_waitcnt lgkmcnt(5)
MF v[64:67],a[160:163],v[32:35],v[64:67]
v_exp_f32 v10, v10
MF v[96:99],a[160:163],v[40:43],v[96:99]
R16 a[84:87],v240,5120
buffer_load_dwordx4 v242, s[12:15], s50 offen offset:2048 sc1 lds
MF v[128:131],a[160:163],v[48:51],v[128:131]
v_exp_f32 v11, v11
MF v[160:163],a[160:163],v[56:59],v[160:163]
R16 a[88:91],v240,6144
PK v8,v8,v9
MF v[68:71],a[164:167],v[32:35],v[68:71]
v_exp_f32 v4, v4
MF v[100:103],a[164:167],v[40:43],v[100:103]
R16 a[92:95],v240,7168
buffer_load_dwordx4 v242, s[12:15], s50 offen offset:3072 sc1 lds
MF v[132:135],a[164:167],v[48:51],v[132:135]
v_exp_f32 v5, v5
MF v[164:167],a[164:167],v[56:59],v[164:167]
R16 a[96:99],v240,8192
s_xor_b32 m0, m0, 0x14000
MF v[72:75],a[168:171],v[32:35],v[72:75]
v_exp_f32 v6, v6
MF v[104:107],a[168:171],v[40:43],v[104:107]
R16 a[100:103],v240,9216
PK v9,v10,v11
MF v[136:139],a[168:171],v[48:51],v[136:139]
v_exp_f32 v7, v7
MF v[168:171],a[168:171],v[56:59],v[168:171]
R16 a[104:107],v240,10240
buffer_load_dwordx4 v242, s[8:11], s50 offen offset:0 sc1 lds
MF v[76:79],a[172:175],v[32:35],v[76:79]
v_exp_f32 v12, v12
MF v[108:111],a[172:175],v[40:43],v[108:111]
R16 a[108:111],v240,11264
PK v2,v4,v5
MF v[140:143],a[172:175],v[48:51],v[140:143]
v_exp_f32 v13, v13
MF v[172:175],a[172:175],v[56:59],v[172:175]
R16 a[112:115],v240,12288
buffer_load_dwordx4 v242, s[8:11], s50 offen offset:1024 sc1 lds
MF v[80:83],a[176:179],v[32:35],v[80:83]
v_exp_f32 v14, v14
MF v[112:115],a[176:179],v[40:43],v[112:115]
R16 a[116:119],v240,13312
PK v3,v6,v7
MF v[144:147],a[176:179],v[48:51],v[144:147]
v_exp_f32 v15, v15
MF v[176:179],a[176:179],v[56:59],v[176:179]
R16 a[120:123],v240,14336
buffer_load_dwordx4 v242, s[8:11], s50 offen offset:2048 sc1 lds
MF v[84:87],a[180:183],v[32:35],v[84:87]
v_exp_f32 v16, v16
MF v[116:119],a[180:183],v[40:43],v[116:119]
R16 a[124:127],v240,15360
v_add_u32 v241, 0x8000, v241
MF v[148:151],a[180:183],v[48:51],v[148:151]
v_exp_f32 v17, v17
MF v[180:183],a[180:183],v[56:59],v[180:183]
buffer_load_dwordx4 v242, s[8:11], s50 offen offset:3072 sc1 lds
v_and_b32 v241, 0x1ffff, v241
MF v[88:91],a[184:187],v[32:35],v[88:91]
v_exp_f32 v18, v18
MF v[120:123],a[184:187],v[40:43],v[120:123]
R16 a[128:131],v241,16384
PK v10,v12,v13
MF v[152:155],a[184:187],v[48:51],v[152:155]
v_exp_f32 v19, v19
MF v[184:187],a[184:187],v[56:59],v[184:187]
R16 a[132:135],v241,17408
s_add_u32 s50, s50, 0x8000
MF v[92:95],a[188:191],v[32:35],v[92:95]
v_exp_f32 v24, v24
MF v[124:127],a[188:191],v[40:43],v[124:127]
R16 a[136:139],v241,18432
PK v11,v14,v15
MF v[156:159],a[188:191],v[48:51],v[156:159]
v_exp_f32 v25, v25
MF v[188:191],a[188:191],v[56:59],v[188:191]
R16 a[140:143],v241,19456
PK v16,v16,v17
MF v[208:211],a[192:195],v[32:35],v[208:211]
v_exp_f32 v26, v26
MF v[212:215],a[192:195],v[40:43],v[212:215]
R16 a[144:147],v241,20480
PK v17,v18,v19
MF v[216:219],a[192:195],v[48:51],v[216:219]
v_exp_f32 v27, v27
MF v[220:223],a[192:195],v[56:59],v[220:223]
PK v24,v24,v25
PK v25,v26,v27
s_waitcnt lgkmcnt(5)
s_branch L_AE_%=
L_HD_%=:
MF v[0:3],a[64:67],a[0:3],v[224:227]
s_mov_b32 s24, s72
s_and_b32 s25, s73, 0xffff
MF v[0:3],a[68:71],a[4:7],v[0:3]
R16 a[96:99],v240,8192
s_mov_b32 s26, 0x400000
MF v[0:3],a[72:75],a[8:11],v[0:3]
v_lshrrev_b32 v252, 9, v242
v_lshlrev_b32 v252, 9, v252
MF v[0:3],a[76:79],a[12:15],v[0:3]
R16 a[100:103],v240,9216
v_add_u32 v252, v252, v242
MF v[8:11],a[64:67],a[16:19],v[196:199]
s_lshl_b32 s48, s30, 2
s_lshr_b32 s31, s29, 1
MF v[8:11],a[68:71],a[20:23],v[8:11]
R16 a[104:107],v240,10240
s_add_u32 s48, s48, s31
MF v[8:11],a[72:75],a[24:27],v[8:11]
s_lshl_b32 s48, s48, 2
s_add_u32 s48, s48, s28
MF v[8:11],a[76:79],a[28:31],v[8:11]
R16 a[108:111],v240,11264
s_lshl_b32 s48, s48, 13
MF v[16:19],a[64:67],a[32:35],v[200:203]
s_and_b32 s31, s29, 1
s_lshl_b32 s31, s31, 9
MF v[16:19],a[68:71],a[36:39],v[16:19]
R16 a[112:115],v240,12288
s_add_u32 s48, s48, s31
MF v[16:19],a[72:75],a[40:43],v[16:19]
s_add_u32 m0, s34, 0x20000
v_mov_b64 v[96:97], 0
MF v[16:19],a[76:79],a[44:47],v[16:19]
R16 a[116:119],v240,13312
buffer_load_dwordx4 v252, s[24:27], s48 offen offset:0 lds
MF v[24:27],a[64:67],a[48:51],v[204:207]
s_add_u32 s48, s48, 0x400
v_mov_b64 v[98:99], 0
MF v[24:27],a[68:71],a[52:55],v[24:27]
R16 a[120:123],v240,14336
buffer_load_dwordx4 v252, s[24:27], s48 offen offset:1024 lds
MF v[24:27],a[72:75],a[56:59],v[24:27]
s_add_u32 s48, s48, 0x400
v_mov_b64 v[100:101], 0
MF v[24:27],a[76:79],a[60:63],v[24:27]
R16 a[124:127],v240,15360
buffer_load_dwordx4 v252, s[24:27], s48 offen offset:2048 lds
s_add_u32 s48, s48, 0x400
v_mov_b64 v[102:103], 0
buffer_load_dwordx4 v252, s[24:27], s48 offen offset:3072 lds
v_mov_b64 v[104:105], 0
v_mov_b64 v[106:107], 0
v_mov_b64 v[108:109], 0
v_mov_b64 v[110:111], 0
v_mov_b64 v[112:113], 0
MF v[12:15],a[80:83],a[16:19],v[228:231]
v_exp_f32 v0, v0
MF v[12:15],a[84:87],a[20:23],v[12:15]
v_mov_b64 v[114:115], 0
v_mov_b64 v[116:117], 0
MF v[12:15],a[88:91],a[24:27],v[12:15]
v_exp_f32 v1, v1
MF v[12:15],a[92:95],a[28:31],v[12:15]
PK v0,v0,v1
v_mov_b64 v[118:119], 0
MF v[20:23],a[80:83],a[32:35],v[200:203]
v_exp_f32 v2, v2
MF v[20:23],a[84:87],a[36:39],v[20:23]
v_mov_b64 v[120:121], 0
v_mov_b64 v[122:123], 0
MF v[20:23],a[88:91],a[40:43],v[20:23]
v_exp_f32 v3, v3
MF v[20:23],a[92:95],a[44:47],v[20:23]
PK v1,v2,v3
v_mov_b64 v[124:125], 0
MF v[28:31],a[80:83],a[48:51],v[204:207]
v_exp_f32 v8, v8
MF v[28:31],a[84:87],a[52:55],v[28:31]
v_mov_b64 v[126:127], 0
v_mov_b64 v[128:129], 0
MF v[28:31],a[88:91],a[56:59],v[28:31]
v_exp_f32 v9, v9
MF v[28:31],a[92:95],a[60:63],v[28:31]
PK v8,v8,v9
v_mov_b64 v[130:131], 0
v_mov_b64 v[132:133], 0
v_mov_b64 v[134:135], 0
v_mov_b64 v[136:137], 0
v_mov_b64 v[138:139], 0
v_mov_b64 v[140:141], 0
v_mov_b64 v[142:143], 0
v_mov_b64 v[144:145], 0
v_mov_b64 v[146:147], 0
v_mov_b64 v[148:149], 0
v_mov_b64 v[150:151], 0
v_mov_b64 v[152:153], 0
v_mov_b64 v[154:155], 0
v_mov_b64 v[156:157], 0
v_mov_b64 v[158:159], 0
v_mov_b64 v[160:161], 0
v_mov_b64 v[162:163], 0
v_mov_b64 v[164:165], 0
v_mov_b64 v[166:167], 0
v_exp_f32 v10, v10
v_exp_f32 v11, v11
s_nop 0
PK v9,v10,v11
)A"
R"A(v_exp_f32 v16, v16
v_exp_f32 v17, v17
s_nop 0
PK v16,v16,v17
v_exp_f32 v18, v18
v_exp_f32 v19, v19
s_nop 0
PK v17,v18,v19
v_exp_f32 v24, v24
v_exp_f32 v25, v25
s_nop 0
PK v24,v24,v25
v_exp_f32 v26, v26
v_exp_f32 v27, v27
s_nop 0
PK v25,v26,v27
s_nop 0
v_mov_b32 v2, 0
s_nop 0
v_mov_b32 v3, 0
v_exp_f32 v12, v12
v_exp_f32 v13, v13
s_nop 0
PK v10,v12,v13
v_exp_f32 v14, v14
v_exp_f32 v15, v15
s_nop 0
PK v11,v14,v15
s_waitcnt lgkmcnt(0)
v_mov_b64 v[168:169], 0
v_mov_b64 v[170:171], 0
v_mov_b64 v[172:173], 0
v_mov_b64 v[174:175], 0
v_mov_b64 v[176:177], 0
v_mov_b64 v[178:179], 0
v_mov_b64 v[180:181], 0
v_mov_b64 v[182:183], 0
v_mov_b64 v[184:185], 0
v_mov_b64 v[186:187], 0
v_mov_b64 v[188:189], 0
v_mov_b64 v[190:191], 0
s_branch L_MD1_%=
L_D0_%=:
MF v[0:3],a[64:67],a[0:3],v[224:227]
v_exp_f32 v52, v52
MF v[0:3],a[68:71],a[4:7],v[0:3]
MF v[0:3],a[72:75],a[8:11],v[0:3]
v_exp_f32 v53, v53
MF v[0:3],a[76:79],a[12:15],v[0:3]
PK v50,v52,v53
MF v[8:11],a[64:67],a[16:19],v[196:199]
v_exp_f32 v54, v54
MF v[8:11],a[68:71],a[20:23],v[8:11]
R16 a[160:163],v241,24576
MF v[8:11],a[72:75],a[24:27],v[8:11]
v_exp_f32 v55, v55
MF v[8:11],a[76:79],a[28:31],v[8:11]
R16 a[164:167],v241,25600
PK v51,v54,v55
MF v[16:19],a[64:67],a[32:35],v[200:203]
v_exp_f32 v60, v60
MF v[16:19],a[68:71],a[36:39],v[16:19]
R16 a[168:171],v241,26624
MF v[16:19],a[72:75],a[40:43],v[16:19]
v_exp_f32 v61, v61
MF v[16:19],a[76:79],a[44:47],v[16:19]
R16 a[172:175],v241,27648
PK v58,v60,v61
MF v[24:27],a[64:67],a[48:51],v[204:207]
v_exp_f32 v62, v62
MF v[24:27],a[68:71],a[52:55],v[24:27]
R16 a[176:179],v241,28672
MF v[24:27],a[72:75],a[56:59],v[24:27]
v_exp_f32 v63, v63
MF v[24:27],a[76:79],a[60:63],v[24:27]
R16 a[180:183],v241,29696
PK v59,v62,v63
R16 a[184:187],v241,30720
R16 a[188:191],v241,31744
MF v[12:15],a[80:83],a[16:19],v[228:231]
v_exp_f32 v0, v0
MF v[12:15],a[84:87],a[20:23],v[12:15]
MF v[12:15],a[88:91],a[24:27],v[12:15]
v_exp_f32 v1, v1
MF v[12:15],a[92:95],a[28:31],v[12:15]
PK v0,v0,v1
MF v[20:23],a[80:83],a[32:35],v[200:203]
v_exp_f32 v2, v2
MF v[20:23],a[84:87],a[36:39],v[20:23]
MF v[20:23],a[88:91],a[40:43],v[20:23]
v_exp_f32 v3, v3
MF v[20:23],a[92:95],a[44:47],v[20:23]
R16 a[96:99],v240,8192
PK v1,v2,v3
MF v[28:31],a[80:83],a[48:51],v[204:207]
v_exp_f32 v8, v8
MF v[28:31],a[84:87],a[52:55],v[28:31]
MF v[28:31],a[88:91],a[56:59],v[28:31]
v_exp_f32 v9, v9
MF v[28:31],a[92:95],a[60:63],v[28:31]
R16 a[100:103],v240,9216
s_waitcnt lgkmcnt(2)
MF v[64:67],a[160:163],v[32:35],v[64:67]
v_exp_f32 v10, v10
MF v[96:99],a[160:163],v[40:43],v[96:99]
PK v8,v8,v9
MF v[128:131],a[160:163],v[48:51],v[128:131]
v_exp_f32 v11, v11
MF v[160:163],a[160:163],v[56:59],v[160:163]
R16 a[104:107],v240,10240
PK v9,v10,v11
MF v[68:71],a[164:167],v[32:35],v[68:71]
v_exp_f32 v16, v16
MF v[100:103],a[164:167],v[40:43],v[100:103]
MF v[132:135],a[164:167],v[48:51],v[132:135]
v_exp_f32 v17, v17
MF v[164:167],a[164:167],v[56:59],v[164:167]
R16 a[108:111],v240,11264
PK v16,v16,v17
MF v[72:75],a[168:171],v[32:35],v[72:75]
v_exp_f32 v18, v18
MF v[104:107],a[168:171],v[40:43],v[104:107]
MF v[136:139],a[168:171],v[48:51],v[136:139]
v_exp_f32 v19, v19
MF v[168:171],a[168:171],v[56:59],v[168:171]
R16 a[112:115],v240,12288
PK v17,v18,v19
MF v[76:79],a[172:175],v[32:35],v[76:79]
v_exp_f32 v24, v24
MF v[108:111],a[172:175],v[40:43],v[108:111]
MF v[140:143],a[172:175],v[48:51],v[140:143]
v_exp_f32 v25, v25
MF v[172:175],a[172:175],v[56:59],v[172:175]
R16 a[116:119],v240,13312
PK v24,v24,v25
MF v[80:83],a[176:179],v[32:35],v[80:83]
v_exp_f32 v26, v26
MF v[112:115],a[176:179],v[40:43],v[112:115]
MF v[144:147],a[176:179],v[48:51],v[144:147]
v_exp_f32 v27, v27
MF v[176:179],a[176:179],v[56:59],v[176:179]
R16 a[120:123],v240,14336
PK v25,v26,v27
MF v[84:87],a[180:183],v[32:35],v[84:87]
MF v[116:119],a[180:183],v[40:43],v[116:119]
MF v[148:151],a[180:183],v[48:51],v[148:151]
MF v[180:183],a[180:183],v[56:59],v[180:183]
R16 a[124:127],v240,15360
v_mov_b32 v2, 0
MF v[88:91],a[184:187],v[32:35],v[88:91]
MF v[120:123],a[184:187],v[40:43],v[120:123]
MF v[152:155],a[184:187],v[48:51],v[152:155]
MF v[184:187],a[184:187],v[56:59],v[184:187]
v_mov_b32 v3, 0
MF v[92:95],a[188:191],v[32:35],v[92:95]
v_exp_f32 v12, v12
MF v[124:127],a[188:191],v[40:43],v[124:127]
MF v[156:159],a[188:191],v[48:51],v[156:159]
v_exp_f32 v13, v13
MF v[188:191],a[188:191],v[56:59],v[188:191]
PK v10,v12,v13
MF v[208:211],a[192:195],v[32:35],v[208:211]
v_exp_f32 v14, v14
MF v[212:215],a[192:195],v[40:43],v[212:215]
MF v[216:219],a[192:195],v[48:51],v[216:219]
v_exp_f32 v15, v15
MF v[220:223],a[192:195],v[56:59],v[220:223]
PK v11,v14,v15
s_waitcnt lgkmcnt(0)
L_MD1_%=:
v_exp_f32 v20, v20
s_waitcnt vmcnt(12)
s_barrier
v_exp_f32 v21, v21
v_add_u32 v241, 0x8000, v241
v_and_b32 v241, 0x1ffff, v241
s_lshl_b32 s49, s30, 3
v_exp_f32 v22, v22
s_add_u32 s49, s49, s29
R16 a[128:131],v241,16384
s_and_b32 m0, s50, 0x1ffff
s_lshl_b32 s49, s49, 2
v_exp_f32 v23, v23
s_add_u32 s49, s49, s28
s_lshl_b32 s49, s49, 2
s_add_u32 s49, s49, 0x4906000
R16 a[132:135],v241,17408
s_xor_b32 m0, m0, 0x14000
MF v[48:51],a[96:99],a[32:35],v[200:203]
v_exp_f32 v28, v28
MF v[48:51],a[100:103],a[36:39],v[48:51]
R16 a[136:139],v241,18432
buffer_load_dwordx4 v242, s[12:15], s50 offen offset:0 sc1 lds
s_and_b32 s72, s76, 0xf9
MF v[48:51],a[104:107],a[40:43],v[48:51]
v_exp_f32 v29, v29
s_add_u32 s72, s72, 0x0
MF v[48:51],a[108:111],a[44:47],v[48:51]
R16 a[140:143],v241,19456
PK v18,v20,v21
s_lshl_b32 s72, s72, 3
MF v[56:59],a[96:99],a[48:51],v[204:207]
v_exp_f32 v30, v30
s_add_u32 s72, s72, s80
s_lshl_b32 s72, s72, 13
s_lshl_b32 s79, s28, 11
s_add_u32 s72, s72, s79
s_add_u32 s72, s72, 0x3800000
MF v[56:59],a[100:103],a[52:55],v[56:59]
R16 a[144:147],v241,20480
buffer_store_dwordx4 a[192:195], v242, s[20:23], s36 offen offset:0 sc1
PK v19,v22,v23
MF v[56:59],a[104:107],a[56:59],v[56:59]
v_exp_f32 v31, v31
MF v[56:59],a[108:111],a[60:63],v[56:59]
R16 a[148:151],v241,21504
buffer_load_dwordx4 v242, s[12:15], s50 offen offset:1024 sc1 lds
PK v26,v28,v29
PK v27,v30,v31
R16 a[152:155],v241,22528
buffer_store_dwordx4 a[192:195], v242, s[20:23], s36 offen offset:1024 sc1
R16 a[156:159],v241,23552
v_add_u32 v240, 0x8000, v240
v_and_b32 v240, 0x1ffff, v240
buffer_load_dwordx4 v242, s[12:15], s50 offen offset:2048 sc1 lds
MF v[52:55],a[112:115],a[32:35],v[200:203]
MF v[52:55],a[116:119],a[36:39],v[52:55]
MF v[52:55],a[120:123],a[40:43],v[52:55]
MF v[52:55],a[124:127],a[44:47],v[52:55]
R16 a[64:67],v240,0
buffer_store_dwordx4 a[192:195], v242, s[20:23], s36 offen offset:2048 sc1
MF v[60:63],a[112:115],a[48:51],v[204:207]
MF v[60:63],a[116:119],a[52:55],v[60:63]
buffer_load_dwordx4 v242, s[12:15], s50 offen offset:3072 sc1 lds
MF v[60:63],a[120:123],a[56:59],v[60:63]
MF v[60:63],a[124:127],a[60:63],v[60:63]
R16 a[68:71],v240,1024
s_waitcnt lgkmcnt(2)
MF v[64:67],a[128:131],v[0:3],v[64:67]
s_xor_b32 m0, m0, 0x14000
MF v[96:99],a[128:131],v[8:11],v[96:99]
MF v[128:131],a[128:131],v[16:19],v[128:131]
MF v[160:163],a[128:131],v[24:27],v[160:163]
R16 a[72:75],v240,2048
MF v[68:71],a[132:135],v[0:3],v[68:71]
v_exp_f32 v48, v48
MF v[100:103],a[132:135],v[8:11],v[100:103]
buffer_load_dwordx4 v242, s[8:11], s50 offen offset:0 sc1 lds
buffer_store_dwordx4 a[192:195], v242, s[20:23], s36 offen offset:3072 sc1
MF v[132:135],a[132:135],v[16:19],v[132:135]
v_exp_f32 v49, v49
MF v[164:167],a[132:135],v[24:27],v[164:167]
R16 a[76:79],v240,3072
PK v48,v48,v49
MF v[72:75],a[136:139],v[0:3],v[72:75]
v_exp_f32 v50, v50
MF v[104:107],a[136:139],v[8:11],v[104:107]
buffer_load_dwordx4 v242, s[8:11], s50 offen offset:1024 sc1 lds
MF v[136:139],a[136:139],v[16:19],v[136:139]
v_exp_f32 v51, v51
MF v[168:171],a[136:139],v[24:27],v[168:171]
R16 a[80:83],v240,4096
PK v49,v50,v51
MF v[76:79],a[140:143],v[0:3],v[76:79]
v_exp_f32 v56, v56
MF v[108:111],a[140:143],v[8:11],v[108:111]
v_mov_b32 v254, s37
v_mov_b32 v255, 0
s_waitcnt vmcnt(14)
MF v[140:143],a[140:143],v[16:19],v[140:143]
buffer_store_dword v254, v255, s[20:23], s49 offen offset:0 sc1
v_exp_f32 v57, v57
MF v[172:175],a[140:143],v[24:27],v[172:175]
R16 a[84:87],v240,5120
PK v56,v56,v57
s_add_u32 s79, s36, 0x20000
MF v[80:83],a[144:147],v[0:3],v[80:83]
buffer_store_dwordx4 a[192:195], v242, s[20:23], s79 offen offset:0 sc1
v_exp_f32 v58, v58
MF v[112:115],a[144:147],v[8:11],v[112:115]
buffer_load_dwordx4 v242, s[8:11], s50 offen offset:2048 sc1 lds
MF v[144:147],a[144:147],v[16:19],v[144:147]
v_exp_f32 v59, v59
MF v[176:179],a[144:147],v[24:27],v[176:179]
R16 a[88:91],v240,6144
PK v57,v58,v59
MF v[84:87],a[148:151],v[0:3],v[84:87]
MF v[116:119],a[148:151],v[8:11],v[116:119]
buffer_load_dwordx4 v242, s[8:11], s50 offen offset:3072 sc1 lds
MF v[148:151],a[148:151],v[16:19],v[148:151]
MF v[180:183],a[148:151],v[24:27],v[180:183]
R16 a[92:95],v240,7168
s_add_u32 s50, s50, 0x8000
MF v[88:91],a[152:155],v[0:3],v[88:91]
MF v[120:123],a[152:155],v[8:11],v[120:123]
MF v[152:155],a[152:155],v[16:19],v[152:155]
MF v[184:187],a[152:155],v[24:27],v[184:187]
MF v[92:95],a[156:159],v[0:3],v[92:95]
MF v[124:127],a[156:159],v[8:11],v[124:127]
MF v[156:159],a[156:159],v[16:19],v[156:159]
MF v[188:191],a[156:159],v[24:27],v[188:191]
MF v[208:211],a[192:195],v[0:3],v[208:211]
MF v[212:215],a[192:195],v[8:11],v[212:215]
MF v[216:219],a[192:195],v[16:19],v[216:219]
MF v[220:223],a[192:195],v[24:27],v[220:223]
s_waitcnt lgkmcnt(0)
s_sub_u32 s90, s90, 1
s_cbranch_scc0 L_SVE_%=
s_swappc_b64 s[18:19], s[16:17]
L_SVE_%=:
v_exp_f32 v52, v52
v_exp_f32 v53, v53
s_nop 0
PK v50,v52,v53
s_add_u32 s79, s36, 0x20000
v_exp_f32 v54, v54
R16 a[160:163],v241,24576
buffer_store_dwordx4 a[192:195], v242, s[20:23], s79 offen offset:1024 sc1
v_exp_f32 v55, v55
R16 a[164:167],v241,25600
PK v51,v54,v55
MF v[16:19],a[64:67],a[32:35],v[200:203]
v_exp_f32 v60, v60
MF v[16:19],a[68:71],a[36:39],v[16:19]
R16 a[168:171],v241,26624
MF v[16:19],a[72:75],a[40:43],v[16:19]
v_exp_f32 v61, v61
MF v[16:19],a[76:79],a[44:47],v[16:19]
R16 a[172:175],v241,27648
PK v58,v60,v61
MF v[24:27],a[64:67],a[48:51],v[204:207]
v_exp_f32 v62, v62
MF v[24:27],a[68:71],a[52:55],v[24:27]
R16 a[176:179],v241,28672
buffer_store_dwordx4 a[192:195], v242, s[20:23], s79 offen offset:2048 sc1
MF v[24:27],a[72:75],a[56:59],v[24:27]
v_exp_f32 v63, v63
MF v[24:27],a[76:79],a[60:63],v[24:27]
R16 a[180:183],v241,29696
PK v59,v62,v63
R16 a[184:187],v241,30720
R16 a[188:191],v241,31744
buffer_store_dwordx4 a[192:195], v242, s[20:23], s79 offen offset:3072 sc1
MF v[20:23],a[80:83],a[32:35],v[200:203]
MF v[20:23],a[84:87],a[36:39],v[20:23]
MF v[20:23],a[88:91],a[40:43],v[20:23]
MF v[20:23],a[92:95],a[44:47],v[20:23]
R16 a[96:99],v240,8192
MF v[28:31],a[80:83],a[48:51],v[204:207]
MF v[28:31],a[84:87],a[52:55],v[28:31]
MF v[28:31],a[88:91],a[56:59],v[28:31]
MF v[28:31],a[92:95],a[60:63],v[28:31]
R16 a[100:103],v240,9216
s_waitcnt lgkmcnt(2)
s_add_u32 s79, s72, 0x0
MF v[128:131],a[160:163],v[48:51],v[128:131]
buffer_store_dwordx4 a[192:195], v242, s[20:23], s79 offen offset:0 sc1
MF v[160:163],a[160:163],v[56:59],v[160:163]
R16 a[104:107],v240,10240
v_exp_f32 v16, v16
MF v[132:135],a[164:167],v[48:51],v[132:135]
v_exp_f32 v17, v17
MF v[164:167],a[164:167],v[56:59],v[164:167]
R16 a[108:111],v240,11264
PK v16,v16,v17
v_exp_f32 v18, v18
MF v[136:139],a[168:171],v[48:51],v[136:139]
v_exp_f32 v19, v19
MF v[168:171],a[168:171],v[56:59],v[168:171]
R16 a[112:115],v240,12288
PK v17,v18,v19
v_exp_f32 v24, v24
s_add_u32 s79, s72, 0x20000
MF v[140:143],a[172:175],v[48:51],v[140:143]
buffer_store_dwordx4 a[192:195], v242, s[20:23], s79 offen offset:0 sc1
v_exp_f32 v25, v25
MF v[172:175],a[172:175],v[56:59],v[172:175]
R16 a[116:119],v240,13312
PK v24,v24,v25
v_exp_f32 v26, v26
MF v[144:147],a[176:179],v[48:51],v[144:147]
v_exp_f32 v27, v27
MF v[176:179],a[176:179],v[56:59],v[176:179]
R16 a[120:123],v240,14336
PK v25,v26,v27
s_add_u32 s79, s72, 0x40000
MF v[148:151],a[180:183],v[48:51],v[148:151]
buffer_store_dwordx4 a[192:195], v242, s[20:23], s79 offen offset:0 sc1
MF v[180:183],a[180:183],v[56:59],v[180:183]
R16 a[124:127],v240,15360
MF v[152:155],a[184:187],v[48:51],v[152:155]
MF v[184:187],a[184:187],v[56:59],v[184:187]
MF v[156:159],a[188:191],v[48:51],v[156:159]
MF v[188:191],a[188:191],v[56:59],v[188:191]
MF v[216:219],a[192:195],v[48:51],v[216:219]
MF v[220:223],a[192:195],v[56:59],v[220:223]
s_waitcnt lgkmcnt(0)
L_MM3_%=:
v_exp_f32 v20, v20
s_waitcnt vmcnt(24)
)A"
R"A(s_barrier
v_exp_f32 v21, v21
v_add_u32 v241, 0x8000, v241
v_and_b32 v241, 0x1ffff, v241
s_add_u32 s79, s72, 0x60000
buffer_store_dwordx4 a[192:195], v242, s[20:23], s79 offen offset:0 sc1
v_exp_f32 v22, v22
R16 a[128:131],v241,16384
s_and_b32 m0, s50, 0x1ffff
v_exp_f32 v23, v23
R16 a[132:135],v241,17408
s_xor_b32 m0, m0, 0x14000
MF v[48:51],a[96:99],a[32:35],v[200:203]
v_exp_f32 v28, v28
MF v[48:51],a[100:103],a[36:39],v[48:51]
R16 a[136:139],v241,18432
buffer_load_dwordx4 v242, s[12:15], s50 offen offset:0 sc1 lds
MF v[48:51],a[104:107],a[40:43],v[48:51]
v_exp_f32 v29, v29
MF v[48:51],a[108:111],a[44:47],v[48:51]
R16 a[140:143],v241,19456
PK v18,v20,v21
MF v[56:59],a[96:99],a[48:51],v[204:207]
v_exp_f32 v30, v30
MF v[56:59],a[100:103],a[52:55],v[56:59]
R16 a[144:147],v241,20480
PK v19,v22,v23
s_add_u32 s79, s80, 0x1
MF v[56:59],a[104:107],a[56:59],v[56:59]
v_exp_f32 v31, v31
s_and_b32 s79, s79, 3
MF v[56:59],a[108:111],a[60:63],v[56:59]
R16 a[148:151],v241,21504
buffer_load_dwordx4 v242, s[12:15], s50 offen offset:1024 sc1 lds
s_lshl_b32 s79, s79, 17
PK v26,v28,v29
PK v27,v30,v31
s_add_u32 s79, s79, s72
s_add_u32 s79, s79, 0x8000
R16 a[152:155],v241,22528
buffer_store_dwordx4 a[192:195], v242, s[20:23], s79 offen offset:0 sc1
R16 a[156:159],v241,23552
v_add_u32 v240, 0x8000, v240
v_and_b32 v240, 0x1ffff, v240
buffer_load_dwordx4 v242, s[12:15], s50 offen offset:2048 sc1 lds
R16 a[64:67],v240,0
s_add_u32 s79, s80, 0x2
MF v[52:55],a[112:115],a[32:35],v[200:203]
s_and_b32 s79, s79, 3
s_lshl_b32 s79, s79, 17
MF v[52:55],a[116:119],a[36:39],v[52:55]
R16 a[68:71],v240,1024
s_add_u32 s79, s79, s72
MF v[52:55],a[120:123],a[40:43],v[52:55]
s_add_u32 s79, s79, 0x8000
MF v[52:55],a[124:127],a[44:47],v[52:55]
R16 a[72:75],v240,2048
buffer_store_dwordx4 a[192:195], v242, s[20:23], s79 offen offset:0 sc1
MF v[60:63],a[112:115],a[48:51],v[204:207]
MF v[60:63],a[116:119],a[52:55],v[60:63]
R16 a[76:79],v240,3072
buffer_load_dwordx4 v242, s[12:15], s50 offen offset:3072 sc1 lds
MF v[60:63],a[120:123],a[56:59],v[60:63]
MF v[60:63],a[124:127],a[60:63],v[60:63]
R16 a[80:83],v240,4096
s_waitcnt lgkmcnt(5)
s_xor_b32 m0, m0, 0x14000
R16 a[84:87],v240,5120
buffer_store_dwordx4 a[192:195], v242, s[20:23], s79 offen offset:1024 sc1
MF v[128:131],a[128:131],v[16:19],v[128:131]
MF v[160:163],a[128:131],v[24:27],v[160:163]
R16 a[88:91],v240,6144
v_exp_f32 v48, v48
R16 a[92:95],v240,7168
buffer_load_dwordx4 v242, s[8:11], s50 offen offset:0 sc1 lds
MF v[132:135],a[132:135],v[16:19],v[132:135]
v_exp_f32 v49, v49
MF v[164:167],a[132:135],v[24:27],v[164:167]
R16 a[96:99],v240,8192
PK v48,v48,v49
v_exp_f32 v50, v50
R16 a[100:103],v240,9216
buffer_load_dwordx4 v242, s[8:11], s50 offen offset:1024 sc1 lds
MF v[136:139],a[136:139],v[16:19],v[136:139]
v_exp_f32 v51, v51
MF v[168:171],a[136:139],v[24:27],v[168:171]
R16 a[104:107],v240,10240
PK v49,v50,v51
v_exp_f32 v56, v56
R16 a[108:111],v240,11264
s_add_u32 s79, s80, 0x3
s_and_b32 s79, s79, 3
MF v[140:143],a[140:143],v[16:19],v[140:143]
v_exp_f32 v57, v57
s_lshl_b32 s79, s79, 17
s_add_u32 s79, s79, s72
s_add_u32 s79, s79, 0x8000
MF v[172:175],a[140:143],v[24:27],v[172:175]
R16 a[112:115],v240,12288
buffer_store_dwordx4 a[192:195], v242, s[20:23], s79 offen offset:0 sc1
PK v56,v56,v57
v_exp_f32 v58, v58
R16 a[116:119],v240,13312
buffer_load_dwordx4 v242, s[8:11], s50 offen offset:2048 sc1 lds
MF v[144:147],a[144:147],v[16:19],v[144:147]
v_exp_f32 v59, v59
MF v[176:179],a[144:147],v[24:27],v[176:179]
R16 a[120:123],v240,14336
PK v57,v58,v59
R16 a[124:127],v240,15360
buffer_load_dwordx4 v242, s[8:11], s50 offen offset:3072 sc1 lds
MF v[148:151],a[148:151],v[16:19],v[148:151]
buffer_store_dwordx4 a[192:195], v242, s[20:23], s79 offen offset:1024 sc1
MF v[180:183],a[148:151],v[24:27],v[180:183]
s_add_u32 s50, s50, 0x8000
MF v[152:155],a[152:155],v[16:19],v[152:155]
MF v[184:187],a[152:155],v[24:27],v[184:187]
MF v[156:159],a[156:159],v[16:19],v[156:159]
MF v[188:191],a[156:159],v[24:27],v[188:191]
MF v[216:219],a[192:195],v[16:19],v[216:219]
MF v[220:223],a[192:195],v[24:27],v[220:223]
s_waitcnt lgkmcnt(0)
s_sub_u32 s90, s90, 1
s_cbranch_scc0 L_SVF_%=
s_swappc_b64 s[18:19], s[16:17]
L_SVF_%=:
L_TM_%=:
s_cmp_eq_u32 s29, 7
s_cbranch_scc1 L_T7M_%=
MF v[0:3],a[64:67],a[32:35],v[200:203]
v_exp_f32 v52, v52
MF v[0:3],a[68:71],a[36:39],v[0:3]
R16 a[160:163],v241,24576
MF v[0:3],a[72:75],a[40:43],v[0:3]
v_exp_f32 v53, v53
MF v[0:3],a[76:79],a[44:47],v[0:3]
R16 a[164:167],v241,25600
PK v50,v52,v53
MF v[8:11],a[64:67],a[48:51],v[204:207]
v_exp_f32 v54, v54
MF v[8:11],a[68:71],a[52:55],v[8:11]
R16 a[168:171],v241,26624
MF v[8:11],a[72:75],a[56:59],v[8:11]
v_exp_f32 v55, v55
MF v[8:11],a[76:79],a[60:63],v[8:11]
R16 a[172:175],v241,27648
PK v51,v54,v55
MF v[4:7],a[80:83],a[32:35],v[200:203]
v_exp_f32 v60, v60
MF v[4:7],a[84:87],a[36:39],v[4:7]
R16 a[176:179],v241,28672
MF v[4:7],a[88:91],a[40:43],v[4:7]
v_exp_f32 v61, v61
MF v[4:7],a[92:95],a[44:47],v[4:7]
R16 a[180:183],v241,29696
PK v58,v60,v61
MF v[12:15],a[80:83],a[48:51],v[204:207]
v_exp_f32 v62, v62
MF v[12:15],a[84:87],a[52:55],v[12:15]
R16 a[184:187],v241,30720
MF v[12:15],a[88:91],a[56:59],v[12:15]
v_exp_f32 v63, v63
MF v[12:15],a[92:95],a[60:63],v[12:15]
R16 a[188:191],v241,31744
PK v59,v62,v63
MF v[16:19],a[96:99],a[32:35],v[200:203]
v_add_u32 v240, 0x8000, v240
v_and_b32 v240, 0x1ffff, v240
MF v[16:19],a[100:103],a[36:39],v[16:19]
s_and_b32 m0, s50, 0x1ffff
MF v[16:19],a[104:107],a[40:43],v[16:19]
s_xor_b32 m0, m0, 0x14000
MF v[16:19],a[108:111],a[44:47],v[16:19]
MF v[24:27],a[96:99],a[48:51],v[204:207]
v_exp_f32 v0, v0
MF v[24:27],a[100:103],a[52:55],v[24:27]
s_waitcnt vmcnt(27)
s_barrier
MF v[24:27],a[104:107],a[56:59],v[24:27]
v_exp_f32 v1, v1
MF v[24:27],a[108:111],a[60:63],v[24:27]
R16 a[64:67],v240,0
PK v0,v0,v1
MF v[20:23],a[112:115],a[32:35],v[200:203]
v_exp_f32 v2, v2
MF v[20:23],a[116:119],a[36:39],v[20:23]
R16 a[68:71],v240,1024
buffer_load_dwordx4 v242, s[12:15], s50 offen offset:0 sc1 lds
MF v[20:23],a[120:123],a[40:43],v[20:23]
v_exp_f32 v3, v3
MF v[20:23],a[124:127],a[44:47],v[20:23]
R16 a[72:75],v240,2048
PK v1,v2,v3
MF v[28:31],a[112:115],a[48:51],v[204:207]
v_exp_f32 v8, v8
MF v[28:31],a[116:119],a[52:55],v[28:31]
R16 a[76:79],v240,3072
buffer_load_dwordx4 v242, s[12:15], s50 offen offset:1024 sc1 lds
MF v[28:31],a[120:123],a[56:59],v[28:31]
v_exp_f32 v9, v9
MF v[28:31],a[124:127],a[60:63],v[28:31]
R16 a[80:83],v240,4096
s_waitcnt lgkmcnt(5)
v_exp_f32 v10, v10
R16 a[84:87],v240,5120
buffer_load_dwordx4 v242, s[12:15], s50 offen offset:2048 sc1 lds
MF v[128:131],a[160:163],v[48:51],v[128:131]
v_exp_f32 v11, v11
MF v[160:163],a[160:163],v[56:59],v[160:163]
R16 a[88:91],v240,6144
PK v8,v8,v9
v_exp_f32 v4, v4
R16 a[92:95],v240,7168
buffer_load_dwordx4 v242, s[12:15], s50 offen offset:3072 sc1 lds
MF v[132:135],a[164:167],v[48:51],v[132:135]
v_exp_f32 v5, v5
MF v[164:167],a[164:167],v[56:59],v[164:167]
R16 a[96:99],v240,8192
s_xor_b32 m0, m0, 0x14000
v_exp_f32 v6, v6
R16 a[100:103],v240,9216
PK v9,v10,v11
MF v[136:139],a[168:171],v[48:51],v[136:139]
v_exp_f32 v7, v7
MF v[168:171],a[168:171],v[56:59],v[168:171]
R16 a[104:107],v240,10240
buffer_load_dwordx4 v242, s[8:11], s50 offen offset:0 sc1 lds
v_exp_f32 v12, v12
R16 a[108:111],v240,11264
PK v2,v4,v5
MF v[140:143],a[172:175],v[48:51],v[140:143]
v_exp_f32 v13, v13
MF v[172:175],a[172:175],v[56:59],v[172:175]
R16 a[112:115],v240,12288
buffer_load_dwordx4 v242, s[8:11], s50 offen offset:1024 sc1 lds
v_exp_f32 v14, v14
R16 a[116:119],v240,13312
PK v3,v6,v7
MF v[144:147],a[176:179],v[48:51],v[144:147]
v_exp_f32 v15, v15
MF v[176:179],a[176:179],v[56:59],v[176:179]
R16 a[120:123],v240,14336
buffer_load_dwordx4 v242, s[8:11], s50 offen offset:2048 sc1 lds
v_exp_f32 v16, v16
R16 a[124:127],v240,15360
v_add_u32 v241, 0x8000, v241
MF v[148:151],a[180:183],v[48:51],v[148:151]
v_exp_f32 v17, v17
MF v[180:183],a[180:183],v[56:59],v[180:183]
buffer_load_dwordx4 v242, s[8:11], s50 offen offset:3072 sc1 lds
v_and_b32 v241, 0x1ffff, v241
v_exp_f32 v18, v18
R16 a[128:131],v241,16384
PK v10,v12,v13
MF v[152:155],a[184:187],v[48:51],v[152:155]
v_exp_f32 v19, v19
MF v[184:187],a[184:187],v[56:59],v[184:187]
R16 a[132:135],v241,17408
s_add_u32 s50, s50, 0x8000
v_exp_f32 v24, v24
R16 a[136:139],v241,18432
PK v11,v14,v15
MF v[156:159],a[188:191],v[48:51],v[156:159]
v_exp_f32 v25, v25
MF v[188:191],a[188:191],v[56:59],v[188:191]
R16 a[140:143],v241,19456
PK v16,v16,v17
v_exp_f32 v26, v26
R16 a[144:147],v241,20480
PK v17,v18,v19
MF v[216:219],a[192:195],v[48:51],v[216:219]
v_exp_f32 v27, v27
MF v[220:223],a[192:195],v[56:59],v[220:223]
PK v24,v24,v25
PK v25,v26,v27
s_waitcnt lgkmcnt(5)
s_branch L_AE_%=
L_D1_%=:
MF v[32:35],a[96:99],a[0:3],v[224:227]
v_exp_f32 v20, v20
MF v[32:35],a[100:103],a[4:7],v[32:35]
s_waitcnt vmcnt(12)
s_barrier
MF v[32:35],a[104:107],a[8:11],v[32:35]
v_exp_f32 v21, v21
MF v[32:35],a[108:111],a[12:15],v[32:35]
v_add_u32 v241, 0x8000, v241
v_and_b32 v241, 0x1ffff, v241
s_lshl_b32 s49, s30, 3
MF v[40:43],a[96:99],a[16:19],v[196:199]
v_exp_f32 v22, v22
s_add_u32 s49, s49, s29
MF v[40:43],a[100:103],a[20:23],v[40:43]
R16 a[128:131],v241,16384
s_and_b32 m0, s50, 0x1ffff
s_lshl_b32 s49, s49, 2
MF v[40:43],a[104:107],a[24:27],v[40:43]
v_exp_f32 v23, v23
s_add_u32 s49, s49, s28
s_lshl_b32 s49, s49, 2
s_add_u32 s49, s49, 0x4906000
MF v[40:43],a[108:111],a[28:31],v[40:43]
R16 a[132:135],v241,17408
s_xor_b32 m0, m0, 0x14000
MF v[48:51],a[96:99],a[32:35],v[200:203]
v_exp_f32 v28, v28
MF v[48:51],a[100:103],a[36:39],v[48:51]
R16 a[136:139],v241,18432
buffer_load_dwordx4 v242, s[12:15], s50 offen offset:0 sc1 lds
s_and_b32 s72, s76, 0xf9
MF v[48:51],a[104:107],a[40:43],v[48:51]
v_exp_f32 v29, v29
s_add_u32 s72, s72, 0x0
MF v[48:51],a[108:111],a[44:47],v[48:51]
R16 a[140:143],v241,19456
PK v18,v20,v21
s_lshl_b32 s72, s72, 3
MF v[56:59],a[96:99],a[48:51],v[204:207]
v_exp_f32 v30, v30
s_add_u32 s72, s72, s80
s_lshl_b32 s72, s72, 13
s_lshl_b32 s79, s28, 11
s_add_u32 s72, s72, s79
s_add_u32 s72, s72, 0x3800000
MF v[56:59],a[100:103],a[52:55],v[56:59]
R16 a[144:147],v241,20480
buffer_store_dwordx4 a[192:195], v242, s[20:23], s36 offen offset:0 sc1
PK v19,v22,v23
MF v[56:59],a[104:107],a[56:59],v[56:59]
v_exp_f32 v31, v31
MF v[56:59],a[108:111],a[60:63],v[56:59]
R16 a[148:151],v241,21504
buffer_load_dwordx4 v242, s[12:15], s50 offen offset:1024 sc1 lds
PK v26,v28,v29
PK v27,v30,v31
R16 a[152:155],v241,22528
buffer_store_dwordx4 a[192:195], v242, s[20:23], s36 offen offset:1024 sc1
R16 a[156:159],v241,23552
v_add_u32 v240, 0x8000, v240
MF v[44:47],a[112:115],a[16:19],v[228:231]
v_exp_f32 v32, v32
MF v[44:47],a[116:119],a[20:23],v[44:47]
buffer_load_dwordx4 v242, s[12:15], s50 offen offset:2048 sc1 lds
v_and_b32 v240, 0x1ffff, v240
MF v[44:47],a[120:123],a[24:27],v[44:47]
v_exp_f32 v33, v33
MF v[44:47],a[124:127],a[28:31],v[44:47]
PK v32,v32,v33
MF v[52:55],a[112:115],a[32:35],v[200:203]
v_exp_f32 v34, v34
MF v[52:55],a[116:119],a[36:39],v[52:55]
MF v[52:55],a[120:123],a[40:43],v[52:55]
v_exp_f32 v35, v35
MF v[52:55],a[124:127],a[44:47],v[52:55]
R16 a[64:67],v240,0
PK v33,v34,v35
MF v[60:63],a[112:115],a[48:51],v[204:207]
v_exp_f32 v40, v40
MF v[60:63],a[116:119],a[52:55],v[60:63]
buffer_load_dwordx4 v242, s[12:15], s50 offen offset:3072 sc1 lds
buffer_store_dwordx4 a[192:195], v242, s[20:23], s36 offen offset:2048 sc1
MF v[60:63],a[120:123],a[56:59],v[60:63]
v_exp_f32 v41, v41
MF v[60:63],a[124:127],a[60:63],v[60:63]
R16 a[68:71],v240,1024
s_waitcnt lgkmcnt(2)
MF v[64:67],a[128:131],v[0:3],v[64:67]
v_exp_f32 v42, v42
MF v[96:99],a[128:131],v[8:11],v[96:99]
s_xor_b32 m0, m0, 0x14000
PK v40,v40,v41
MF v[128:131],a[128:131],v[16:19],v[128:131]
v_exp_f32 v43, v43
MF v[160:163],a[128:131],v[24:27],v[160:163]
R16 a[72:75],v240,2048
PK v41,v42,v43
MF v[68:71],a[132:135],v[0:3],v[68:71]
v_exp_f32 v48, v48
MF v[100:103],a[132:135],v[8:11],v[100:103]
buffer_load_dwordx4 v242, s[8:11], s50 offen offset:0 sc1 lds
buffer_store_dwordx4 a[192:195], v242, s[20:23], s36 offen offset:3072 sc1
MF v[132:135],a[132:135],v[16:19],v[132:135]
v_exp_f32 v49, v49
MF v[164:167],a[132:135],v[24:27],v[164:167]
R16 a[76:79],v240,3072
PK v48,v48,v49
MF v[72:75],a[136:139],v[0:3],v[72:75]
v_exp_f32 v50, v50
MF v[104:107],a[136:139],v[8:11],v[104:107]
buffer_load_dwordx4 v242, s[8:11], s50 offen offset:1024 sc1 lds
MF v[136:139],a[136:139],v[16:19],v[136:139]
v_exp_f32 v51, v51
MF v[168:171],a[136:139],v[24:27],v[168:171]
R16 a[80:83],v240,4096
PK v49,v50,v51
MF v[76:79],a[140:143],v[0:3],v[76:79]
v_exp_f32 v56, v56
MF v[108:111],a[140:143],v[8:11],v[108:111]
v_mov_b32 v254, s37
v_mov_b32 v255, 0
)A"
R"A(s_waitcnt vmcnt(14)
MF v[140:143],a[140:143],v[16:19],v[140:143]
buffer_store_dword v254, v255, s[20:23], s49 offen offset:0 sc1
v_exp_f32 v57, v57
MF v[172:175],a[140:143],v[24:27],v[172:175]
R16 a[84:87],v240,5120
PK v56,v56,v57
s_add_u32 s79, s36, 0x20000
MF v[80:83],a[144:147],v[0:3],v[80:83]
buffer_store_dwordx4 a[192:195], v242, s[20:23], s79 offen offset:0 sc1
v_exp_f32 v58, v58
MF v[112:115],a[144:147],v[8:11],v[112:115]
buffer_load_dwordx4 v242, s[8:11], s50 offen offset:2048 sc1 lds
MF v[144:147],a[144:147],v[16:19],v[144:147]
v_exp_f32 v59, v59
MF v[176:179],a[144:147],v[24:27],v[176:179]
R16 a[88:91],v240,6144
PK v57,v58,v59
MF v[84:87],a[148:151],v[0:3],v[84:87]
MF v[116:119],a[148:151],v[8:11],v[116:119]
buffer_load_dwordx4 v242, s[8:11], s50 offen offset:3072 sc1 lds
MF v[148:151],a[148:151],v[16:19],v[148:151]
MF v[180:183],a[148:151],v[24:27],v[180:183]
R16 a[92:95],v240,7168
s_add_u32 s50, s50, 0x8000
MF v[88:91],a[152:155],v[0:3],v[88:91]
v_mov_b32 v34, 0
MF v[120:123],a[152:155],v[8:11],v[120:123]
MF v[152:155],a[152:155],v[16:19],v[152:155]
MF v[184:187],a[152:155],v[24:27],v[184:187]
v_mov_b32 v35, 0
MF v[92:95],a[156:159],v[0:3],v[92:95]
v_exp_f32 v44, v44
MF v[124:127],a[156:159],v[8:11],v[124:127]
MF v[156:159],a[156:159],v[16:19],v[156:159]
v_exp_f32 v45, v45
MF v[188:191],a[156:159],v[24:27],v[188:191]
PK v42,v44,v45
MF v[208:211],a[192:195],v[0:3],v[208:211]
v_exp_f32 v46, v46
MF v[212:215],a[192:195],v[8:11],v[212:215]
MF v[216:219],a[192:195],v[16:19],v[216:219]
v_exp_f32 v47, v47
MF v[220:223],a[192:195],v[24:27],v[220:223]
PK v43,v46,v47
s_waitcnt lgkmcnt(0)
s_sub_u32 s90, s90, 1
s_cbranch_scc0 L_SVG_%=
s_swappc_b64 s[18:19], s[16:17]
L_SVG_%=:
v_exp_f32 v52, v52
v_exp_f32 v53, v53
s_nop 0
PK v50,v52,v53
s_add_u32 s79, s36, 0x20000
v_exp_f32 v54, v54
R16 a[160:163],v241,24576
buffer_store_dwordx4 a[192:195], v242, s[20:23], s79 offen offset:1024 sc1
v_exp_f32 v55, v55
R16 a[164:167],v241,25600
PK v51,v54,v55
MF v[16:19],a[64:67],a[32:35],v[200:203]
v_exp_f32 v60, v60
MF v[16:19],a[68:71],a[36:39],v[16:19]
R16 a[168:171],v241,26624
MF v[16:19],a[72:75],a[40:43],v[16:19]
v_exp_f32 v61, v61
MF v[16:19],a[76:79],a[44:47],v[16:19]
R16 a[172:175],v241,27648
PK v58,v60,v61
MF v[24:27],a[64:67],a[48:51],v[204:207]
v_exp_f32 v62, v62
MF v[24:27],a[68:71],a[52:55],v[24:27]
R16 a[176:179],v241,28672
buffer_store_dwordx4 a[192:195], v242, s[20:23], s79 offen offset:2048 sc1
MF v[24:27],a[72:75],a[56:59],v[24:27]
v_exp_f32 v63, v63
MF v[24:27],a[76:79],a[60:63],v[24:27]
R16 a[180:183],v241,29696
PK v59,v62,v63
R16 a[184:187],v241,30720
R16 a[188:191],v241,31744
buffer_store_dwordx4 a[192:195], v242, s[20:23], s79 offen offset:3072 sc1
MF v[20:23],a[80:83],a[32:35],v[200:203]
MF v[20:23],a[84:87],a[36:39],v[20:23]
MF v[20:23],a[88:91],a[40:43],v[20:23]
MF v[20:23],a[92:95],a[44:47],v[20:23]
R16 a[96:99],v240,8192
MF v[28:31],a[80:83],a[48:51],v[204:207]
MF v[28:31],a[84:87],a[52:55],v[28:31]
MF v[28:31],a[88:91],a[56:59],v[28:31]
MF v[28:31],a[92:95],a[60:63],v[28:31]
R16 a[100:103],v240,9216
s_waitcnt lgkmcnt(2)
MF v[64:67],a[160:163],v[32:35],v[64:67]
MF v[96:99],a[160:163],v[40:43],v[96:99]
s_add_u32 s79, s72, 0x0
MF v[128:131],a[160:163],v[48:51],v[128:131]
buffer_store_dwordx4 a[192:195], v242, s[20:23], s79 offen offset:0 sc1
MF v[160:163],a[160:163],v[56:59],v[160:163]
R16 a[104:107],v240,10240
MF v[68:71],a[164:167],v[32:35],v[68:71]
v_exp_f32 v16, v16
MF v[100:103],a[164:167],v[40:43],v[100:103]
MF v[132:135],a[164:167],v[48:51],v[132:135]
v_exp_f32 v17, v17
MF v[164:167],a[164:167],v[56:59],v[164:167]
R16 a[108:111],v240,11264
PK v16,v16,v17
MF v[72:75],a[168:171],v[32:35],v[72:75]
v_exp_f32 v18, v18
MF v[104:107],a[168:171],v[40:43],v[104:107]
MF v[136:139],a[168:171],v[48:51],v[136:139]
v_exp_f32 v19, v19
MF v[168:171],a[168:171],v[56:59],v[168:171]
R16 a[112:115],v240,12288
PK v17,v18,v19
MF v[76:79],a[172:175],v[32:35],v[76:79]
v_exp_f32 v24, v24
MF v[108:111],a[172:175],v[40:43],v[108:111]
s_add_u32 s79, s72, 0x20000
MF v[140:143],a[172:175],v[48:51],v[140:143]
buffer_store_dwordx4 a[192:195], v242, s[20:23], s79 offen offset:0 sc1
v_exp_f32 v25, v25
MF v[172:175],a[172:175],v[56:59],v[172:175]
R16 a[116:119],v240,13312
PK v24,v24,v25
MF v[80:83],a[176:179],v[32:35],v[80:83]
v_exp_f32 v26, v26
MF v[112:115],a[176:179],v[40:43],v[112:115]
MF v[144:147],a[176:179],v[48:51],v[144:147]
v_exp_f32 v27, v27
MF v[176:179],a[176:179],v[56:59],v[176:179]
R16 a[120:123],v240,14336
PK v25,v26,v27
MF v[84:87],a[180:183],v[32:35],v[84:87]
MF v[116:119],a[180:183],v[40:43],v[116:119]
s_add_u32 s79, s72, 0x40000
MF v[148:151],a[180:183],v[48:51],v[148:151]
buffer_store_dwordx4 a[192:195], v242, s[20:23], s79 offen offset:0 sc1
MF v[180:183],a[180:183],v[56:59],v[180:183]
R16 a[124:127],v240,15360
MF v[88:91],a[184:187],v[32:35],v[88:91]
MF v[120:123],a[184:187],v[40:43],v[120:123]
MF v[152:155],a[184:187],v[48:51],v[152:155]
MF v[184:187],a[184:187],v[56:59],v[184:187]
MF v[92:95],a[188:191],v[32:35],v[92:95]
MF v[124:127],a[188:191],v[40:43],v[124:127]
MF v[156:159],a[188:191],v[48:51],v[156:159]
MF v[188:191],a[188:191],v[56:59],v[188:191]
MF v[208:211],a[192:195],v[32:35],v[208:211]
MF v[212:215],a[192:195],v[40:43],v[212:215]
MF v[216:219],a[192:195],v[48:51],v[216:219]
MF v[220:223],a[192:195],v[56:59],v[220:223]
s_waitcnt lgkmcnt(0)
s_branch L_MM3_%=
L_D2_%=:
MF v[0:3],a[64:67],a[0:3],v[224:227]
v_exp_f32 v52, v52
MF v[0:3],a[68:71],a[4:7],v[0:3]
MF v[0:3],a[72:75],a[8:11],v[0:3]
v_exp_f32 v53, v53
MF v[0:3],a[76:79],a[12:15],v[0:3]
PK v50,v52,v53
s_add_u32 s79, s36, 0x20000
MF v[8:11],a[64:67],a[16:19],v[196:199]
v_exp_f32 v54, v54
MF v[8:11],a[68:71],a[20:23],v[8:11]
R16 a[160:163],v241,24576
buffer_store_dwordx4 a[192:195], v242, s[20:23], s79 offen offset:1024 sc1
MF v[8:11],a[72:75],a[24:27],v[8:11]
v_exp_f32 v55, v55
MF v[8:11],a[76:79],a[28:31],v[8:11]
R16 a[164:167],v241,25600
PK v51,v54,v55
MF v[16:19],a[64:67],a[32:35],v[200:203]
v_exp_f32 v60, v60
MF v[16:19],a[68:71],a[36:39],v[16:19]
R16 a[168:171],v241,26624
MF v[16:19],a[72:75],a[40:43],v[16:19]
v_exp_f32 v61, v61
MF v[16:19],a[76:79],a[44:47],v[16:19]
R16 a[172:175],v241,27648
PK v58,v60,v61
MF v[24:27],a[64:67],a[48:51],v[204:207]
v_exp_f32 v62, v62
MF v[24:27],a[68:71],a[52:55],v[24:27]
R16 a[176:179],v241,28672
buffer_store_dwordx4 a[192:195], v242, s[20:23], s79 offen offset:2048 sc1
MF v[24:27],a[72:75],a[56:59],v[24:27]
v_exp_f32 v63, v63
MF v[24:27],a[76:79],a[60:63],v[24:27]
R16 a[180:183],v241,29696
PK v59,v62,v63
R16 a[184:187],v241,30720
R16 a[188:191],v241,31744
MF v[12:15],a[80:83],a[16:19],v[228:231]
v_exp_f32 v0, v0
MF v[12:15],a[84:87],a[20:23],v[12:15]
MF v[12:15],a[88:91],a[24:27],v[12:15]
v_exp_f32 v1, v1
MF v[12:15],a[92:95],a[28:31],v[12:15]
buffer_store_dwordx4 a[192:195], v242, s[20:23], s79 offen offset:3072 sc1
PK v0,v0,v1
MF v[20:23],a[80:83],a[32:35],v[200:203]
v_exp_f32 v2, v2
MF v[20:23],a[84:87],a[36:39],v[20:23]
MF v[20:23],a[88:91],a[40:43],v[20:23]
v_exp_f32 v3, v3
MF v[20:23],a[92:95],a[44:47],v[20:23]
R16 a[96:99],v240,8192
PK v1,v2,v3
MF v[28:31],a[80:83],a[48:51],v[204:207]
v_exp_f32 v8, v8
MF v[28:31],a[84:87],a[52:55],v[28:31]
MF v[28:31],a[88:91],a[56:59],v[28:31]
v_exp_f32 v9, v9
MF v[28:31],a[92:95],a[60:63],v[28:31]
R16 a[100:103],v240,9216
s_waitcnt lgkmcnt(2)
MF v[64:67],a[160:163],v[32:35],v[64:67]
v_exp_f32 v10, v10
MF v[96:99],a[160:163],v[40:43],v[96:99]
PK v8,v8,v9
s_add_u32 s79, s72, 0x0
MF v[128:131],a[160:163],v[48:51],v[128:131]
buffer_store_dwordx4 a[192:195], v242, s[20:23], s79 offen offset:0 sc1
v_exp_f32 v11, v11
MF v[160:163],a[160:163],v[56:59],v[160:163]
R16 a[104:107],v240,10240
PK v9,v10,v11
MF v[68:71],a[164:167],v[32:35],v[68:71]
v_exp_f32 v16, v16
MF v[100:103],a[164:167],v[40:43],v[100:103]
MF v[132:135],a[164:167],v[48:51],v[132:135]
v_exp_f32 v17, v17
MF v[164:167],a[164:167],v[56:59],v[164:167]
R16 a[108:111],v240,11264
PK v16,v16,v17
MF v[72:75],a[168:171],v[32:35],v[72:75]
v_exp_f32 v18, v18
MF v[104:107],a[168:171],v[40:43],v[104:107]
MF v[136:139],a[168:171],v[48:51],v[136:139]
v_exp_f32 v19, v19
MF v[168:171],a[168:171],v[56:59],v[168:171]
R16 a[112:115],v240,12288
PK v17,v18,v19
MF v[76:79],a[172:175],v[32:35],v[76:79]
v_exp_f32 v24, v24
MF v[108:111],a[172:175],v[40:43],v[108:111]
s_add_u32 s79, s72, 0x20000
MF v[140:143],a[172:175],v[48:51],v[140:143]
buffer_store_dwordx4 a[192:195], v242, s[20:23], s79 offen offset:0 sc1
v_exp_f32 v25, v25
MF v[172:175],a[172:175],v[56:59],v[172:175]
R16 a[116:119],v240,13312
PK v24,v24,v25
MF v[80:83],a[176:179],v[32:35],v[80:83]
v_exp_f32 v26, v26
MF v[112:115],a[176:179],v[40:43],v[112:115]
MF v[144:147],a[176:179],v[48:51],v[144:147]
v_exp_f32 v27, v27
MF v[176:179],a[176:179],v[56:59],v[176:179]
R16 a[120:123],v240,14336
PK v25,v26,v27
MF v[84:87],a[180:183],v[32:35],v[84:87]
MF v[116:119],a[180:183],v[40:43],v[116:119]
s_add_u32 s79, s72, 0x40000
MF v[148:151],a[180:183],v[48:51],v[148:151]
buffer_store_dwordx4 a[192:195], v242, s[20:23], s79 offen offset:0 sc1
MF v[180:183],a[180:183],v[56:59],v[180:183]
R16 a[124:127],v240,15360
v_mov_b32 v2, 0
MF v[88:91],a[184:187],v[32:35],v[88:91]
MF v[120:123],a[184:187],v[40:43],v[120:123]
MF v[152:155],a[184:187],v[48:51],v[152:155]
MF v[184:187],a[184:187],v[56:59],v[184:187]
v_mov_b32 v3, 0
MF v[92:95],a[188:191],v[32:35],v[92:95]
v_exp_f32 v12, v12
MF v[124:127],a[188:191],v[40:43],v[124:127]
MF v[156:159],a[188:191],v[48:51],v[156:159]
v_exp_f32 v13, v13
MF v[188:191],a[188:191],v[56:59],v[188:191]
PK v10,v12,v13
MF v[208:211],a[192:195],v[32:35],v[208:211]
v_exp_f32 v14, v14
MF v[212:215],a[192:195],v[40:43],v[212:215]
MF v[216:219],a[192:195],v[48:51],v[216:219]
v_exp_f32 v15, v15
MF v[220:223],a[192:195],v[56:59],v[220:223]
PK v11,v14,v15
s_waitcnt lgkmcnt(0)
v_exp_f32 v20, v20
s_waitcnt vmcnt(24)
s_barrier
v_exp_f32 v21, v21
v_add_u32 v241, 0x8000, v241
v_and_b32 v241, 0x1ffff, v241
s_add_u32 s79, s72, 0x60000
buffer_store_dwordx4 a[192:195], v242, s[20:23], s79 offen offset:0 sc1
v_exp_f32 v22, v22
R16 a[128:131],v241,16384
s_and_b32 m0, s50, 0x1ffff
v_exp_f32 v23, v23
R16 a[132:135],v241,17408
s_xor_b32 m0, m0, 0x14000
MF v[48:51],a[96:99],a[32:35],v[200:203]
v_exp_f32 v28, v28
MF v[48:51],a[100:103],a[36:39],v[48:51]
R16 a[136:139],v241,18432
buffer_load_dwordx4 v242, s[12:15], s50 offen offset:0 sc1 lds
MF v[48:51],a[104:107],a[40:43],v[48:51]
v_exp_f32 v29, v29
MF v[48:51],a[108:111],a[44:47],v[48:51]
R16 a[140:143],v241,19456
PK v18,v20,v21
MF v[56:59],a[96:99],a[48:51],v[204:207]
v_exp_f32 v30, v30
MF v[56:59],a[100:103],a[52:55],v[56:59]
R16 a[144:147],v241,20480
PK v19,v22,v23
s_add_u32 s79, s80, 0x1
MF v[56:59],a[104:107],a[56:59],v[56:59]
v_exp_f32 v31, v31
s_and_b32 s79, s79, 3
MF v[56:59],a[108:111],a[60:63],v[56:59]
R16 a[148:151],v241,21504
buffer_load_dwordx4 v242, s[12:15], s50 offen offset:1024 sc1 lds
s_lshl_b32 s79, s79, 17
PK v26,v28,v29
PK v27,v30,v31
s_add_u32 s79, s79, s72
s_add_u32 s79, s79, 0x8000
R16 a[152:155],v241,22528
buffer_store_dwordx4 a[192:195], v242, s[20:23], s79 offen offset:0 sc1
R16 a[156:159],v241,23552
v_add_u32 v240, 0x8000, v240
v_and_b32 v240, 0x1ffff, v240
buffer_load_dwordx4 v242, s[12:15], s50 offen offset:2048 sc1 lds
R16 a[64:67],v240,0
s_add_u32 s79, s80, 0x2
MF v[52:55],a[112:115],a[32:35],v[200:203]
s_and_b32 s79, s79, 3
s_lshl_b32 s79, s79, 17
MF v[52:55],a[116:119],a[36:39],v[52:55]
R16 a[68:71],v240,1024
s_add_u32 s79, s79, s72
MF v[52:55],a[120:123],a[40:43],v[52:55]
s_add_u32 s79, s79, 0x8000
MF v[52:55],a[124:127],a[44:47],v[52:55]
R16 a[72:75],v240,2048
buffer_store_dwordx4 a[192:195], v242, s[20:23], s79 offen offset:0 sc1
MF v[60:63],a[112:115],a[48:51],v[204:207]
MF v[60:63],a[116:119],a[52:55],v[60:63]
R16 a[76:79],v240,3072
buffer_load_dwordx4 v242, s[12:15], s50 offen offset:3072 sc1 lds
MF v[60:63],a[120:123],a[56:59],v[60:63]
MF v[60:63],a[124:127],a[60:63],v[60:63]
R16 a[80:83],v240,4096
s_waitcnt lgkmcnt(5)
MF v[64:67],a[128:131],v[0:3],v[64:67]
s_xor_b32 m0, m0, 0x14000
MF v[96:99],a[128:131],v[8:11],v[96:99]
R16 a[84:87],v240,5120
buffer_store_dwordx4 a[192:195], v242, s[20:23], s79 offen offset:1024 sc1
MF v[128:131],a[128:131],v[16:19],v[128:131]
MF v[160:163],a[128:131],v[24:27],v[160:163]
R16 a[88:91],v240,6144
MF v[68:71],a[132:135],v[0:3],v[68:71]
v_exp_f32 v48, v48
MF v[100:103],a[132:135],v[8:11],v[100:103]
R16 a[92:95],v240,7168
buffer_load_dwordx4 v242, s[8:11], s50 offen offset:0 sc1 lds
MF v[132:135],a[132:135],v[16:19],v[132:135]
v_exp_f32 v49, v49
MF v[164:167],a[132:135],v[24:27],v[164:167]
R16 a[96:99],v240,8192
PK v48,v48,v49
MF v[72:75],a[136:139],v[0:3],v[72:75]
v_exp_f32 v50, v50
MF v[104:107],a[136:139],v[8:11],v[104:107]
R16 a[100:103],v240,9216
buffer_load_dwordx4 v242, s[8:11], s50 offen offset:1024 sc1 lds
MF v[136:139],a[136:139],v[16:19],v[136:139]
v_exp_f32 v51, v51
MF v[168:171],a[136:139],v[24:27],v[168:171]
R16 a[104:107],v240,10240
PK v49,v50,v51
MF v[76:79],a[140:143],v[0:3],v[76:79]
v_exp_f32 v56, v56
MF v[108:111],a[140:143],v[8:11],v[108:111]
R16 a[108:111],v240,11264
s_add_u32 s79, s80, 0x3
s_and_b32 s79, s79, 3
MF v[140:143],a[140:143],v[16:19],v[140:143]
v_exp_f32 v57, v57
s_lshl_b32 s79, s79, 17
s_add_u32 s79, s79, s72
s_add_u32 s79, s79, 0x8000
)A"
R"A(MF v[172:175],a[140:143],v[24:27],v[172:175]
R16 a[112:115],v240,12288
buffer_store_dwordx4 a[192:195], v242, s[20:23], s79 offen offset:0 sc1
PK v56,v56,v57
MF v[80:83],a[144:147],v[0:3],v[80:83]
v_exp_f32 v58, v58
MF v[112:115],a[144:147],v[8:11],v[112:115]
R16 a[116:119],v240,13312
buffer_load_dwordx4 v242, s[8:11], s50 offen offset:2048 sc1 lds
MF v[144:147],a[144:147],v[16:19],v[144:147]
v_exp_f32 v59, v59
MF v[176:179],a[144:147],v[24:27],v[176:179]
R16 a[120:123],v240,14336
PK v57,v58,v59
MF v[84:87],a[148:151],v[0:3],v[84:87]
MF v[116:119],a[148:151],v[8:11],v[116:119]
R16 a[124:127],v240,15360
buffer_load_dwordx4 v242, s[8:11], s50 offen offset:3072 sc1 lds
MF v[148:151],a[148:151],v[16:19],v[148:151]
buffer_store_dwordx4 a[192:195], v242, s[20:23], s79 offen offset:1024 sc1
MF v[180:183],a[148:151],v[24:27],v[180:183]
s_add_u32 s50, s50, 0x8000
MF v[88:91],a[152:155],v[0:3],v[88:91]
MF v[120:123],a[152:155],v[8:11],v[120:123]
MF v[152:155],a[152:155],v[16:19],v[152:155]
MF v[184:187],a[152:155],v[24:27],v[184:187]
MF v[92:95],a[156:159],v[0:3],v[92:95]
MF v[124:127],a[156:159],v[8:11],v[124:127]
MF v[156:159],a[156:159],v[16:19],v[156:159]
MF v[188:191],a[156:159],v[24:27],v[188:191]
MF v[208:211],a[192:195],v[0:3],v[208:211]
MF v[212:215],a[192:195],v[8:11],v[212:215]
MF v[216:219],a[192:195],v[16:19],v[216:219]
MF v[220:223],a[192:195],v[24:27],v[220:223]
s_waitcnt lgkmcnt(0)
s_sub_u32 s90, s90, 1
s_cbranch_scc0 L_SVH_%=
s_swappc_b64 s[18:19], s[16:17]
L_SVH_%=:
s_branch L_TM_%=
L_T7M_%=:
s_cmp_eq_u32 s28, 1
s_cbranch_scc1 L_T71_%=
s_cmp_eq_u32 s28, 2
s_cbranch_scc1 L_T72_%=
v_lshrrev_b32 v251, 7, v242
v_min_u32 v251, 2, v251
s_mul_i32 s52, s81, 3
v_add_u32 v251, s52, v251
v_lshrrev_b32 v251, 1, v251
v_lshlrev_b32 v251, 7, v251
v_bfe_u32 v250, v242, 4, 1
v_lshl_add_u32 v250, v250, 13, v251
s_lshl_b32 s52, s28, 2
s_add_u32 s52, s52, s80
s_lshl_b32 s52, s52, 14
s_add_u32 s52, s52, 0x4990000
buffer_load_dword v251, v250, s[20:23], s52 offen offset:0 sc1
MF v[0:3],a[64:67],a[32:35],v[232:235]
v_exp_f32 v52, v52
MF v[0:3],a[68:71],a[36:39],v[0:3]
R16 a[160:163],v241,24576
s_add_u32 s79, s53, 0x3000
MF v[0:3],a[72:75],a[40:43],v[0:3]
buffer_load_dwordx4 a[0:3], v242, s[20:23], s79 offen offset:3072 sc1
v_exp_f32 v53, v53
MF v[0:3],a[76:79],a[44:47],v[0:3]
R16 a[164:167],v241,25600
PK v50,v52,v53
MF v[8:11],a[64:67],a[48:51],v[204:207]
v_exp_f32 v54, v54
MF v[8:11],a[68:71],a[52:55],v[8:11]
R16 a[168:171],v241,26624
s_add_u32 s79, s53, 0x4000
MF v[8:11],a[72:75],a[56:59],v[8:11]
buffer_load_dwordx4 a[4:7], v242, s[20:23], s79 offen offset:0 sc1
v_exp_f32 v55, v55
MF v[8:11],a[76:79],a[60:63],v[8:11]
R16 a[172:175],v241,27648
buffer_load_dwordx4 a[8:11], v242, s[20:23], s79 offen offset:1024 sc1
PK v51,v54,v55
v_exp_f32 v60, v60
R16 a[176:179],v241,28672
buffer_load_dwordx4 a[12:15], v242, s[20:23], s79 offen offset:2048 sc1
v_exp_f32 v61, v61
R16 a[180:183],v241,29696
PK v58,v60,v61
MF v[12:15],a[80:83],a[48:51],v[236:239]
v_exp_f32 v62, v62
MF v[12:15],a[84:87],a[52:55],v[12:15]
R16 a[184:187],v241,30720
buffer_load_dwordx4 a[16:19], v242, s[20:23], s79 offen offset:3072 sc1
s_add_u32 s79, s53, 0x5000
MF v[12:15],a[88:91],a[56:59],v[12:15]
buffer_load_dwordx4 a[20:23], v242, s[20:23], s79 offen offset:0 sc1
v_exp_f32 v63, v63
MF v[12:15],a[92:95],a[60:63],v[12:15]
R16 a[188:191],v241,31744
PK v59,v62,v63
v_add_u32 v240, 0x8000, v240
v_and_b32 v240, 0x1ffff, v240
buffer_load_dwordx4 a[24:27], v242, s[20:23], s79 offen offset:1024 sc1
buffer_load_dwordx4 a[28:31], v242, s[20:23], s79 offen offset:2048 sc1
v_exp_f32 v0, v0
s_waitcnt vmcnt(10)
s_barrier
v_exp_f32 v1, v1
R16 a[64:67],v240,0
PK v0,v0,v1
v_exp_f32 v2, v2
R16 a[68:71],v240,1024
s_waitcnt vmcnt(8)
v_cmp_ne_u32 vcc, s37, v251
s_or_b64 s[46:47], s[46:47], vcc
v_exp_f32 v3, v3
s_lshl_b32 s48, s28, 13
R16 a[72:75],v240,2048
PK v1,v2,v3
s_add_u32 m0, s48, 0x20000
v_exp_f32 v8, v8
R16 a[76:79],v240,3072
buffer_load_dwordx4 v242, s[20:23], s60 offen offset:0 sc1 lds
buffer_load_dwordx4 v242, s[20:23], s60 offen offset:1024 sc1 lds
buffer_load_dwordx4 v242, s[20:23], s60 offen offset:2048 sc1 lds
v_exp_f32 v9, v9
R16 a[80:83],v240,4096
buffer_load_dwordx4 v242, s[20:23], s60 offen offset:3072 sc1 lds
s_waitcnt lgkmcnt(5)
v_exp_f32 v10, v10
R16 a[84:87],v240,5120
PK v8,v8,v9
MF v[128:131],a[160:163],v[48:51],v[128:131]
v_exp_f32 v11, v11
MF v[160:163],a[160:163],v[56:59],v[160:163]
R16 a[88:91],v240,6144
PK v9,v10,v11
s_add_u32 m0, m0, 0x1000
s_add_u32 s48, s60, 0x1000
R16 a[92:95],v240,7168
buffer_load_dwordx4 v242, s[20:23], s48 offen offset:0 sc1 lds
MF v[132:135],a[164:167],v[48:51],v[132:135]
buffer_load_dwordx4 v242, s[20:23], s48 offen offset:1024 sc1 lds
MF v[164:167],a[164:167],v[56:59],v[164:167]
R16 a[96:99],v240,8192
v_mov_b32 v2, 0
buffer_load_dwordx4 v242, s[20:23], s48 offen offset:2048 sc1 lds
R16 a[100:103],v240,9216
buffer_load_dwordx4 v242, s[20:23], s48 offen offset:3072 sc1 lds
MF v[136:139],a[168:171],v[48:51],v[136:139]
MF v[168:171],a[168:171],v[56:59],v[168:171]
R16 a[104:107],v240,10240
v_mov_b32 v3, 0
v_exp_f32 v12, v12
R16 a[108:111],v240,11264
s_cmp_lt_u32 s28, 2
s_cselect_b32 s48, 0x8000, 0
s_xor_b32 s48, s48, s54
MF v[140:143],a[172:175],v[48:51],v[140:143]
v_exp_f32 v13, v13
s_xor_b32 m0, s48, 0x8000
MF v[172:175],a[172:175],v[56:59],v[172:175]
R16 a[112:115],v240,12288
PK v10,v12,v13
v_exp_f32 v14, v14
R16 a[116:119],v240,13312
buffer_load_dwordx4 v242, s[20:23], s61 offen offset:0 sc1 lds
buffer_load_dwordx4 v242, s[20:23], s61 offen offset:1024 sc1 lds
MF v[144:147],a[176:179],v[48:51],v[144:147]
v_exp_f32 v15, v15
MF v[176:179],a[176:179],v[56:59],v[176:179]
R16 a[120:123],v240,14336
PK v11,v14,v15
buffer_load_dwordx4 v242, s[20:23], s61 offen offset:2048 sc1 lds
buffer_load_dwordx4 v242, s[20:23], s61 offen offset:3072 sc1 lds
R16 a[124:127],v240,15360
v_add_u32 v241, 0x8000, v241
MF v[148:151],a[180:183],v[48:51],v[148:151]
v_and_b32 v241, 0x1ffff, v241
s_add_u32 m0, m0, 0x1000
MF v[180:183],a[180:183],v[56:59],v[180:183]
s_add_u32 s48, s61, 0x1000
buffer_load_dwordx4 v242, s[20:23], s48 offen offset:0 sc1 lds
buffer_load_dwordx4 v242, s[20:23], s48 offen offset:1024 sc1 lds
R16 a[128:131],v241,16384
MF v[152:155],a[184:187],v[48:51],v[152:155]
MF v[184:187],a[184:187],v[56:59],v[184:187]
R16 a[132:135],v241,17408
buffer_load_dwordx4 v242, s[20:23], s48 offen offset:2048 sc1 lds
R16 a[136:139],v241,18432
buffer_load_dwordx4 v242, s[20:23], s48 offen offset:3072 sc1 lds
MF v[156:159],a[188:191],v[48:51],v[156:159]
MF v[188:191],a[188:191],v[56:59],v[188:191]
R16 a[140:143],v241,19456
R16 a[144:147],v241,20480
MF v[216:219],a[192:195],v[48:51],v[216:219]
MF v[220:223],a[192:195],v[56:59],v[220:223]
s_waitcnt lgkmcnt(5)
s_branch L_AE7_%=
L_T71_%=:
v_lshrrev_b32 v251, 7, v242
v_min_u32 v251, 2, v251
s_mul_i32 s52, s81, 3
v_add_u32 v251, s52, v251
v_lshrrev_b32 v251, 1, v251
v_lshlrev_b32 v251, 7, v251
v_bfe_u32 v250, v242, 4, 1
v_lshl_add_u32 v250, v250, 13, v251
s_lshl_b32 s52, s28, 2
s_add_u32 s52, s52, s80
s_lshl_b32 s52, s52, 14
s_add_u32 s52, s52, 0x4990000
buffer_load_dword v251, v250, s[20:23], s52 offen offset:0 sc1
MF v[0:3],a[64:67],a[32:35],v[200:203]
v_exp_f32 v52, v52
MF v[0:3],a[68:71],a[36:39],v[0:3]
R16 a[160:163],v241,24576
s_add_u32 s79, s53, 0x3000
MF v[0:3],a[72:75],a[40:43],v[0:3]
buffer_load_dwordx4 a[0:3], v242, s[20:23], s79 offen offset:3072 sc1
v_exp_f32 v53, v53
MF v[0:3],a[76:79],a[44:47],v[0:3]
R16 a[164:167],v241,25600
PK v50,v52,v53
MF v[8:11],a[64:67],a[48:51],v[204:207]
v_exp_f32 v54, v54
MF v[8:11],a[68:71],a[52:55],v[8:11]
R16 a[168:171],v241,26624
s_add_u32 s79, s53, 0x4000
MF v[8:11],a[72:75],a[56:59],v[8:11]
buffer_load_dwordx4 a[4:7], v242, s[20:23], s79 offen offset:0 sc1
v_exp_f32 v55, v55
MF v[8:11],a[76:79],a[60:63],v[8:11]
R16 a[172:175],v241,27648
buffer_load_dwordx4 a[8:11], v242, s[20:23], s79 offen offset:1024 sc1
PK v51,v54,v55
MF v[4:7],a[80:83],a[32:35],v[200:203]
v_exp_f32 v60, v60
MF v[4:7],a[84:87],a[36:39],v[4:7]
R16 a[176:179],v241,28672
buffer_load_dwordx4 a[12:15], v242, s[20:23], s79 offen offset:2048 sc1
MF v[4:7],a[88:91],a[40:43],v[4:7]
v_exp_f32 v61, v61
MF v[4:7],a[92:95],a[44:47],v[4:7]
R16 a[180:183],v241,29696
PK v58,v60,v61
MF v[12:15],a[80:83],a[48:51],v[204:207]
v_exp_f32 v62, v62
MF v[12:15],a[84:87],a[52:55],v[12:15]
R16 a[184:187],v241,30720
buffer_load_dwordx4 a[16:19], v242, s[20:23], s79 offen offset:3072 sc1
s_add_u32 s79, s53, 0x5000
MF v[12:15],a[88:91],a[56:59],v[12:15]
buffer_load_dwordx4 a[20:23], v242, s[20:23], s79 offen offset:0 sc1
v_exp_f32 v63, v63
MF v[12:15],a[92:95],a[60:63],v[12:15]
R16 a[188:191],v241,31744
PK v59,v62,v63
MF v[16:19],a[96:99],a[32:35],v[232:235]
v_add_u32 v240, 0x8000, v240
v_and_b32 v240, 0x1ffff, v240
MF v[16:19],a[100:103],a[36:39],v[16:19]
buffer_load_dwordx4 a[24:27], v242, s[20:23], s79 offen offset:1024 sc1
buffer_load_dwordx4 a[28:31], v242, s[20:23], s79 offen offset:2048 sc1
MF v[16:19],a[104:107],a[40:43],v[16:19]
MF v[16:19],a[108:111],a[44:47],v[16:19]
MF v[24:27],a[96:99],a[48:51],v[204:207]
v_exp_f32 v0, v0
MF v[24:27],a[100:103],a[52:55],v[24:27]
s_waitcnt vmcnt(10)
s_barrier
MF v[24:27],a[104:107],a[56:59],v[24:27]
v_exp_f32 v1, v1
MF v[24:27],a[108:111],a[60:63],v[24:27]
R16 a[64:67],v240,0
PK v0,v0,v1
v_exp_f32 v2, v2
R16 a[68:71],v240,1024
s_waitcnt vmcnt(8)
v_cmp_ne_u32 vcc, s37, v251
s_or_b64 s[46:47], s[46:47], vcc
v_exp_f32 v3, v3
s_lshl_b32 s48, s28, 13
R16 a[72:75],v240,2048
PK v1,v2,v3
s_add_u32 m0, s48, 0x20000
MF v[28:31],a[112:115],a[48:51],v[236:239]
v_exp_f32 v8, v8
MF v[28:31],a[116:119],a[52:55],v[28:31]
R16 a[76:79],v240,3072
buffer_load_dwordx4 v242, s[20:23], s60 offen offset:0 sc1 lds
buffer_load_dwordx4 v242, s[20:23], s60 offen offset:1024 sc1 lds
MF v[28:31],a[120:123],a[56:59],v[28:31]
buffer_load_dwordx4 v242, s[20:23], s60 offen offset:2048 sc1 lds
v_exp_f32 v9, v9
MF v[28:31],a[124:127],a[60:63],v[28:31]
R16 a[80:83],v240,4096
buffer_load_dwordx4 v242, s[20:23], s60 offen offset:3072 sc1 lds
s_waitcnt lgkmcnt(5)
v_exp_f32 v10, v10
R16 a[84:87],v240,5120
PK v8,v8,v9
MF v[128:131],a[160:163],v[48:51],v[128:131]
v_exp_f32 v11, v11
MF v[160:163],a[160:163],v[56:59],v[160:163]
R16 a[88:91],v240,6144
PK v9,v10,v11
v_exp_f32 v4, v4
R16 a[92:95],v240,7168
s_add_u32 m0, m0, 0x1000
s_add_u32 s48, s60, 0x1000
MF v[132:135],a[164:167],v[48:51],v[132:135]
v_exp_f32 v5, v5
MF v[164:167],a[164:167],v[56:59],v[164:167]
R16 a[96:99],v240,8192
PK v2,v4,v5
v_exp_f32 v6, v6
R16 a[100:103],v240,9216
buffer_load_dwordx4 v242, s[20:23], s48 offen offset:0 sc1 lds
buffer_load_dwordx4 v242, s[20:23], s48 offen offset:1024 sc1 lds
MF v[136:139],a[168:171],v[48:51],v[136:139]
buffer_load_dwordx4 v242, s[20:23], s48 offen offset:2048 sc1 lds
v_exp_f32 v7, v7
MF v[168:171],a[168:171],v[56:59],v[168:171]
R16 a[104:107],v240,10240
buffer_load_dwordx4 v242, s[20:23], s48 offen offset:3072 sc1 lds
PK v3,v6,v7
v_exp_f32 v12, v12
R16 a[108:111],v240,11264
s_cmp_lt_u32 s28, 2
s_cselect_b32 s48, 0x8000, 0
s_xor_b32 s48, s48, s54
MF v[140:143],a[172:175],v[48:51],v[140:143]
v_exp_f32 v13, v13
s_xor_b32 m0, s48, 0x8000
MF v[172:175],a[172:175],v[56:59],v[172:175]
R16 a[112:115],v240,12288
PK v10,v12,v13
v_exp_f32 v14, v14
R16 a[116:119],v240,13312
buffer_load_dwordx4 v242, s[20:23], s61 offen offset:0 sc1 lds
buffer_load_dwordx4 v242, s[20:23], s61 offen offset:1024 sc1 lds
MF v[144:147],a[176:179],v[48:51],v[144:147]
buffer_load_dwordx4 v242, s[20:23], s61 offen offset:2048 sc1 lds
v_exp_f32 v15, v15
MF v[176:179],a[176:179],v[56:59],v[176:179]
R16 a[120:123],v240,14336
buffer_load_dwordx4 v242, s[20:23], s61 offen offset:3072 sc1 lds
PK v11,v14,v15
v_exp_f32 v16, v16
R16 a[124:127],v240,15360
v_add_u32 v241, 0x8000, v241
MF v[148:151],a[180:183],v[48:51],v[148:151]
v_exp_f32 v17, v17
MF v[180:183],a[180:183],v[56:59],v[180:183]
v_and_b32 v241, 0x1ffff, v241
PK v16,v16,v17
v_exp_f32 v18, v18
R16 a[128:131],v241,16384
s_add_u32 m0, m0, 0x1000
s_add_u32 s48, s61, 0x1000
MF v[152:155],a[184:187],v[48:51],v[152:155]
v_exp_f32 v19, v19
MF v[184:187],a[184:187],v[56:59],v[184:187]
R16 a[132:135],v241,17408
PK v17,v18,v19
v_exp_f32 v24, v24
R16 a[136:139],v241,18432
buffer_load_dwordx4 v242, s[20:23], s48 offen offset:0 sc1 lds
buffer_load_dwordx4 v242, s[20:23], s48 offen offset:1024 sc1 lds
MF v[156:159],a[188:191],v[48:51],v[156:159]
buffer_load_dwordx4 v242, s[20:23], s48 offen offset:2048 sc1 lds
v_exp_f32 v25, v25
MF v[188:191],a[188:191],v[56:59],v[188:191]
R16 a[140:143],v241,19456
buffer_load_dwordx4 v242, s[20:23], s48 offen offset:3072 sc1 lds
PK v24,v24,v25
v_exp_f32 v26, v26
R16 a[144:147],v241,20480
MF v[216:219],a[192:195],v[48:51],v[216:219]
v_exp_f32 v27, v27
MF v[220:223],a[192:195],v[56:59],v[220:223]
PK v25,v26,v27
s_waitcnt lgkmcnt(5)
s_branch L_AE7_%=
L_T72_%=:
v_lshrrev_b32 v251, 7, v242
v_min_u32 v251, 2, v251
s_mul_i32 s52, s81, 3
v_add_u32 v251, s52, v251
v_lshrrev_b32 v251, 1, v251
v_lshlrev_b32 v251, 7, v251
v_bfe_u32 v250, v242, 4, 1
v_lshl_add_u32 v250, v250, 13, v251
s_lshl_b32 s52, s28, 2
s_add_u32 s52, s52, s80
s_lshl_b32 s52, s52, 14
s_add_u32 s52, s52, 0x4990000
buffer_load_dword v251, v250, s[20:23], s52 offen offset:0 sc1
MF v[0:3],a[64:67],a[32:35],v[200:203]
v_exp_f32 v52, v52
)A"
R"A(MF v[0:3],a[68:71],a[36:39],v[0:3]
R16 a[160:163],v241,24576
s_add_u32 s79, s53, 0x3000
MF v[0:3],a[72:75],a[40:43],v[0:3]
buffer_load_dwordx4 a[0:3], v242, s[20:23], s79 offen offset:3072 sc1
v_exp_f32 v53, v53
MF v[0:3],a[76:79],a[44:47],v[0:3]
R16 a[164:167],v241,25600
PK v50,v52,v53
MF v[8:11],a[64:67],a[48:51],v[204:207]
v_exp_f32 v54, v54
MF v[8:11],a[68:71],a[52:55],v[8:11]
R16 a[168:171],v241,26624
s_add_u32 s79, s53, 0x4000
MF v[8:11],a[72:75],a[56:59],v[8:11]
buffer_load_dwordx4 a[4:7], v242, s[20:23], s79 offen offset:0 sc1
v_exp_f32 v55, v55
MF v[8:11],a[76:79],a[60:63],v[8:11]
R16 a[172:175],v241,27648
buffer_load_dwordx4 a[8:11], v242, s[20:23], s79 offen offset:1024 sc1
PK v51,v54,v55
MF v[4:7],a[80:83],a[32:35],v[200:203]
v_exp_f32 v60, v60
MF v[4:7],a[84:87],a[36:39],v[4:7]
R16 a[176:179],v241,28672
buffer_load_dwordx4 a[12:15], v242, s[20:23], s79 offen offset:2048 sc1
MF v[4:7],a[88:91],a[40:43],v[4:7]
v_exp_f32 v61, v61
MF v[4:7],a[92:95],a[44:47],v[4:7]
R16 a[180:183],v241,29696
PK v58,v60,v61
MF v[12:15],a[80:83],a[48:51],v[204:207]
v_exp_f32 v62, v62
MF v[12:15],a[84:87],a[52:55],v[12:15]
R16 a[184:187],v241,30720
buffer_load_dwordx4 a[16:19], v242, s[20:23], s79 offen offset:3072 sc1
s_add_u32 s79, s53, 0x5000
MF v[12:15],a[88:91],a[56:59],v[12:15]
buffer_load_dwordx4 a[20:23], v242, s[20:23], s79 offen offset:0 sc1
v_exp_f32 v63, v63
MF v[12:15],a[92:95],a[60:63],v[12:15]
R16 a[188:191],v241,31744
PK v59,v62,v63
MF v[16:19],a[96:99],a[32:35],v[200:203]
v_add_u32 v240, 0x8000, v240
v_and_b32 v240, 0x1ffff, v240
MF v[16:19],a[100:103],a[36:39],v[16:19]
buffer_load_dwordx4 a[24:27], v242, s[20:23], s79 offen offset:1024 sc1
buffer_load_dwordx4 a[28:31], v242, s[20:23], s79 offen offset:2048 sc1
MF v[16:19],a[104:107],a[40:43],v[16:19]
MF v[16:19],a[108:111],a[44:47],v[16:19]
MF v[24:27],a[96:99],a[48:51],v[204:207]
v_exp_f32 v0, v0
MF v[24:27],a[100:103],a[52:55],v[24:27]
s_waitcnt vmcnt(10)
s_barrier
MF v[24:27],a[104:107],a[56:59],v[24:27]
v_exp_f32 v1, v1
MF v[24:27],a[108:111],a[60:63],v[24:27]
R16 a[64:67],v240,0
PK v0,v0,v1
MF v[20:23],a[112:115],a[32:35],v[200:203]
v_exp_f32 v2, v2
MF v[20:23],a[116:119],a[36:39],v[20:23]
R16 a[68:71],v240,1024
s_waitcnt vmcnt(8)
v_cmp_ne_u32 vcc, s37, v251
s_or_b64 s[46:47], s[46:47], vcc
MF v[20:23],a[120:123],a[40:43],v[20:23]
v_exp_f32 v3, v3
s_lshl_b32 s48, s28, 13
MF v[20:23],a[124:127],a[44:47],v[20:23]
R16 a[72:75],v240,2048
PK v1,v2,v3
s_add_u32 m0, s48, 0x20000
MF v[28:31],a[112:115],a[48:51],v[204:207]
v_exp_f32 v8, v8
MF v[28:31],a[116:119],a[52:55],v[28:31]
R16 a[76:79],v240,3072
buffer_load_dwordx4 v242, s[20:23], s60 offen offset:0 sc1 lds
buffer_load_dwordx4 v242, s[20:23], s60 offen offset:1024 sc1 lds
MF v[28:31],a[120:123],a[56:59],v[28:31]
buffer_load_dwordx4 v242, s[20:23], s60 offen offset:2048 sc1 lds
v_exp_f32 v9, v9
MF v[28:31],a[124:127],a[60:63],v[28:31]
R16 a[80:83],v240,4096
buffer_load_dwordx4 v242, s[20:23], s60 offen offset:3072 sc1 lds
s_waitcnt lgkmcnt(5)
v_exp_f32 v10, v10
R16 a[84:87],v240,5120
PK v8,v8,v9
MF v[128:131],a[160:163],v[48:51],v[128:131]
v_exp_f32 v11, v11
MF v[160:163],a[160:163],v[56:59],v[160:163]
R16 a[88:91],v240,6144
PK v9,v10,v11
v_exp_f32 v4, v4
R16 a[92:95],v240,7168
s_add_u32 m0, m0, 0x1000
s_add_u32 s48, s60, 0x1000
MF v[132:135],a[164:167],v[48:51],v[132:135]
v_exp_f32 v5, v5
MF v[164:167],a[164:167],v[56:59],v[164:167]
R16 a[96:99],v240,8192
PK v2,v4,v5
v_exp_f32 v6, v6
R16 a[100:103],v240,9216
buffer_load_dwordx4 v242, s[20:23], s48 offen offset:0 sc1 lds
buffer_load_dwordx4 v242, s[20:23], s48 offen offset:1024 sc1 lds
MF v[136:139],a[168:171],v[48:51],v[136:139]
buffer_load_dwordx4 v242, s[20:23], s48 offen offset:2048 sc1 lds
v_exp_f32 v7, v7
MF v[168:171],a[168:171],v[56:59],v[168:171]
R16 a[104:107],v240,10240
buffer_load_dwordx4 v242, s[20:23], s48 offen offset:3072 sc1 lds
PK v3,v6,v7
v_exp_f32 v12, v12
R16 a[108:111],v240,11264
s_cmp_lt_u32 s28, 2
s_cselect_b32 s48, 0x8000, 0
s_xor_b32 s48, s48, s54
MF v[140:143],a[172:175],v[48:51],v[140:143]
v_exp_f32 v13, v13
s_xor_b32 m0, s48, 0x8000
MF v[172:175],a[172:175],v[56:59],v[172:175]
R16 a[112:115],v240,12288
PK v10,v12,v13
v_exp_f32 v14, v14
R16 a[116:119],v240,13312
buffer_load_dwordx4 v242, s[20:23], s61 offen offset:0 sc1 lds
buffer_load_dwordx4 v242, s[20:23], s61 offen offset:1024 sc1 lds
MF v[144:147],a[176:179],v[48:51],v[144:147]
buffer_load_dwordx4 v242, s[20:23], s61 offen offset:2048 sc1 lds
v_exp_f32 v15, v15
MF v[176:179],a[176:179],v[56:59],v[176:179]
R16 a[120:123],v240,14336
buffer_load_dwordx4 v242, s[20:23], s61 offen offset:3072 sc1 lds
PK v11,v14,v15
v_exp_f32 v16, v16
R16 a[124:127],v240,15360
v_add_u32 v241, 0x8000, v241
MF v[148:151],a[180:183],v[48:51],v[148:151]
v_exp_f32 v17, v17
MF v[180:183],a[180:183],v[56:59],v[180:183]
v_and_b32 v241, 0x1ffff, v241
PK v16,v16,v17
v_exp_f32 v18, v18
R16 a[128:131],v241,16384
s_add_u32 m0, m0, 0x1000
s_add_u32 s48, s61, 0x1000
MF v[152:155],a[184:187],v[48:51],v[152:155]
v_exp_f32 v19, v19
MF v[184:187],a[184:187],v[56:59],v[184:187]
R16 a[132:135],v241,17408
PK v17,v18,v19
v_exp_f32 v24, v24
R16 a[136:139],v241,18432
buffer_load_dwordx4 v242, s[20:23], s48 offen offset:0 sc1 lds
buffer_load_dwordx4 v242, s[20:23], s48 offen offset:1024 sc1 lds
MF v[156:159],a[188:191],v[48:51],v[156:159]
buffer_load_dwordx4 v242, s[20:23], s48 offen offset:2048 sc1 lds
v_exp_f32 v25, v25
MF v[188:191],a[188:191],v[56:59],v[188:191]
R16 a[140:143],v241,19456
buffer_load_dwordx4 v242, s[20:23], s48 offen offset:3072 sc1 lds
PK v24,v24,v25
v_exp_f32 v26, v26
R16 a[144:147],v241,20480
MF v[216:219],a[192:195],v[48:51],v[216:219]
v_exp_f32 v27, v27
MF v[220:223],a[192:195],v[56:59],v[220:223]
PK v25,v26,v27
s_waitcnt lgkmcnt(5)
s_branch L_AE7_%=
L_T73_%=:
v_lshrrev_b32 v251, 7, v242
v_min_u32 v251, 2, v251
s_mul_i32 s52, s81, 3
v_add_u32 v251, s52, v251
v_lshrrev_b32 v251, 1, v251
v_lshlrev_b32 v251, 7, v251
v_bfe_u32 v250, v242, 4, 1
v_lshl_add_u32 v250, v250, 13, v251
s_lshl_b32 s52, s28, 2
s_add_u32 s52, s52, s80
s_lshl_b32 s52, s52, 14
s_add_u32 s52, s52, 0x4990000
buffer_load_dword v251, v250, s[20:23], s52 offen offset:0 sc1
MF v[0:3],a[64:67],a[32:35],v[200:203]
v_exp_f32 v52, v52
MF v[0:3],a[68:71],a[36:39],v[0:3]
R16 a[160:163],v241,24576
s_add_u32 s79, s53, 0x3000
MF v[0:3],a[72:75],a[40:43],v[0:3]
buffer_load_dwordx4 a[0:3], v242, s[20:23], s79 offen offset:3072 sc1
v_exp_f32 v53, v53
MF v[0:3],a[76:79],a[44:47],v[0:3]
R16 a[164:167],v241,25600
PK v50,v52,v53
MF v[8:11],a[64:67],a[48:51],v[204:207]
v_exp_f32 v54, v54
MF v[8:11],a[68:71],a[52:55],v[8:11]
R16 a[168:171],v241,26624
s_add_u32 s79, s53, 0x4000
MF v[8:11],a[72:75],a[56:59],v[8:11]
buffer_load_dwordx4 a[4:7], v242, s[20:23], s79 offen offset:0 sc1
v_exp_f32 v55, v55
MF v[8:11],a[76:79],a[60:63],v[8:11]
R16 a[172:175],v241,27648
buffer_load_dwordx4 a[8:11], v242, s[20:23], s79 offen offset:1024 sc1
PK v51,v54,v55
MF v[4:7],a[80:83],a[32:35],v[200:203]
v_exp_f32 v60, v60
MF v[4:7],a[84:87],a[36:39],v[4:7]
R16 a[176:179],v241,28672
buffer_load_dwordx4 a[12:15], v242, s[20:23], s79 offen offset:2048 sc1
MF v[4:7],a[88:91],a[40:43],v[4:7]
v_exp_f32 v61, v61
MF v[4:7],a[92:95],a[44:47],v[4:7]
R16 a[180:183],v241,29696
PK v58,v60,v61
MF v[12:15],a[80:83],a[48:51],v[204:207]
v_exp_f32 v62, v62
MF v[12:15],a[84:87],a[52:55],v[12:15]
R16 a[184:187],v241,30720
buffer_load_dwordx4 a[16:19], v242, s[20:23], s79 offen offset:3072 sc1
s_add_u32 s79, s53, 0x5000
MF v[12:15],a[88:91],a[56:59],v[12:15]
buffer_load_dwordx4 a[20:23], v242, s[20:23], s79 offen offset:0 sc1
v_exp_f32 v63, v63
MF v[12:15],a[92:95],a[60:63],v[12:15]
R16 a[188:191],v241,31744
PK v59,v62,v63
MF v[16:19],a[96:99],a[32:35],v[200:203]
v_add_u32 v240, 0x8000, v240
v_and_b32 v240, 0x1ffff, v240
MF v[16:19],a[100:103],a[36:39],v[16:19]
buffer_load_dwordx4 a[24:27], v242, s[20:23], s79 offen offset:1024 sc1
buffer_load_dwordx4 a[28:31], v242, s[20:23], s79 offen offset:2048 sc1
MF v[16:19],a[104:107],a[40:43],v[16:19]
MF v[16:19],a[108:111],a[44:47],v[16:19]
MF v[24:27],a[96:99],a[48:51],v[204:207]
v_exp_f32 v0, v0
MF v[24:27],a[100:103],a[52:55],v[24:27]
s_waitcnt vmcnt(10)
s_barrier
MF v[24:27],a[104:107],a[56:59],v[24:27]
v_exp_f32 v1, v1
MF v[24:27],a[108:111],a[60:63],v[24:27]
R16 a[64:67],v240,0
PK v0,v0,v1
MF v[20:23],a[112:115],a[32:35],v[200:203]
v_exp_f32 v2, v2
MF v[20:23],a[116:119],a[36:39],v[20:23]
R16 a[68:71],v240,1024
s_waitcnt vmcnt(8)
v_cmp_ne_u32 vcc, s37, v251
s_or_b64 s[46:47], s[46:47], vcc
MF v[20:23],a[120:123],a[40:43],v[20:23]
v_exp_f32 v3, v3
s_lshl_b32 s48, s28, 13
MF v[20:23],a[124:127],a[44:47],v[20:23]
R16 a[72:75],v240,2048
PK v1,v2,v3
s_add_u32 m0, s48, 0x20000
MF v[28:31],a[112:115],a[48:51],v[204:207]
v_exp_f32 v8, v8
MF v[28:31],a[116:119],a[52:55],v[28:31]
R16 a[76:79],v240,3072
buffer_load_dwordx4 v242, s[20:23], s60 offen offset:0 sc1 lds
buffer_load_dwordx4 v242, s[20:23], s60 offen offset:1024 sc1 lds
MF v[28:31],a[120:123],a[56:59],v[28:31]
buffer_load_dwordx4 v242, s[20:23], s60 offen offset:2048 sc1 lds
v_exp_f32 v9, v9
MF v[28:31],a[124:127],a[60:63],v[28:31]
R16 a[80:83],v240,4096
buffer_load_dwordx4 v242, s[20:23], s60 offen offset:3072 sc1 lds
s_waitcnt lgkmcnt(5)
MF v[64:67],a[160:163],v[32:35],v[64:67]
v_exp_f32 v10, v10
MF v[96:99],a[160:163],v[40:43],v[96:99]
R16 a[84:87],v240,5120
PK v8,v8,v9
MF v[128:131],a[160:163],v[48:51],v[128:131]
v_exp_f32 v11, v11
MF v[160:163],a[160:163],v[56:59],v[160:163]
R16 a[88:91],v240,6144
PK v9,v10,v11
MF v[68:71],a[164:167],v[32:35],v[68:71]
v_exp_f32 v4, v4
MF v[100:103],a[164:167],v[40:43],v[100:103]
R16 a[92:95],v240,7168
s_add_u32 m0, m0, 0x1000
s_add_u32 s48, s60, 0x1000
MF v[132:135],a[164:167],v[48:51],v[132:135]
v_exp_f32 v5, v5
MF v[164:167],a[164:167],v[56:59],v[164:167]
R16 a[96:99],v240,8192
PK v2,v4,v5
MF v[72:75],a[168:171],v[32:35],v[72:75]
v_exp_f32 v6, v6
MF v[104:107],a[168:171],v[40:43],v[104:107]
R16 a[100:103],v240,9216
buffer_load_dwordx4 v242, s[20:23], s48 offen offset:0 sc1 lds
buffer_load_dwordx4 v242, s[20:23], s48 offen offset:1024 sc1 lds
MF v[136:139],a[168:171],v[48:51],v[136:139]
buffer_load_dwordx4 v242, s[20:23], s48 offen offset:2048 sc1 lds
v_exp_f32 v7, v7
MF v[168:171],a[168:171],v[56:59],v[168:171]
R16 a[104:107],v240,10240
buffer_load_dwordx4 v242, s[20:23], s48 offen offset:3072 sc1 lds
PK v3,v6,v7
MF v[76:79],a[172:175],v[32:35],v[76:79]
v_exp_f32 v12, v12
MF v[108:111],a[172:175],v[40:43],v[108:111]
R16 a[108:111],v240,11264
s_cmp_lt_u32 s28, 2
s_cselect_b32 s48, 0x8000, 0
s_xor_b32 s48, s48, s54
MF v[140:143],a[172:175],v[48:51],v[140:143]
v_exp_f32 v13, v13
s_xor_b32 m0, s48, 0x8000
MF v[172:175],a[172:175],v[56:59],v[172:175]
R16 a[112:115],v240,12288
PK v10,v12,v13
MF v[80:83],a[176:179],v[32:35],v[80:83]
v_exp_f32 v14, v14
MF v[112:115],a[176:179],v[40:43],v[112:115]
R16 a[116:119],v240,13312
buffer_load_dwordx4 v242, s[20:23], s61 offen offset:0 sc1 lds
buffer_load_dwordx4 v242, s[20:23], s61 offen offset:1024 sc1 lds
MF v[144:147],a[176:179],v[48:51],v[144:147]
buffer_load_dwordx4 v242, s[20:23], s61 offen offset:2048 sc1 lds
v_exp_f32 v15, v15
MF v[176:179],a[176:179],v[56:59],v[176:179]
R16 a[120:123],v240,14336
buffer_load_dwordx4 v242, s[20:23], s61 offen offset:3072 sc1 lds
PK v11,v14,v15
MF v[84:87],a[180:183],v[32:35],v[84:87]
v_exp_f32 v16, v16
MF v[116:119],a[180:183],v[40:43],v[116:119]
R16 a[124:127],v240,15360
v_add_u32 v241, 0x8000, v241
MF v[148:151],a[180:183],v[48:51],v[148:151]
v_exp_f32 v17, v17
MF v[180:183],a[180:183],v[56:59],v[180:183]
v_and_b32 v241, 0x1ffff, v241
PK v16,v16,v17
MF v[88:91],a[184:187],v[32:35],v[88:91]
v_exp_f32 v18, v18
MF v[120:123],a[184:187],v[40:43],v[120:123]
R16 a[128:131],v241,16384
s_add_u32 m0, m0, 0x1000
s_add_u32 s48, s61, 0x1000
MF v[152:155],a[184:187],v[48:51],v[152:155]
v_exp_f32 v19, v19
MF v[184:187],a[184:187],v[56:59],v[184:187]
R16 a[132:135],v241,17408
PK v17,v18,v19
MF v[92:95],a[188:191],v[32:35],v[92:95]
v_exp_f32 v24, v24
MF v[124:127],a[188:191],v[40:43],v[124:127]
R16 a[136:139],v241,18432
buffer_load_dwordx4 v242, s[20:23], s48 offen offset:0 sc1 lds
buffer_load_dwordx4 v242, s[20:23], s48 offen offset:1024 sc1 lds
MF v[156:159],a[188:191],v[48:51],v[156:159]
buffer_load_dwordx4 v242, s[20:23], s48 offen offset:2048 sc1 lds
v_exp_f32 v25, v25
MF v[188:191],a[188:191],v[56:59],v[188:191]
R16 a[140:143],v241,19456
buffer_load_dwordx4 v242, s[20:23], s48 offen offset:3072 sc1 lds
PK v24,v24,v25
MF v[208:211],a[192:195],v[32:35],v[208:211]
v_exp_f32 v26, v26
MF v[212:215],a[192:195],v[40:43],v[212:215]
R16 a[144:147],v241,20480
MF v[216:219],a[192:195],v[48:51],v[216:219]
v_exp_f32 v27, v27
MF v[220:223],a[192:195],v[56:59],v[220:223]
PK v25,v26,v27
s_waitcnt lgkmcnt(5)
L_AE7_%=:
s_nop 7
s_mov_b32 s92, 0x53800000
s_mov_b32 s93, 0x2b800000
v_rcp_f32 v210, v208
v_cmp_nle_f32_e64 s[40:41], v208, s92
v_cmp_nle_f32_e64 s[42:43], s93, v208
s_or_b64 s[38:39], s[38:39], s[40:41]
s_or_b64 s[38:39], s[38:39], s[42:43]
v_pk_mul_f32 v[64:65], v[64:65], v[210:211] op_sel_hi:[1,0]
v_pk_mul_f32 v[66:67], v[66:67], v[210:211] op_sel_hi:[1,0]
v_pk_mul_f32 v[68:69], v[68:69], v[210:211] op_sel_hi:[1,0]
v_pk_mul_f32 v[70:71], v[70:71], v[210:211] op_sel_hi:[1,0]
PK v64,v64,v65
PK v65,v66,v67
PK v66,v68,v69
PK v67,v70,v71
buffer_store_dwordx4 v[64:67], v242, s[20:23], s35 offen offset:0 sc1
s_add_u32 s79, s53, 0x5000
)A"
R"A(s_nop 0
buffer_load_dwordx4 v[64:67], v242, s[20:23], s79 offen offset:3072 sc1
s_add_u32 s79, s53, 0x6000
buffer_load_dwordx4 v[68:71], v242, s[20:23], s79 offen offset:0 sc1
v_pk_mul_f32 v[72:73], v[72:73], v[210:211] op_sel_hi:[1,0]
v_pk_mul_f32 v[74:75], v[74:75], v[210:211] op_sel_hi:[1,0]
v_pk_mul_f32 v[76:77], v[76:77], v[210:211] op_sel_hi:[1,0]
v_pk_mul_f32 v[78:79], v[78:79], v[210:211] op_sel_hi:[1,0]
PK v72,v72,v73
PK v73,v74,v75
PK v74,v76,v77
PK v75,v78,v79
buffer_store_dwordx4 v[72:75], v242, s[20:23], s35 offen offset:1024 sc1
s_add_u32 s79, s53, 0x6000
s_nop 0
buffer_load_dwordx4 v[72:75], v242, s[20:23], s79 offen offset:1024 sc1
buffer_load_dwordx4 v[76:79], v242, s[20:23], s79 offen offset:2048 sc1
v_pk_mul_f32 v[80:81], v[80:81], v[210:211] op_sel_hi:[1,0]
v_pk_mul_f32 v[82:83], v[82:83], v[210:211] op_sel_hi:[1,0]
v_pk_mul_f32 v[84:85], v[84:85], v[210:211] op_sel_hi:[1,0]
v_pk_mul_f32 v[86:87], v[86:87], v[210:211] op_sel_hi:[1,0]
PK v80,v80,v81
PK v81,v82,v83
PK v82,v84,v85
PK v83,v86,v87
buffer_store_dwordx4 v[80:83], v242, s[20:23], s35 offen offset:2048 sc1
s_add_u32 s79, s53, 0x6000
s_nop 0
buffer_load_dwordx4 v[80:83], v242, s[20:23], s79 offen offset:3072 sc1
s_add_u32 s79, s53, 0x7000
buffer_load_dwordx4 v[84:87], v242, s[20:23], s79 offen offset:0 sc1
v_pk_mul_f32 v[88:89], v[88:89], v[210:211] op_sel_hi:[1,0]
v_pk_mul_f32 v[90:91], v[90:91], v[210:211] op_sel_hi:[1,0]
v_pk_mul_f32 v[92:93], v[92:93], v[210:211] op_sel_hi:[1,0]
v_pk_mul_f32 v[94:95], v[94:95], v[210:211] op_sel_hi:[1,0]
PK v88,v88,v89
PK v89,v90,v91
PK v90,v92,v93
PK v91,v94,v95
buffer_store_dwordx4 v[88:91], v242, s[20:23], s35 offen offset:3072 sc1
s_add_u32 s79, s53, 0x7000
s_nop 0
buffer_load_dwordx4 v[88:91], v242, s[20:23], s79 offen offset:1024 sc1
buffer_load_dwordx4 v[92:95], v242, s[20:23], s79 offen offset:2048 sc1
v_rcp_f32 v214, v212
v_cmp_nle_f32_e64 s[40:41], v212, s92
v_cmp_nle_f32_e64 s[42:43], s93, v212
s_or_b64 s[38:39], s[38:39], s[40:41]
s_or_b64 s[38:39], s[38:39], s[42:43]
v_pk_mul_f32 v[96:97], v[96:97], v[214:215] op_sel_hi:[1,0]
v_pk_mul_f32 v[98:99], v[98:99], v[214:215] op_sel_hi:[1,0]
v_pk_mul_f32 v[100:101], v[100:101], v[214:215] op_sel_hi:[1,0]
v_pk_mul_f32 v[102:103], v[102:103], v[214:215] op_sel_hi:[1,0]
PK v96,v96,v97
PK v97,v98,v99
PK v98,v100,v101
PK v99,v102,v103
s_add_u32 s35, s35, 0x20000
buffer_store_dwordx4 v[96:99], v242, s[20:23], s35 offen offset:0 sc1
s_add_u32 s79, s53, 0x7000
s_nop 0
buffer_load_dwordx4 v[96:99], v242, s[20:23], s79 offen offset:3072 sc1
s_add_u32 s79, s53, 0x8000
buffer_load_dwordx4 v[100:103], v242, s[20:23], s79 offen offset:0 sc1
v_pk_mul_f32 v[104:105], v[104:105], v[214:215] op_sel_hi:[1,0]
v_pk_mul_f32 v[106:107], v[106:107], v[214:215] op_sel_hi:[1,0]
v_pk_mul_f32 v[108:109], v[108:109], v[214:215] op_sel_hi:[1,0]
v_pk_mul_f32 v[110:111], v[110:111], v[214:215] op_sel_hi:[1,0]
PK v104,v104,v105
PK v105,v106,v107
PK v106,v108,v109
PK v107,v110,v111
buffer_store_dwordx4 v[104:107], v242, s[20:23], s35 offen offset:1024 sc1
s_add_u32 s79, s53, 0x8000
s_nop 0
buffer_load_dwordx4 v[104:107], v242, s[20:23], s79 offen offset:1024 sc1
buffer_load_dwordx4 v[108:111], v242, s[20:23], s79 offen offset:2048 sc1
v_pk_mul_f32 v[112:113], v[112:113], v[214:215] op_sel_hi:[1,0]
v_pk_mul_f32 v[114:115], v[114:115], v[214:215] op_sel_hi:[1,0]
v_pk_mul_f32 v[116:117], v[116:117], v[214:215] op_sel_hi:[1,0]
v_pk_mul_f32 v[118:119], v[118:119], v[214:215] op_sel_hi:[1,0]
PK v112,v112,v113
PK v113,v114,v115
PK v114,v116,v117
PK v115,v118,v119
buffer_store_dwordx4 v[112:115], v242, s[20:23], s35 offen offset:2048 sc1
s_add_u32 s79, s53, 0x8000
s_nop 0
buffer_load_dwordx4 v[112:115], v242, s[20:23], s79 offen offset:3072 sc1
s_add_u32 s79, s53, 0x9000
buffer_load_dwordx4 v[116:119], v242, s[20:23], s79 offen offset:0 sc1
v_pk_mul_f32 v[120:121], v[120:121], v[214:215] op_sel_hi:[1,0]
v_pk_mul_f32 v[122:123], v[122:123], v[214:215] op_sel_hi:[1,0]
v_pk_mul_f32 v[124:125], v[124:125], v[214:215] op_sel_hi:[1,0]
v_pk_mul_f32 v[126:127], v[126:127], v[214:215] op_sel_hi:[1,0]
PK v120,v120,v121
PK v121,v122,v123
PK v122,v124,v125
PK v123,v126,v127
buffer_store_dwordx4 v[120:123], v242, s[20:23], s35 offen offset:3072 sc1
s_add_u32 s79, s53, 0x9000
s_nop 0
buffer_load_dwordx4 v[120:123], v242, s[20:23], s79 offen offset:1024 sc1
buffer_load_dwordx4 v[124:127], v242, s[20:23], s79 offen offset:2048 sc1
s_sub_u32 s49, s37, 1
s_cmp_eq_u64 s[38:39], 0
s_cselect_b32 s89, s37, s49
s_cmp_eq_u32 s28, 0
s_cbranch_scc1 L_E10_%=
s_cmp_eq_u32 s28, 1
s_cbranch_scc1 L_E11_%=
s_cmp_eq_u32 s28, 2
s_cbranch_scc1 L_E12_%=
s_branch L_E13_%=
L_AE_%=:
s_nop 7
s_mov_b32 s48, 0x53800000
s_mov_b32 s49, 0x2b800000
v_rcp_f32 v210, v208
v_cmp_nle_f32_e64 s[40:41], v208, s48
v_cmp_nle_f32_e64 s[42:43], s49, v208
s_or_b64 s[38:39], s[38:39], s[40:41]
s_or_b64 s[38:39], s[38:39], s[42:43]
v_pk_mul_f32 v[64:65], v[64:65], v[210:211] op_sel_hi:[1,0]
v_pk_mul_f32 v[66:67], v[66:67], v[210:211] op_sel_hi:[1,0]
v_pk_mul_f32 v[68:69], v[68:69], v[210:211] op_sel_hi:[1,0]
v_pk_mul_f32 v[70:71], v[70:71], v[210:211] op_sel_hi:[1,0]
PK v64,v64,v65
PK v65,v66,v67
PK v66,v68,v69
PK v67,v70,v71
buffer_store_dwordx4 v[64:67], v242, s[20:23], s35 offen offset:0 sc1
v_pk_mul_f32 v[72:73], v[72:73], v[210:211] op_sel_hi:[1,0]
v_pk_mul_f32 v[74:75], v[74:75], v[210:211] op_sel_hi:[1,0]
v_pk_mul_f32 v[76:77], v[76:77], v[210:211] op_sel_hi:[1,0]
v_pk_mul_f32 v[78:79], v[78:79], v[210:211] op_sel_hi:[1,0]
PK v72,v72,v73
PK v73,v74,v75
PK v74,v76,v77
PK v75,v78,v79
buffer_store_dwordx4 v[72:75], v242, s[20:23], s35 offen offset:1024 sc1
v_pk_mul_f32 v[80:81], v[80:81], v[210:211] op_sel_hi:[1,0]
v_pk_mul_f32 v[82:83], v[82:83], v[210:211] op_sel_hi:[1,0]
v_pk_mul_f32 v[84:85], v[84:85], v[210:211] op_sel_hi:[1,0]
v_pk_mul_f32 v[86:87], v[86:87], v[210:211] op_sel_hi:[1,0]
PK v80,v80,v81
PK v81,v82,v83
PK v82,v84,v85
PK v83,v86,v87
buffer_store_dwordx4 v[80:83], v242, s[20:23], s35 offen offset:2048 sc1
v_pk_mul_f32 v[88:89], v[88:89], v[210:211] op_sel_hi:[1,0]
v_pk_mul_f32 v[90:91], v[90:91], v[210:211] op_sel_hi:[1,0]
v_pk_mul_f32 v[92:93], v[92:93], v[210:211] op_sel_hi:[1,0]
v_pk_mul_f32 v[94:95], v[94:95], v[210:211] op_sel_hi:[1,0]
PK v88,v88,v89
PK v89,v90,v91
PK v90,v92,v93
PK v91,v94,v95
buffer_store_dwordx4 v[88:91], v242, s[20:23], s35 offen offset:3072 sc1
v_rcp_f32 v214, v212
v_cmp_nle_f32_e64 s[40:41], v212, s48
v_cmp_nle_f32_e64 s[42:43], s49, v212
s_or_b64 s[38:39], s[38:39], s[40:41]
s_or_b64 s[38:39], s[38:39], s[42:43]
v_pk_mul_f32 v[96:97], v[96:97], v[214:215] op_sel_hi:[1,0]
v_pk_mul_f32 v[98:99], v[98:99], v[214:215] op_sel_hi:[1,0]
v_pk_mul_f32 v[100:101], v[100:101], v[214:215] op_sel_hi:[1,0]
v_pk_mul_f32 v[102:103], v[102:103], v[214:215] op_sel_hi:[1,0]
PK v96,v96,v97
PK v97,v98,v99
PK v98,v100,v101
PK v99,v102,v103
s_add_u32 s35, s35, 0x20000
buffer_store_dwordx4 v[96:99], v242, s[20:23], s35 offen offset:0 sc1
v_pk_mul_f32 v[104:105], v[104:105], v[214:215] op_sel_hi:[1,0]
v_pk_mul_f32 v[106:107], v[106:107], v[214:215] op_sel_hi:[1,0]
v_pk_mul_f32 v[108:109], v[108:109], v[214:215] op_sel_hi:[1,0]
v_pk_mul_f32 v[110:111], v[110:111], v[214:215] op_sel_hi:[1,0]
PK v104,v104,v105
PK v105,v106,v107
PK v106,v108,v109
PK v107,v110,v111
buffer_store_dwordx4 v[104:107], v242, s[20:23], s35 offen offset:1024 sc1
v_pk_mul_f32 v[112:113], v[112:113], v[214:215] op_sel_hi:[1,0]
v_pk_mul_f32 v[114:115], v[114:115], v[214:215] op_sel_hi:[1,0]
v_pk_mul_f32 v[116:117], v[116:117], v[214:215] op_sel_hi:[1,0]
v_pk_mul_f32 v[118:119], v[118:119], v[214:215] op_sel_hi:[1,0]
PK v112,v112,v113
PK v113,v114,v115
PK v114,v116,v117
PK v115,v118,v119
buffer_store_dwordx4 v[112:115], v242, s[20:23], s35 offen offset:2048 sc1
v_pk_mul_f32 v[120:121], v[120:121], v[214:215] op_sel_hi:[1,0]
v_pk_mul_f32 v[122:123], v[122:123], v[214:215] op_sel_hi:[1,0]
v_pk_mul_f32 v[124:125], v[124:125], v[214:215] op_sel_hi:[1,0]
v_pk_mul_f32 v[126:127], v[126:127], v[214:215] op_sel_hi:[1,0]
PK v120,v120,v121
PK v121,v122,v123
PK v122,v124,v125
PK v123,v126,v127
buffer_store_dwordx4 v[120:123], v242, s[20:23], s35 offen offset:3072 sc1
s_sub_u32 s49, s37, 1
s_cmp_eq_u64 s[38:39], 0
s_cselect_b32 s89, s37, s49
s_cmp_eq_u32 s29, 6
s_cbranch_scc1 L_S6_%=
s_lshl_b32 s31, s29, 1
s_sub_u32 s31, 9, s31
MF v[32:35],a[64:67],a[32:35],v[200:203]
v_exp_f32 v20, v20
MF v[32:35],a[68:71],a[36:39],v[32:35]
R16 a[148:151],v241,21504
MF v[32:35],a[72:75],a[40:43],v[32:35]
v_exp_f32 v21, v21
MF v[32:35],a[76:79],a[44:47],v[32:35]
R16 a[152:155],v241,22528
PK v18,v20,v21
MF v[40:43],a[64:67],a[48:51],v[204:207]
v_exp_f32 v22, v22
MF v[40:43],a[68:71],a[52:55],v[40:43]
R16 a[156:159],v241,23552
R16 a[160:163],v241,24576
MF v[40:43],a[72:75],a[56:59],v[40:43]
v_exp_f32 v23, v23
MF v[40:43],a[76:79],a[60:63],v[40:43]
R16 a[164:167],v241,25600
PK v19,v22,v23
MF v[36:39],a[80:83],a[32:35],v[200:203]
v_exp_f32 v28, v28
MF v[36:39],a[84:87],a[36:39],v[36:39]
R16 a[168:171],v241,26624
MF v[36:39],a[88:91],a[40:43],v[36:39]
v_exp_f32 v29, v29
MF v[36:39],a[92:95],a[44:47],v[36:39]
R16 a[172:175],v241,27648
R16 a[176:179],v241,28672
MF v[44:47],a[80:83],a[48:51],v[204:207]
v_exp_f32 v30, v30
MF v[44:47],a[84:87],a[52:55],v[44:47]
R16 a[180:183],v241,29696
PK v26,v28,v29
MF v[44:47],a[88:91],a[56:59],v[44:47]
v_exp_f32 v31, v31
MF v[44:47],a[92:95],a[60:63],v[44:47]
R16 a[184:187],v241,30720
PK v27,v30,v31
MF v[48:51],a[96:99],a[32:35],v[200:203]
R16 a[188:191],v241,31744
v_add_u32 v240, 0x8000, v240
MF v[48:51],a[100:103],a[36:39],v[48:51]
v_and_b32 v240, 0x1ffff, v240
s_and_b32 m0, s50, 0x1ffff
MF v[48:51],a[104:107],a[40:43],v[48:51]
s_xor_b32 m0, m0, 0x14000
MF v[48:51],a[108:111],a[44:47],v[48:51]
MF v[56:59],a[96:99],a[48:51],v[204:207]
v_exp_f32 v32, v32
MF v[56:59],a[100:103],a[52:55],v[56:59]
s_waitcnt vmcnt(23)
s_barrier
MF v[56:59],a[104:107],a[56:59],v[56:59]
v_exp_f32 v33, v33
MF v[56:59],a[108:111],a[60:63],v[56:59]
R16 a[64:67],v240,0
PK v32,v32,v33
MF v[52:55],a[112:115],a[32:35],v[200:203]
v_exp_f32 v34, v34
MF v[52:55],a[116:119],a[36:39],v[52:55]
R16 a[68:71],v240,1024
buffer_load_dwordx4 v242, s[12:15], s50 offen offset:0 sc1 lds
MF v[52:55],a[120:123],a[40:43],v[52:55]
v_exp_f32 v35, v35
MF v[52:55],a[124:127],a[44:47],v[52:55]
R16 a[72:75],v240,2048
PK v33,v34,v35
MF v[60:63],a[112:115],a[48:51],v[204:207]
v_exp_f32 v40, v40
MF v[60:63],a[116:119],a[52:55],v[60:63]
R16 a[76:79],v240,3072
buffer_load_dwordx4 v242, s[12:15], s50 offen offset:1024 sc1 lds
MF v[60:63],a[120:123],a[56:59],v[60:63]
v_exp_f32 v41, v41
MF v[60:63],a[124:127],a[60:63],v[60:63]
R16 a[80:83],v240,4096
s_waitcnt lgkmcnt(5)
MF v[128:131],a[128:131],v[0:3],v[128:131]
v_exp_f32 v42, v42
MF v[160:163],a[128:131],v[8:11],v[160:163]
R16 a[84:87],v240,5120
buffer_load_dwordx4 v242, s[12:15], s50 offen offset:2048 sc1 lds
MF v[132:135],a[132:135],v[0:3],v[132:135]
v_exp_f32 v43, v43
MF v[164:167],a[132:135],v[8:11],v[164:167]
R16 a[88:91],v240,6144
PK v40,v40,v41
MF v[136:139],a[136:139],v[0:3],v[136:139]
v_exp_f32 v36, v36
MF v[168:171],a[136:139],v[8:11],v[168:171]
R16 a[92:95],v240,7168
buffer_load_dwordx4 v242, s[12:15], s50 offen offset:3072 sc1 lds
MF v[140:143],a[140:143],v[0:3],v[140:143]
v_exp_f32 v37, v37
MF v[172:175],a[140:143],v[8:11],v[172:175]
R16 a[96:99],v240,8192
s_xor_b32 m0, m0, 0x14000
MF v[144:147],a[144:147],v[0:3],v[144:147]
v_exp_f32 v38, v38
MF v[176:179],a[144:147],v[8:11],v[176:179]
R16 a[100:103],v240,9216
PK v41,v42,v43
MF v[148:151],a[148:151],v[0:3],v[148:151]
v_exp_f32 v39, v39
MF v[180:183],a[148:151],v[8:11],v[180:183]
R16 a[104:107],v240,10240
buffer_load_dwordx4 v242, s[8:11], s50 offen offset:0 sc1 lds
MF v[152:155],a[152:155],v[0:3],v[152:155]
v_exp_f32 v44, v44
MF v[184:187],a[152:155],v[8:11],v[184:187]
R16 a[108:111],v240,11264
PK v34,v36,v37
MF v[156:159],a[156:159],v[0:3],v[156:159]
v_exp_f32 v45, v45
MF v[188:191],a[156:159],v[8:11],v[188:191]
R16 a[112:115],v240,12288
buffer_load_dwordx4 v242, s[8:11], s50 offen offset:1024 sc1 lds
MF v[216:219],a[192:195],v[0:3],v[216:219]
v_exp_f32 v46, v46
MF v[220:223],a[192:195],v[8:11],v[220:223]
R16 a[116:119],v240,13312
PK v35,v38,v39
MF v[128:131],a[160:163],v[16:19],v[128:131]
v_exp_f32 v47, v47
MF v[160:163],a[160:163],v[24:27],v[160:163]
R16 a[120:123],v240,14336
buffer_load_dwordx4 v242, s[8:11], s50 offen offset:2048 sc1 lds
MF v[132:135],a[164:167],v[16:19],v[132:135]
v_exp_f32 v48, v48
MF v[164:167],a[164:167],v[24:27],v[164:167]
R16 a[124:127],v240,15360
v_add_u32 v241, 0x8000, v241
MF v[136:139],a[168:171],v[16:19],v[136:139]
v_exp_f32 v49, v49
MF v[168:171],a[168:171],v[24:27],v[168:171]
buffer_load_dwordx4 v242, s[8:11], s50 offen offset:3072 sc1 lds
v_and_b32 v241, 0x1ffff, v241
MF v[140:143],a[172:175],v[16:19],v[140:143]
v_exp_f32 v50, v50
MF v[172:175],a[172:175],v[24:27],v[172:175]
R16 a[128:131],v241,16384
PK v42,v44,v45
v_mov_b32 v252, s89
MF v[144:147],a[176:179],v[16:19],v[144:147]
v_exp_f32 v51, v51
v_mov_b32 v253, 0
MF v[176:179],a[176:179],v[24:27],v[176:179]
R16 a[132:135],v241,17408
s_add_u32 s50, s50, 0x8000
s_waitcnt vmcnt(8)
MF v[148:151],a[180:183],v[16:19],v[148:151]
buffer_store_dword v252, v253, s[20:23], s86 offen offset:0 sc1
v_exp_f32 v56, v56
MF v[180:183],a[180:183],v[24:27],v[180:183]
R16 a[136:139],v241,18432
PK v43,v46,v47
MF v[152:155],a[184:187],v[16:19],v[152:155]
v_exp_f32 v57, v57
MF v[184:187],a[184:187],v[24:27],v[184:187]
R16 a[140:143],v241,19456
PK v48,v48,v49
MF v[156:159],a[188:191],v[16:19],v[156:159]
v_exp_f32 v58, v58
MF v[188:191],a[188:191],v[24:27],v[188:191]
R16 a[144:147],v241,20480
PK v49,v50,v51
MF v[216:219],a[192:195],v[16:19],v[216:219]
v_exp_f32 v59, v59
MF v[220:223],a[192:195],v[24:27],v[220:223]
PK v56,v56,v57
PK v57,v58,v59
s_waitcnt lgkmcnt(5)
s_sub_u32 s90, s90, 1
s_cbranch_scc0 L_SVI_%=
s_swappc_b64 s[18:19], s[16:17]
L_SVI_%=:
s_cmp_eq_u32 s29, 5
s_cbranch_scc1 L_L2X_%=
L_L2_%=:
MF v[0:3],a[64:67],a[32:35],v[200:203]
v_exp_f32 v52, v52
MF v[0:3],a[68:71],a[36:39],v[0:3]
R16 a[148:151],v241,21504
MF v[0:3],a[72:75],a[40:43],v[0:3]
v_exp_f32 v53, v53
MF v[0:3],a[76:79],a[44:47],v[0:3]
R16 a[152:155],v241,22528
PK v50,v52,v53
MF v[8:11],a[64:67],a[48:51],v[204:207]
v_exp_f32 v54, v54
MF v[8:11],a[68:71],a[52:55],v[8:11]
R16 a[156:159],v241,23552
)A"
R"A(R16 a[160:163],v241,24576
MF v[8:11],a[72:75],a[56:59],v[8:11]
v_exp_f32 v55, v55
MF v[8:11],a[76:79],a[60:63],v[8:11]
R16 a[164:167],v241,25600
PK v51,v54,v55
MF v[4:7],a[80:83],a[32:35],v[200:203]
v_exp_f32 v60, v60
MF v[4:7],a[84:87],a[36:39],v[4:7]
R16 a[168:171],v241,26624
MF v[4:7],a[88:91],a[40:43],v[4:7]
v_exp_f32 v61, v61
MF v[4:7],a[92:95],a[44:47],v[4:7]
R16 a[172:175],v241,27648
R16 a[176:179],v241,28672
MF v[12:15],a[80:83],a[48:51],v[204:207]
v_exp_f32 v62, v62
MF v[12:15],a[84:87],a[52:55],v[12:15]
R16 a[180:183],v241,29696
PK v58,v60,v61
MF v[12:15],a[88:91],a[56:59],v[12:15]
v_exp_f32 v63, v63
MF v[12:15],a[92:95],a[60:63],v[12:15]
R16 a[184:187],v241,30720
PK v59,v62,v63
MF v[16:19],a[96:99],a[32:35],v[200:203]
R16 a[188:191],v241,31744
v_add_u32 v240, 0x8000, v240
MF v[16:19],a[100:103],a[36:39],v[16:19]
v_and_b32 v240, 0x1ffff, v240
s_and_b32 m0, s50, 0x1ffff
MF v[16:19],a[104:107],a[40:43],v[16:19]
s_xor_b32 m0, m0, 0x14000
MF v[16:19],a[108:111],a[44:47],v[16:19]
MF v[24:27],a[96:99],a[48:51],v[204:207]
v_exp_f32 v0, v0
MF v[24:27],a[100:103],a[52:55],v[24:27]
s_waitcnt vmcnt(12)
s_barrier
MF v[24:27],a[104:107],a[56:59],v[24:27]
v_exp_f32 v1, v1
MF v[24:27],a[108:111],a[60:63],v[24:27]
R16 a[64:67],v240,0
PK v0,v0,v1
MF v[20:23],a[112:115],a[32:35],v[200:203]
v_exp_f32 v2, v2
MF v[20:23],a[116:119],a[36:39],v[20:23]
R16 a[68:71],v240,1024
buffer_load_dwordx4 v242, s[12:15], s50 offen offset:0 sc1 lds
MF v[20:23],a[120:123],a[40:43],v[20:23]
v_exp_f32 v3, v3
MF v[20:23],a[124:127],a[44:47],v[20:23]
R16 a[72:75],v240,2048
PK v1,v2,v3
MF v[28:31],a[112:115],a[48:51],v[204:207]
v_exp_f32 v8, v8
MF v[28:31],a[116:119],a[52:55],v[28:31]
R16 a[76:79],v240,3072
buffer_load_dwordx4 v242, s[12:15], s50 offen offset:1024 sc1 lds
MF v[28:31],a[120:123],a[56:59],v[28:31]
v_exp_f32 v9, v9
MF v[28:31],a[124:127],a[60:63],v[28:31]
R16 a[80:83],v240,4096
s_waitcnt lgkmcnt(5)
MF v[128:131],a[128:131],v[32:35],v[128:131]
v_exp_f32 v10, v10
MF v[160:163],a[128:131],v[40:43],v[160:163]
R16 a[84:87],v240,5120
buffer_load_dwordx4 v242, s[12:15], s50 offen offset:2048 sc1 lds
MF v[132:135],a[132:135],v[32:35],v[132:135]
v_exp_f32 v11, v11
MF v[164:167],a[132:135],v[40:43],v[164:167]
R16 a[88:91],v240,6144
PK v8,v8,v9
MF v[136:139],a[136:139],v[32:35],v[136:139]
v_exp_f32 v4, v4
MF v[168:171],a[136:139],v[40:43],v[168:171]
R16 a[92:95],v240,7168
buffer_load_dwordx4 v242, s[12:15], s50 offen offset:3072 sc1 lds
MF v[140:143],a[140:143],v[32:35],v[140:143]
v_exp_f32 v5, v5
MF v[172:175],a[140:143],v[40:43],v[172:175]
R16 a[96:99],v240,8192
s_xor_b32 m0, m0, 0x14000
MF v[144:147],a[144:147],v[32:35],v[144:147]
v_exp_f32 v6, v6
MF v[176:179],a[144:147],v[40:43],v[176:179]
R16 a[100:103],v240,9216
PK v9,v10,v11
MF v[148:151],a[148:151],v[32:35],v[148:151]
v_exp_f32 v7, v7
MF v[180:183],a[148:151],v[40:43],v[180:183]
R16 a[104:107],v240,10240
buffer_load_dwordx4 v242, s[8:11], s50 offen offset:0 sc1 lds
MF v[152:155],a[152:155],v[32:35],v[152:155]
v_exp_f32 v12, v12
MF v[184:187],a[152:155],v[40:43],v[184:187]
R16 a[108:111],v240,11264
PK v2,v4,v5
MF v[156:159],a[156:159],v[32:35],v[156:159]
v_exp_f32 v13, v13
MF v[188:191],a[156:159],v[40:43],v[188:191]
R16 a[112:115],v240,12288
buffer_load_dwordx4 v242, s[8:11], s50 offen offset:1024 sc1 lds
MF v[216:219],a[192:195],v[32:35],v[216:219]
v_exp_f32 v14, v14
MF v[220:223],a[192:195],v[40:43],v[220:223]
R16 a[116:119],v240,13312
PK v3,v6,v7
MF v[128:131],a[160:163],v[48:51],v[128:131]
v_exp_f32 v15, v15
MF v[160:163],a[160:163],v[56:59],v[160:163]
R16 a[120:123],v240,14336
buffer_load_dwordx4 v242, s[8:11], s50 offen offset:2048 sc1 lds
MF v[132:135],a[164:167],v[48:51],v[132:135]
v_exp_f32 v16, v16
MF v[164:167],a[164:167],v[56:59],v[164:167]
R16 a[124:127],v240,15360
v_add_u32 v241, 0x8000, v241
MF v[136:139],a[168:171],v[48:51],v[136:139]
v_exp_f32 v17, v17
MF v[168:171],a[168:171],v[56:59],v[168:171]
buffer_load_dwordx4 v242, s[8:11], s50 offen offset:3072 sc1 lds
v_and_b32 v241, 0x1ffff, v241
MF v[140:143],a[172:175],v[48:51],v[140:143]
v_exp_f32 v18, v18
MF v[172:175],a[172:175],v[56:59],v[172:175]
R16 a[128:131],v241,16384
PK v10,v12,v13
MF v[144:147],a[176:179],v[48:51],v[144:147]
v_exp_f32 v19, v19
MF v[176:179],a[176:179],v[56:59],v[176:179]
R16 a[132:135],v241,17408
s_add_u32 s50, s50, 0x8000
MF v[148:151],a[180:183],v[48:51],v[148:151]
v_exp_f32 v24, v24
MF v[180:183],a[180:183],v[56:59],v[180:183]
R16 a[136:139],v241,18432
PK v11,v14,v15
MF v[152:155],a[184:187],v[48:51],v[152:155]
v_exp_f32 v25, v25
MF v[184:187],a[184:187],v[56:59],v[184:187]
R16 a[140:143],v241,19456
PK v16,v16,v17
MF v[156:159],a[188:191],v[48:51],v[156:159]
v_exp_f32 v26, v26
MF v[188:191],a[188:191],v[56:59],v[188:191]
R16 a[144:147],v241,20480
PK v17,v18,v19
MF v[216:219],a[192:195],v[48:51],v[216:219]
v_exp_f32 v27, v27
MF v[220:223],a[192:195],v[56:59],v[220:223]
PK v24,v24,v25
PK v25,v26,v27
s_waitcnt lgkmcnt(5)
MF v[32:35],a[64:67],a[32:35],v[200:203]
v_exp_f32 v20, v20
MF v[32:35],a[68:71],a[36:39],v[32:35]
R16 a[148:151],v241,21504
MF v[32:35],a[72:75],a[40:43],v[32:35]
v_exp_f32 v21, v21
MF v[32:35],a[76:79],a[44:47],v[32:35]
R16 a[152:155],v241,22528
PK v18,v20,v21
MF v[40:43],a[64:67],a[48:51],v[204:207]
v_exp_f32 v22, v22
MF v[40:43],a[68:71],a[52:55],v[40:43]
R16 a[156:159],v241,23552
R16 a[160:163],v241,24576
MF v[40:43],a[72:75],a[56:59],v[40:43]
v_exp_f32 v23, v23
MF v[40:43],a[76:79],a[60:63],v[40:43]
R16 a[164:167],v241,25600
PK v19,v22,v23
MF v[36:39],a[80:83],a[32:35],v[200:203]
v_exp_f32 v28, v28
MF v[36:39],a[84:87],a[36:39],v[36:39]
R16 a[168:171],v241,26624
MF v[36:39],a[88:91],a[40:43],v[36:39]
v_exp_f32 v29, v29
MF v[36:39],a[92:95],a[44:47],v[36:39]
R16 a[172:175],v241,27648
R16 a[176:179],v241,28672
MF v[44:47],a[80:83],a[48:51],v[204:207]
v_exp_f32 v30, v30
MF v[44:47],a[84:87],a[52:55],v[44:47]
R16 a[180:183],v241,29696
PK v26,v28,v29
MF v[44:47],a[88:91],a[56:59],v[44:47]
v_exp_f32 v31, v31
MF v[44:47],a[92:95],a[60:63],v[44:47]
R16 a[184:187],v241,30720
PK v27,v30,v31
MF v[48:51],a[96:99],a[32:35],v[200:203]
R16 a[188:191],v241,31744
v_add_u32 v240, 0x8000, v240
MF v[48:51],a[100:103],a[36:39],v[48:51]
v_and_b32 v240, 0x1ffff, v240
s_and_b32 m0, s50, 0x1ffff
MF v[48:51],a[104:107],a[40:43],v[48:51]
s_xor_b32 m0, m0, 0x14000
MF v[48:51],a[108:111],a[44:47],v[48:51]
MF v[56:59],a[96:99],a[48:51],v[204:207]
v_exp_f32 v32, v32
MF v[56:59],a[100:103],a[52:55],v[56:59]
s_waitcnt vmcnt(12)
s_barrier
MF v[56:59],a[104:107],a[56:59],v[56:59]
v_exp_f32 v33, v33
MF v[56:59],a[108:111],a[60:63],v[56:59]
R16 a[64:67],v240,0
PK v32,v32,v33
MF v[52:55],a[112:115],a[32:35],v[200:203]
v_exp_f32 v34, v34
MF v[52:55],a[116:119],a[36:39],v[52:55]
R16 a[68:71],v240,1024
buffer_load_dwordx4 v242, s[12:15], s50 offen offset:0 sc1 lds
MF v[52:55],a[120:123],a[40:43],v[52:55]
v_exp_f32 v35, v35
MF v[52:55],a[124:127],a[44:47],v[52:55]
R16 a[72:75],v240,2048
PK v33,v34,v35
MF v[60:63],a[112:115],a[48:51],v[204:207]
v_exp_f32 v40, v40
MF v[60:63],a[116:119],a[52:55],v[60:63]
R16 a[76:79],v240,3072
buffer_load_dwordx4 v242, s[12:15], s50 offen offset:1024 sc1 lds
MF v[60:63],a[120:123],a[56:59],v[60:63]
v_exp_f32 v41, v41
MF v[60:63],a[124:127],a[60:63],v[60:63]
R16 a[80:83],v240,4096
s_waitcnt lgkmcnt(5)
MF v[128:131],a[128:131],v[0:3],v[128:131]
v_exp_f32 v42, v42
MF v[160:163],a[128:131],v[8:11],v[160:163]
R16 a[84:87],v240,5120
buffer_load_dwordx4 v242, s[12:15], s50 offen offset:2048 sc1 lds
MF v[132:135],a[132:135],v[0:3],v[132:135]
v_exp_f32 v43, v43
MF v[164:167],a[132:135],v[8:11],v[164:167]
R16 a[88:91],v240,6144
PK v40,v40,v41
MF v[136:139],a[136:139],v[0:3],v[136:139]
v_exp_f32 v36, v36
MF v[168:171],a[136:139],v[8:11],v[168:171]
R16 a[92:95],v240,7168
buffer_load_dwordx4 v242, s[12:15], s50 offen offset:3072 sc1 lds
MF v[140:143],a[140:143],v[0:3],v[140:143]
v_exp_f32 v37, v37
MF v[172:175],a[140:143],v[8:11],v[172:175]
R16 a[96:99],v240,8192
s_xor_b32 m0, m0, 0x14000
MF v[144:147],a[144:147],v[0:3],v[144:147]
v_exp_f32 v38, v38
MF v[176:179],a[144:147],v[8:11],v[176:179]
R16 a[100:103],v240,9216
PK v41,v42,v43
MF v[148:151],a[148:151],v[0:3],v[148:151]
v_exp_f32 v39, v39
MF v[180:183],a[148:151],v[8:11],v[180:183]
R16 a[104:107],v240,10240
buffer_load_dwordx4 v242, s[8:11], s50 offen offset:0 sc1 lds
MF v[152:155],a[152:155],v[0:3],v[152:155]
v_exp_f32 v44, v44
MF v[184:187],a[152:155],v[8:11],v[184:187]
R16 a[108:111],v240,11264
PK v34,v36,v37
MF v[156:159],a[156:159],v[0:3],v[156:159]
v_exp_f32 v45, v45
MF v[188:191],a[156:159],v[8:11],v[188:191]
R16 a[112:115],v240,12288
buffer_load_dwordx4 v242, s[8:11], s50 offen offset:1024 sc1 lds
MF v[216:219],a[192:195],v[0:3],v[216:219]
v_exp_f32 v46, v46
MF v[220:223],a[192:195],v[8:11],v[220:223]
R16 a[116:119],v240,13312
PK v35,v38,v39
MF v[128:131],a[160:163],v[16:19],v[128:131]
v_exp_f32 v47, v47
MF v[160:163],a[160:163],v[24:27],v[160:163]
R16 a[120:123],v240,14336
buffer_load_dwordx4 v242, s[8:11], s50 offen offset:2048 sc1 lds
MF v[132:135],a[164:167],v[16:19],v[132:135]
v_exp_f32 v48, v48
MF v[164:167],a[164:167],v[24:27],v[164:167]
R16 a[124:127],v240,15360
v_add_u32 v241, 0x8000, v241
MF v[136:139],a[168:171],v[16:19],v[136:139]
v_exp_f32 v49, v49
MF v[168:171],a[168:171],v[24:27],v[168:171]
buffer_load_dwordx4 v242, s[8:11], s50 offen offset:3072 sc1 lds
v_and_b32 v241, 0x1ffff, v241
MF v[140:143],a[172:175],v[16:19],v[140:143]
v_exp_f32 v50, v50
MF v[172:175],a[172:175],v[24:27],v[172:175]
R16 a[128:131],v241,16384
PK v42,v44,v45
MF v[144:147],a[176:179],v[16:19],v[144:147]
v_exp_f32 v51, v51
MF v[176:179],a[176:179],v[24:27],v[176:179]
R16 a[132:135],v241,17408
s_add_u32 s50, s50, 0x8000
MF v[148:151],a[180:183],v[16:19],v[148:151]
v_exp_f32 v56, v56
MF v[180:183],a[180:183],v[24:27],v[180:183]
R16 a[136:139],v241,18432
PK v43,v46,v47
MF v[152:155],a[184:187],v[16:19],v[152:155]
v_exp_f32 v57, v57
MF v[184:187],a[184:187],v[24:27],v[184:187]
R16 a[140:143],v241,19456
PK v48,v48,v49
MF v[156:159],a[188:191],v[16:19],v[156:159]
v_exp_f32 v58, v58
MF v[188:191],a[188:191],v[24:27],v[188:191]
R16 a[144:147],v241,20480
PK v49,v50,v51
MF v[216:219],a[192:195],v[16:19],v[216:219]
v_exp_f32 v59, v59
MF v[220:223],a[192:195],v[24:27],v[220:223]
PK v56,v56,v57
PK v57,v58,v59
s_waitcnt lgkmcnt(5)
s_sub_u32 s90, s90, 1
s_cbranch_scc0 L_SVJ_%=
s_swappc_b64 s[18:19], s[16:17]
L_SVJ_%=:
s_sub_u32 s31, s31, 1
s_cbranch_scc0 L_L2_%=
L_L2X_%=:
v_bfe_u32 v250, v242, 4, 1
v_bfe_u32 v251, v242, 5, 2
v_lshl_add_u32 v250, v251, 3, v250
v_lshlrev_b32 v250, 7, v250
v_bfe_u32 v251, v242, 7, 2
v_lshl_add_u32 v250, v251, 5, v250
s_lshl_b32 s79, s80, 8
s_lshl_b32 s49, s82, 3
s_add_u32 s79, s79, s49
s_lshr_b32 s49, s28, 1
s_lshl_b32 s49, s49, 2
s_add_u32 s79, s79, s49
s_add_u32 s52, s79, 0x4905000
buffer_load_dword v251, v250, s[20:23], s52 offen offset:0 sc1
MF v[0:3],a[64:67],a[32:35],v[200:203]
v_exp_f32 v52, v52
MF v[0:3],a[68:71],a[36:39],v[0:3]
R16 a[148:151],v241,21504
MF v[0:3],a[72:75],a[40:43],v[0:3]
v_exp_f32 v53, v53
MF v[0:3],a[76:79],a[44:47],v[0:3]
R16 a[152:155],v241,22528
PK v50,v52,v53
MF v[8:11],a[64:67],a[48:51],v[204:207]
v_exp_f32 v54, v54
MF v[8:11],a[68:71],a[52:55],v[8:11]
R16 a[156:159],v241,23552
R16 a[160:163],v241,24576
MF v[8:11],a[72:75],a[56:59],v[8:11]
v_exp_f32 v55, v55
MF v[8:11],a[76:79],a[60:63],v[8:11]
R16 a[164:167],v241,25600
PK v51,v54,v55
MF v[4:7],a[80:83],a[32:35],v[200:203]
v_exp_f32 v60, v60
MF v[4:7],a[84:87],a[36:39],v[4:7]
R16 a[168:171],v241,26624
MF v[4:7],a[88:91],a[40:43],v[4:7]
v_exp_f32 v61, v61
MF v[4:7],a[92:95],a[44:47],v[4:7]
R16 a[172:175],v241,27648
R16 a[176:179],v241,28672
MF v[12:15],a[80:83],a[48:51],v[204:207]
v_exp_f32 v62, v62
MF v[12:15],a[84:87],a[52:55],v[12:15]
R16 a[180:183],v241,29696
PK v58,v60,v61
MF v[12:15],a[88:91],a[56:59],v[12:15]
v_exp_f32 v63, v63
MF v[12:15],a[92:95],a[60:63],v[12:15]
R16 a[184:187],v241,30720
PK v59,v62,v63
MF v[16:19],a[96:99],a[32:35],v[200:203]
R16 a[188:191],v241,31744
v_add_u32 v240, 0x8000, v240
MF v[16:19],a[100:103],a[36:39],v[16:19]
v_and_b32 v240, 0x1ffff, v240
s_and_b32 m0, s50, 0x1ffff
MF v[16:19],a[104:107],a[40:43],v[16:19]
s_xor_b32 m0, m0, 0x14000
MF v[16:19],a[108:111],a[44:47],v[16:19]
MF v[24:27],a[96:99],a[48:51],v[204:207]
v_exp_f32 v0, v0
MF v[24:27],a[100:103],a[52:55],v[24:27]
s_waitcnt vmcnt(13)
s_barrier
MF v[24:27],a[104:107],a[56:59],v[24:27]
v_exp_f32 v1, v1
MF v[24:27],a[108:111],a[60:63],v[24:27]
R16 a[64:67],v240,0
)A"
R"A(PK v0,v0,v1
MF v[20:23],a[112:115],a[32:35],v[200:203]
v_exp_f32 v2, v2
MF v[20:23],a[116:119],a[36:39],v[20:23]
R16 a[68:71],v240,1024
buffer_load_dwordx4 v242, s[12:15], s50 offen offset:0 sc1 lds
MF v[20:23],a[120:123],a[40:43],v[20:23]
v_exp_f32 v3, v3
MF v[20:23],a[124:127],a[44:47],v[20:23]
R16 a[72:75],v240,2048
PK v1,v2,v3
MF v[28:31],a[112:115],a[48:51],v[204:207]
v_exp_f32 v8, v8
MF v[28:31],a[116:119],a[52:55],v[28:31]
R16 a[76:79],v240,3072
buffer_load_dwordx4 v242, s[12:15], s50 offen offset:1024 sc1 lds
MF v[28:31],a[120:123],a[56:59],v[28:31]
v_exp_f32 v9, v9
MF v[28:31],a[124:127],a[60:63],v[28:31]
R16 a[80:83],v240,4096
s_waitcnt lgkmcnt(5)
MF v[128:131],a[128:131],v[32:35],v[128:131]
v_exp_f32 v10, v10
MF v[160:163],a[128:131],v[40:43],v[160:163]
R16 a[84:87],v240,5120
buffer_load_dwordx4 v242, s[12:15], s50 offen offset:2048 sc1 lds
s_waitcnt vmcnt(3)
MF v[132:135],a[132:135],v[32:35],v[132:135]
v_exp_f32 v11, v11
v_cmp_ne_u32 vcc, s37, v251
MF v[164:167],a[132:135],v[40:43],v[164:167]
R16 a[88:91],v240,6144
PK v8,v8,v9
s_nop 0
MF v[136:139],a[136:139],v[32:35],v[136:139]
v_exp_f32 v4, v4
s_cbranch_vccnz L_SP_WF_%=
L_RT_WF_%=:
MF v[168:171],a[136:139],v[40:43],v[168:171]
R16 a[92:95],v240,7168
buffer_load_dwordx4 v242, s[12:15], s50 offen offset:3072 sc1 lds
s_add_u32 s79, s53, 0x0
MF v[140:143],a[140:143],v[32:35],v[140:143]
buffer_load_dwordx4 a[196:199], v242, s[20:23], s79 offen offset:0 sc1
v_exp_f32 v5, v5
MF v[172:175],a[140:143],v[40:43],v[172:175]
R16 a[96:99],v240,8192
s_xor_b32 m0, m0, 0x14000
MF v[144:147],a[144:147],v[32:35],v[144:147]
v_exp_f32 v6, v6
MF v[176:179],a[144:147],v[40:43],v[176:179]
R16 a[100:103],v240,9216
buffer_load_dwordx4 a[200:203], v242, s[20:23], s79 offen offset:1024 sc1
PK v9,v10,v11
MF v[148:151],a[148:151],v[32:35],v[148:151]
v_exp_f32 v7, v7
MF v[180:183],a[148:151],v[40:43],v[180:183]
R16 a[104:107],v240,10240
buffer_load_dwordx4 v242, s[8:11], s50 offen offset:0 sc1 lds
MF v[152:155],a[152:155],v[32:35],v[152:155]
v_exp_f32 v12, v12
MF v[184:187],a[152:155],v[40:43],v[184:187]
R16 a[108:111],v240,11264
PK v2,v4,v5
MF v[156:159],a[156:159],v[32:35],v[156:159]
buffer_load_dwordx4 a[204:207], v242, s[20:23], s79 offen offset:2048 sc1
v_exp_f32 v13, v13
MF v[188:191],a[156:159],v[40:43],v[188:191]
R16 a[112:115],v240,12288
buffer_load_dwordx4 v242, s[8:11], s50 offen offset:1024 sc1 lds
MF v[216:219],a[192:195],v[32:35],v[216:219]
v_exp_f32 v14, v14
MF v[220:223],a[192:195],v[40:43],v[220:223]
R16 a[116:119],v240,13312
PK v3,v6,v7
MF v[128:131],a[160:163],v[48:51],v[128:131]
v_exp_f32 v15, v15
MF v[160:163],a[160:163],v[56:59],v[160:163]
R16 a[120:123],v240,14336
buffer_load_dwordx4 v242, s[8:11], s50 offen offset:2048 sc1 lds
buffer_load_dwordx4 a[208:211], v242, s[20:23], s79 offen offset:3072 sc1
MF v[132:135],a[164:167],v[48:51],v[132:135]
v_exp_f32 v16, v16
MF v[164:167],a[164:167],v[56:59],v[164:167]
R16 a[124:127],v240,15360
v_add_u32 v241, 0x8000, v241
MF v[136:139],a[168:171],v[48:51],v[136:139]
v_exp_f32 v17, v17
MF v[168:171],a[168:171],v[56:59],v[168:171]
buffer_load_dwordx4 v242, s[8:11], s50 offen offset:3072 sc1 lds
v_and_b32 v241, 0x1ffff, v241
MF v[140:143],a[172:175],v[48:51],v[140:143]
v_exp_f32 v18, v18
s_add_u32 s79, s53, 0x1000
MF v[172:175],a[172:175],v[56:59],v[172:175]
R16 a[128:131],v241,16384
buffer_load_dwordx4 a[212:215], v242, s[20:23], s79 offen offset:0 sc1
PK v10,v12,v13
MF v[144:147],a[176:179],v[48:51],v[144:147]
v_exp_f32 v19, v19
MF v[176:179],a[176:179],v[56:59],v[176:179]
R16 a[132:135],v241,17408
s_add_u32 s50, s50, 0x8000
MF v[148:151],a[180:183],v[48:51],v[148:151]
v_exp_f32 v24, v24
MF v[180:183],a[180:183],v[56:59],v[180:183]
R16 a[136:139],v241,18432
buffer_load_dwordx4 a[216:219], v242, s[20:23], s79 offen offset:1024 sc1
PK v11,v14,v15
MF v[152:155],a[184:187],v[48:51],v[152:155]
v_exp_f32 v25, v25
MF v[184:187],a[184:187],v[56:59],v[184:187]
R16 a[140:143],v241,19456
PK v16,v16,v17
MF v[156:159],a[188:191],v[48:51],v[156:159]
v_exp_f32 v26, v26
MF v[188:191],a[188:191],v[56:59],v[188:191]
R16 a[144:147],v241,20480
PK v17,v18,v19
MF v[216:219],a[192:195],v[48:51],v[216:219]
v_exp_f32 v27, v27
MF v[220:223],a[192:195],v[56:59],v[220:223]
PK v24,v24,v25
PK v25,v26,v27
s_waitcnt lgkmcnt(5)
MF v[32:35],a[64:67],a[32:35],v[200:203]
v_exp_f32 v20, v20
MF v[32:35],a[68:71],a[36:39],v[32:35]
R16 a[148:151],v241,21504
s_add_u32 s79, s53, 0x1000
MF v[32:35],a[72:75],a[40:43],v[32:35]
buffer_load_dwordx4 a[220:223], v242, s[20:23], s79 offen offset:2048 sc1
v_exp_f32 v21, v21
MF v[32:35],a[76:79],a[44:47],v[32:35]
R16 a[152:155],v241,22528
PK v18,v20,v21
MF v[40:43],a[64:67],a[48:51],v[204:207]
v_exp_f32 v22, v22
MF v[40:43],a[68:71],a[52:55],v[40:43]
R16 a[156:159],v241,23552
R16 a[160:163],v241,24576
MF v[40:43],a[72:75],a[56:59],v[40:43]
v_exp_f32 v23, v23
MF v[40:43],a[76:79],a[60:63],v[40:43]
R16 a[164:167],v241,25600
PK v19,v22,v23
MF v[36:39],a[80:83],a[32:35],v[200:203]
v_exp_f32 v28, v28
MF v[36:39],a[84:87],a[36:39],v[36:39]
R16 a[168:171],v241,26624
buffer_load_dwordx4 a[224:227], v242, s[20:23], s79 offen offset:3072 sc1
MF v[36:39],a[88:91],a[40:43],v[36:39]
v_exp_f32 v29, v29
MF v[36:39],a[92:95],a[44:47],v[36:39]
R16 a[172:175],v241,27648
R16 a[176:179],v241,28672
MF v[44:47],a[80:83],a[48:51],v[204:207]
v_exp_f32 v30, v30
MF v[44:47],a[84:87],a[52:55],v[44:47]
R16 a[180:183],v241,29696
PK v26,v28,v29
s_add_u32 s79, s53, 0x2000
MF v[44:47],a[88:91],a[56:59],v[44:47]
buffer_load_dwordx4 a[228:231], v242, s[20:23], s79 offen offset:0 sc1
v_exp_f32 v31, v31
MF v[44:47],a[92:95],a[60:63],v[44:47]
R16 a[184:187],v241,30720
PK v27,v30,v31
MF v[48:51],a[96:99],a[32:35],v[200:203]
R16 a[188:191],v241,31744
v_add_u32 v240, 0x8000, v240
MF v[48:51],a[100:103],a[36:39],v[48:51]
v_and_b32 v240, 0x1ffff, v240
s_and_b32 m0, s50, 0x1ffff
MF v[48:51],a[104:107],a[40:43],v[48:51]
s_xor_b32 m0, m0, 0x14000
MF v[48:51],a[108:111],a[44:47],v[48:51]
buffer_load_dwordx4 a[232:235], v242, s[20:23], s79 offen offset:1024 sc1
MF v[56:59],a[96:99],a[48:51],v[204:207]
v_exp_f32 v32, v32
MF v[56:59],a[100:103],a[52:55],v[56:59]
s_waitcnt vmcnt(23)
s_barrier
MF v[56:59],a[104:107],a[56:59],v[56:59]
v_exp_f32 v33, v33
MF v[56:59],a[108:111],a[60:63],v[56:59]
R16 a[64:67],v240,0
PK v32,v32,v33
MF v[52:55],a[112:115],a[32:35],v[200:203]
v_exp_f32 v34, v34
MF v[52:55],a[116:119],a[36:39],v[52:55]
R16 a[68:71],v240,1024
buffer_load_dwordx4 v242, s[12:15], s50 offen offset:0 sc1 lds
buffer_load_dwordx4 a[236:239], v242, s[20:23], s79 offen offset:2048 sc1
MF v[52:55],a[120:123],a[40:43],v[52:55]
v_exp_f32 v35, v35
MF v[52:55],a[124:127],a[44:47],v[52:55]
R16 a[72:75],v240,2048
PK v33,v34,v35
MF v[60:63],a[112:115],a[48:51],v[204:207]
v_exp_f32 v40, v40
MF v[60:63],a[116:119],a[52:55],v[60:63]
R16 a[76:79],v240,3072
buffer_load_dwordx4 v242, s[12:15], s50 offen offset:1024 sc1 lds
MF v[60:63],a[120:123],a[56:59],v[60:63]
v_exp_f32 v41, v41
MF v[60:63],a[124:127],a[60:63],v[60:63]
R16 a[80:83],v240,4096
buffer_load_dwordx4 a[240:243], v242, s[20:23], s79 offen offset:3072 sc1
s_waitcnt lgkmcnt(5)
MF v[128:131],a[128:131],v[0:3],v[128:131]
v_exp_f32 v42, v42
MF v[160:163],a[128:131],v[8:11],v[160:163]
R16 a[84:87],v240,5120
buffer_load_dwordx4 v242, s[12:15], s50 offen offset:2048 sc1 lds
MF v[132:135],a[132:135],v[0:3],v[132:135]
v_exp_f32 v43, v43
MF v[164:167],a[132:135],v[8:11],v[164:167]
R16 a[88:91],v240,6144
PK v40,v40,v41
MF v[136:139],a[136:139],v[0:3],v[136:139]
v_exp_f32 v36, v36
MF v[168:171],a[136:139],v[8:11],v[168:171]
R16 a[92:95],v240,7168
buffer_load_dwordx4 v242, s[12:15], s50 offen offset:3072 sc1 lds
s_add_u32 s79, s53, 0x3000
MF v[140:143],a[140:143],v[0:3],v[140:143]
buffer_load_dwordx4 a[244:247], v242, s[20:23], s79 offen offset:0 sc1
v_exp_f32 v37, v37
MF v[172:175],a[140:143],v[8:11],v[172:175]
R16 a[96:99],v240,8192
s_xor_b32 m0, m0, 0x14000
MF v[144:147],a[144:147],v[0:3],v[144:147]
v_exp_f32 v38, v38
MF v[176:179],a[144:147],v[8:11],v[176:179]
R16 a[100:103],v240,9216
PK v41,v42,v43
MF v[148:151],a[148:151],v[0:3],v[148:151]
v_exp_f32 v39, v39
MF v[180:183],a[148:151],v[8:11],v[180:183]
R16 a[104:107],v240,10240
buffer_load_dwordx4 v242, s[8:11], s50 offen offset:0 sc1 lds
buffer_load_dwordx4 a[248:251], v242, s[20:23], s79 offen offset:1024 sc1
MF v[152:155],a[152:155],v[0:3],v[152:155]
v_exp_f32 v44, v44
MF v[184:187],a[152:155],v[8:11],v[184:187]
R16 a[108:111],v240,11264
PK v34,v36,v37
MF v[156:159],a[156:159],v[0:3],v[156:159]
v_exp_f32 v45, v45
MF v[188:191],a[156:159],v[8:11],v[188:191]
R16 a[112:115],v240,12288
buffer_load_dwordx4 v242, s[8:11], s50 offen offset:1024 sc1 lds
MF v[216:219],a[192:195],v[0:3],v[216:219]
v_exp_f32 v46, v46
MF v[220:223],a[192:195],v[8:11],v[220:223]
R16 a[116:119],v240,13312
buffer_load_dwordx4 a[252:255], v242, s[20:23], s79 offen offset:2048 sc1
PK v35,v38,v39
MF v[128:131],a[160:163],v[16:19],v[128:131]
v_exp_f32 v47, v47
MF v[160:163],a[160:163],v[24:27],v[160:163]
R16 a[120:123],v240,14336
buffer_load_dwordx4 v242, s[8:11], s50 offen offset:2048 sc1 lds
MF v[132:135],a[164:167],v[16:19],v[132:135]
v_exp_f32 v48, v48
MF v[164:167],a[164:167],v[24:27],v[164:167]
R16 a[124:127],v240,15360
v_add_u32 v241, 0x8000, v241
MF v[136:139],a[168:171],v[16:19],v[136:139]
v_exp_f32 v49, v49
MF v[168:171],a[168:171],v[24:27],v[168:171]
buffer_load_dwordx4 v242, s[8:11], s50 offen offset:3072 sc1 lds
buffer_load_dwordx4 a[0:3], v242, s[20:23], s79 offen offset:3072 sc1
v_and_b32 v241, 0x1ffff, v241
MF v[140:143],a[172:175],v[16:19],v[140:143]
v_exp_f32 v50, v50
MF v[172:175],a[172:175],v[24:27],v[172:175]
R16 a[128:131],v241,16384
PK v42,v44,v45
MF v[144:147],a[176:179],v[16:19],v[144:147]
v_exp_f32 v51, v51
MF v[176:179],a[176:179],v[24:27],v[176:179]
R16 a[132:135],v241,17408
s_add_u32 s50, s50, 0x8000
MF v[148:151],a[180:183],v[16:19],v[148:151]
v_exp_f32 v56, v56
MF v[180:183],a[180:183],v[24:27],v[180:183]
R16 a[136:139],v241,18432
PK v43,v46,v47
s_add_u32 s79, s53, 0x4000
MF v[152:155],a[184:187],v[16:19],v[152:155]
buffer_load_dwordx4 a[4:7], v242, s[20:23], s79 offen offset:0 sc1
v_exp_f32 v57, v57
MF v[184:187],a[184:187],v[24:27],v[184:187]
R16 a[140:143],v241,19456
PK v48,v48,v49
MF v[156:159],a[188:191],v[16:19],v[156:159]
v_exp_f32 v58, v58
MF v[188:191],a[188:191],v[24:27],v[188:191]
R16 a[144:147],v241,20480
PK v49,v50,v51
MF v[216:219],a[192:195],v[16:19],v[216:219]
v_exp_f32 v59, v59
MF v[220:223],a[192:195],v[24:27],v[220:223]
PK v56,v56,v57
PK v57,v58,v59
s_waitcnt lgkmcnt(5)
MF v[0:3],a[64:67],a[32:35],v[200:203]
v_exp_f32 v52, v52
MF v[0:3],a[68:71],a[36:39],v[0:3]
R16 a[148:151],v241,21504
s_add_u32 s79, s53, 0x4000
MF v[0:3],a[72:75],a[40:43],v[0:3]
buffer_load_dwordx4 a[8:11], v242, s[20:23], s79 offen offset:1024 sc1
v_exp_f32 v53, v53
MF v[0:3],a[76:79],a[44:47],v[0:3]
R16 a[152:155],v241,22528
PK v50,v52,v53
MF v[8:11],a[64:67],a[48:51],v[204:207]
v_exp_f32 v54, v54
MF v[8:11],a[68:71],a[52:55],v[8:11]
R16 a[156:159],v241,23552
R16 a[160:163],v241,24576
MF v[8:11],a[72:75],a[56:59],v[8:11]
v_exp_f32 v55, v55
MF v[8:11],a[76:79],a[60:63],v[8:11]
R16 a[164:167],v241,25600
PK v51,v54,v55
MF v[4:7],a[80:83],a[32:35],v[200:203]
v_exp_f32 v60, v60
MF v[4:7],a[84:87],a[36:39],v[4:7]
R16 a[168:171],v241,26624
buffer_load_dwordx4 a[12:15], v242, s[20:23], s79 offen offset:2048 sc1
MF v[4:7],a[88:91],a[40:43],v[4:7]
v_exp_f32 v61, v61
MF v[4:7],a[92:95],a[44:47],v[4:7]
R16 a[172:175],v241,27648
R16 a[176:179],v241,28672
MF v[12:15],a[80:83],a[48:51],v[204:207]
v_exp_f32 v62, v62
MF v[12:15],a[84:87],a[52:55],v[12:15]
R16 a[180:183],v241,29696
buffer_load_dwordx4 a[16:19], v242, s[20:23], s79 offen offset:3072 sc1
PK v58,v60,v61
MF v[12:15],a[88:91],a[56:59],v[12:15]
v_exp_f32 v63, v63
MF v[12:15],a[92:95],a[60:63],v[12:15]
R16 a[184:187],v241,30720
PK v59,v62,v63
MF v[16:19],a[96:99],a[32:35],v[200:203]
R16 a[188:191],v241,31744
v_add_u32 v240, 0x8000, v240
MF v[16:19],a[100:103],a[36:39],v[16:19]
v_and_b32 v240, 0x1ffff, v240
s_and_b32 m0, s50, 0x1ffff
MF v[16:19],a[104:107],a[40:43],v[16:19]
s_xor_b32 m0, m0, 0x14000
MF v[16:19],a[108:111],a[44:47],v[16:19]
s_add_u32 s79, s53, 0x5000
MF v[24:27],a[96:99],a[48:51],v[204:207]
buffer_load_dwordx4 a[20:23], v242, s[20:23], s79 offen offset:0 sc1
v_exp_f32 v0, v0
MF v[24:27],a[100:103],a[52:55],v[24:27]
s_waitcnt vmcnt(33)
s_barrier
MF v[24:27],a[104:107],a[56:59],v[24:27]
v_exp_f32 v1, v1
MF v[24:27],a[108:111],a[60:63],v[24:27]
R16 a[64:67],v240,0
PK v0,v0,v1
MF v[20:23],a[112:115],a[32:35],v[200:203]
v_exp_f32 v2, v2
MF v[20:23],a[116:119],a[36:39],v[20:23]
R16 a[68:71],v240,1024
buffer_load_dwordx4 v242, s[12:15], s50 offen offset:0 sc1 lds
buffer_load_dwordx4 a[24:27], v242, s[20:23], s79 offen offset:1024 sc1
MF v[20:23],a[120:123],a[40:43],v[20:23]
v_exp_f32 v3, v3
MF v[20:23],a[124:127],a[44:47],v[20:23]
R16 a[72:75],v240,2048
PK v1,v2,v3
MF v[28:31],a[112:115],a[48:51],v[204:207]
v_exp_f32 v8, v8
MF v[28:31],a[116:119],a[52:55],v[28:31]
R16 a[76:79],v240,3072
buffer_load_dwordx4 v242, s[12:15], s50 offen offset:1024 sc1 lds
MF v[28:31],a[120:123],a[56:59],v[28:31]
v_exp_f32 v9, v9
MF v[28:31],a[124:127],a[60:63],v[28:31]
R16 a[80:83],v240,4096
buffer_load_dwordx4 a[28:31], v242, s[20:23], s79 offen offset:2048 sc1
s_waitcnt lgkmcnt(5)
MF v[128:131],a[128:131],v[32:35],v[128:131]
v_exp_f32 v10, v10
MF v[160:163],a[128:131],v[40:43],v[160:163]
R16 a[84:87],v240,5120
buffer_load_dwordx4 v242, s[12:15], s50 offen offset:2048 sc1 lds
MF v[132:135],a[132:135],v[32:35],v[132:135]
v_exp_f32 v11, v11
)A"
R"A(MF v[164:167],a[132:135],v[40:43],v[164:167]
R16 a[88:91],v240,6144
PK v8,v8,v9
MF v[136:139],a[136:139],v[32:35],v[136:139]
v_exp_f32 v4, v4
MF v[168:171],a[136:139],v[40:43],v[168:171]
R16 a[92:95],v240,7168
buffer_load_dwordx4 v242, s[12:15], s50 offen offset:3072 sc1 lds
buffer_load_dwordx4 v[64:67], v242, s[20:23], s79 offen offset:3072 sc1
MF v[140:143],a[140:143],v[32:35],v[140:143]
v_exp_f32 v5, v5
MF v[172:175],a[140:143],v[40:43],v[172:175]
R16 a[96:99],v240,8192
s_xor_b32 m0, m0, 0x14000
MF v[144:147],a[144:147],v[32:35],v[144:147]
v_exp_f32 v6, v6
MF v[176:179],a[144:147],v[40:43],v[176:179]
R16 a[100:103],v240,9216
PK v9,v10,v11
MF v[148:151],a[148:151],v[32:35],v[148:151]
v_exp_f32 v7, v7
MF v[180:183],a[148:151],v[40:43],v[180:183]
R16 a[104:107],v240,10240
buffer_load_dwordx4 v242, s[8:11], s50 offen offset:0 sc1 lds
s_add_u32 s79, s53, 0x6000
MF v[152:155],a[152:155],v[32:35],v[152:155]
buffer_load_dwordx4 v[68:71], v242, s[20:23], s79 offen offset:0 sc1
v_exp_f32 v12, v12
MF v[184:187],a[152:155],v[40:43],v[184:187]
R16 a[108:111],v240,11264
PK v2,v4,v5
MF v[156:159],a[156:159],v[32:35],v[156:159]
v_exp_f32 v13, v13
MF v[188:191],a[156:159],v[40:43],v[188:191]
R16 a[112:115],v240,12288
buffer_load_dwordx4 v242, s[8:11], s50 offen offset:1024 sc1 lds
MF v[216:219],a[192:195],v[32:35],v[216:219]
v_exp_f32 v14, v14
MF v[220:223],a[192:195],v[40:43],v[220:223]
R16 a[116:119],v240,13312
buffer_load_dwordx4 v[72:75], v242, s[20:23], s79 offen offset:1024 sc1
PK v3,v6,v7
MF v[128:131],a[160:163],v[48:51],v[128:131]
v_exp_f32 v15, v15
MF v[160:163],a[160:163],v[56:59],v[160:163]
R16 a[120:123],v240,14336
buffer_load_dwordx4 v242, s[8:11], s50 offen offset:2048 sc1 lds
MF v[132:135],a[164:167],v[48:51],v[132:135]
v_exp_f32 v16, v16
MF v[164:167],a[164:167],v[56:59],v[164:167]
R16 a[124:127],v240,15360
v_add_u32 v241, 0x8000, v241
MF v[136:139],a[168:171],v[48:51],v[136:139]
v_exp_f32 v17, v17
MF v[168:171],a[168:171],v[56:59],v[168:171]
buffer_load_dwordx4 v242, s[8:11], s50 offen offset:3072 sc1 lds
buffer_load_dwordx4 v[76:79], v242, s[20:23], s79 offen offset:2048 sc1
v_and_b32 v241, 0x1ffff, v241
MF v[140:143],a[172:175],v[48:51],v[140:143]
v_exp_f32 v18, v18
MF v[172:175],a[172:175],v[56:59],v[172:175]
R16 a[128:131],v241,16384
PK v10,v12,v13
MF v[144:147],a[176:179],v[48:51],v[144:147]
v_exp_f32 v19, v19
MF v[176:179],a[176:179],v[56:59],v[176:179]
R16 a[132:135],v241,17408
s_add_u32 s50, s50, 0x8000
MF v[148:151],a[180:183],v[48:51],v[148:151]
v_exp_f32 v24, v24
MF v[180:183],a[180:183],v[56:59],v[180:183]
R16 a[136:139],v241,18432
buffer_load_dwordx4 v[80:83], v242, s[20:23], s79 offen offset:3072 sc1
PK v11,v14,v15
MF v[152:155],a[184:187],v[48:51],v[152:155]
v_exp_f32 v25, v25
MF v[184:187],a[184:187],v[56:59],v[184:187]
R16 a[140:143],v241,19456
PK v16,v16,v17
MF v[156:159],a[188:191],v[48:51],v[156:159]
v_exp_f32 v26, v26
MF v[188:191],a[188:191],v[56:59],v[188:191]
R16 a[144:147],v241,20480
PK v17,v18,v19
MF v[216:219],a[192:195],v[48:51],v[216:219]
v_exp_f32 v27, v27
MF v[220:223],a[192:195],v[56:59],v[220:223]
PK v24,v24,v25
PK v25,v26,v27
s_waitcnt lgkmcnt(5)
MF v[32:35],a[64:67],a[32:35],v[200:203]
v_exp_f32 v20, v20
MF v[32:35],a[68:71],a[36:39],v[32:35]
R16 a[148:151],v241,21504
s_add_u32 s79, s53, 0x7000
MF v[32:35],a[72:75],a[40:43],v[32:35]
buffer_load_dwordx4 v[84:87], v242, s[20:23], s79 offen offset:0 sc1
v_exp_f32 v21, v21
MF v[32:35],a[76:79],a[44:47],v[32:35]
R16 a[152:155],v241,22528
PK v18,v20,v21
MF v[40:43],a[64:67],a[48:51],v[204:207]
v_exp_f32 v22, v22
MF v[40:43],a[68:71],a[52:55],v[40:43]
R16 a[156:159],v241,23552
R16 a[160:163],v241,24576
MF v[40:43],a[72:75],a[56:59],v[40:43]
v_exp_f32 v23, v23
MF v[40:43],a[76:79],a[60:63],v[40:43]
R16 a[164:167],v241,25600
PK v19,v22,v23
MF v[36:39],a[80:83],a[32:35],v[200:203]
v_exp_f32 v28, v28
MF v[36:39],a[84:87],a[36:39],v[36:39]
R16 a[168:171],v241,26624
buffer_load_dwordx4 v[88:91], v242, s[20:23], s79 offen offset:1024 sc1
MF v[36:39],a[88:91],a[40:43],v[36:39]
v_exp_f32 v29, v29
MF v[36:39],a[92:95],a[44:47],v[36:39]
R16 a[172:175],v241,27648
R16 a[176:179],v241,28672
buffer_load_dwordx4 v[92:95], v242, s[20:23], s79 offen offset:2048 sc1
MF v[44:47],a[80:83],a[48:51],v[204:207]
v_exp_f32 v30, v30
MF v[44:47],a[84:87],a[52:55],v[44:47]
R16 a[180:183],v241,29696
PK v26,v28,v29
MF v[44:47],a[88:91],a[56:59],v[44:47]
v_exp_f32 v31, v31
MF v[44:47],a[92:95],a[60:63],v[44:47]
R16 a[184:187],v241,30720
PK v27,v30,v31
MF v[48:51],a[96:99],a[32:35],v[200:203]
R16 a[188:191],v241,31744
v_add_u32 v240, 0x8000, v240
MF v[48:51],a[100:103],a[36:39],v[48:51]
v_and_b32 v240, 0x1ffff, v240
s_and_b32 m0, s50, 0x1ffff
MF v[48:51],a[104:107],a[40:43],v[48:51]
buffer_load_dwordx4 v[96:99], v242, s[20:23], s79 offen offset:3072 sc1
s_xor_b32 m0, m0, 0x14000
MF v[48:51],a[108:111],a[44:47],v[48:51]
MF v[56:59],a[96:99],a[48:51],v[204:207]
v_exp_f32 v32, v32
MF v[56:59],a[100:103],a[52:55],v[56:59]
s_waitcnt vmcnt(32)
s_barrier
s_add_u32 s79, s53, 0x8000
MF v[56:59],a[104:107],a[56:59],v[56:59]
buffer_load_dwordx4 v[100:103], v242, s[20:23], s79 offen offset:0 sc1
v_exp_f32 v33, v33
MF v[56:59],a[108:111],a[60:63],v[56:59]
R16 a[64:67],v240,0
PK v32,v32,v33
MF v[52:55],a[112:115],a[32:35],v[200:203]
v_exp_f32 v34, v34
MF v[52:55],a[116:119],a[36:39],v[52:55]
R16 a[68:71],v240,1024
buffer_load_dwordx4 v242, s[12:15], s50 offen offset:0 sc1 lds
MF v[52:55],a[120:123],a[40:43],v[52:55]
buffer_load_dwordx4 v[104:107], v242, s[20:23], s79 offen offset:1024 sc1
v_exp_f32 v35, v35
MF v[52:55],a[124:127],a[44:47],v[52:55]
R16 a[72:75],v240,2048
PK v33,v34,v35
MF v[60:63],a[112:115],a[48:51],v[204:207]
v_exp_f32 v40, v40
MF v[60:63],a[116:119],a[52:55],v[60:63]
R16 a[76:79],v240,3072
buffer_load_dwordx4 v242, s[12:15], s50 offen offset:1024 sc1 lds
MF v[60:63],a[120:123],a[56:59],v[60:63]
v_exp_f32 v41, v41
MF v[60:63],a[124:127],a[60:63],v[60:63]
R16 a[80:83],v240,4096
buffer_load_dwordx4 v[108:111], v242, s[20:23], s79 offen offset:2048 sc1
s_waitcnt lgkmcnt(5)
MF v[128:131],a[128:131],v[0:3],v[128:131]
v_exp_f32 v42, v42
MF v[160:163],a[128:131],v[8:11],v[160:163]
R16 a[84:87],v240,5120
buffer_load_dwordx4 v242, s[12:15], s50 offen offset:2048 sc1 lds
MF v[132:135],a[132:135],v[0:3],v[132:135]
v_exp_f32 v43, v43
MF v[164:167],a[132:135],v[8:11],v[164:167]
R16 a[88:91],v240,6144
PK v40,v40,v41
MF v[136:139],a[136:139],v[0:3],v[136:139]
buffer_load_dwordx4 v[112:115], v242, s[20:23], s79 offen offset:3072 sc1
v_exp_f32 v36, v36
MF v[168:171],a[136:139],v[8:11],v[168:171]
R16 a[92:95],v240,7168
buffer_load_dwordx4 v242, s[12:15], s50 offen offset:3072 sc1 lds
MF v[140:143],a[140:143],v[0:3],v[140:143]
v_exp_f32 v37, v37
MF v[172:175],a[140:143],v[8:11],v[172:175]
R16 a[96:99],v240,8192
s_xor_b32 m0, m0, 0x14000
MF v[144:147],a[144:147],v[0:3],v[144:147]
v_exp_f32 v38, v38
MF v[176:179],a[144:147],v[8:11],v[176:179]
R16 a[100:103],v240,9216
PK v41,v42,v43
s_add_u32 s79, s53, 0x9000
MF v[148:151],a[148:151],v[0:3],v[148:151]
buffer_load_dwordx4 v[116:119], v242, s[20:23], s79 offen offset:0 sc1
v_exp_f32 v39, v39
MF v[180:183],a[148:151],v[8:11],v[180:183]
R16 a[104:107],v240,10240
buffer_load_dwordx4 v242, s[8:11], s50 offen offset:0 sc1 lds
MF v[152:155],a[152:155],v[0:3],v[152:155]
v_exp_f32 v44, v44
MF v[184:187],a[152:155],v[8:11],v[184:187]
R16 a[108:111],v240,11264
PK v34,v36,v37
MF v[156:159],a[156:159],v[0:3],v[156:159]
buffer_load_dwordx4 v[120:123], v242, s[20:23], s79 offen offset:1024 sc1
v_exp_f32 v45, v45
MF v[188:191],a[156:159],v[8:11],v[188:191]
R16 a[112:115],v240,12288
buffer_load_dwordx4 v242, s[8:11], s50 offen offset:1024 sc1 lds
MF v[216:219],a[192:195],v[0:3],v[216:219]
v_exp_f32 v46, v46
MF v[220:223],a[192:195],v[8:11],v[220:223]
R16 a[116:119],v240,13312
PK v35,v38,v39
MF v[128:131],a[160:163],v[16:19],v[128:131]
v_exp_f32 v47, v47
MF v[160:163],a[160:163],v[24:27],v[160:163]
R16 a[120:123],v240,14336
buffer_load_dwordx4 v242, s[8:11], s50 offen offset:2048 sc1 lds
buffer_load_dwordx4 v[124:127], v242, s[20:23], s79 offen offset:2048 sc1
MF v[132:135],a[164:167],v[16:19],v[132:135]
v_exp_f32 v48, v48
MF v[164:167],a[164:167],v[24:27],v[164:167]
R16 a[124:127],v240,15360
v_add_u32 v241, 0x8000, v241
MF v[136:139],a[168:171],v[16:19],v[136:139]
v_exp_f32 v49, v49
v_lshrrev_b32 v251, 7, v242
MF v[168:171],a[168:171],v[24:27],v[168:171]
buffer_load_dwordx4 v242, s[8:11], s50 offen offset:3072 sc1 lds
v_and_b32 v241, 0x1ffff, v241
v_min_u32 v251, 2, v251
MF v[140:143],a[172:175],v[16:19],v[140:143]
v_exp_f32 v50, v50
s_mul_i32 s52, s81, 3
MF v[172:175],a[172:175],v[24:27],v[172:175]
R16 a[128:131],v241,16384
PK v42,v44,v45
v_add_u32 v251, s52, v251
MF v[144:147],a[176:179],v[16:19],v[144:147]
v_exp_f32 v51, v51
v_lshrrev_b32 v251, 1, v251
MF v[176:179],a[176:179],v[24:27],v[176:179]
R16 a[132:135],v241,17408
s_add_u32 s50, s50, 0x8000
v_lshlrev_b32 v251, 7, v251
MF v[148:151],a[180:183],v[16:19],v[148:151]
v_exp_f32 v56, v56
v_bfe_u32 v250, v242, 4, 1
MF v[180:183],a[180:183],v[24:27],v[180:183]
R16 a[136:139],v241,18432
PK v43,v46,v47
v_lshl_add_u32 v250, v250, 13, v251
MF v[152:155],a[184:187],v[16:19],v[152:155]
v_exp_f32 v57, v57
s_lshl_b32 s52, s28, 2
MF v[184:187],a[184:187],v[24:27],v[184:187]
R16 a[140:143],v241,19456
PK v48,v48,v49
s_add_u32 s52, s52, s80
MF v[156:159],a[188:191],v[16:19],v[156:159]
v_exp_f32 v58, v58
s_lshl_b32 s52, s52, 14
MF v[188:191],a[188:191],v[24:27],v[188:191]
R16 a[144:147],v241,20480
PK v49,v50,v51
s_add_u32 s52, s52, 0x4990000
MF v[216:219],a[192:195],v[16:19],v[216:219]
buffer_load_dword v251, v250, s[20:23], s52 offen offset:0 sc1
v_exp_f32 v59, v59
MF v[220:223],a[192:195],v[24:27],v[220:223]
PK v56,v56,v57
PK v57,v58,v59
s_waitcnt lgkmcnt(5)
MF v[0:3],a[64:67],a[32:35],v[200:203]
v_exp_f32 v52, v52
MF v[0:3],a[68:71],a[36:39],v[0:3]
R16 a[148:151],v241,21504
MF v[0:3],a[72:75],a[40:43],v[0:3]
v_exp_f32 v53, v53
MF v[0:3],a[76:79],a[44:47],v[0:3]
R16 a[152:155],v241,22528
PK v50,v52,v53
MF v[8:11],a[64:67],a[48:51],v[204:207]
v_exp_f32 v54, v54
MF v[8:11],a[68:71],a[52:55],v[8:11]
R16 a[156:159],v241,23552
R16 a[160:163],v241,24576
MF v[8:11],a[72:75],a[56:59],v[8:11]
v_exp_f32 v55, v55
MF v[8:11],a[76:79],a[60:63],v[8:11]
R16 a[164:167],v241,25600
PK v51,v54,v55
MF v[4:7],a[80:83],a[32:35],v[200:203]
v_exp_f32 v60, v60
MF v[4:7],a[84:87],a[36:39],v[4:7]
R16 a[168:171],v241,26624
MF v[4:7],a[88:91],a[40:43],v[4:7]
v_exp_f32 v61, v61
MF v[4:7],a[92:95],a[44:47],v[4:7]
R16 a[172:175],v241,27648
R16 a[176:179],v241,28672
MF v[12:15],a[80:83],a[48:51],v[204:207]
v_exp_f32 v62, v62
MF v[12:15],a[84:87],a[52:55],v[12:15]
R16 a[180:183],v241,29696
PK v58,v60,v61
MF v[12:15],a[88:91],a[56:59],v[12:15]
v_exp_f32 v63, v63
MF v[12:15],a[92:95],a[60:63],v[12:15]
R16 a[184:187],v241,30720
PK v59,v62,v63
MF v[16:19],a[96:99],a[32:35],v[200:203]
R16 a[188:191],v241,31744
v_add_u32 v240, 0x8000, v240
MF v[16:19],a[100:103],a[36:39],v[16:19]
v_and_b32 v240, 0x1ffff, v240
s_and_b32 m0, s50, 0x1ffff
MF v[16:19],a[104:107],a[40:43],v[16:19]
s_xor_b32 m0, m0, 0x14000
MF v[16:19],a[108:111],a[44:47],v[16:19]
MF v[24:27],a[96:99],a[48:51],v[204:207]
v_exp_f32 v0, v0
MF v[24:27],a[100:103],a[52:55],v[24:27]
s_waitcnt vmcnt(29)
s_barrier
MF v[24:27],a[104:107],a[56:59],v[24:27]
v_exp_f32 v1, v1
MF v[24:27],a[108:111],a[60:63],v[24:27]
R16 a[64:67],v240,0
PK v0,v0,v1
MF v[20:23],a[112:115],a[32:35],v[200:203]
v_exp_f32 v2, v2
MF v[20:23],a[116:119],a[36:39],v[20:23]
R16 a[68:71],v240,1024
buffer_load_dwordx4 v242, s[12:15], s50 offen offset:0 sc1 lds
MF v[20:23],a[120:123],a[40:43],v[20:23]
v_exp_f32 v3, v3
MF v[20:23],a[124:127],a[44:47],v[20:23]
R16 a[72:75],v240,2048
PK v1,v2,v3
MF v[28:31],a[112:115],a[48:51],v[204:207]
v_exp_f32 v8, v8
MF v[28:31],a[116:119],a[52:55],v[28:31]
R16 a[76:79],v240,3072
buffer_load_dwordx4 v242, s[12:15], s50 offen offset:1024 sc1 lds
MF v[28:31],a[120:123],a[56:59],v[28:31]
v_exp_f32 v9, v9
MF v[28:31],a[124:127],a[60:63],v[28:31]
R16 a[80:83],v240,4096
s_waitcnt lgkmcnt(5)
MF v[128:131],a[128:131],v[32:35],v[128:131]
v_exp_f32 v10, v10
MF v[160:163],a[128:131],v[40:43],v[160:163]
R16 a[84:87],v240,5120
buffer_load_dwordx4 v242, s[12:15], s50 offen offset:2048 sc1 lds
MF v[132:135],a[132:135],v[32:35],v[132:135]
v_exp_f32 v11, v11
MF v[164:167],a[132:135],v[40:43],v[164:167]
R16 a[88:91],v240,6144
PK v8,v8,v9
MF v[136:139],a[136:139],v[32:35],v[136:139]
v_exp_f32 v4, v4
MF v[168:171],a[136:139],v[40:43],v[168:171]
R16 a[92:95],v240,7168
buffer_load_dwordx4 v242, s[12:15], s50 offen offset:3072 sc1 lds
MF v[140:143],a[140:143],v[32:35],v[140:143]
v_exp_f32 v5, v5
MF v[172:175],a[140:143],v[40:43],v[172:175]
R16 a[96:99],v240,8192
s_add_u32 s50, s50, 0x8000
s_waitcnt vmcnt(4)
MF v[144:147],a[144:147],v[32:35],v[144:147]
v_exp_f32 v6, v6
v_cmp_ne_u32 vcc, s37, v251
MF v[176:179],a[144:147],v[40:43],v[176:179]
R16 a[100:103],v240,9216
PK v9,v10,v11
s_or_b64 s[46:47], s[46:47], vcc
MF v[148:151],a[148:151],v[32:35],v[148:151]
v_exp_f32 v7, v7
s_lshl_b32 s48, s28, 13
MF v[180:183],a[148:151],v[40:43],v[180:183]
R16 a[104:107],v240,10240
PK v2,v4,v5
s_add_u32 m0, s48, 0x20000
MF v[152:155],a[152:155],v[32:35],v[152:155]
buffer_load_dwordx4 v242, s[20:23], s60 offen offset:0 sc1 lds
v_exp_f32 v12, v12
)A"
R"A(MF v[184:187],a[152:155],v[40:43],v[184:187]
R16 a[108:111],v240,11264
buffer_load_dwordx4 v242, s[20:23], s60 offen offset:1024 sc1 lds
PK v3,v6,v7
MF v[156:159],a[156:159],v[32:35],v[156:159]
v_exp_f32 v13, v13
MF v[188:191],a[156:159],v[40:43],v[188:191]
R16 a[112:115],v240,12288
PK v10,v12,v13
MF v[216:219],a[192:195],v[32:35],v[216:219]
v_exp_f32 v14, v14
MF v[220:223],a[192:195],v[40:43],v[220:223]
R16 a[116:119],v240,13312
buffer_load_dwordx4 v242, s[20:23], s60 offen offset:2048 sc1 lds
MF v[128:131],a[160:163],v[48:51],v[128:131]
buffer_load_dwordx4 v242, s[20:23], s60 offen offset:3072 sc1 lds
v_exp_f32 v15, v15
MF v[160:163],a[160:163],v[56:59],v[160:163]
R16 a[120:123],v240,14336
PK v11,v14,v15
MF v[132:135],a[164:167],v[48:51],v[132:135]
v_exp_f32 v16, v16
MF v[164:167],a[164:167],v[56:59],v[164:167]
R16 a[124:127],v240,15360
v_add_u32 v241, 0x8000, v241
s_add_u32 m0, m0, 0x1000
MF v[136:139],a[168:171],v[48:51],v[136:139]
v_exp_f32 v17, v17
s_add_u32 s48, s60, 0x1000
MF v[168:171],a[168:171],v[56:59],v[168:171]
v_and_b32 v241, 0x1ffff, v241
PK v16,v16,v17
MF v[140:143],a[172:175],v[48:51],v[140:143]
v_exp_f32 v18, v18
MF v[172:175],a[172:175],v[56:59],v[172:175]
R16 a[128:131],v241,16384
buffer_load_dwordx4 v242, s[20:23], s48 offen offset:0 sc1 lds
buffer_load_dwordx4 v242, s[20:23], s48 offen offset:1024 sc1 lds
MF v[144:147],a[176:179],v[48:51],v[144:147]
v_exp_f32 v19, v19
MF v[176:179],a[176:179],v[56:59],v[176:179]
R16 a[132:135],v241,17408
PK v17,v18,v19
MF v[148:151],a[180:183],v[48:51],v[148:151]
v_exp_f32 v24, v24
MF v[180:183],a[180:183],v[56:59],v[180:183]
R16 a[136:139],v241,18432
buffer_load_dwordx4 v242, s[20:23], s48 offen offset:2048 sc1 lds
MF v[152:155],a[184:187],v[48:51],v[152:155]
v_exp_f32 v25, v25
MF v[184:187],a[184:187],v[56:59],v[184:187]
R16 a[140:143],v241,19456
PK v24,v24,v25
MF v[156:159],a[188:191],v[48:51],v[156:159]
v_exp_f32 v26, v26
MF v[188:191],a[188:191],v[56:59],v[188:191]
R16 a[144:147],v241,20480
buffer_load_dwordx4 v242, s[20:23], s48 offen offset:3072 sc1 lds
MF v[216:219],a[192:195],v[48:51],v[216:219]
v_exp_f32 v27, v27
MF v[220:223],a[192:195],v[56:59],v[220:223]
PK v25,v26,v27
s_waitcnt lgkmcnt(5)
MF v[32:35],a[64:67],a[32:35],v[200:203]
v_exp_f32 v20, v20
MF v[32:35],a[68:71],a[36:39],v[32:35]
R16 a[148:151],v241,21504
MF v[32:35],a[72:75],a[40:43],v[32:35]
v_exp_f32 v21, v21
MF v[32:35],a[76:79],a[44:47],v[32:35]
R16 a[152:155],v241,22528
PK v18,v20,v21
MF v[40:43],a[64:67],a[48:51],v[204:207]
v_exp_f32 v22, v22
MF v[40:43],a[68:71],a[52:55],v[40:43]
R16 a[156:159],v241,23552
R16 a[160:163],v241,24576
MF v[40:43],a[72:75],a[56:59],v[40:43]
v_exp_f32 v23, v23
MF v[40:43],a[76:79],a[60:63],v[40:43]
R16 a[164:167],v241,25600
PK v19,v22,v23
MF v[36:39],a[80:83],a[32:35],v[200:203]
v_exp_f32 v28, v28
MF v[36:39],a[84:87],a[36:39],v[36:39]
R16 a[168:171],v241,26624
MF v[36:39],a[88:91],a[40:43],v[36:39]
v_exp_f32 v29, v29
MF v[36:39],a[92:95],a[44:47],v[36:39]
R16 a[172:175],v241,27648
R16 a[176:179],v241,28672
MF v[44:47],a[80:83],a[48:51],v[204:207]
v_exp_f32 v30, v30
MF v[44:47],a[84:87],a[52:55],v[44:47]
R16 a[180:183],v241,29696
PK v26,v28,v29
MF v[44:47],a[88:91],a[56:59],v[44:47]
v_exp_f32 v31, v31
MF v[44:47],a[92:95],a[60:63],v[44:47]
R16 a[184:187],v241,30720
PK v27,v30,v31
MF v[48:51],a[96:99],a[32:35],v[200:203]
R16 a[188:191],v241,31744
v_add_u32 v240, 0x8000, v240
MF v[48:51],a[100:103],a[36:39],v[48:51]
v_and_b32 v240, 0x1ffff, v240
s_and_b32 m0, s50, 0x1ffff
MF v[48:51],a[104:107],a[40:43],v[48:51]
s_xor_b32 m0, m0, 0x14000
MF v[48:51],a[108:111],a[44:47],v[48:51]
MF v[56:59],a[96:99],a[48:51],v[204:207]
v_exp_f32 v32, v32
MF v[56:59],a[100:103],a[52:55],v[56:59]
s_waitcnt vmcnt(20)
s_barrier
MF v[56:59],a[104:107],a[56:59],v[56:59]
v_exp_f32 v33, v33
MF v[56:59],a[108:111],a[60:63],v[56:59]
R16 a[64:67],v240,0
PK v32,v32,v33
MF v[52:55],a[112:115],a[32:35],v[200:203]
v_exp_f32 v34, v34
MF v[52:55],a[116:119],a[36:39],v[52:55]
R16 a[68:71],v240,1024
buffer_load_dwordx4 v242, s[12:15], s50 offen offset:0 sc1 lds
MF v[52:55],a[120:123],a[40:43],v[52:55]
v_exp_f32 v35, v35
MF v[52:55],a[124:127],a[44:47],v[52:55]
R16 a[72:75],v240,2048
PK v33,v34,v35
MF v[60:63],a[112:115],a[48:51],v[204:207]
v_exp_f32 v40, v40
MF v[60:63],a[116:119],a[52:55],v[60:63]
R16 a[76:79],v240,3072
buffer_load_dwordx4 v242, s[12:15], s50 offen offset:1024 sc1 lds
MF v[60:63],a[120:123],a[56:59],v[60:63]
v_exp_f32 v41, v41
MF v[60:63],a[124:127],a[60:63],v[60:63]
R16 a[80:83],v240,4096
s_waitcnt lgkmcnt(5)
MF v[128:131],a[128:131],v[0:3],v[128:131]
v_exp_f32 v42, v42
MF v[160:163],a[128:131],v[8:11],v[160:163]
R16 a[84:87],v240,5120
buffer_load_dwordx4 v242, s[12:15], s50 offen offset:2048 sc1 lds
MF v[132:135],a[132:135],v[0:3],v[132:135]
v_exp_f32 v43, v43
MF v[164:167],a[132:135],v[8:11],v[164:167]
R16 a[88:91],v240,6144
PK v40,v40,v41
MF v[136:139],a[136:139],v[0:3],v[136:139]
v_exp_f32 v36, v36
MF v[168:171],a[136:139],v[8:11],v[168:171]
R16 a[92:95],v240,7168
buffer_load_dwordx4 v242, s[12:15], s50 offen offset:3072 sc1 lds
MF v[140:143],a[140:143],v[0:3],v[140:143]
v_exp_f32 v37, v37
MF v[172:175],a[140:143],v[8:11],v[172:175]
R16 a[96:99],v240,8192
s_add_u32 s50, s50, 0x8000
MF v[144:147],a[144:147],v[0:3],v[144:147]
v_exp_f32 v38, v38
MF v[176:179],a[144:147],v[8:11],v[176:179]
R16 a[100:103],v240,9216
PK v41,v42,v43
s_cmp_lt_u32 s28, 2
s_cselect_b32 s48, 0x8000, 0
s_xor_b32 s48, s48, s54
MF v[148:151],a[148:151],v[0:3],v[148:151]
v_exp_f32 v39, v39
s_xor_b32 m0, s48, 0x8000
MF v[180:183],a[148:151],v[8:11],v[180:183]
R16 a[104:107],v240,10240
buffer_load_dwordx4 v242, s[20:23], s61 offen offset:0 sc1 lds
PK v34,v36,v37
MF v[152:155],a[152:155],v[0:3],v[152:155]
buffer_load_dwordx4 v242, s[20:23], s61 offen offset:1024 sc1 lds
v_exp_f32 v44, v44
MF v[184:187],a[152:155],v[8:11],v[184:187]
R16 a[108:111],v240,11264
PK v35,v38,v39
MF v[156:159],a[156:159],v[0:3],v[156:159]
v_exp_f32 v45, v45
MF v[188:191],a[156:159],v[8:11],v[188:191]
R16 a[112:115],v240,12288
PK v42,v44,v45
MF v[216:219],a[192:195],v[0:3],v[216:219]
v_exp_f32 v46, v46
MF v[220:223],a[192:195],v[8:11],v[220:223]
R16 a[116:119],v240,13312
buffer_load_dwordx4 v242, s[20:23], s61 offen offset:2048 sc1 lds
MF v[128:131],a[160:163],v[16:19],v[128:131]
buffer_load_dwordx4 v242, s[20:23], s61 offen offset:3072 sc1 lds
v_exp_f32 v47, v47
MF v[160:163],a[160:163],v[24:27],v[160:163]
R16 a[120:123],v240,14336
PK v43,v46,v47
MF v[132:135],a[164:167],v[16:19],v[132:135]
v_exp_f32 v48, v48
MF v[164:167],a[164:167],v[24:27],v[164:167]
R16 a[124:127],v240,15360
v_add_u32 v241, 0x8000, v241
s_add_u32 m0, m0, 0x1000
MF v[136:139],a[168:171],v[16:19],v[136:139]
v_exp_f32 v49, v49
s_add_u32 s48, s61, 0x1000
MF v[168:171],a[168:171],v[24:27],v[168:171]
v_and_b32 v241, 0x1ffff, v241
PK v48,v48,v49
MF v[140:143],a[172:175],v[16:19],v[140:143]
v_exp_f32 v50, v50
MF v[172:175],a[172:175],v[24:27],v[172:175]
R16 a[128:131],v241,16384
buffer_load_dwordx4 v242, s[20:23], s48 offen offset:0 sc1 lds
buffer_load_dwordx4 v242, s[20:23], s48 offen offset:1024 sc1 lds
MF v[144:147],a[176:179],v[16:19],v[144:147]
v_exp_f32 v51, v51
MF v[176:179],a[176:179],v[24:27],v[176:179]
R16 a[132:135],v241,17408
PK v49,v50,v51
MF v[148:151],a[180:183],v[16:19],v[148:151]
v_exp_f32 v56, v56
MF v[180:183],a[180:183],v[24:27],v[180:183]
R16 a[136:139],v241,18432
buffer_load_dwordx4 v242, s[20:23], s48 offen offset:2048 sc1 lds
MF v[152:155],a[184:187],v[16:19],v[152:155]
v_exp_f32 v57, v57
MF v[184:187],a[184:187],v[24:27],v[184:187]
R16 a[140:143],v241,19456
PK v56,v56,v57
MF v[156:159],a[188:191],v[16:19],v[156:159]
v_exp_f32 v58, v58
MF v[188:191],a[188:191],v[24:27],v[188:191]
R16 a[144:147],v241,20480
buffer_load_dwordx4 v242, s[20:23], s48 offen offset:3072 sc1 lds
MF v[216:219],a[192:195],v[16:19],v[216:219]
v_exp_f32 v59, v59
MF v[220:223],a[192:195],v[24:27],v[220:223]
PK v57,v58,v59
s_waitcnt lgkmcnt(5)
s_branch L_EE_%=
L_S6_%=:
MF v[32:35],a[64:67],a[32:35],v[200:203]
v_exp_f32 v20, v20
MF v[32:35],a[68:71],a[36:39],v[32:35]
R16 a[148:151],v241,21504
s_add_u32 s79, s53, 0x3000
MF v[32:35],a[72:75],a[40:43],v[32:35]
buffer_load_dwordx4 a[0:3], v242, s[20:23], s79 offen offset:3072 sc1
v_exp_f32 v21, v21
MF v[32:35],a[76:79],a[44:47],v[32:35]
R16 a[152:155],v241,22528
PK v18,v20,v21
MF v[40:43],a[64:67],a[48:51],v[204:207]
v_exp_f32 v22, v22
MF v[40:43],a[68:71],a[52:55],v[40:43]
R16 a[156:159],v241,23552
R16 a[160:163],v241,24576
MF v[40:43],a[72:75],a[56:59],v[40:43]
v_exp_f32 v23, v23
MF v[40:43],a[76:79],a[60:63],v[40:43]
R16 a[164:167],v241,25600
PK v19,v22,v23
MF v[36:39],a[80:83],a[32:35],v[200:203]
v_exp_f32 v28, v28
MF v[36:39],a[84:87],a[36:39],v[36:39]
R16 a[168:171],v241,26624
s_add_u32 s79, s53, 0x4000
MF v[36:39],a[88:91],a[40:43],v[36:39]
buffer_load_dwordx4 a[4:7], v242, s[20:23], s79 offen offset:0 sc1
v_exp_f32 v29, v29
MF v[36:39],a[92:95],a[44:47],v[36:39]
R16 a[172:175],v241,27648
R16 a[176:179],v241,28672
buffer_load_dwordx4 a[8:11], v242, s[20:23], s79 offen offset:1024 sc1
MF v[44:47],a[80:83],a[48:51],v[204:207]
v_exp_f32 v30, v30
MF v[44:47],a[84:87],a[52:55],v[44:47]
R16 a[180:183],v241,29696
PK v26,v28,v29
MF v[44:47],a[88:91],a[56:59],v[44:47]
v_exp_f32 v31, v31
MF v[44:47],a[92:95],a[60:63],v[44:47]
R16 a[184:187],v241,30720
PK v27,v30,v31
MF v[48:51],a[96:99],a[32:35],v[200:203]
R16 a[188:191],v241,31744
v_add_u32 v240, 0x8000, v240
MF v[48:51],a[100:103],a[36:39],v[48:51]
v_and_b32 v240, 0x1ffff, v240
s_and_b32 m0, s50, 0x1ffff
MF v[48:51],a[104:107],a[40:43],v[48:51]
buffer_load_dwordx4 a[12:15], v242, s[20:23], s79 offen offset:2048 sc1
s_xor_b32 m0, m0, 0x14000
MF v[48:51],a[108:111],a[44:47],v[48:51]
MF v[56:59],a[96:99],a[48:51],v[204:207]
v_exp_f32 v32, v32
MF v[56:59],a[100:103],a[52:55],v[56:59]
buffer_load_dwordx4 a[16:19], v242, s[20:23], s79 offen offset:3072 sc1
s_waitcnt vmcnt(28)
s_barrier
MF v[56:59],a[104:107],a[56:59],v[56:59]
v_exp_f32 v33, v33
MF v[56:59],a[108:111],a[60:63],v[56:59]
R16 a[64:67],v240,0
PK v32,v32,v33
MF v[52:55],a[112:115],a[32:35],v[200:203]
v_exp_f32 v34, v34
MF v[52:55],a[116:119],a[36:39],v[52:55]
R16 a[68:71],v240,1024
buffer_load_dwordx4 v242, s[12:15], s50 offen offset:0 sc1 lds
MF v[52:55],a[120:123],a[40:43],v[52:55]
v_exp_f32 v35, v35
s_add_u32 s79, s53, 0x5000
MF v[52:55],a[124:127],a[44:47],v[52:55]
R16 a[72:75],v240,2048
buffer_load_dwordx4 a[20:23], v242, s[20:23], s79 offen offset:0 sc1
PK v33,v34,v35
MF v[60:63],a[112:115],a[48:51],v[204:207]
v_exp_f32 v40, v40
MF v[60:63],a[116:119],a[52:55],v[60:63]
R16 a[76:79],v240,3072
buffer_load_dwordx4 v242, s[12:15], s50 offen offset:1024 sc1 lds
MF v[60:63],a[120:123],a[56:59],v[60:63]
v_exp_f32 v41, v41
MF v[60:63],a[124:127],a[60:63],v[60:63]
R16 a[80:83],v240,4096
buffer_load_dwordx4 a[24:27], v242, s[20:23], s79 offen offset:1024 sc1
s_waitcnt lgkmcnt(5)
MF v[128:131],a[128:131],v[0:3],v[128:131]
v_exp_f32 v42, v42
MF v[160:163],a[128:131],v[8:11],v[160:163]
R16 a[84:87],v240,5120
buffer_load_dwordx4 v242, s[12:15], s50 offen offset:2048 sc1 lds
MF v[132:135],a[132:135],v[0:3],v[132:135]
v_exp_f32 v43, v43
MF v[164:167],a[132:135],v[8:11],v[164:167]
R16 a[88:91],v240,6144
PK v40,v40,v41
MF v[136:139],a[136:139],v[0:3],v[136:139]
buffer_load_dwordx4 a[28:31], v242, s[20:23], s79 offen offset:2048 sc1
v_exp_f32 v36, v36
MF v[168:171],a[136:139],v[8:11],v[168:171]
R16 a[92:95],v240,7168
buffer_load_dwordx4 v242, s[12:15], s50 offen offset:3072 sc1 lds
MF v[140:143],a[140:143],v[0:3],v[140:143]
v_exp_f32 v37, v37
MF v[172:175],a[140:143],v[8:11],v[172:175]
R16 a[96:99],v240,8192
s_xor_b32 m0, m0, 0x14000
MF v[144:147],a[144:147],v[0:3],v[144:147]
v_exp_f32 v38, v38
MF v[176:179],a[144:147],v[8:11],v[176:179]
R16 a[100:103],v240,9216
buffer_load_dwordx4 v[64:67], v242, s[20:23], s79 offen offset:3072 sc1
PK v41,v42,v43
MF v[148:151],a[148:151],v[0:3],v[148:151]
v_exp_f32 v39, v39
MF v[180:183],a[148:151],v[8:11],v[180:183]
R16 a[104:107],v240,10240
buffer_load_dwordx4 v242, s[8:11], s50 offen offset:0 sc1 lds
MF v[152:155],a[152:155],v[0:3],v[152:155]
v_exp_f32 v44, v44
MF v[184:187],a[152:155],v[8:11],v[184:187]
R16 a[108:111],v240,11264
PK v34,v36,v37
MF v[156:159],a[156:159],v[0:3],v[156:159]
v_exp_f32 v45, v45
s_add_u32 s79, s53, 0x6000
MF v[188:191],a[156:159],v[8:11],v[188:191]
R16 a[112:115],v240,12288
buffer_load_dwordx4 v242, s[8:11], s50 offen offset:1024 sc1 lds
buffer_load_dwordx4 v[68:71], v242, s[20:23], s79 offen offset:0 sc1
MF v[216:219],a[192:195],v[0:3],v[216:219]
v_exp_f32 v46, v46
MF v[220:223],a[192:195],v[8:11],v[220:223]
R16 a[116:119],v240,13312
PK v35,v38,v39
MF v[128:131],a[160:163],v[16:19],v[128:131]
v_exp_f32 v47, v47
MF v[160:163],a[160:163],v[24:27],v[160:163]
R16 a[120:123],v240,14336
buffer_load_dwordx4 v242, s[8:11], s50 offen offset:2048 sc1 lds
MF v[132:135],a[164:167],v[16:19],v[132:135]
v_exp_f32 v48, v48
v_lshrrev_b32 v251, 7, v242
MF v[164:167],a[164:167],v[24:27],v[164:167]
R16 a[124:127],v240,15360
v_add_u32 v241, 0x8000, v241
v_min_u32 v251, 2, v251
MF v[136:139],a[168:171],v[16:19],v[136:139]
v_exp_f32 v49, v49
s_mul_i32 s52, s81, 3
MF v[168:171],a[168:171],v[24:27],v[168:171]
buffer_load_dwordx4 v242, s[8:11], s50 offen offset:3072 sc1 lds
v_and_b32 v241, 0x1ffff, v241
v_add_u32 v251, s52, v251
MF v[140:143],a[172:175],v[16:19],v[140:143]
v_exp_f32 v50, v50
v_lshrrev_b32 v251, 1, v251
)A"
R"A(MF v[172:175],a[172:175],v[24:27],v[172:175]
R16 a[128:131],v241,16384
PK v42,v44,v45
v_lshlrev_b32 v251, 7, v251
v_mov_b32 v252, s89
MF v[144:147],a[176:179],v[16:19],v[144:147]
v_exp_f32 v51, v51
v_bfe_u32 v250, v242, 4, 1
v_mov_b32 v253, 0
MF v[176:179],a[176:179],v[24:27],v[176:179]
R16 a[132:135],v241,17408
s_add_u32 s50, s50, 0x8000
v_lshl_add_u32 v250, v250, 13, v251
s_waitcnt vmcnt(18)
MF v[148:151],a[180:183],v[16:19],v[148:151]
buffer_store_dword v252, v253, s[20:23], s86 offen offset:0 sc1
v_exp_f32 v56, v56
s_lshl_b32 s52, s28, 2
MF v[180:183],a[180:183],v[24:27],v[180:183]
R16 a[136:139],v241,18432
PK v43,v46,v47
s_add_u32 s52, s52, s80
MF v[152:155],a[184:187],v[16:19],v[152:155]
v_exp_f32 v57, v57
s_lshl_b32 s52, s52, 14
MF v[184:187],a[184:187],v[24:27],v[184:187]
R16 a[140:143],v241,19456
PK v48,v48,v49
s_add_u32 s52, s52, 0x4990000
MF v[156:159],a[188:191],v[16:19],v[156:159]
buffer_load_dword v251, v250, s[20:23], s52 offen offset:0 sc1
v_exp_f32 v58, v58
MF v[188:191],a[188:191],v[24:27],v[188:191]
R16 a[144:147],v241,20480
PK v49,v50,v51
MF v[216:219],a[192:195],v[16:19],v[216:219]
v_exp_f32 v59, v59
MF v[220:223],a[192:195],v[24:27],v[220:223]
PK v56,v56,v57
PK v57,v58,v59
s_waitcnt lgkmcnt(5)
MF v[0:3],a[64:67],a[32:35],v[200:203]
v_exp_f32 v52, v52
MF v[0:3],a[68:71],a[36:39],v[0:3]
R16 a[148:151],v241,21504
s_add_u32 s79, s53, 0x6000
MF v[0:3],a[72:75],a[40:43],v[0:3]
buffer_load_dwordx4 v[72:75], v242, s[20:23], s79 offen offset:1024 sc1
v_exp_f32 v53, v53
MF v[0:3],a[76:79],a[44:47],v[0:3]
R16 a[152:155],v241,22528
PK v50,v52,v53
MF v[8:11],a[64:67],a[48:51],v[204:207]
v_exp_f32 v54, v54
MF v[8:11],a[68:71],a[52:55],v[8:11]
R16 a[156:159],v241,23552
R16 a[160:163],v241,24576
MF v[8:11],a[72:75],a[56:59],v[8:11]
v_exp_f32 v55, v55
MF v[8:11],a[76:79],a[60:63],v[8:11]
R16 a[164:167],v241,25600
PK v51,v54,v55
MF v[4:7],a[80:83],a[32:35],v[200:203]
v_exp_f32 v60, v60
MF v[4:7],a[84:87],a[36:39],v[4:7]
R16 a[168:171],v241,26624
buffer_load_dwordx4 v[76:79], v242, s[20:23], s79 offen offset:2048 sc1
MF v[4:7],a[88:91],a[40:43],v[4:7]
v_exp_f32 v61, v61
MF v[4:7],a[92:95],a[44:47],v[4:7]
R16 a[172:175],v241,27648
R16 a[176:179],v241,28672
buffer_load_dwordx4 v[80:83], v242, s[20:23], s79 offen offset:3072 sc1
MF v[12:15],a[80:83],a[48:51],v[204:207]
v_exp_f32 v62, v62
MF v[12:15],a[84:87],a[52:55],v[12:15]
R16 a[180:183],v241,29696
PK v58,v60,v61
MF v[12:15],a[88:91],a[56:59],v[12:15]
v_exp_f32 v63, v63
MF v[12:15],a[92:95],a[60:63],v[12:15]
R16 a[184:187],v241,30720
PK v59,v62,v63
MF v[16:19],a[96:99],a[32:35],v[200:203]
R16 a[188:191],v241,31744
v_add_u32 v240, 0x8000, v240
MF v[16:19],a[100:103],a[36:39],v[16:19]
v_and_b32 v240, 0x1ffff, v240
s_and_b32 m0, s50, 0x1ffff
MF v[16:19],a[104:107],a[40:43],v[16:19]
s_xor_b32 m0, m0, 0x14000
s_add_u32 s79, s53, 0x7000
MF v[16:19],a[108:111],a[44:47],v[16:19]
buffer_load_dwordx4 v[84:87], v242, s[20:23], s79 offen offset:0 sc1
MF v[24:27],a[96:99],a[48:51],v[204:207]
v_exp_f32 v0, v0
MF v[24:27],a[100:103],a[52:55],v[24:27]
buffer_load_dwordx4 v[88:91], v242, s[20:23], s79 offen offset:1024 sc1
s_waitcnt vmcnt(37)
s_barrier
MF v[24:27],a[104:107],a[56:59],v[24:27]
v_exp_f32 v1, v1
MF v[24:27],a[108:111],a[60:63],v[24:27]
R16 a[64:67],v240,0
PK v0,v0,v1
MF v[20:23],a[112:115],a[32:35],v[200:203]
v_exp_f32 v2, v2
MF v[20:23],a[116:119],a[36:39],v[20:23]
R16 a[68:71],v240,1024
buffer_load_dwordx4 v242, s[12:15], s50 offen offset:0 sc1 lds
MF v[20:23],a[120:123],a[40:43],v[20:23]
buffer_load_dwordx4 v[92:95], v242, s[20:23], s79 offen offset:2048 sc1
v_exp_f32 v3, v3
MF v[20:23],a[124:127],a[44:47],v[20:23]
R16 a[72:75],v240,2048
PK v1,v2,v3
MF v[28:31],a[112:115],a[48:51],v[204:207]
v_exp_f32 v8, v8
MF v[28:31],a[116:119],a[52:55],v[28:31]
R16 a[76:79],v240,3072
buffer_load_dwordx4 v242, s[12:15], s50 offen offset:1024 sc1 lds
MF v[28:31],a[120:123],a[56:59],v[28:31]
v_exp_f32 v9, v9
MF v[28:31],a[124:127],a[60:63],v[28:31]
R16 a[80:83],v240,4096
buffer_load_dwordx4 v[96:99], v242, s[20:23], s79 offen offset:3072 sc1
s_waitcnt lgkmcnt(5)
MF v[128:131],a[128:131],v[32:35],v[128:131]
v_exp_f32 v10, v10
MF v[160:163],a[128:131],v[40:43],v[160:163]
R16 a[84:87],v240,5120
buffer_load_dwordx4 v242, s[12:15], s50 offen offset:2048 sc1 lds
MF v[132:135],a[132:135],v[32:35],v[132:135]
v_exp_f32 v11, v11
MF v[164:167],a[132:135],v[40:43],v[164:167]
R16 a[88:91],v240,6144
PK v8,v8,v9
MF v[136:139],a[136:139],v[32:35],v[136:139]
v_exp_f32 v4, v4
MF v[168:171],a[136:139],v[40:43],v[168:171]
R16 a[92:95],v240,7168
buffer_load_dwordx4 v242, s[12:15], s50 offen offset:3072 sc1 lds
MF v[140:143],a[140:143],v[32:35],v[140:143]
v_exp_f32 v5, v5
MF v[172:175],a[140:143],v[40:43],v[172:175]
R16 a[96:99],v240,8192
s_add_u32 s50, s50, 0x8000
s_waitcnt vmcnt(11)
MF v[144:147],a[144:147],v[32:35],v[144:147]
v_exp_f32 v6, v6
v_cmp_ne_u32 vcc, s37, v251
MF v[176:179],a[144:147],v[40:43],v[176:179]
R16 a[100:103],v240,9216
PK v9,v10,v11
s_or_b64 s[46:47], s[46:47], vcc
MF v[148:151],a[148:151],v[32:35],v[148:151]
v_exp_f32 v7, v7
s_lshl_b32 s48, s28, 13
MF v[180:183],a[148:151],v[40:43],v[180:183]
R16 a[104:107],v240,10240
PK v2,v4,v5
s_add_u32 m0, s48, 0x20000
MF v[152:155],a[152:155],v[32:35],v[152:155]
buffer_load_dwordx4 v242, s[20:23], s60 offen offset:0 sc1 lds
v_exp_f32 v12, v12
MF v[184:187],a[152:155],v[40:43],v[184:187]
R16 a[108:111],v240,11264
buffer_load_dwordx4 v242, s[20:23], s60 offen offset:1024 sc1 lds
PK v3,v6,v7
MF v[156:159],a[156:159],v[32:35],v[156:159]
v_exp_f32 v13, v13
MF v[188:191],a[156:159],v[40:43],v[188:191]
R16 a[112:115],v240,12288
PK v10,v12,v13
MF v[216:219],a[192:195],v[32:35],v[216:219]
v_exp_f32 v14, v14
MF v[220:223],a[192:195],v[40:43],v[220:223]
R16 a[116:119],v240,13312
buffer_load_dwordx4 v242, s[20:23], s60 offen offset:2048 sc1 lds
MF v[128:131],a[160:163],v[48:51],v[128:131]
buffer_load_dwordx4 v242, s[20:23], s60 offen offset:3072 sc1 lds
v_exp_f32 v15, v15
MF v[160:163],a[160:163],v[56:59],v[160:163]
R16 a[120:123],v240,14336
PK v11,v14,v15
MF v[132:135],a[164:167],v[48:51],v[132:135]
v_exp_f32 v16, v16
MF v[164:167],a[164:167],v[56:59],v[164:167]
R16 a[124:127],v240,15360
v_add_u32 v241, 0x8000, v241
s_add_u32 m0, m0, 0x1000
MF v[136:139],a[168:171],v[48:51],v[136:139]
v_exp_f32 v17, v17
s_add_u32 s48, s60, 0x1000
MF v[168:171],a[168:171],v[56:59],v[168:171]
v_and_b32 v241, 0x1ffff, v241
PK v16,v16,v17
MF v[140:143],a[172:175],v[48:51],v[140:143]
v_exp_f32 v18, v18
MF v[172:175],a[172:175],v[56:59],v[172:175]
R16 a[128:131],v241,16384
buffer_load_dwordx4 v242, s[20:23], s48 offen offset:0 sc1 lds
buffer_load_dwordx4 v242, s[20:23], s48 offen offset:1024 sc1 lds
MF v[144:147],a[176:179],v[48:51],v[144:147]
v_exp_f32 v19, v19
MF v[176:179],a[176:179],v[56:59],v[176:179]
R16 a[132:135],v241,17408
PK v17,v18,v19
MF v[148:151],a[180:183],v[48:51],v[148:151]
v_exp_f32 v24, v24
MF v[180:183],a[180:183],v[56:59],v[180:183]
R16 a[136:139],v241,18432
buffer_load_dwordx4 v242, s[20:23], s48 offen offset:2048 sc1 lds
MF v[152:155],a[184:187],v[48:51],v[152:155]
v_exp_f32 v25, v25
MF v[184:187],a[184:187],v[56:59],v[184:187]
R16 a[140:143],v241,19456
PK v24,v24,v25
MF v[156:159],a[188:191],v[48:51],v[156:159]
v_exp_f32 v26, v26
MF v[188:191],a[188:191],v[56:59],v[188:191]
R16 a[144:147],v241,20480
buffer_load_dwordx4 v242, s[20:23], s48 offen offset:3072 sc1 lds
MF v[216:219],a[192:195],v[48:51],v[216:219]
v_exp_f32 v27, v27
MF v[220:223],a[192:195],v[56:59],v[220:223]
PK v25,v26,v27
s_waitcnt lgkmcnt(5)
MF v[32:35],a[64:67],a[32:35],v[200:203]
v_exp_f32 v20, v20
MF v[32:35],a[68:71],a[36:39],v[32:35]
R16 a[148:151],v241,21504
s_add_u32 s79, s53, 0x8000
MF v[32:35],a[72:75],a[40:43],v[32:35]
buffer_load_dwordx4 v[100:103], v242, s[20:23], s79 offen offset:0 sc1
v_exp_f32 v21, v21
MF v[32:35],a[76:79],a[44:47],v[32:35]
R16 a[152:155],v241,22528
PK v18,v20,v21
MF v[40:43],a[64:67],a[48:51],v[204:207]
v_exp_f32 v22, v22
MF v[40:43],a[68:71],a[52:55],v[40:43]
R16 a[156:159],v241,23552
R16 a[160:163],v241,24576
MF v[40:43],a[72:75],a[56:59],v[40:43]
v_exp_f32 v23, v23
MF v[40:43],a[76:79],a[60:63],v[40:43]
R16 a[164:167],v241,25600
PK v19,v22,v23
MF v[36:39],a[80:83],a[32:35],v[200:203]
v_exp_f32 v28, v28
MF v[36:39],a[84:87],a[36:39],v[36:39]
R16 a[168:171],v241,26624
buffer_load_dwordx4 v[104:107], v242, s[20:23], s79 offen offset:1024 sc1
MF v[36:39],a[88:91],a[40:43],v[36:39]
v_exp_f32 v29, v29
MF v[36:39],a[92:95],a[44:47],v[36:39]
R16 a[172:175],v241,27648
R16 a[176:179],v241,28672
buffer_load_dwordx4 v[108:111], v242, s[20:23], s79 offen offset:2048 sc1
MF v[44:47],a[80:83],a[48:51],v[204:207]
v_exp_f32 v30, v30
MF v[44:47],a[84:87],a[52:55],v[44:47]
R16 a[180:183],v241,29696
PK v26,v28,v29
MF v[44:47],a[88:91],a[56:59],v[44:47]
v_exp_f32 v31, v31
MF v[44:47],a[92:95],a[60:63],v[44:47]
R16 a[184:187],v241,30720
PK v27,v30,v31
MF v[48:51],a[96:99],a[32:35],v[200:203]
R16 a[188:191],v241,31744
v_add_u32 v240, 0x8000, v240
MF v[48:51],a[100:103],a[36:39],v[48:51]
v_and_b32 v240, 0x1ffff, v240
s_and_b32 m0, s50, 0x1ffff
MF v[48:51],a[104:107],a[40:43],v[48:51]
buffer_load_dwordx4 v[112:115], v242, s[20:23], s79 offen offset:3072 sc1
s_xor_b32 m0, m0, 0x14000
MF v[48:51],a[108:111],a[44:47],v[48:51]
MF v[56:59],a[96:99],a[48:51],v[204:207]
v_exp_f32 v32, v32
MF v[56:59],a[100:103],a[52:55],v[56:59]
s_waitcnt vmcnt(31)
s_barrier
s_add_u32 s79, s53, 0x9000
MF v[56:59],a[104:107],a[56:59],v[56:59]
buffer_load_dwordx4 v[116:119], v242, s[20:23], s79 offen offset:0 sc1
v_exp_f32 v33, v33
MF v[56:59],a[108:111],a[60:63],v[56:59]
R16 a[64:67],v240,0
PK v32,v32,v33
MF v[52:55],a[112:115],a[32:35],v[200:203]
v_exp_f32 v34, v34
MF v[52:55],a[116:119],a[36:39],v[52:55]
R16 a[68:71],v240,1024
buffer_load_dwordx4 v242, s[12:15], s50 offen offset:0 sc1 lds
MF v[52:55],a[120:123],a[40:43],v[52:55]
buffer_load_dwordx4 v[120:123], v242, s[20:23], s79 offen offset:1024 sc1
v_exp_f32 v35, v35
MF v[52:55],a[124:127],a[44:47],v[52:55]
R16 a[72:75],v240,2048
PK v33,v34,v35
MF v[60:63],a[112:115],a[48:51],v[204:207]
v_exp_f32 v40, v40
MF v[60:63],a[116:119],a[52:55],v[60:63]
R16 a[76:79],v240,3072
buffer_load_dwordx4 v242, s[12:15], s50 offen offset:1024 sc1 lds
MF v[60:63],a[120:123],a[56:59],v[60:63]
v_exp_f32 v41, v41
MF v[60:63],a[124:127],a[60:63],v[60:63]
R16 a[80:83],v240,4096
buffer_load_dwordx4 v[124:127], v242, s[20:23], s79 offen offset:2048 sc1
s_waitcnt lgkmcnt(5)
MF v[128:131],a[128:131],v[0:3],v[128:131]
v_exp_f32 v42, v42
MF v[160:163],a[128:131],v[8:11],v[160:163]
R16 a[84:87],v240,5120
buffer_load_dwordx4 v242, s[12:15], s50 offen offset:2048 sc1 lds
MF v[132:135],a[132:135],v[0:3],v[132:135]
v_exp_f32 v43, v43
MF v[164:167],a[132:135],v[8:11],v[164:167]
R16 a[88:91],v240,6144
PK v40,v40,v41
MF v[136:139],a[136:139],v[0:3],v[136:139]
v_exp_f32 v36, v36
MF v[168:171],a[136:139],v[8:11],v[168:171]
R16 a[92:95],v240,7168
buffer_load_dwordx4 v242, s[12:15], s50 offen offset:3072 sc1 lds
MF v[140:143],a[140:143],v[0:3],v[140:143]
v_exp_f32 v37, v37
MF v[172:175],a[140:143],v[8:11],v[172:175]
R16 a[96:99],v240,8192
s_add_u32 s50, s50, 0x8000
MF v[144:147],a[144:147],v[0:3],v[144:147]
v_exp_f32 v38, v38
MF v[176:179],a[144:147],v[8:11],v[176:179]
R16 a[100:103],v240,9216
PK v41,v42,v43
s_cmp_lt_u32 s28, 2
s_cselect_b32 s48, 0x8000, 0
s_xor_b32 s48, s48, s54
MF v[148:151],a[148:151],v[0:3],v[148:151]
v_exp_f32 v39, v39
s_xor_b32 m0, s48, 0x8000
MF v[180:183],a[148:151],v[8:11],v[180:183]
R16 a[104:107],v240,10240
buffer_load_dwordx4 v242, s[20:23], s61 offen offset:0 sc1 lds
PK v34,v36,v37
MF v[152:155],a[152:155],v[0:3],v[152:155]
buffer_load_dwordx4 v242, s[20:23], s61 offen offset:1024 sc1 lds
v_exp_f32 v44, v44
MF v[184:187],a[152:155],v[8:11],v[184:187]
R16 a[108:111],v240,11264
PK v35,v38,v39
MF v[156:159],a[156:159],v[0:3],v[156:159]
v_exp_f32 v45, v45
MF v[188:191],a[156:159],v[8:11],v[188:191]
R16 a[112:115],v240,12288
PK v42,v44,v45
MF v[216:219],a[192:195],v[0:3],v[216:219]
v_exp_f32 v46, v46
MF v[220:223],a[192:195],v[8:11],v[220:223]
R16 a[116:119],v240,13312
buffer_load_dwordx4 v242, s[20:23], s61 offen offset:2048 sc1 lds
MF v[128:131],a[160:163],v[16:19],v[128:131]
buffer_load_dwordx4 v242, s[20:23], s61 offen offset:3072 sc1 lds
v_exp_f32 v47, v47
MF v[160:163],a[160:163],v[24:27],v[160:163]
R16 a[120:123],v240,14336
PK v43,v46,v47
MF v[132:135],a[164:167],v[16:19],v[132:135]
v_exp_f32 v48, v48
MF v[164:167],a[164:167],v[24:27],v[164:167]
R16 a[124:127],v240,15360
v_add_u32 v241, 0x8000, v241
s_add_u32 m0, m0, 0x1000
MF v[136:139],a[168:171],v[16:19],v[136:139]
v_exp_f32 v49, v49
s_add_u32 s48, s61, 0x1000
MF v[168:171],a[168:171],v[24:27],v[168:171]
v_and_b32 v241, 0x1ffff, v241
PK v48,v48,v49
MF v[140:143],a[172:175],v[16:19],v[140:143]
v_exp_f32 v50, v50
MF v[172:175],a[172:175],v[24:27],v[172:175]
R16 a[128:131],v241,16384
buffer_load_dwordx4 v242, s[20:23], s48 offen offset:0 sc1 lds
buffer_load_dwordx4 v242, s[20:23], s48 offen offset:1024 sc1 lds
MF v[144:147],a[176:179],v[16:19],v[144:147]
v_exp_f32 v51, v51
MF v[176:179],a[176:179],v[24:27],v[176:179]
R16 a[132:135],v241,17408
PK v49,v50,v51
MF v[148:151],a[180:183],v[16:19],v[148:151]
v_exp_f32 v56, v56
MF v[180:183],a[180:183],v[24:27],v[180:183]
R16 a[136:139],v241,18432
buffer_load_dwordx4 v242, s[20:23], s48 offen offset:2048 sc1 lds
MF v[152:155],a[184:187],v[16:19],v[152:155]
)A"
R"A(v_exp_f32 v57, v57
MF v[184:187],a[184:187],v[24:27],v[184:187]
R16 a[140:143],v241,19456
PK v56,v56,v57
MF v[156:159],a[188:191],v[16:19],v[156:159]
v_exp_f32 v58, v58
MF v[188:191],a[188:191],v[24:27],v[188:191]
R16 a[144:147],v241,20480
buffer_load_dwordx4 v242, s[20:23], s48 offen offset:3072 sc1 lds
MF v[216:219],a[192:195],v[16:19],v[216:219]
v_exp_f32 v59, v59
MF v[220:223],a[192:195],v[24:27],v[220:223]
PK v57,v58,v59
s_waitcnt lgkmcnt(5)
L_EE_%=:
s_cmp_eq_u32 s28, 1
s_cbranch_scc1 L_E01_%=
s_cmp_eq_u32 s28, 2
s_cbranch_scc1 L_E02_%=
s_cmp_eq_u32 s28, 3
s_cbranch_scc1 L_E03_%=
MF v[0:3],a[64:67],a[32:35],v[232:235]
v_exp_f32 v52, v52
MF v[0:3],a[68:71],a[36:39],v[0:3]
R16 a[148:151],v241,21504
MF v[0:3],a[72:75],a[40:43],v[0:3]
v_exp_f32 v53, v53
MF v[0:3],a[76:79],a[44:47],v[0:3]
R16 a[152:155],v241,22528
PK v50,v52,v53
MF v[8:11],a[64:67],a[48:51],v[204:207]
v_exp_f32 v54, v54
MF v[8:11],a[68:71],a[52:55],v[8:11]
R16 a[156:159],v241,23552
R16 a[160:163],v241,24576
MF v[8:11],a[72:75],a[56:59],v[8:11]
v_exp_f32 v55, v55
MF v[8:11],a[76:79],a[60:63],v[8:11]
R16 a[164:167],v241,25600
PK v51,v54,v55
v_exp_f32 v60, v60
R16 a[168:171],v241,26624
v_exp_f32 v61, v61
R16 a[172:175],v241,27648
R16 a[176:179],v241,28672
MF v[12:15],a[80:83],a[48:51],v[236:239]
v_exp_f32 v62, v62
MF v[12:15],a[84:87],a[52:55],v[12:15]
R16 a[180:183],v241,29696
PK v58,v60,v61
MF v[12:15],a[88:91],a[56:59],v[12:15]
v_exp_f32 v63, v63
MF v[12:15],a[92:95],a[60:63],v[12:15]
R16 a[184:187],v241,30720
PK v59,v62,v63
R16 a[188:191],v241,31744
v_add_u32 v240, 0x8000, v240
v_and_b32 v240, 0x1ffff, v240
v_exp_f32 v0, v0
s_waitcnt vmcnt(8)
s_barrier
v_exp_f32 v1, v1
R16 a[64:67],v240,0
PK v0,v0,v1
v_exp_f32 v2, v2
R16 a[68:71],v240,1024
v_exp_f32 v3, v3
R16 a[72:75],v240,2048
PK v1,v2,v3
v_exp_f32 v8, v8
R16 a[76:79],v240,3072
v_exp_f32 v9, v9
R16 a[80:83],v240,4096
s_waitcnt lgkmcnt(5)
MF v[128:131],a[128:131],v[32:35],v[128:131]
v_exp_f32 v10, v10
MF v[160:163],a[128:131],v[40:43],v[160:163]
R16 a[84:87],v240,5120
PK v8,v8,v9
MF v[132:135],a[132:135],v[32:35],v[132:135]
v_exp_f32 v11, v11
MF v[164:167],a[132:135],v[40:43],v[164:167]
R16 a[88:91],v240,6144
PK v9,v10,v11
MF v[136:139],a[136:139],v[32:35],v[136:139]
MF v[168:171],a[136:139],v[40:43],v[168:171]
R16 a[92:95],v240,7168
MF v[140:143],a[140:143],v[32:35],v[140:143]
MF v[172:175],a[140:143],v[40:43],v[172:175]
R16 a[96:99],v240,8192
v_mov_b32 v2, 0
MF v[144:147],a[144:147],v[32:35],v[144:147]
MF v[176:179],a[144:147],v[40:43],v[176:179]
R16 a[100:103],v240,9216
MF v[148:151],a[148:151],v[32:35],v[148:151]
MF v[180:183],a[148:151],v[40:43],v[180:183]
R16 a[104:107],v240,10240
v_mov_b32 v3, 0
MF v[152:155],a[152:155],v[32:35],v[152:155]
v_exp_f32 v12, v12
MF v[184:187],a[152:155],v[40:43],v[184:187]
R16 a[108:111],v240,11264
MF v[156:159],a[156:159],v[32:35],v[156:159]
v_exp_f32 v13, v13
MF v[188:191],a[156:159],v[40:43],v[188:191]
R16 a[112:115],v240,12288
PK v10,v12,v13
MF v[216:219],a[192:195],v[32:35],v[216:219]
v_exp_f32 v14, v14
MF v[220:223],a[192:195],v[40:43],v[220:223]
R16 a[116:119],v240,13312
MF v[128:131],a[160:163],v[48:51],v[128:131]
v_exp_f32 v15, v15
MF v[160:163],a[160:163],v[56:59],v[160:163]
R16 a[120:123],v240,14336
PK v11,v14,v15
MF v[132:135],a[164:167],v[48:51],v[132:135]
MF v[164:167],a[164:167],v[56:59],v[164:167]
R16 a[124:127],v240,15360
v_add_u32 v241, 0x8000, v241
MF v[136:139],a[168:171],v[48:51],v[136:139]
v_and_b32 v241, 0x1ffff, v241
MF v[168:171],a[168:171],v[56:59],v[168:171]
MF v[140:143],a[172:175],v[48:51],v[140:143]
MF v[172:175],a[172:175],v[56:59],v[172:175]
R16 a[128:131],v241,16384
MF v[144:147],a[176:179],v[48:51],v[144:147]
MF v[176:179],a[176:179],v[56:59],v[176:179]
R16 a[132:135],v241,17408
MF v[148:151],a[180:183],v[48:51],v[148:151]
MF v[180:183],a[180:183],v[56:59],v[180:183]
R16 a[136:139],v241,18432
MF v[152:155],a[184:187],v[48:51],v[152:155]
MF v[184:187],a[184:187],v[56:59],v[184:187]
R16 a[140:143],v241,19456
MF v[156:159],a[188:191],v[48:51],v[156:159]
MF v[188:191],a[188:191],v[56:59],v[188:191]
R16 a[144:147],v241,20480
MF v[216:219],a[192:195],v[48:51],v[216:219]
MF v[220:223],a[192:195],v[56:59],v[220:223]
s_waitcnt lgkmcnt(5)
L_E10_%=:
R16 a[148:151],v241,21504
R16 a[152:155],v241,22528
s_cmp_lt_u32 s28, 2
s_cselect_b32 s48, 0x14000, 0
s_xor_b32 s48, s48, s54
R16 a[156:159],v241,23552
s_xor_b32 m0, s48, 0x4000
R16 a[160:163],v241,24576
buffer_load_dwordx4 v242, s[20:23], s62 offen offset:0 sc1 lds
buffer_load_dwordx4 v242, s[20:23], s62 offen offset:1024 sc1 lds
R16 a[164:167],v241,25600
R16 a[168:171],v241,26624
buffer_load_dwordx4 v242, s[20:23], s62 offen offset:2048 sc1 lds
R16 a[172:175],v241,27648
R16 a[176:179],v241,28672
R16 a[180:183],v241,29696
buffer_load_dwordx4 v242, s[20:23], s62 offen offset:3072 sc1 lds
R16 a[184:187],v241,30720
R16 a[188:191],v241,31744
s_add_u32 m0, m0, 0x1000
s_add_u32 s48, s62, 0x1000
buffer_load_dwordx4 v242, s[20:23], s48 offen offset:0 sc1 lds
s_waitcnt vmcnt(13)
s_barrier
buffer_load_dwordx4 v242, s[20:23], s48 offen offset:1024 sc1 lds
buffer_load_dwordx4 v242, s[20:23], s48 offen offset:2048 sc1 lds
buffer_load_dwordx4 v242, s[20:23], s48 offen offset:3072 sc1 lds
s_waitcnt lgkmcnt(0)
MF v[128:131],a[128:131],v[0:3],v[128:131]
MF v[160:163],a[128:131],v[8:11],v[160:163]
MF v[132:135],a[132:135],v[0:3],v[132:135]
MF v[164:167],a[132:135],v[8:11],v[164:167]
s_add_u32 s79, s53, 0x9000
MF v[136:139],a[136:139],v[0:3],v[136:139]
buffer_load_dwordx4 a[64:67], v242, s[20:23], s79 offen offset:3072 sc1
MF v[168:171],a[136:139],v[8:11],v[168:171]
s_add_u32 s79, s53, 0xa000
MF v[140:143],a[140:143],v[0:3],v[140:143]
buffer_load_dwordx4 a[68:71], v242, s[20:23], s79 offen offset:0 sc1
MF v[172:175],a[140:143],v[8:11],v[172:175]
MF v[144:147],a[144:147],v[0:3],v[144:147]
buffer_load_dwordx4 a[72:75], v242, s[20:23], s79 offen offset:1024 sc1
MF v[176:179],a[144:147],v[8:11],v[176:179]
MF v[148:151],a[148:151],v[0:3],v[148:151]
MF v[180:183],a[148:151],v[8:11],v[180:183]
buffer_load_dwordx4 a[76:79], v242, s[20:23], s79 offen offset:2048 sc1
MF v[152:155],a[152:155],v[0:3],v[152:155]
MF v[184:187],a[152:155],v[8:11],v[184:187]
MF v[156:159],a[156:159],v[0:3],v[156:159]
buffer_load_dwordx4 a[80:83], v242, s[20:23], s79 offen offset:3072 sc1
MF v[188:191],a[156:159],v[8:11],v[188:191]
MF v[216:219],a[192:195],v[0:3],v[216:219]
MF v[220:223],a[192:195],v[8:11],v[220:223]
s_add_u32 s79, s53, 0xb000
buffer_load_dwordx4 a[84:87], v242, s[20:23], s79 offen offset:0 sc1
v_add_u32 v241, 0x8000, v241
v_and_b32 v241, 0x1ffff, v241
buffer_load_dwordx4 a[88:91], v242, s[20:23], s79 offen offset:1024 sc1
buffer_load_dwordx4 a[92:95], v242, s[20:23], s79 offen offset:2048 sc1
R16 a[128:131],v241,16384
v_mov_b32 v252, s89
buffer_load_dwordx4 a[96:99], v242, s[20:23], s79 offen offset:3072 sc1
v_mov_b32 v253, 0
R16 a[132:135],v241,17408
s_waitcnt vmcnt(19)
buffer_store_dword v252, v253, s[20:23], s86 offen offset:0 sc1
R16 a[136:139],v241,18432
s_add_u32 s79, s53, 0xc000
buffer_load_dwordx4 a[100:103], v242, s[20:23], s79 offen offset:0 sc1
R16 a[140:143],v241,19456
buffer_load_dwordx4 a[104:107], v242, s[20:23], s79 offen offset:1024 sc1
R16 a[144:147],v241,20480
R16 a[148:151],v241,21504
R16 a[152:155],v241,22528
R16 a[156:159],v241,23552
R16 a[160:163],v241,24576
R16 a[164:167],v241,25600
R16 a[168:171],v241,26624
R16 a[172:175],v241,27648
R16 a[176:179],v241,28672
R16 a[180:183],v241,29696
R16 a[184:187],v241,30720
R16 a[188:191],v241,31744
s_and_b32 s48, s54, 0x10000
s_xor_b32 s48, s48, 0x18000
s_lshl_b32 s49, s28, 2
v_mov_b32 v252, s48
v_add_u32 v253, s49, v252
s_cmp_lg_u64 s[46:47], 0
s_cselect_b32 s49, 2, 0
v_mov_b32 v254, s49
ds_write_b32 v253, v254 offset:0
s_waitcnt vmcnt(32) lgkmcnt(0)
s_barrier
R16 v[248:251],v252,0
s_add_u32 s79, s53, 0xc000
buffer_load_dwordx4 a[108:111], v242, s[20:23], s79 offen offset:2048 sc1
v_add_u32 v240, 0x20000, v242
buffer_load_dwordx4 a[112:115], v242, s[20:23], s79 offen offset:3072 sc1
s_and_b32 s48, s54, 0x10000
v_add_u32 v241, s48, v242
v_xor_b32 v243, 0x10000, v241
s_and_b32 s8, s76, 0xf9
s_add_u32 s8, s8, 0x0
s_add_u32 s79, s53, 0xd000
s_lshl_b32 s8, s8, 3
buffer_load_dwordx4 a[116:119], v242, s[20:23], s79 offen offset:0 sc1
s_add_u32 s8, s8, s80
buffer_load_dwordx4 a[120:123], v242, s[20:23], s79 offen offset:1024 sc1
s_lshl_b32 s8, s8, 13
s_lshl_b32 s15, s28, 11
s_add_u32 s8, s8, s15
s_add_u32 s8, s8, 0x3800000
s_add_u32 s9, s8, 0x8000
buffer_load_dwordx4 a[124:127], v242, s[20:23], s79 offen offset:2048 sc1
s_and_b32 s12, s76, 0xf9
s_lshl_b32 s15, s80, 1
s_add_u32 s12, s12, s15
buffer_load_dwordx4 a[32:35], v242, s[20:23], s79 offen offset:3072 sc1
s_lshl_b32 s12, s12, 3
s_add_u32 s12, s12, 0x0
s_lshl_b32 s12, s12, 13
s_lshl_b32 s15, s28, 11
s_add_u32 s12, s12, s15
s_add_u32 s79, s53, 0xe000
s_add_u32 s12, s12, 0x3800000
buffer_load_dwordx4 a[36:39], v242, s[20:23], s79 offen offset:0 sc1
s_lshl_b32 s10, s76, 2
buffer_load_dwordx4 a[40:43], v242, s[20:23], s79 offen offset:1024 sc1
s_add_u32 s10, s10, s28
s_lshl_b32 s10, s10, 7
s_add_u32 s10, s10, 0x49d0000
s_lshl_b32 s11, s80, 10
s_sub_u32 s11, s10, s11
buffer_load_dwordx4 a[44:47], v242, s[20:23], s79 offen offset:2048 sc1
buffer_load_dwordx4 a[48:51], v242, s[20:23], s79 offen offset:3072 sc1
R16 a[128:131],v240,0
R16 a[132:135],v240,1024
R16 a[136:139],v240,2048
s_add_u32 s79, s53, 0xf000
buffer_load_dwordx4 a[52:55], v242, s[20:23], s79 offen offset:0 sc1
R16 a[140:143],v240,3072
buffer_load_dwordx4 a[56:59], v242, s[20:23], s79 offen offset:1024 sc1
R16 a[144:147],v240,4096
R16 a[148:151],v240,5120
buffer_load_dwordx4 a[60:63], v242, s[20:23], s79 offen offset:2048 sc1
R16 a[152:155],v240,6144
buffer_load_dwordx4 v[224:227], v242, s[20:23], s79 offen offset:3072 sc1
R16 a[156:159],v240,7168
R16 a[160:163],v240,8192
R16 a[164:167],v240,9216
R16 a[168:171],v240,10240
R16 a[172:175],v240,11264
s_nop 7
s_branch L_BE_%=
L_E01_%=:
MF v[0:3],a[64:67],a[32:35],v[200:203]
v_exp_f32 v52, v52
MF v[0:3],a[68:71],a[36:39],v[0:3]
R16 a[148:151],v241,21504
MF v[0:3],a[72:75],a[40:43],v[0:3]
v_exp_f32 v53, v53
MF v[0:3],a[76:79],a[44:47],v[0:3]
R16 a[152:155],v241,22528
PK v50,v52,v53
MF v[8:11],a[64:67],a[48:51],v[204:207]
v_exp_f32 v54, v54
MF v[8:11],a[68:71],a[52:55],v[8:11]
R16 a[156:159],v241,23552
R16 a[160:163],v241,24576
MF v[8:11],a[72:75],a[56:59],v[8:11]
v_exp_f32 v55, v55
MF v[8:11],a[76:79],a[60:63],v[8:11]
R16 a[164:167],v241,25600
PK v51,v54,v55
MF v[4:7],a[80:83],a[32:35],v[200:203]
v_exp_f32 v60, v60
MF v[4:7],a[84:87],a[36:39],v[4:7]
R16 a[168:171],v241,26624
MF v[4:7],a[88:91],a[40:43],v[4:7]
v_exp_f32 v61, v61
MF v[4:7],a[92:95],a[44:47],v[4:7]
R16 a[172:175],v241,27648
R16 a[176:179],v241,28672
MF v[12:15],a[80:83],a[48:51],v[204:207]
v_exp_f32 v62, v62
MF v[12:15],a[84:87],a[52:55],v[12:15]
R16 a[180:183],v241,29696
PK v58,v60,v61
MF v[12:15],a[88:91],a[56:59],v[12:15]
v_exp_f32 v63, v63
MF v[12:15],a[92:95],a[60:63],v[12:15]
R16 a[184:187],v241,30720
PK v59,v62,v63
MF v[16:19],a[96:99],a[32:35],v[232:235]
R16 a[188:191],v241,31744
v_add_u32 v240, 0x8000, v240
MF v[16:19],a[100:103],a[36:39],v[16:19]
v_and_b32 v240, 0x1ffff, v240
MF v[16:19],a[104:107],a[40:43],v[16:19]
MF v[16:19],a[108:111],a[44:47],v[16:19]
MF v[24:27],a[96:99],a[48:51],v[204:207]
v_exp_f32 v0, v0
MF v[24:27],a[100:103],a[52:55],v[24:27]
s_waitcnt vmcnt(8)
s_barrier
MF v[24:27],a[104:107],a[56:59],v[24:27]
v_exp_f32 v1, v1
MF v[24:27],a[108:111],a[60:63],v[24:27]
R16 a[64:67],v240,0
PK v0,v0,v1
v_exp_f32 v2, v2
R16 a[68:71],v240,1024
v_exp_f32 v3, v3
R16 a[72:75],v240,2048
PK v1,v2,v3
MF v[28:31],a[112:115],a[48:51],v[236:239]
v_exp_f32 v8, v8
MF v[28:31],a[116:119],a[52:55],v[28:31]
R16 a[76:79],v240,3072
MF v[28:31],a[120:123],a[56:59],v[28:31]
v_exp_f32 v9, v9
MF v[28:31],a[124:127],a[60:63],v[28:31]
R16 a[80:83],v240,4096
s_waitcnt lgkmcnt(5)
MF v[128:131],a[128:131],v[32:35],v[128:131]
v_exp_f32 v10, v10
MF v[160:163],a[128:131],v[40:43],v[160:163]
R16 a[84:87],v240,5120
PK v8,v8,v9
MF v[132:135],a[132:135],v[32:35],v[132:135]
v_exp_f32 v11, v11
MF v[164:167],a[132:135],v[40:43],v[164:167]
R16 a[88:91],v240,6144
PK v9,v10,v11
MF v[136:139],a[136:139],v[32:35],v[136:139]
v_exp_f32 v4, v4
MF v[168:171],a[136:139],v[40:43],v[168:171]
R16 a[92:95],v240,7168
MF v[140:143],a[140:143],v[32:35],v[140:143]
v_exp_f32 v5, v5
MF v[172:175],a[140:143],v[40:43],v[172:175]
R16 a[96:99],v240,8192
PK v2,v4,v5
MF v[144:147],a[144:147],v[32:35],v[144:147]
v_exp_f32 v6, v6
MF v[176:179],a[144:147],v[40:43],v[176:179]
R16 a[100:103],v240,9216
MF v[148:151],a[148:151],v[32:35],v[148:151]
v_exp_f32 v7, v7
MF v[180:183],a[148:151],v[40:43],v[180:183]
R16 a[104:107],v240,10240
PK v3,v6,v7
MF v[152:155],a[152:155],v[32:35],v[152:155]
v_exp_f32 v12, v12
MF v[184:187],a[152:155],v[40:43],v[184:187]
R16 a[108:111],v240,11264
MF v[156:159],a[156:159],v[32:35],v[156:159]
)A"
R"A(v_exp_f32 v13, v13
MF v[188:191],a[156:159],v[40:43],v[188:191]
R16 a[112:115],v240,12288
PK v10,v12,v13
MF v[216:219],a[192:195],v[32:35],v[216:219]
v_exp_f32 v14, v14
MF v[220:223],a[192:195],v[40:43],v[220:223]
R16 a[116:119],v240,13312
MF v[128:131],a[160:163],v[48:51],v[128:131]
v_exp_f32 v15, v15
MF v[160:163],a[160:163],v[56:59],v[160:163]
R16 a[120:123],v240,14336
PK v11,v14,v15
MF v[132:135],a[164:167],v[48:51],v[132:135]
v_exp_f32 v16, v16
MF v[164:167],a[164:167],v[56:59],v[164:167]
R16 a[124:127],v240,15360
v_add_u32 v241, 0x8000, v241
MF v[136:139],a[168:171],v[48:51],v[136:139]
v_exp_f32 v17, v17
MF v[168:171],a[168:171],v[56:59],v[168:171]
v_and_b32 v241, 0x1ffff, v241
PK v16,v16,v17
MF v[140:143],a[172:175],v[48:51],v[140:143]
v_exp_f32 v18, v18
MF v[172:175],a[172:175],v[56:59],v[172:175]
R16 a[128:131],v241,16384
MF v[144:147],a[176:179],v[48:51],v[144:147]
v_exp_f32 v19, v19
MF v[176:179],a[176:179],v[56:59],v[176:179]
R16 a[132:135],v241,17408
PK v17,v18,v19
MF v[148:151],a[180:183],v[48:51],v[148:151]
v_exp_f32 v24, v24
MF v[180:183],a[180:183],v[56:59],v[180:183]
R16 a[136:139],v241,18432
MF v[152:155],a[184:187],v[48:51],v[152:155]
v_exp_f32 v25, v25
MF v[184:187],a[184:187],v[56:59],v[184:187]
R16 a[140:143],v241,19456
PK v24,v24,v25
MF v[156:159],a[188:191],v[48:51],v[156:159]
v_exp_f32 v26, v26
MF v[188:191],a[188:191],v[56:59],v[188:191]
R16 a[144:147],v241,20480
MF v[216:219],a[192:195],v[48:51],v[216:219]
v_exp_f32 v27, v27
MF v[220:223],a[192:195],v[56:59],v[220:223]
PK v25,v26,v27
s_waitcnt lgkmcnt(5)
L_E11_%=:
R16 a[148:151],v241,21504
s_cmp_lt_u32 s28, 2
s_cselect_b32 s48, 0x14000, 0
s_xor_b32 s48, s48, s54
R16 a[152:155],v241,22528
s_xor_b32 m0, s48, 0x4000
s_nop 0
buffer_load_dwordx4 v242, s[20:23], s62 offen offset:0 sc1 lds
v_mov_b32 v18, 0
R16 a[156:159],v241,23552
R16 a[160:163],v241,24576
buffer_load_dwordx4 v242, s[20:23], s62 offen offset:1024 sc1 lds
R16 a[164:167],v241,25600
v_mov_b32 v19, 0
v_exp_f32 v28, v28
R16 a[168:171],v241,26624
buffer_load_dwordx4 v242, s[20:23], s62 offen offset:2048 sc1 lds
v_exp_f32 v29, v29
R16 a[172:175],v241,27648
R16 a[176:179],v241,28672
v_exp_f32 v30, v30
R16 a[180:183],v241,29696
PK v26,v28,v29
v_exp_f32 v31, v31
R16 a[184:187],v241,30720
PK v27,v30,v31
R16 a[188:191],v241,31744
buffer_load_dwordx4 v242, s[20:23], s62 offen offset:3072 sc1 lds
s_add_u32 m0, m0, 0x1000
s_add_u32 s48, s62, 0x1000
buffer_load_dwordx4 v242, s[20:23], s48 offen offset:0 sc1 lds
s_waitcnt vmcnt(13)
s_barrier
buffer_load_dwordx4 v242, s[20:23], s48 offen offset:1024 sc1 lds
buffer_load_dwordx4 v242, s[20:23], s48 offen offset:2048 sc1 lds
buffer_load_dwordx4 v242, s[20:23], s48 offen offset:3072 sc1 lds
s_waitcnt lgkmcnt(0)
MF v[128:131],a[128:131],v[0:3],v[128:131]
MF v[160:163],a[128:131],v[8:11],v[160:163]
MF v[132:135],a[132:135],v[0:3],v[132:135]
MF v[164:167],a[132:135],v[8:11],v[164:167]
s_add_u32 s79, s53, 0x9000
MF v[136:139],a[136:139],v[0:3],v[136:139]
buffer_load_dwordx4 a[64:67], v242, s[20:23], s79 offen offset:3072 sc1
MF v[168:171],a[136:139],v[8:11],v[168:171]
s_add_u32 s79, s53, 0xa000
MF v[140:143],a[140:143],v[0:3],v[140:143]
buffer_load_dwordx4 a[68:71], v242, s[20:23], s79 offen offset:0 sc1
MF v[172:175],a[140:143],v[8:11],v[172:175]
MF v[144:147],a[144:147],v[0:3],v[144:147]
buffer_load_dwordx4 a[72:75], v242, s[20:23], s79 offen offset:1024 sc1
MF v[176:179],a[144:147],v[8:11],v[176:179]
MF v[148:151],a[148:151],v[0:3],v[148:151]
MF v[180:183],a[148:151],v[8:11],v[180:183]
buffer_load_dwordx4 a[76:79], v242, s[20:23], s79 offen offset:2048 sc1
MF v[152:155],a[152:155],v[0:3],v[152:155]
MF v[184:187],a[152:155],v[8:11],v[184:187]
MF v[156:159],a[156:159],v[0:3],v[156:159]
buffer_load_dwordx4 a[80:83], v242, s[20:23], s79 offen offset:3072 sc1
MF v[188:191],a[156:159],v[8:11],v[188:191]
MF v[216:219],a[192:195],v[0:3],v[216:219]
MF v[220:223],a[192:195],v[8:11],v[220:223]
s_add_u32 s79, s53, 0xb000
MF v[128:131],a[160:163],v[16:19],v[128:131]
buffer_load_dwordx4 a[84:87], v242, s[20:23], s79 offen offset:0 sc1
MF v[160:163],a[160:163],v[24:27],v[160:163]
v_add_u32 v241, 0x8000, v241
v_and_b32 v241, 0x1ffff, v241
MF v[132:135],a[164:167],v[16:19],v[132:135]
buffer_load_dwordx4 a[88:91], v242, s[20:23], s79 offen offset:1024 sc1
MF v[164:167],a[164:167],v[24:27],v[164:167]
MF v[136:139],a[168:171],v[16:19],v[136:139]
MF v[168:171],a[168:171],v[24:27],v[168:171]
buffer_load_dwordx4 a[92:95], v242, s[20:23], s79 offen offset:2048 sc1
MF v[140:143],a[172:175],v[16:19],v[140:143]
MF v[172:175],a[172:175],v[24:27],v[172:175]
R16 a[128:131],v241,16384
v_mov_b32 v252, s89
MF v[144:147],a[176:179],v[16:19],v[144:147]
buffer_load_dwordx4 a[96:99], v242, s[20:23], s79 offen offset:3072 sc1
v_mov_b32 v253, 0
MF v[176:179],a[176:179],v[24:27],v[176:179]
R16 a[132:135],v241,17408
s_waitcnt vmcnt(19)
MF v[148:151],a[180:183],v[16:19],v[148:151]
buffer_store_dword v252, v253, s[20:23], s86 offen offset:0 sc1
MF v[180:183],a[180:183],v[24:27],v[180:183]
R16 a[136:139],v241,18432
s_add_u32 s79, s53, 0xc000
MF v[152:155],a[184:187],v[16:19],v[152:155]
buffer_load_dwordx4 a[100:103], v242, s[20:23], s79 offen offset:0 sc1
MF v[184:187],a[184:187],v[24:27],v[184:187]
R16 a[140:143],v241,19456
buffer_load_dwordx4 a[104:107], v242, s[20:23], s79 offen offset:1024 sc1
MF v[156:159],a[188:191],v[16:19],v[156:159]
MF v[188:191],a[188:191],v[24:27],v[188:191]
R16 a[144:147],v241,20480
MF v[216:219],a[192:195],v[16:19],v[216:219]
MF v[220:223],a[192:195],v[24:27],v[220:223]
R16 a[148:151],v241,21504
R16 a[152:155],v241,22528
R16 a[156:159],v241,23552
R16 a[160:163],v241,24576
R16 a[164:167],v241,25600
R16 a[168:171],v241,26624
R16 a[172:175],v241,27648
R16 a[176:179],v241,28672
R16 a[180:183],v241,29696
R16 a[184:187],v241,30720
R16 a[188:191],v241,31744
s_and_b32 s48, s54, 0x10000
s_xor_b32 s48, s48, 0x18000
s_lshl_b32 s49, s28, 2
v_mov_b32 v252, s48
v_add_u32 v253, s49, v252
s_cmp_lg_u64 s[46:47], 0
s_cselect_b32 s49, 2, 0
v_mov_b32 v254, s49
ds_write_b32 v253, v254 offset:0
s_waitcnt vmcnt(32) lgkmcnt(0)
s_barrier
R16 v[248:251],v252,0
s_add_u32 s79, s53, 0xc000
buffer_load_dwordx4 a[108:111], v242, s[20:23], s79 offen offset:2048 sc1
v_add_u32 v240, 0x20000, v242
buffer_load_dwordx4 a[112:115], v242, s[20:23], s79 offen offset:3072 sc1
s_and_b32 s48, s54, 0x10000
v_add_u32 v241, s48, v242
v_xor_b32 v243, 0x10000, v241
s_and_b32 s8, s76, 0xf9
s_add_u32 s8, s8, 0x0
s_add_u32 s79, s53, 0xd000
s_lshl_b32 s8, s8, 3
buffer_load_dwordx4 a[116:119], v242, s[20:23], s79 offen offset:0 sc1
s_add_u32 s8, s8, s80
buffer_load_dwordx4 a[120:123], v242, s[20:23], s79 offen offset:1024 sc1
s_lshl_b32 s8, s8, 13
s_lshl_b32 s15, s28, 11
s_add_u32 s8, s8, s15
s_add_u32 s8, s8, 0x3800000
s_add_u32 s9, s8, 0x8000
buffer_load_dwordx4 a[124:127], v242, s[20:23], s79 offen offset:2048 sc1
s_and_b32 s12, s76, 0xf9
s_lshl_b32 s15, s80, 1
s_add_u32 s12, s12, s15
buffer_load_dwordx4 a[32:35], v242, s[20:23], s79 offen offset:3072 sc1
s_lshl_b32 s12, s12, 3
s_add_u32 s12, s12, 0x0
s_lshl_b32 s12, s12, 13
s_lshl_b32 s15, s28, 11
s_add_u32 s12, s12, s15
s_add_u32 s79, s53, 0xe000
s_add_u32 s12, s12, 0x3800000
buffer_load_dwordx4 a[36:39], v242, s[20:23], s79 offen offset:0 sc1
s_lshl_b32 s10, s76, 2
buffer_load_dwordx4 a[40:43], v242, s[20:23], s79 offen offset:1024 sc1
s_add_u32 s10, s10, s28
s_lshl_b32 s10, s10, 7
s_add_u32 s10, s10, 0x49d0000
s_lshl_b32 s11, s80, 10
s_sub_u32 s11, s10, s11
buffer_load_dwordx4 a[44:47], v242, s[20:23], s79 offen offset:2048 sc1
buffer_load_dwordx4 a[48:51], v242, s[20:23], s79 offen offset:3072 sc1
R16 a[128:131],v240,0
R16 a[132:135],v240,1024
R16 a[136:139],v240,2048
s_add_u32 s79, s53, 0xf000
buffer_load_dwordx4 a[52:55], v242, s[20:23], s79 offen offset:0 sc1
R16 a[140:143],v240,3072
buffer_load_dwordx4 a[56:59], v242, s[20:23], s79 offen offset:1024 sc1
R16 a[144:147],v240,4096
R16 a[148:151],v240,5120
buffer_load_dwordx4 a[60:63], v242, s[20:23], s79 offen offset:2048 sc1
R16 a[152:155],v240,6144
buffer_load_dwordx4 v[224:227], v242, s[20:23], s79 offen offset:3072 sc1
R16 a[156:159],v240,7168
R16 a[160:163],v240,8192
R16 a[164:167],v240,9216
R16 a[168:171],v240,10240
R16 a[172:175],v240,11264
s_nop 7
s_branch L_BE_%=
L_E02_%=:
MF v[0:3],a[64:67],a[32:35],v[200:203]
v_exp_f32 v52, v52
MF v[0:3],a[68:71],a[36:39],v[0:3]
R16 a[148:151],v241,21504
MF v[0:3],a[72:75],a[40:43],v[0:3]
v_exp_f32 v53, v53
MF v[0:3],a[76:79],a[44:47],v[0:3]
R16 a[152:155],v241,22528
PK v50,v52,v53
MF v[8:11],a[64:67],a[48:51],v[204:207]
v_exp_f32 v54, v54
MF v[8:11],a[68:71],a[52:55],v[8:11]
R16 a[156:159],v241,23552
R16 a[160:163],v241,24576
MF v[8:11],a[72:75],a[56:59],v[8:11]
v_exp_f32 v55, v55
MF v[8:11],a[76:79],a[60:63],v[8:11]
R16 a[164:167],v241,25600
PK v51,v54,v55
MF v[4:7],a[80:83],a[32:35],v[200:203]
v_exp_f32 v60, v60
MF v[4:7],a[84:87],a[36:39],v[4:7]
R16 a[168:171],v241,26624
MF v[4:7],a[88:91],a[40:43],v[4:7]
v_exp_f32 v61, v61
MF v[4:7],a[92:95],a[44:47],v[4:7]
R16 a[172:175],v241,27648
R16 a[176:179],v241,28672
MF v[12:15],a[80:83],a[48:51],v[204:207]
v_exp_f32 v62, v62
MF v[12:15],a[84:87],a[52:55],v[12:15]
R16 a[180:183],v241,29696
PK v58,v60,v61
MF v[12:15],a[88:91],a[56:59],v[12:15]
v_exp_f32 v63, v63
MF v[12:15],a[92:95],a[60:63],v[12:15]
R16 a[184:187],v241,30720
PK v59,v62,v63
MF v[16:19],a[96:99],a[32:35],v[200:203]
R16 a[188:191],v241,31744
v_add_u32 v240, 0x8000, v240
MF v[16:19],a[100:103],a[36:39],v[16:19]
v_and_b32 v240, 0x1ffff, v240
MF v[16:19],a[104:107],a[40:43],v[16:19]
MF v[16:19],a[108:111],a[44:47],v[16:19]
MF v[24:27],a[96:99],a[48:51],v[204:207]
v_exp_f32 v0, v0
MF v[24:27],a[100:103],a[52:55],v[24:27]
s_waitcnt vmcnt(8)
s_barrier
MF v[24:27],a[104:107],a[56:59],v[24:27]
v_exp_f32 v1, v1
MF v[24:27],a[108:111],a[60:63],v[24:27]
R16 a[64:67],v240,0
PK v0,v0,v1
MF v[20:23],a[112:115],a[32:35],v[200:203]
v_exp_f32 v2, v2
MF v[20:23],a[116:119],a[36:39],v[20:23]
R16 a[68:71],v240,1024
MF v[20:23],a[120:123],a[40:43],v[20:23]
v_exp_f32 v3, v3
MF v[20:23],a[124:127],a[44:47],v[20:23]
R16 a[72:75],v240,2048
PK v1,v2,v3
MF v[28:31],a[112:115],a[48:51],v[204:207]
v_exp_f32 v8, v8
MF v[28:31],a[116:119],a[52:55],v[28:31]
R16 a[76:79],v240,3072
MF v[28:31],a[120:123],a[56:59],v[28:31]
v_exp_f32 v9, v9
MF v[28:31],a[124:127],a[60:63],v[28:31]
R16 a[80:83],v240,4096
s_waitcnt lgkmcnt(5)
MF v[128:131],a[128:131],v[32:35],v[128:131]
v_exp_f32 v10, v10
MF v[160:163],a[128:131],v[40:43],v[160:163]
R16 a[84:87],v240,5120
PK v8,v8,v9
MF v[132:135],a[132:135],v[32:35],v[132:135]
v_exp_f32 v11, v11
MF v[164:167],a[132:135],v[40:43],v[164:167]
R16 a[88:91],v240,6144
PK v9,v10,v11
MF v[136:139],a[136:139],v[32:35],v[136:139]
v_exp_f32 v4, v4
MF v[168:171],a[136:139],v[40:43],v[168:171]
R16 a[92:95],v240,7168
MF v[140:143],a[140:143],v[32:35],v[140:143]
v_exp_f32 v5, v5
MF v[172:175],a[140:143],v[40:43],v[172:175]
R16 a[96:99],v240,8192
PK v2,v4,v5
MF v[144:147],a[144:147],v[32:35],v[144:147]
v_exp_f32 v6, v6
MF v[176:179],a[144:147],v[40:43],v[176:179]
R16 a[100:103],v240,9216
MF v[148:151],a[148:151],v[32:35],v[148:151]
v_exp_f32 v7, v7
MF v[180:183],a[148:151],v[40:43],v[180:183]
R16 a[104:107],v240,10240
PK v3,v6,v7
MF v[152:155],a[152:155],v[32:35],v[152:155]
v_exp_f32 v12, v12
MF v[184:187],a[152:155],v[40:43],v[184:187]
R16 a[108:111],v240,11264
MF v[156:159],a[156:159],v[32:35],v[156:159]
v_exp_f32 v13, v13
MF v[188:191],a[156:159],v[40:43],v[188:191]
R16 a[112:115],v240,12288
PK v10,v12,v13
MF v[216:219],a[192:195],v[32:35],v[216:219]
v_exp_f32 v14, v14
MF v[220:223],a[192:195],v[40:43],v[220:223]
R16 a[116:119],v240,13312
MF v[128:131],a[160:163],v[48:51],v[128:131]
v_exp_f32 v15, v15
MF v[160:163],a[160:163],v[56:59],v[160:163]
R16 a[120:123],v240,14336
PK v11,v14,v15
MF v[132:135],a[164:167],v[48:51],v[132:135]
v_exp_f32 v16, v16
MF v[164:167],a[164:167],v[56:59],v[164:167]
R16 a[124:127],v240,15360
v_add_u32 v241, 0x8000, v241
MF v[136:139],a[168:171],v[48:51],v[136:139]
v_exp_f32 v17, v17
MF v[168:171],a[168:171],v[56:59],v[168:171]
v_and_b32 v241, 0x1ffff, v241
PK v16,v16,v17
MF v[140:143],a[172:175],v[48:51],v[140:143]
v_exp_f32 v18, v18
MF v[172:175],a[172:175],v[56:59],v[172:175]
R16 a[128:131],v241,16384
MF v[144:147],a[176:179],v[48:51],v[144:147]
v_exp_f32 v19, v19
MF v[176:179],a[176:179],v[56:59],v[176:179]
R16 a[132:135],v241,17408
PK v17,v18,v19
MF v[148:151],a[180:183],v[48:51],v[148:151]
v_exp_f32 v24, v24
MF v[180:183],a[180:183],v[56:59],v[180:183]
R16 a[136:139],v241,18432
MF v[152:155],a[184:187],v[48:51],v[152:155]
v_exp_f32 v25, v25
MF v[184:187],a[184:187],v[56:59],v[184:187]
R16 a[140:143],v241,19456
PK v24,v24,v25
MF v[156:159],a[188:191],v[48:51],v[156:159]
v_exp_f32 v26, v26
MF v[188:191],a[188:191],v[56:59],v[188:191]
R16 a[144:147],v241,20480
MF v[216:219],a[192:195],v[48:51],v[216:219]
v_exp_f32 v27, v27
MF v[220:223],a[192:195],v[56:59],v[220:223]
PK v25,v26,v27
s_waitcnt lgkmcnt(5)
L_E12_%=:
MF v[32:35],a[64:67],a[32:35],v[232:235]
v_exp_f32 v20, v20
MF v[32:35],a[68:71],a[36:39],v[32:35]
R16 a[148:151],v241,21504
s_cmp_lt_u32 s28, 2
s_cselect_b32 s48, 0x14000, 0
s_xor_b32 s48, s48, s54
MF v[32:35],a[72:75],a[40:43],v[32:35]
v_exp_f32 v21, v21
)A"
R"A(s_xor_b32 m0, s48, 0x4000
MF v[32:35],a[76:79],a[44:47],v[32:35]
R16 a[152:155],v241,22528
buffer_load_dwordx4 v242, s[20:23], s62 offen offset:0 sc1 lds
PK v18,v20,v21
MF v[40:43],a[64:67],a[48:51],v[204:207]
v_exp_f32 v22, v22
MF v[40:43],a[68:71],a[52:55],v[40:43]
R16 a[156:159],v241,23552
R16 a[160:163],v241,24576
buffer_load_dwordx4 v242, s[20:23], s62 offen offset:1024 sc1 lds
MF v[40:43],a[72:75],a[56:59],v[40:43]
v_exp_f32 v23, v23
MF v[40:43],a[76:79],a[60:63],v[40:43]
R16 a[164:167],v241,25600
PK v19,v22,v23
v_exp_f32 v28, v28
R16 a[168:171],v241,26624
buffer_load_dwordx4 v242, s[20:23], s62 offen offset:2048 sc1 lds
v_exp_f32 v29, v29
R16 a[172:175],v241,27648
R16 a[176:179],v241,28672
MF v[44:47],a[80:83],a[48:51],v[236:239]
v_exp_f32 v30, v30
MF v[44:47],a[84:87],a[52:55],v[44:47]
R16 a[180:183],v241,29696
PK v26,v28,v29
MF v[44:47],a[88:91],a[56:59],v[44:47]
v_exp_f32 v31, v31
MF v[44:47],a[92:95],a[60:63],v[44:47]
R16 a[184:187],v241,30720
PK v27,v30,v31
R16 a[188:191],v241,31744
buffer_load_dwordx4 v242, s[20:23], s62 offen offset:3072 sc1 lds
s_add_u32 m0, m0, 0x1000
s_add_u32 s48, s62, 0x1000
buffer_load_dwordx4 v242, s[20:23], s48 offen offset:0 sc1 lds
v_exp_f32 v32, v32
s_waitcnt vmcnt(13)
s_barrier
v_exp_f32 v33, v33
buffer_load_dwordx4 v242, s[20:23], s48 offen offset:1024 sc1 lds
PK v32,v32,v33
v_exp_f32 v34, v34
buffer_load_dwordx4 v242, s[20:23], s48 offen offset:2048 sc1 lds
v_exp_f32 v35, v35
s_nop 0
PK v33,v34,v35
v_exp_f32 v40, v40
buffer_load_dwordx4 v242, s[20:23], s48 offen offset:3072 sc1 lds
v_exp_f32 v41, v41
s_waitcnt lgkmcnt(0)
PK v40,v40,v41
MF v[128:131],a[128:131],v[0:3],v[128:131]
v_exp_f32 v42, v42
MF v[160:163],a[128:131],v[8:11],v[160:163]
MF v[132:135],a[132:135],v[0:3],v[132:135]
v_exp_f32 v43, v43
MF v[164:167],a[132:135],v[8:11],v[164:167]
PK v41,v42,v43
s_add_u32 s79, s53, 0x9000
MF v[136:139],a[136:139],v[0:3],v[136:139]
buffer_load_dwordx4 a[64:67], v242, s[20:23], s79 offen offset:3072 sc1
MF v[168:171],a[136:139],v[8:11],v[168:171]
s_add_u32 s79, s53, 0xa000
MF v[140:143],a[140:143],v[0:3],v[140:143]
buffer_load_dwordx4 a[68:71], v242, s[20:23], s79 offen offset:0 sc1
MF v[172:175],a[140:143],v[8:11],v[172:175]
v_mov_b32 v34, 0
MF v[144:147],a[144:147],v[0:3],v[144:147]
buffer_load_dwordx4 a[72:75], v242, s[20:23], s79 offen offset:1024 sc1
MF v[176:179],a[144:147],v[8:11],v[176:179]
MF v[148:151],a[148:151],v[0:3],v[148:151]
MF v[180:183],a[148:151],v[8:11],v[180:183]
buffer_load_dwordx4 a[76:79], v242, s[20:23], s79 offen offset:2048 sc1
v_mov_b32 v35, 0
MF v[152:155],a[152:155],v[0:3],v[152:155]
v_exp_f32 v44, v44
MF v[184:187],a[152:155],v[8:11],v[184:187]
MF v[156:159],a[156:159],v[0:3],v[156:159]
v_exp_f32 v45, v45
MF v[188:191],a[156:159],v[8:11],v[188:191]
buffer_load_dwordx4 a[80:83], v242, s[20:23], s79 offen offset:3072 sc1
PK v42,v44,v45
MF v[216:219],a[192:195],v[0:3],v[216:219]
v_exp_f32 v46, v46
MF v[220:223],a[192:195],v[8:11],v[220:223]
s_add_u32 s79, s53, 0xb000
MF v[128:131],a[160:163],v[16:19],v[128:131]
v_exp_f32 v47, v47
MF v[160:163],a[160:163],v[24:27],v[160:163]
v_add_u32 v241, 0x8000, v241
v_and_b32 v241, 0x1ffff, v241
MF v[132:135],a[164:167],v[16:19],v[132:135]
buffer_load_dwordx4 a[84:87], v242, s[20:23], s79 offen offset:0 sc1
PK v43,v46,v47
MF v[164:167],a[164:167],v[24:27],v[164:167]
buffer_load_dwordx4 a[88:91], v242, s[20:23], s79 offen offset:1024 sc1
MF v[136:139],a[168:171],v[16:19],v[136:139]
MF v[168:171],a[168:171],v[24:27],v[168:171]
buffer_load_dwordx4 a[92:95], v242, s[20:23], s79 offen offset:2048 sc1
MF v[140:143],a[172:175],v[16:19],v[140:143]
MF v[172:175],a[172:175],v[24:27],v[172:175]
R16 a[128:131],v241,16384
v_mov_b32 v252, s89
MF v[144:147],a[176:179],v[16:19],v[144:147]
buffer_load_dwordx4 a[96:99], v242, s[20:23], s79 offen offset:3072 sc1
v_mov_b32 v253, 0
MF v[176:179],a[176:179],v[24:27],v[176:179]
R16 a[132:135],v241,17408
s_waitcnt vmcnt(19)
MF v[148:151],a[180:183],v[16:19],v[148:151]
buffer_store_dword v252, v253, s[20:23], s86 offen offset:0 sc1
MF v[180:183],a[180:183],v[24:27],v[180:183]
R16 a[136:139],v241,18432
s_add_u32 s79, s53, 0xc000
MF v[152:155],a[184:187],v[16:19],v[152:155]
buffer_load_dwordx4 a[100:103], v242, s[20:23], s79 offen offset:0 sc1
MF v[184:187],a[184:187],v[24:27],v[184:187]
R16 a[140:143],v241,19456
buffer_load_dwordx4 a[104:107], v242, s[20:23], s79 offen offset:1024 sc1
MF v[156:159],a[188:191],v[16:19],v[156:159]
MF v[188:191],a[188:191],v[24:27],v[188:191]
R16 a[144:147],v241,20480
MF v[216:219],a[192:195],v[16:19],v[216:219]
MF v[220:223],a[192:195],v[24:27],v[220:223]
R16 a[148:151],v241,21504
R16 a[152:155],v241,22528
R16 a[156:159],v241,23552
R16 a[160:163],v241,24576
R16 a[164:167],v241,25600
R16 a[168:171],v241,26624
R16 a[172:175],v241,27648
R16 a[176:179],v241,28672
R16 a[180:183],v241,29696
R16 a[184:187],v241,30720
R16 a[188:191],v241,31744
s_and_b32 s48, s54, 0x10000
s_xor_b32 s48, s48, 0x18000
s_lshl_b32 s49, s28, 2
v_mov_b32 v252, s48
v_add_u32 v253, s49, v252
s_cmp_lg_u64 s[46:47], 0
s_cselect_b32 s49, 2, 0
v_mov_b32 v254, s49
ds_write_b32 v253, v254 offset:0
s_waitcnt vmcnt(32) lgkmcnt(0)
s_barrier
MF v[128:131],a[128:131],v[32:35],v[128:131]
R16 v[248:251],v252,0
s_add_u32 s79, s53, 0xc000
MF v[160:163],a[128:131],v[40:43],v[160:163]
buffer_load_dwordx4 a[108:111], v242, s[20:23], s79 offen offset:2048 sc1
v_add_u32 v240, 0x20000, v242
MF v[132:135],a[132:135],v[32:35],v[132:135]
buffer_load_dwordx4 a[112:115], v242, s[20:23], s79 offen offset:3072 sc1
s_and_b32 s48, s54, 0x10000
MF v[164:167],a[132:135],v[40:43],v[164:167]
v_add_u32 v241, s48, v242
v_xor_b32 v243, 0x10000, v241
MF v[136:139],a[136:139],v[32:35],v[136:139]
s_and_b32 s8, s76, 0xf9
s_add_u32 s8, s8, 0x0
MF v[168:171],a[136:139],v[40:43],v[168:171]
s_add_u32 s79, s53, 0xd000
s_lshl_b32 s8, s8, 3
MF v[140:143],a[140:143],v[32:35],v[140:143]
buffer_load_dwordx4 a[116:119], v242, s[20:23], s79 offen offset:0 sc1
s_add_u32 s8, s8, s80
MF v[172:175],a[140:143],v[40:43],v[172:175]
buffer_load_dwordx4 a[120:123], v242, s[20:23], s79 offen offset:1024 sc1
s_lshl_b32 s8, s8, 13
MF v[144:147],a[144:147],v[32:35],v[144:147]
s_lshl_b32 s15, s28, 11
s_add_u32 s8, s8, s15
MF v[176:179],a[144:147],v[40:43],v[176:179]
s_add_u32 s8, s8, 0x3800000
s_add_u32 s9, s8, 0x8000
MF v[148:151],a[148:151],v[32:35],v[148:151]
buffer_load_dwordx4 a[124:127], v242, s[20:23], s79 offen offset:2048 sc1
s_and_b32 s12, s76, 0xf9
MF v[180:183],a[148:151],v[40:43],v[180:183]
s_lshl_b32 s15, s80, 1
s_add_u32 s12, s12, s15
MF v[152:155],a[152:155],v[32:35],v[152:155]
buffer_load_dwordx4 a[32:35], v242, s[20:23], s79 offen offset:3072 sc1
s_lshl_b32 s12, s12, 3
MF v[184:187],a[152:155],v[40:43],v[184:187]
s_add_u32 s12, s12, 0x0
s_lshl_b32 s12, s12, 13
MF v[156:159],a[156:159],v[32:35],v[156:159]
s_lshl_b32 s15, s28, 11
s_add_u32 s12, s12, s15
MF v[188:191],a[156:159],v[40:43],v[188:191]
s_add_u32 s79, s53, 0xe000
s_add_u32 s12, s12, 0x3800000
MF v[216:219],a[192:195],v[32:35],v[216:219]
buffer_load_dwordx4 a[36:39], v242, s[20:23], s79 offen offset:0 sc1
s_lshl_b32 s10, s76, 2
MF v[220:223],a[192:195],v[40:43],v[220:223]
buffer_load_dwordx4 a[40:43], v242, s[20:23], s79 offen offset:1024 sc1
s_add_u32 s10, s10, s28
s_lshl_b32 s10, s10, 7
s_add_u32 s10, s10, 0x49d0000
s_lshl_b32 s11, s80, 10
s_sub_u32 s11, s10, s11
buffer_load_dwordx4 a[44:47], v242, s[20:23], s79 offen offset:2048 sc1
buffer_load_dwordx4 a[48:51], v242, s[20:23], s79 offen offset:3072 sc1
R16 a[128:131],v240,0
R16 a[132:135],v240,1024
R16 a[136:139],v240,2048
s_add_u32 s79, s53, 0xf000
buffer_load_dwordx4 a[52:55], v242, s[20:23], s79 offen offset:0 sc1
R16 a[140:143],v240,3072
buffer_load_dwordx4 a[56:59], v242, s[20:23], s79 offen offset:1024 sc1
R16 a[144:147],v240,4096
R16 a[148:151],v240,5120
buffer_load_dwordx4 a[60:63], v242, s[20:23], s79 offen offset:2048 sc1
R16 a[152:155],v240,6144
buffer_load_dwordx4 v[224:227], v242, s[20:23], s79 offen offset:3072 sc1
R16 a[156:159],v240,7168
R16 a[160:163],v240,8192
R16 a[164:167],v240,9216
R16 a[168:171],v240,10240
R16 a[172:175],v240,11264
s_nop 7
s_branch L_BE_%=
L_E03_%=:
MF v[0:3],a[64:67],a[32:35],v[200:203]
v_exp_f32 v52, v52
MF v[0:3],a[68:71],a[36:39],v[0:3]
R16 a[148:151],v241,21504
MF v[0:3],a[72:75],a[40:43],v[0:3]
v_exp_f32 v53, v53
MF v[0:3],a[76:79],a[44:47],v[0:3]
R16 a[152:155],v241,22528
PK v50,v52,v53
MF v[8:11],a[64:67],a[48:51],v[204:207]
v_exp_f32 v54, v54
MF v[8:11],a[68:71],a[52:55],v[8:11]
R16 a[156:159],v241,23552
R16 a[160:163],v241,24576
MF v[8:11],a[72:75],a[56:59],v[8:11]
v_exp_f32 v55, v55
MF v[8:11],a[76:79],a[60:63],v[8:11]
R16 a[164:167],v241,25600
PK v51,v54,v55
MF v[4:7],a[80:83],a[32:35],v[200:203]
v_exp_f32 v60, v60
MF v[4:7],a[84:87],a[36:39],v[4:7]
R16 a[168:171],v241,26624
MF v[4:7],a[88:91],a[40:43],v[4:7]
v_exp_f32 v61, v61
MF v[4:7],a[92:95],a[44:47],v[4:7]
R16 a[172:175],v241,27648
R16 a[176:179],v241,28672
MF v[12:15],a[80:83],a[48:51],v[204:207]
v_exp_f32 v62, v62
MF v[12:15],a[84:87],a[52:55],v[12:15]
R16 a[180:183],v241,29696
PK v58,v60,v61
MF v[12:15],a[88:91],a[56:59],v[12:15]
v_exp_f32 v63, v63
MF v[12:15],a[92:95],a[60:63],v[12:15]
R16 a[184:187],v241,30720
PK v59,v62,v63
MF v[16:19],a[96:99],a[32:35],v[200:203]
R16 a[188:191],v241,31744
v_add_u32 v240, 0x8000, v240
MF v[16:19],a[100:103],a[36:39],v[16:19]
v_and_b32 v240, 0x1ffff, v240
MF v[16:19],a[104:107],a[40:43],v[16:19]
MF v[16:19],a[108:111],a[44:47],v[16:19]
MF v[24:27],a[96:99],a[48:51],v[204:207]
v_exp_f32 v0, v0
MF v[24:27],a[100:103],a[52:55],v[24:27]
s_waitcnt vmcnt(8)
s_barrier
MF v[24:27],a[104:107],a[56:59],v[24:27]
v_exp_f32 v1, v1
MF v[24:27],a[108:111],a[60:63],v[24:27]
R16 a[64:67],v240,0
PK v0,v0,v1
MF v[20:23],a[112:115],a[32:35],v[200:203]
v_exp_f32 v2, v2
MF v[20:23],a[116:119],a[36:39],v[20:23]
R16 a[68:71],v240,1024
MF v[20:23],a[120:123],a[40:43],v[20:23]
v_exp_f32 v3, v3
MF v[20:23],a[124:127],a[44:47],v[20:23]
R16 a[72:75],v240,2048
PK v1,v2,v3
MF v[28:31],a[112:115],a[48:51],v[204:207]
v_exp_f32 v8, v8
MF v[28:31],a[116:119],a[52:55],v[28:31]
R16 a[76:79],v240,3072
MF v[28:31],a[120:123],a[56:59],v[28:31]
v_exp_f32 v9, v9
MF v[28:31],a[124:127],a[60:63],v[28:31]
R16 a[80:83],v240,4096
s_waitcnt lgkmcnt(5)
MF v[128:131],a[128:131],v[32:35],v[128:131]
v_exp_f32 v10, v10
MF v[160:163],a[128:131],v[40:43],v[160:163]
R16 a[84:87],v240,5120
PK v8,v8,v9
MF v[132:135],a[132:135],v[32:35],v[132:135]
v_exp_f32 v11, v11
MF v[164:167],a[132:135],v[40:43],v[164:167]
R16 a[88:91],v240,6144
PK v9,v10,v11
MF v[136:139],a[136:139],v[32:35],v[136:139]
v_exp_f32 v4, v4
MF v[168:171],a[136:139],v[40:43],v[168:171]
R16 a[92:95],v240,7168
MF v[140:143],a[140:143],v[32:35],v[140:143]
v_exp_f32 v5, v5
MF v[172:175],a[140:143],v[40:43],v[172:175]
R16 a[96:99],v240,8192
PK v2,v4,v5
MF v[144:147],a[144:147],v[32:35],v[144:147]
v_exp_f32 v6, v6
MF v[176:179],a[144:147],v[40:43],v[176:179]
R16 a[100:103],v240,9216
MF v[148:151],a[148:151],v[32:35],v[148:151]
v_exp_f32 v7, v7
MF v[180:183],a[148:151],v[40:43],v[180:183]
R16 a[104:107],v240,10240
PK v3,v6,v7
MF v[152:155],a[152:155],v[32:35],v[152:155]
v_exp_f32 v12, v12
MF v[184:187],a[152:155],v[40:43],v[184:187]
R16 a[108:111],v240,11264
MF v[156:159],a[156:159],v[32:35],v[156:159]
v_exp_f32 v13, v13
MF v[188:191],a[156:159],v[40:43],v[188:191]
R16 a[112:115],v240,12288
PK v10,v12,v13
MF v[216:219],a[192:195],v[32:35],v[216:219]
v_exp_f32 v14, v14
MF v[220:223],a[192:195],v[40:43],v[220:223]
R16 a[116:119],v240,13312
MF v[128:131],a[160:163],v[48:51],v[128:131]
v_exp_f32 v15, v15
MF v[160:163],a[160:163],v[56:59],v[160:163]
R16 a[120:123],v240,14336
PK v11,v14,v15
MF v[132:135],a[164:167],v[48:51],v[132:135]
v_exp_f32 v16, v16
MF v[164:167],a[164:167],v[56:59],v[164:167]
R16 a[124:127],v240,15360
v_add_u32 v241, 0x8000, v241
MF v[136:139],a[168:171],v[48:51],v[136:139]
v_exp_f32 v17, v17
MF v[168:171],a[168:171],v[56:59],v[168:171]
v_and_b32 v241, 0x1ffff, v241
PK v16,v16,v17
MF v[140:143],a[172:175],v[48:51],v[140:143]
v_exp_f32 v18, v18
MF v[172:175],a[172:175],v[56:59],v[172:175]
R16 a[128:131],v241,16384
MF v[144:147],a[176:179],v[48:51],v[144:147]
v_exp_f32 v19, v19
MF v[176:179],a[176:179],v[56:59],v[176:179]
R16 a[132:135],v241,17408
PK v17,v18,v19
MF v[148:151],a[180:183],v[48:51],v[148:151]
v_exp_f32 v24, v24
MF v[180:183],a[180:183],v[56:59],v[180:183]
R16 a[136:139],v241,18432
MF v[152:155],a[184:187],v[48:51],v[152:155]
v_exp_f32 v25, v25
MF v[184:187],a[184:187],v[56:59],v[184:187]
R16 a[140:143],v241,19456
PK v24,v24,v25
MF v[156:159],a[188:191],v[48:51],v[156:159]
v_exp_f32 v26, v26
MF v[188:191],a[188:191],v[56:59],v[188:191]
R16 a[144:147],v241,20480
MF v[216:219],a[192:195],v[48:51],v[216:219]
v_exp_f32 v27, v27
MF v[220:223],a[192:195],v[56:59],v[220:223]
PK v25,v26,v27
s_waitcnt lgkmcnt(5)
L_E13_%=:
MF v[32:35],a[64:67],a[32:35],v[200:203]
v_exp_f32 v20, v20
MF v[32:35],a[68:71],a[36:39],v[32:35]
R16 a[148:151],v241,21504
s_cmp_lt_u32 s28, 2
s_cselect_b32 s48, 0x14000, 0
s_xor_b32 s48, s48, s54
MF v[32:35],a[72:75],a[40:43],v[32:35]
v_exp_f32 v21, v21
s_xor_b32 m0, s48, 0x4000
MF v[32:35],a[76:79],a[44:47],v[32:35]
R16 a[152:155],v241,22528
buffer_load_dwordx4 v242, s[20:23], s62 offen offset:0 sc1 lds
PK v18,v20,v21
MF v[40:43],a[64:67],a[48:51],v[204:207]
)A"
R"A(v_exp_f32 v22, v22
MF v[40:43],a[68:71],a[52:55],v[40:43]
R16 a[156:159],v241,23552
R16 a[160:163],v241,24576
buffer_load_dwordx4 v242, s[20:23], s62 offen offset:1024 sc1 lds
MF v[40:43],a[72:75],a[56:59],v[40:43]
v_exp_f32 v23, v23
MF v[40:43],a[76:79],a[60:63],v[40:43]
R16 a[164:167],v241,25600
PK v19,v22,v23
MF v[36:39],a[80:83],a[32:35],v[200:203]
v_exp_f32 v28, v28
MF v[36:39],a[84:87],a[36:39],v[36:39]
R16 a[168:171],v241,26624
buffer_load_dwordx4 v242, s[20:23], s62 offen offset:2048 sc1 lds
MF v[36:39],a[88:91],a[40:43],v[36:39]
v_exp_f32 v29, v29
MF v[36:39],a[92:95],a[44:47],v[36:39]
R16 a[172:175],v241,27648
R16 a[176:179],v241,28672
MF v[44:47],a[80:83],a[48:51],v[204:207]
v_exp_f32 v30, v30
MF v[44:47],a[84:87],a[52:55],v[44:47]
R16 a[180:183],v241,29696
PK v26,v28,v29
MF v[44:47],a[88:91],a[56:59],v[44:47]
v_exp_f32 v31, v31
MF v[44:47],a[92:95],a[60:63],v[44:47]
R16 a[184:187],v241,30720
PK v27,v30,v31
MF v[48:51],a[96:99],a[32:35],v[232:235]
R16 a[188:191],v241,31744
buffer_load_dwordx4 v242, s[20:23], s62 offen offset:3072 sc1 lds
MF v[48:51],a[100:103],a[36:39],v[48:51]
s_add_u32 m0, m0, 0x1000
s_add_u32 s48, s62, 0x1000
MF v[48:51],a[104:107],a[40:43],v[48:51]
buffer_load_dwordx4 v242, s[20:23], s48 offen offset:0 sc1 lds
MF v[48:51],a[108:111],a[44:47],v[48:51]
MF v[56:59],a[96:99],a[48:51],v[204:207]
v_exp_f32 v32, v32
MF v[56:59],a[100:103],a[52:55],v[56:59]
s_waitcnt vmcnt(13)
s_barrier
MF v[56:59],a[104:107],a[56:59],v[56:59]
v_exp_f32 v33, v33
MF v[56:59],a[108:111],a[60:63],v[56:59]
buffer_load_dwordx4 v242, s[20:23], s48 offen offset:1024 sc1 lds
PK v32,v32,v33
v_exp_f32 v34, v34
buffer_load_dwordx4 v242, s[20:23], s48 offen offset:2048 sc1 lds
v_exp_f32 v35, v35
s_nop 0
PK v33,v34,v35
MF v[60:63],a[112:115],a[48:51],v[236:239]
v_exp_f32 v40, v40
MF v[60:63],a[116:119],a[52:55],v[60:63]
buffer_load_dwordx4 v242, s[20:23], s48 offen offset:3072 sc1 lds
MF v[60:63],a[120:123],a[56:59],v[60:63]
v_exp_f32 v41, v41
MF v[60:63],a[124:127],a[60:63],v[60:63]
s_waitcnt lgkmcnt(0)
PK v40,v40,v41
MF v[128:131],a[128:131],v[0:3],v[128:131]
v_exp_f32 v42, v42
MF v[160:163],a[128:131],v[8:11],v[160:163]
MF v[132:135],a[132:135],v[0:3],v[132:135]
v_exp_f32 v43, v43
MF v[164:167],a[132:135],v[8:11],v[164:167]
PK v41,v42,v43
s_add_u32 s79, s53, 0x9000
MF v[136:139],a[136:139],v[0:3],v[136:139]
v_exp_f32 v36, v36
MF v[168:171],a[136:139],v[8:11],v[168:171]
buffer_load_dwordx4 a[64:67], v242, s[20:23], s79 offen offset:3072 sc1
s_add_u32 s79, s53, 0xa000
MF v[140:143],a[140:143],v[0:3],v[140:143]
v_exp_f32 v37, v37
MF v[172:175],a[140:143],v[8:11],v[172:175]
buffer_load_dwordx4 a[68:71], v242, s[20:23], s79 offen offset:0 sc1
PK v34,v36,v37
MF v[144:147],a[144:147],v[0:3],v[144:147]
v_exp_f32 v38, v38
MF v[176:179],a[144:147],v[8:11],v[176:179]
buffer_load_dwordx4 a[72:75], v242, s[20:23], s79 offen offset:1024 sc1
MF v[148:151],a[148:151],v[0:3],v[148:151]
v_exp_f32 v39, v39
MF v[180:183],a[148:151],v[8:11],v[180:183]
buffer_load_dwordx4 a[76:79], v242, s[20:23], s79 offen offset:2048 sc1
PK v35,v38,v39
MF v[152:155],a[152:155],v[0:3],v[152:155]
v_exp_f32 v44, v44
MF v[184:187],a[152:155],v[8:11],v[184:187]
MF v[156:159],a[156:159],v[0:3],v[156:159]
v_exp_f32 v45, v45
MF v[188:191],a[156:159],v[8:11],v[188:191]
buffer_load_dwordx4 a[80:83], v242, s[20:23], s79 offen offset:3072 sc1
PK v42,v44,v45
MF v[216:219],a[192:195],v[0:3],v[216:219]
v_exp_f32 v46, v46
MF v[220:223],a[192:195],v[8:11],v[220:223]
s_add_u32 s79, s53, 0xb000
MF v[128:131],a[160:163],v[16:19],v[128:131]
buffer_load_dwordx4 a[84:87], v242, s[20:23], s79 offen offset:0 sc1
v_exp_f32 v47, v47
MF v[160:163],a[160:163],v[24:27],v[160:163]
v_add_u32 v241, 0x8000, v241
v_and_b32 v241, 0x1ffff, v241
MF v[132:135],a[164:167],v[16:19],v[132:135]
v_exp_f32 v48, v48
MF v[164:167],a[164:167],v[24:27],v[164:167]
buffer_load_dwordx4 a[88:91], v242, s[20:23], s79 offen offset:1024 sc1
PK v43,v46,v47
MF v[136:139],a[168:171],v[16:19],v[136:139]
v_exp_f32 v49, v49
MF v[168:171],a[168:171],v[24:27],v[168:171]
buffer_load_dwordx4 a[92:95], v242, s[20:23], s79 offen offset:2048 sc1
PK v48,v48,v49
MF v[140:143],a[172:175],v[16:19],v[140:143]
v_exp_f32 v50, v50
MF v[172:175],a[172:175],v[24:27],v[172:175]
R16 a[128:131],v241,16384
v_mov_b32 v252, s89
v_mov_b32 v253, 0
MF v[144:147],a[176:179],v[16:19],v[144:147]
v_exp_f32 v51, v51
s_waitcnt vmcnt(18)
MF v[176:179],a[176:179],v[24:27],v[176:179]
R16 a[132:135],v241,17408
buffer_store_dword v252, v253, s[20:23], s86 offen offset:0 sc1
PK v49,v50,v51
MF v[148:151],a[180:183],v[16:19],v[148:151]
v_exp_f32 v56, v56
MF v[180:183],a[180:183],v[24:27],v[180:183]
R16 a[136:139],v241,18432
buffer_load_dwordx4 a[96:99], v242, s[20:23], s79 offen offset:3072 sc1
s_add_u32 s79, s53, 0xc000
MF v[152:155],a[184:187],v[16:19],v[152:155]
buffer_load_dwordx4 a[100:103], v242, s[20:23], s79 offen offset:0 sc1
v_exp_f32 v57, v57
MF v[184:187],a[184:187],v[24:27],v[184:187]
R16 a[140:143],v241,19456
PK v56,v56,v57
MF v[156:159],a[188:191],v[16:19],v[156:159]
v_exp_f32 v58, v58
MF v[188:191],a[188:191],v[24:27],v[188:191]
R16 a[144:147],v241,20480
buffer_load_dwordx4 a[104:107], v242, s[20:23], s79 offen offset:1024 sc1
MF v[216:219],a[192:195],v[16:19],v[216:219]
v_exp_f32 v59, v59
MF v[220:223],a[192:195],v[24:27],v[220:223]
PK v57,v58,v59
R16 a[148:151],v241,21504
R16 a[152:155],v241,22528
R16 a[156:159],v241,23552
R16 a[160:163],v241,24576
R16 a[164:167],v241,25600
R16 a[168:171],v241,26624
R16 a[172:175],v241,27648
R16 a[176:179],v241,28672
R16 a[180:183],v241,29696
R16 a[184:187],v241,30720
R16 a[188:191],v241,31744
s_and_b32 s48, s54, 0x10000
s_xor_b32 s48, s48, 0x18000
s_lshl_b32 s49, s28, 2
v_mov_b32 v252, s48
v_add_u32 v253, s49, v252
s_cmp_lg_u64 s[46:47], 0
s_cselect_b32 s49, 2, 0
v_mov_b32 v254, s49
ds_write_b32 v253, v254 offset:0
s_nop 0
v_mov_b32 v50, 0
s_nop 0
v_mov_b32 v51, 0
v_exp_f32 v60, v60
v_exp_f32 v61, v61
s_nop 0
PK v58,v60,v61
v_exp_f32 v62, v62
v_exp_f32 v63, v63
s_nop 0
PK v59,v62,v63
s_waitcnt vmcnt(32) lgkmcnt(0)
s_barrier
MF v[128:131],a[128:131],v[32:35],v[128:131]
R16 v[248:251],v252,0
s_add_u32 s79, s53, 0xc000
MF v[160:163],a[128:131],v[40:43],v[160:163]
buffer_load_dwordx4 a[108:111], v242, s[20:23], s79 offen offset:2048 sc1
v_add_u32 v240, 0x20000, v242
MF v[132:135],a[132:135],v[32:35],v[132:135]
buffer_load_dwordx4 a[112:115], v242, s[20:23], s79 offen offset:3072 sc1
s_and_b32 s48, s54, 0x10000
MF v[164:167],a[132:135],v[40:43],v[164:167]
v_add_u32 v241, s48, v242
v_xor_b32 v243, 0x10000, v241
MF v[136:139],a[136:139],v[32:35],v[136:139]
s_and_b32 s8, s76, 0xf9
s_add_u32 s8, s8, 0x0
MF v[168:171],a[136:139],v[40:43],v[168:171]
s_add_u32 s79, s53, 0xd000
s_lshl_b32 s8, s8, 3
MF v[140:143],a[140:143],v[32:35],v[140:143]
buffer_load_dwordx4 a[116:119], v242, s[20:23], s79 offen offset:0 sc1
s_add_u32 s8, s8, s80
MF v[172:175],a[140:143],v[40:43],v[172:175]
buffer_load_dwordx4 a[120:123], v242, s[20:23], s79 offen offset:1024 sc1
s_lshl_b32 s8, s8, 13
MF v[144:147],a[144:147],v[32:35],v[144:147]
s_lshl_b32 s15, s28, 11
s_add_u32 s8, s8, s15
MF v[176:179],a[144:147],v[40:43],v[176:179]
s_add_u32 s8, s8, 0x3800000
s_add_u32 s9, s8, 0x8000
MF v[148:151],a[148:151],v[32:35],v[148:151]
buffer_load_dwordx4 a[124:127], v242, s[20:23], s79 offen offset:2048 sc1
s_and_b32 s12, s76, 0xf9
MF v[180:183],a[148:151],v[40:43],v[180:183]
s_lshl_b32 s15, s80, 1
s_add_u32 s12, s12, s15
MF v[152:155],a[152:155],v[32:35],v[152:155]
buffer_load_dwordx4 a[32:35], v242, s[20:23], s79 offen offset:3072 sc1
s_lshl_b32 s12, s12, 3
MF v[184:187],a[152:155],v[40:43],v[184:187]
s_add_u32 s12, s12, 0x0
s_lshl_b32 s12, s12, 13
MF v[156:159],a[156:159],v[32:35],v[156:159]
s_lshl_b32 s15, s28, 11
s_add_u32 s12, s12, s15
MF v[188:191],a[156:159],v[40:43],v[188:191]
s_add_u32 s79, s53, 0xe000
s_add_u32 s12, s12, 0x3800000
MF v[216:219],a[192:195],v[32:35],v[216:219]
buffer_load_dwordx4 a[36:39], v242, s[20:23], s79 offen offset:0 sc1
s_lshl_b32 s10, s76, 2
MF v[220:223],a[192:195],v[40:43],v[220:223]
buffer_load_dwordx4 a[40:43], v242, s[20:23], s79 offen offset:1024 sc1
s_add_u32 s10, s10, s28
MF v[128:131],a[160:163],v[48:51],v[128:131]
s_lshl_b32 s10, s10, 7
s_add_u32 s10, s10, 0x49d0000
MF v[160:163],a[160:163],v[56:59],v[160:163]
s_lshl_b32 s11, s80, 10
s_sub_u32 s11, s10, s11
MF v[132:135],a[164:167],v[48:51],v[132:135]
buffer_load_dwordx4 a[44:47], v242, s[20:23], s79 offen offset:2048 sc1
MF v[164:167],a[164:167],v[56:59],v[164:167]
MF v[136:139],a[168:171],v[48:51],v[136:139]
buffer_load_dwordx4 a[48:51], v242, s[20:23], s79 offen offset:3072 sc1
R16 a[128:131],v240,0
MF v[168:171],a[168:171],v[56:59],v[168:171]
R16 a[132:135],v240,1024
MF v[140:143],a[172:175],v[48:51],v[140:143]
MF v[172:175],a[172:175],v[56:59],v[172:175]
R16 a[136:139],v240,2048
s_add_u32 s79, s53, 0xf000
MF v[144:147],a[176:179],v[48:51],v[144:147]
buffer_load_dwordx4 a[52:55], v242, s[20:23], s79 offen offset:0 sc1
R16 a[140:143],v240,3072
MF v[176:179],a[176:179],v[56:59],v[176:179]
buffer_load_dwordx4 a[56:59], v242, s[20:23], s79 offen offset:1024 sc1
MF v[148:151],a[180:183],v[48:51],v[148:151]
R16 a[144:147],v240,4096
MF v[180:183],a[180:183],v[56:59],v[180:183]
R16 a[148:151],v240,5120
MF v[152:155],a[184:187],v[48:51],v[152:155]
buffer_load_dwordx4 a[60:63], v242, s[20:23], s79 offen offset:2048 sc1
MF v[184:187],a[184:187],v[56:59],v[184:187]
R16 a[152:155],v240,6144
MF v[156:159],a[188:191],v[48:51],v[156:159]
buffer_load_dwordx4 v[224:227], v242, s[20:23], s79 offen offset:3072 sc1
R16 a[156:159],v240,7168
MF v[188:191],a[188:191],v[56:59],v[188:191]
MF v[216:219],a[192:195],v[48:51],v[216:219]
MF v[220:223],a[192:195],v[56:59],v[220:223]
R16 a[160:163],v240,8192
R16 a[164:167],v240,9216
R16 a[168:171],v240,10240
R16 a[172:175],v240,11264
s_nop 7
L_BE_%=:
s_mov_b32 s48, 0x53800000
s_mov_b32 s49, 0x2b800000
v_rcp_f32 v218, v216
v_cmp_nle_f32_e64 s[40:41], v216, s48
v_cmp_nle_f32_e64 s[42:43], s49, v216
s_or_b64 s[38:39], s[38:39], s[40:41]
s_or_b64 s[38:39], s[38:39], s[42:43]
v_rcp_f32 v222, v220
v_cmp_nle_f32_e64 s[40:41], v220, s48
v_cmp_nle_f32_e64 s[42:43], s49, v220
s_or_b64 s[38:39], s[38:39], s[40:41]
s_or_b64 s[38:39], s[38:39], s[42:43]
s_waitcnt lgkmcnt(12)
v_or3_b32 v248, v248, v249, v250
v_or_b32 v248, v248, v251
s_nop 0
v_readfirstlane_b32 s48, v248
s_nop 0
s_cmp_lg_u64 s[38:39], 0
s_addc_u32 s48, s48, 0
s_cmp_lg_u32 s48, 0
s_cbranch_scc1 L_EXIT1_%=
L_OP0_%=:
s_waitcnt vmcnt(63) lgkmcnt(8)
MF v[0:3],a[196:199],a[128:131],0
R16 a[176:179],v240,12288
R16 a[180:183],v240,13312
v_mul_f32 v128, v128, v218
MF v[0:3],a[204:207],a[132:135],v[0:3]
v_mul_f32 v129, v129, v218
v_mul_f32 v130, v130, v218
v_mul_f32 v131, v131, v218
MF v[0:3],a[212:215],a[136:139],v[0:3]
v_mul_f32 v132, v132, v218
v_mul_f32 v133, v133, v218
v_mul_f32 v134, v134, v218
MF v[0:3],a[220:223],a[140:143],v[0:3]
v_mul_f32 v135, v135, v218
PK v128,v128,v129
PK v129,v130,v131
MF v[4:7],a[200:203],a[128:131],0
R16 a[184:187],v240,14336
R16 a[188:191],v240,15360
PK v130,v132,v133
MF v[4:7],a[208:211],a[132:135],v[4:7]
PK v131,v134,v135
MF v[4:7],a[216:219],a[136:139],v[4:7]
buffer_store_dwordx4 v[128:131], v242, s[20:23], s36 offen offset:0 sc1
v_mul_f32 v136, v136, v218
v_mul_f32 v137, v137, v218
MF v[4:7],a[224:227],a[140:143],v[4:7]
s_waitcnt vmcnt(63) lgkmcnt(8)
v_mul_f32 v138, v138, v218
v_mul_f32 v139, v139, v218
MF v[0:3],a[228:231],a[144:147],v[0:3]
R16 a[128:131],v240,16384
R16 a[132:135],v240,17408
v_mul_f32 v140, v140, v218
MF v[0:3],a[236:239],a[148:151],v[0:3]
v_mul_f32 v141, v141, v218
v_mul_f32 v142, v142, v218
v_mul_f32 v143, v143, v218
MF v[0:3],a[244:247],a[152:155],v[0:3]
PK v136,v136,v137
PK v137,v138,v139
PK v138,v140,v141
MF v[0:3],a[252:255],a[156:159],v[0:3]
PK v139,v142,v143
MF v[4:7],a[232:235],a[144:147],v[4:7]
R16 a[136:139],v240,18432
R16 a[140:143],v240,19456
MF v[4:7],a[240:243],a[148:151],v[4:7]
buffer_store_dwordx4 v[136:139], v242, s[20:23], s36 offen offset:1024 sc1
v_mul_f32 v144, v144, v218
v_mul_f32 v145, v145, v218
MF v[4:7],a[248:251],a[152:155],v[4:7]
v_mul_f32 v146, v146, v218
v_mul_f32 v147, v147, v218
v_mul_f32 v148, v148, v218
MF v[4:7],a[0:3],a[156:159],v[4:7]
s_waitcnt vmcnt(58) lgkmcnt(8)
v_mul_f32 v149, v149, v218
v_mul_f32 v150, v150, v218
MF v[0:3],a[4:7],a[160:163],v[0:3]
R16 a[144:147],v240,20480
R16 a[148:151],v240,21504
v_mul_f32 v151, v151, v218
MF v[0:3],a[12:15],a[164:167],v[0:3]
PK v144,v144,v145
PK v145,v146,v147
PK v146,v148,v149
MF v[0:3],a[20:23],a[168:171],v[0:3]
PK v147,v150,v151
MF v[0:3],a[28:31],a[172:175],v[0:3]
buffer_store_dwordx4 v[144:147], v242, s[20:23], s36 offen offset:2048 sc1
v_mul_f32 v152, v152, v218
v_mul_f32 v153, v153, v218
MF v[4:7],a[8:11],a[160:163],v[4:7]
R16 a[152:155],v240,22528
R16 a[156:159],v240,23552
s_add_u32 s95, s81, 0x30
MF v[4:7],a[16:19],a[164:167],v[4:7]
v_mov_b32 v206, s95
v_lshrrev_b32 v206, 1, v206
v_lshlrev_b32 v206, 7, v206
MF v[4:7],a[24:27],a[168:171],v[4:7]
v_bfe_u32 v207, v242, 4, 1
v_lshl_add_u32 v207, v207, 13, v206
s_lshl_b32 s95, s28, 2
MF v[4:7],v[64:67],a[172:175],v[4:7]
s_waitcnt vmcnt(47) lgkmcnt(8)
s_add_u32 s95, s95, s80
s_lshl_b32 s95, s95, 14
MF v[0:3],v[68:71],a[176:179],v[0:3]
R16 a[160:163],v240,24576
R16 a[164:167],v240,25600
s_add_u32 s95, s95, 0x4990000
)A"
R"A(MF v[0:3],v[76:79],a[180:183],v[0:3]
buffer_load_dword v206, v207, s[20:23], s95 offen offset:0 sc1
v_mul_f32 v154, v154, v218
v_mul_f32 v155, v155, v218
MF v[0:3],v[84:87],a[184:187],v[0:3]
v_mul_f32 v156, v156, v218
v_mul_f32 v157, v157, v218
v_mul_f32 v158, v158, v218
MF v[0:3],v[92:95],a[188:191],v[0:3]
v_mul_f32 v159, v159, v218
PK v152,v152,v153
PK v153,v154,v155
MF v[4:7],v[72:75],a[176:179],v[4:7]
R16 a[168:171],v240,26624
R16 a[172:175],v240,27648
PK v154,v156,v157
MF v[4:7],v[80:83],a[180:183],v[4:7]
PK v155,v158,v159
MF v[4:7],v[88:91],a[184:187],v[4:7]
buffer_store_dwordx4 v[152:155], v242, s[20:23], s36 offen offset:3072 sc1
v_mul_f32 v160, v160, v222
v_mul_f32 v161, v161, v222
MF v[4:7],v[96:99],a[188:191],v[4:7]
s_waitcnt vmcnt(30) lgkmcnt(8)
v_mul_f32 v162, v162, v222
v_mul_f32 v163, v163, v222
MF v[0:3],v[100:103],a[128:131],v[0:3]
R16 a[176:179],v240,28672
R16 a[180:183],v240,29696
v_mul_f32 v164, v164, v222
MF v[0:3],v[108:111],a[132:135],v[0:3]
v_mul_f32 v165, v165, v222
v_mul_f32 v166, v166, v222
v_mul_f32 v167, v167, v222
MF v[0:3],v[116:119],a[136:139],v[0:3]
PK v160,v160,v161
PK v161,v162,v163
PK v162,v164,v165
MF v[0:3],v[124:127],a[140:143],v[0:3]
PK v163,v166,v167
s_add_u32 s36, s36, 0x20000
MF v[4:7],v[104:107],a[128:131],v[4:7]
R16 a[184:187],v240,30720
R16 a[188:191],v240,31744
MF v[4:7],v[112:115],a[132:135],v[4:7]
buffer_store_dwordx4 v[160:163], v242, s[20:23], s36 offen offset:0 sc1
v_mul_f32 v168, v168, v222
v_mul_f32 v169, v169, v222
MF v[4:7],v[120:123],a[136:139],v[4:7]
s_waitcnt vmcnt(40)
s_barrier
v_mul_f32 v170, v170, v222
MF v[4:7],a[64:67],a[140:143],v[4:7]
s_waitcnt vmcnt(22) lgkmcnt(8)
v_mul_f32 v171, v171, v222
v_mul_f32 v172, v172, v222
MF v[0:3],a[68:71],a[144:147],v[0:3]
R16 a[128:131],v241,0
R16 a[132:135],v241,1024
v_mul_f32 v173, v173, v222
MF v[0:3],a[76:79],a[148:151],v[0:3]
v_mul_f32 v174, v174, v222
v_mul_f32 v175, v175, v222
PK v168,v168,v169
MF v[0:3],a[84:87],a[152:155],v[0:3]
PK v169,v170,v171
PK v170,v172,v173
PK v171,v174,v175
MF v[0:3],a[92:95],a[156:159],v[0:3]
buffer_store_dwordx4 v[168:171], v242, s[20:23], s36 offen offset:1024 sc1
v_mul_f32 v176, v176, v222
v_mul_f32 v177, v177, v222
MF v[4:7],a[72:75],a[144:147],v[4:7]
R16 a[136:139],v241,2048
R16 a[140:143],v241,3072
v_mul_f32 v178, v178, v222
MF v[4:7],a[80:83],a[148:151],v[4:7]
v_mul_f32 v179, v179, v222
v_mul_f32 v180, v180, v222
v_mul_f32 v181, v181, v222
MF v[4:7],a[88:91],a[152:155],v[4:7]
v_mul_f32 v182, v182, v222
v_mul_f32 v183, v183, v222
PK v176,v176,v177
MF v[4:7],a[96:99],a[156:159],v[4:7]
s_waitcnt vmcnt(15) lgkmcnt(8)
PK v177,v178,v179
PK v178,v180,v181
MF v[0:3],a[100:103],a[160:163],v[0:3]
R16 a[144:147],v241,4096
R16 a[148:151],v241,5120
PK v179,v182,v183
MF v[0:3],a[108:111],a[164:167],v[0:3]
buffer_store_dwordx4 v[176:179], v242, s[20:23], s36 offen offset:2048 sc1
v_mul_f32 v184, v184, v222
v_mul_f32 v185, v185, v222
MF v[0:3],a[116:119],a[168:171],v[0:3]
v_mul_f32 v186, v186, v222
v_mul_f32 v187, v187, v222
v_mul_f32 v188, v188, v222
MF v[0:3],a[124:127],a[172:175],v[0:3]
v_mul_f32 v189, v189, v222
v_mul_f32 v190, v190, v222
v_mul_f32 v191, v191, v222
MF v[4:7],a[104:107],a[160:163],v[4:7]
R16 a[152:155],v241,6144
R16 a[156:159],v241,7168
PK v184,v184,v185
MF v[4:7],a[112:115],a[164:167],v[4:7]
PK v185,v186,v187
PK v186,v188,v189
PK v187,v190,v191
MF v[4:7],a[120:123],a[168:171],v[4:7]
buffer_store_dwordx4 v[184:187], v242, s[20:23], s36 offen offset:3072 sc1
s_mov_b32 s24, s74
s_and_b32 s25, s75, 0xffff
MF v[4:7],a[32:35],a[172:175],v[4:7]
s_waitcnt vmcnt(9) lgkmcnt(8)
s_mov_b32 s26, 0x200000
s_mov_b32 s27, 0x20000
MF v[0:3],a[36:39],a[176:179],v[0:3]
R16 a[160:163],v241,8192
R16 a[164:167],v241,9216
v_bfe_u32 v244, v242, 4, 4
MF v[0:3],a[44:47],a[180:183],v[0:3]
v_lshlrev_b32 v244, 10, v244
v_lshrrev_b32 v200, 8, v242
v_lshl_add_u32 v244, v200, 4, v244
MF v[0:3],a[52:55],a[184:187],v[0:3]
buffer_load_dwordx4 v[36:39], v242, s[20:23], s68 offen offset:0 sc1
buffer_load_dwordx4 v[32:35], v244, s[56:59], s84 offen offset:0
MF v[0:3],a[60:63],a[188:191],v[0:3]
buffer_load_dwordx4 v[44:47], v242, s[20:23], s69 offen offset:0 sc1
buffer_load_dwordx4 v[40:43], v244, s[56:59], s85 offen offset:0
v_and_b32 v205, 0x70, v242
MF v[4:7],a[40:43],a[176:179],v[4:7]
R16 a[168:171],v241,10240
R16 a[172:175],v241,11264
v_lshlrev_b32 v205, 3, v205
MF v[4:7],a[48:51],a[180:183],v[4:7]
v_bfe_u32 v200, v242, 7, 1
v_lshl_add_u32 v205, v200, 5, v205
v_bfe_u32 v200, v242, 8, 1
MF v[4:7],a[56:59],a[184:187],v[4:7]
MF v[4:7],v[224:227],a[188:191],v[4:7]
s_waitcnt lgkmcnt(8)
MF v[8:11],a[196:199],a[128:131],0
R16 a[176:179],v241,12288
R16 a[180:183],v241,13312
MF v[8:11],a[204:207],a[132:135],v[8:11]
v_lshl_add_u32 v205, v200, 6, v205
v_bfe_u32 v200, v242, 9, 1
MF v[8:11],a[212:215],a[136:139],v[8:11]
v_lshl_add_u32 v205, v200, 10, v205
MF v[8:11],a[220:223],a[140:143],v[8:11]
buffer_load_dwordx4 v[192:195], v205, s[20:23], s94 offen offset:0 sc1
buffer_load_dwordx4 v[196:199], v205, s[20:23], s94 offen offset:16 sc1
MF v[12:15],a[200:203],a[128:131],0
R16 a[184:187],v241,14336
R16 a[188:191],v241,15360
MF v[12:15],a[208:211],a[132:135],v[12:15]
PK v24,v0,v1
PK v25,v2,v3
MF v[12:15],a[216:219],a[136:139],v[12:15]
PK v26,v4,v5
PK v27,v6,v7
MF v[12:15],a[224:227],a[140:143],v[12:15]
s_waitcnt lgkmcnt(8)
s_add_u32 s52, s8, 0x0
MF v[8:11],a[228:231],a[144:147],v[8:11]
R16 a[128:131],v241,32768
R16 a[132:135],v241,33792
MF v[8:11],a[236:239],a[148:151],v[8:11]
buffer_store_dwordx4 v[24:27], v242, s[20:23], s52 offen offset:0 sc1
MF v[8:11],a[244:247],a[152:155],v[8:11]
MF v[8:11],a[252:255],a[156:159],v[8:11]
MF v[12:15],a[232:235],a[144:147],v[12:15]
R16 a[136:139],v241,34816
R16 a[140:143],v241,35840
MF v[12:15],a[240:243],a[148:151],v[12:15]
MF v[12:15],a[248:251],a[152:155],v[12:15]
MF v[12:15],a[0:3],a[156:159],v[12:15]
s_waitcnt lgkmcnt(8)
MF v[8:11],a[4:7],a[160:163],v[8:11]
R16 a[144:147],v241,36864
R16 a[148:151],v241,37888
MF v[8:11],a[12:15],a[164:167],v[8:11]
MF v[8:11],a[20:23],a[168:171],v[8:11]
MF v[8:11],a[28:31],a[172:175],v[8:11]
MF v[12:15],a[8:11],a[160:163],v[12:15]
R16 a[152:155],v241,38912
R16 a[156:159],v241,39936
MF v[12:15],a[16:19],a[164:167],v[12:15]
MF v[12:15],a[24:27],a[168:171],v[12:15]
MF v[12:15],v[64:67],a[172:175],v[12:15]
s_waitcnt lgkmcnt(8)
MF v[8:11],v[68:71],a[176:179],v[8:11]
R16 a[160:163],v241,40960
R16 a[164:167],v241,41984
MF v[8:11],v[76:79],a[180:183],v[8:11]
s_waitcnt vmcnt(12)
v_cmp_ne_u32 vcc, s37, v206
MF v[8:11],v[84:87],a[184:187],v[8:11]
s_nop 0
s_cbranch_vccnz L_SP_A3_%=
L_RT_A3_%=:
MF v[8:11],v[92:95],a[188:191],v[8:11]
v_bfe_u32 v206, v242, 7, 1
v_lshlrev_b32 v206, 1, v206
MF v[12:15],v[72:75],a[176:179],v[12:15]
R16 a[168:171],v241,43008
R16 a[172:175],v241,44032
MF v[12:15],v[80:83],a[180:183],v[12:15]
s_lshl_b32 s48, s81, 2
s_add_u32 s48, s48, 0x40
MF v[12:15],v[88:91],a[184:187],v[12:15]
v_add_u32 v206, s48, v206
v_lshrrev_b32 v206, 1, v206
MF v[12:15],v[96:99],a[188:191],v[12:15]
s_waitcnt lgkmcnt(8)
v_lshlrev_b32 v206, 7, v206
MF v[8:11],v[100:103],a[128:131],v[8:11]
R16 a[176:179],v241,45056
R16 a[180:183],v241,46080
MF v[8:11],v[108:111],a[132:135],v[8:11]
v_bfe_u32 v207, v242, 4, 1
v_lshl_add_u32 v207, v207, 13, v206
MF v[8:11],v[116:119],a[136:139],v[8:11]
buffer_load_dword v206, v207, s[20:23], s95 offen offset:0 sc1
MF v[8:11],v[124:127],a[140:143],v[8:11]
s_cmp_lt_u32 s28, 2
s_cselect_b32 s48, 0x14000, 0
s_xor_b32 s48, s48, s54
MF v[12:15],v[104:107],a[128:131],v[12:15]
R16 a[184:187],v241,47104
R16 a[188:191],v241,48128
MF v[12:15],v[112:115],a[132:135],v[12:15]
s_xor_b32 m0, s48, 0xc000
MF v[12:15],v[120:123],a[136:139],v[12:15]
s_waitcnt vmcnt(43)
s_barrier
MF v[12:15],a[64:67],a[140:143],v[12:15]
buffer_load_dwordx4 v242, s[20:23], s63 offen offset:0 sc1 lds
s_waitcnt lgkmcnt(8)
MF v[8:11],a[68:71],a[144:147],v[8:11]
R16 a[128:131],v243,0
R16 a[132:135],v243,1024
MF v[8:11],a[76:79],a[148:151],v[8:11]
buffer_load_dwordx4 v242, s[20:23], s63 offen offset:1024 sc1 lds
buffer_load_dwordx4 v242, s[20:23], s63 offen offset:2048 sc1 lds
MF v[8:11],a[84:87],a[152:155],v[8:11]
buffer_load_dwordx4 v242, s[20:23], s63 offen offset:3072 sc1 lds
s_add_u32 m0, m0, 0x1000
MF v[8:11],a[92:95],a[156:159],v[8:11]
s_add_u32 s48, s63, 0x1000
MF v[12:15],a[72:75],a[144:147],v[12:15]
R16 a[136:139],v243,2048
R16 a[140:143],v243,3072
MF v[12:15],a[80:83],a[148:151],v[12:15]
buffer_load_dwordx4 v242, s[20:23], s48 offen offset:0 sc1 lds
buffer_load_dwordx4 v242, s[20:23], s48 offen offset:1024 sc1 lds
MF v[12:15],a[88:91],a[152:155],v[12:15]
buffer_load_dwordx4 v242, s[20:23], s48 offen offset:2048 sc1 lds
buffer_load_dwordx4 v242, s[20:23], s48 offen offset:3072 sc1 lds
MF v[12:15],a[96:99],a[156:159],v[12:15]
s_waitcnt lgkmcnt(8)
v_mov_b32 v203, s37
MF v[8:11],a[100:103],a[160:163],v[8:11]
R16 a[144:147],v243,4096
R16 a[148:151],v243,5120
MF v[8:11],a[108:111],a[164:167],v[8:11]
v_mov_b32 v204, 0
s_waitcnt vmcnt(16)
MF v[8:11],a[116:119],a[168:171],v[8:11]
buffer_store_dword v203, v204, s[20:23], s88 offen offset:0 sc1
s_waitcnt vmcnt(11)
MF v[8:11],a[124:127],a[172:175],v[8:11]
v_lshlrev_b32 v56, 16, v36
v_and_b32 v57, 0xffff0000, v36
MF v[12:15],a[104:107],a[160:163],v[12:15]
R16 a[152:155],v243,6144
R16 a[156:159],v243,7168
MF v[12:15],a[112:115],a[164:167],v[12:15]
v_lshlrev_b32 v58, 16, v37
v_and_b32 v59, 0xffff0000, v37
MF v[12:15],a[120:123],a[168:171],v[12:15]
v_lshlrev_b32 v60, 16, v38
v_and_b32 v61, 0xffff0000, v38
MF v[12:15],a[32:35],a[172:175],v[12:15]
s_waitcnt lgkmcnt(8)
v_lshlrev_b32 v62, 16, v39
MF v[8:11],a[36:39],a[176:179],v[8:11]
R16 a[160:163],v243,8192
R16 a[164:167],v243,9216
MF v[8:11],a[44:47],a[180:183],v[8:11]
v_and_b32 v63, 0xffff0000, v39
MF v[8:11],a[52:55],a[184:187],v[8:11]
s_cmp_eq_u32 s70, 0
s_cbranch_scc1 L_Y0_%=
L_RT_Y0_%=:
MF v[8:11],a[60:63],a[188:191],v[8:11]
v_mul_f32 v184, 0xbfb8aa3b, v56
v_mul_f32 v185, 0xbfb8aa3b, v57
MF v[12:15],a[40:43],a[176:179],v[12:15]
R16 a[168:171],v243,10240
R16 a[172:175],v243,11264
MF v[12:15],a[48:51],a[180:183],v[12:15]
v_mul_f32 v186, 0xbfb8aa3b, v58
v_mul_f32 v187, 0xbfb8aa3b, v59
MF v[12:15],a[56:59],a[184:187],v[12:15]
MF v[12:15],v[224:227],a[188:191],v[12:15]
s_waitcnt lgkmcnt(8)
MF v[0:3],a[196:199],a[128:131],0
R16 a[176:179],v243,12288
R16 a[180:183],v243,13312
MF v[0:3],a[204:207],a[132:135],v[0:3]
MF v[0:3],a[212:215],a[136:139],v[0:3]
MF v[0:3],a[220:223],a[140:143],v[0:3]
MF v[4:7],a[200:203],a[128:131],0
R16 a[184:187],v243,14336
R16 a[188:191],v243,15360
MF v[4:7],a[208:211],a[132:135],v[4:7]
PK v28,v8,v9
PK v29,v10,v11
MF v[4:7],a[216:219],a[136:139],v[4:7]
PK v30,v12,v13
PK v31,v14,v15
MF v[4:7],a[224:227],a[140:143],v[4:7]
s_waitcnt lgkmcnt(8)
s_add_u32 s52, s8, 0x20000
MF v[0:3],a[228:231],a[144:147],v[0:3]
R16 a[128:131],v241,16384
R16 a[132:135],v241,17408
MF v[0:3],a[236:239],a[148:151],v[0:3]
buffer_store_dwordx4 v[28:31], v242, s[20:23], s52 offen offset:0 sc1
v_mul_f32 v188, 0xbfb8aa3b, v60
MF v[0:3],a[244:247],a[152:155],v[0:3]
v_mul_f32 v189, 0xbfb8aa3b, v61
v_mul_f32 v190, 0xbfb8aa3b, v62
MF v[0:3],a[252:255],a[156:159],v[0:3]
v_mul_f32 v191, 0xbfb8aa3b, v63
MF v[4:7],a[232:235],a[144:147],v[4:7]
R16 a[136:139],v241,18432
R16 a[140:143],v241,19456
MF v[4:7],a[240:243],a[148:151],v[4:7]
v_exp_f32 v184, v184
MF v[4:7],a[248:251],a[152:155],v[4:7]
v_exp_f32 v185, v185
MF v[4:7],a[0:3],a[156:159],v[4:7]
s_waitcnt lgkmcnt(8)
MF v[0:3],a[4:7],a[160:163],v[0:3]
R16 a[144:147],v241,20480
R16 a[148:151],v241,21504
MF v[0:3],a[12:15],a[164:167],v[0:3]
v_exp_f32 v186, v186
MF v[0:3],a[20:23],a[168:171],v[0:3]
v_exp_f32 v187, v187
MF v[0:3],a[28:31],a[172:175],v[0:3]
v_exp_f32 v188, v188
MF v[4:7],a[8:11],a[160:163],v[4:7]
R16 a[152:155],v241,22528
R16 a[156:159],v241,23552
MF v[4:7],a[16:19],a[164:167],v[4:7]
v_exp_f32 v189, v189
MF v[4:7],a[24:27],a[168:171],v[4:7]
v_exp_f32 v190, v190
MF v[4:7],v[64:67],a[172:175],v[4:7]
s_waitcnt lgkmcnt(8)
MF v[0:3],v[68:71],a[176:179],v[0:3]
R16 a[160:163],v241,24576
R16 a[164:167],v241,25600
MF v[0:3],v[76:79],a[180:183],v[0:3]
v_exp_f32 v191, v191
MF v[0:3],v[84:87],a[184:187],v[0:3]
v_add_f32 v184, 1.0, v184
v_add_f32 v185, 1.0, v185
MF v[0:3],v[92:95],a[188:191],v[0:3]
v_add_f32 v186, 1.0, v186
v_add_f32 v187, 1.0, v187
MF v[4:7],v[72:75],a[176:179],v[4:7]
R16 a[168:171],v241,26624
R16 a[172:175],v241,27648
MF v[4:7],v[80:83],a[180:183],v[4:7]
v_add_f32 v188, 1.0, v188
v_add_f32 v189, 1.0, v189
MF v[4:7],v[88:91],a[184:187],v[4:7]
v_add_f32 v190, 1.0, v190
v_add_f32 v191, 1.0, v191
MF v[4:7],v[96:99],a[188:191],v[4:7]
s_waitcnt lgkmcnt(8)
MF v[0:3],v[100:103],a[128:131],v[0:3]
R16 a[176:179],v241,28672
R16 a[180:183],v241,29696
MF v[0:3],v[108:111],a[132:135],v[0:3]
v_rcp_f32 v184, v184
MF v[0:3],v[116:119],a[136:139],v[0:3]
v_rcp_f32 v185, v185
MF v[0:3],v[124:127],a[140:143],v[0:3]
v_rcp_f32 v186, v186
MF v[4:7],v[104:107],a[128:131],v[4:7]
)A"
R"A(R16 a[184:187],v241,30720
R16 a[188:191],v241,31744
MF v[4:7],v[112:115],a[132:135],v[4:7]
v_rcp_f32 v187, v187
MF v[4:7],v[120:123],a[136:139],v[4:7]
v_rcp_f32 v188, v188
MF v[4:7],a[64:67],a[140:143],v[4:7]
s_waitcnt lgkmcnt(8)
MF v[0:3],a[68:71],a[144:147],v[0:3]
v_rcp_f32 v189, v189
MF v[0:3],a[76:79],a[148:151],v[0:3]
v_rcp_f32 v190, v190
MF v[0:3],a[84:87],a[152:155],v[0:3]
v_rcp_f32 v191, v191
MF v[0:3],a[92:95],a[156:159],v[0:3]
v_mul_f32 v56, v56, v184
v_mul_f32 v57, v57, v185
MF v[4:7],a[72:75],a[144:147],v[4:7]
v_mul_f32 v58, v58, v186
v_mul_f32 v59, v59, v187
MF v[4:7],a[80:83],a[148:151],v[4:7]
v_mul_f32 v60, v60, v188
v_mul_f32 v61, v61, v189
MF v[4:7],a[88:91],a[152:155],v[4:7]
s_waitcnt vmcnt(2)
s_barrier
MF v[4:7],a[96:99],a[156:159],v[4:7]
R16 a[128:131],v243,32768
s_waitcnt lgkmcnt(5)
MF v[0:3],a[100:103],a[160:163],v[0:3]
R16 a[132:135],v243,33792
R16 a[136:139],v243,34816
MF v[0:3],a[108:111],a[164:167],v[0:3]
R16 a[140:143],v243,35840
R16 a[144:147],v243,36864
MF v[0:3],a[116:119],a[168:171],v[0:3]
R16 a[148:151],v243,37888
v_mul_f32 v62, v62, v190
MF v[0:3],a[124:127],a[172:175],v[0:3]
v_mul_f32 v63, v63, v191
PK v56,v56,v57
MF v[4:7],a[104:107],a[160:163],v[4:7]
R16 a[152:155],v243,38912
R16 a[156:159],v243,39936
MF v[4:7],a[112:115],a[164:167],v[4:7]
v_and_b32 v57, 0xffff0000, v56
v_lshlrev_b32 v56, 16, v56
MF v[4:7],a[120:123],a[168:171],v[4:7]
PK v58,v58,v59
v_and_b32 v59, 0xffff0000, v58
MF v[4:7],a[32:35],a[172:175],v[4:7]
s_waitcnt lgkmcnt(8)
v_lshlrev_b32 v58, 16, v58
MF v[0:3],a[36:39],a[176:179],v[0:3]
R16 a[160:163],v243,40960
R16 a[164:167],v243,41984
MF v[0:3],a[44:47],a[180:183],v[0:3]
PK v60,v60,v61
v_and_b32 v61, 0xffff0000, v60
MF v[0:3],a[52:55],a[184:187],v[0:3]
v_lshlrev_b32 v60, 16, v60
PK v62,v62,v63
MF v[0:3],a[60:63],a[188:191],v[0:3]
v_and_b32 v63, 0xffff0000, v62
v_lshlrev_b32 v62, 16, v62
MF v[4:7],a[40:43],a[176:179],v[4:7]
R16 a[168:171],v243,43008
R16 a[172:175],v243,44032
MF v[4:7],a[48:51],a[180:183],v[4:7]
MF v[4:7],a[56:59],a[184:187],v[4:7]
MF v[4:7],v[224:227],a[188:191],v[4:7]
s_waitcnt lgkmcnt(8)
s_waitcnt vmcnt(10)
v_cmp_ne_u32 vcc, s37, v206
s_nop 0
s_cbranch_vccz L_OE_%=
MF v[8:11],a[196:199],a[128:131],0
R16 a[176:179],v243,45056
R16 a[180:183],v243,46080
MF v[8:11],a[204:207],a[132:135],v[8:11]
buffer_load_dword v206, v207, s[20:23], s95 offen offset:0 sc1
MF v[8:11],a[212:215],a[136:139],v[8:11]
MF v[8:11],a[220:223],a[140:143],v[8:11]
MF v[12:15],a[200:203],a[128:131],0
R16 a[184:187],v243,47104
R16 a[188:191],v243,48128
MF v[12:15],a[208:211],a[132:135],v[12:15]
PK v24,v0,v1
PK v25,v2,v3
MF v[12:15],a[216:219],a[136:139],v[12:15]
PK v26,v4,v5
PK v27,v6,v7
MF v[12:15],a[224:227],a[140:143],v[12:15]
s_waitcnt lgkmcnt(8)
s_add_u32 s52, s8, 0x40000
MF v[8:11],a[228:231],a[144:147],v[8:11]
R16 a[128:131],v241,49152
R16 a[132:135],v241,50176
MF v[8:11],a[236:239],a[148:151],v[8:11]
buffer_store_dwordx4 v[24:27], v242, s[20:23], s52 offen offset:0 sc1
MF v[8:11],a[244:247],a[152:155],v[8:11]
MF v[8:11],a[252:255],a[156:159],v[8:11]
MF v[12:15],a[232:235],a[144:147],v[12:15]
R16 a[136:139],v241,51200
R16 a[140:143],v241,52224
MF v[12:15],a[240:243],a[148:151],v[12:15]
MF v[12:15],a[248:251],a[152:155],v[12:15]
MF v[12:15],a[0:3],a[156:159],v[12:15]
s_waitcnt lgkmcnt(8)
MF v[8:11],a[4:7],a[160:163],v[8:11]
R16 a[144:147],v241,53248
R16 a[148:151],v241,54272
MF v[8:11],a[12:15],a[164:167],v[8:11]
MF v[8:11],a[20:23],a[168:171],v[8:11]
MF v[8:11],a[28:31],a[172:175],v[8:11]
MF v[12:15],a[8:11],a[160:163],v[12:15]
R16 a[152:155],v241,55296
R16 a[156:159],v241,56320
MF v[12:15],a[16:19],a[164:167],v[12:15]
MF v[12:15],a[24:27],a[168:171],v[12:15]
MF v[12:15],v[64:67],a[172:175],v[12:15]
s_waitcnt lgkmcnt(8)
MF v[8:11],v[68:71],a[176:179],v[8:11]
R16 a[160:163],v241,57344
R16 a[164:167],v241,58368
MF v[8:11],v[76:79],a[180:183],v[8:11]
MF v[8:11],v[84:87],a[184:187],v[8:11]
MF v[8:11],v[92:95],a[188:191],v[8:11]
MF v[12:15],v[72:75],a[176:179],v[12:15]
R16 a[168:171],v241,59392
R16 a[172:175],v241,60416
MF v[12:15],v[80:83],a[180:183],v[12:15]
MF v[12:15],v[88:91],a[184:187],v[12:15]
MF v[12:15],v[96:99],a[188:191],v[12:15]
s_waitcnt lgkmcnt(8)
MF v[8:11],v[100:103],a[128:131],v[8:11]
R16 a[176:179],v241,61440
R16 a[180:183],v241,62464
MF v[8:11],v[108:111],a[132:135],v[8:11]
MF v[8:11],v[116:119],a[136:139],v[8:11]
MF v[8:11],v[124:127],a[140:143],v[8:11]
MF v[12:15],v[104:107],a[128:131],v[12:15]
R16 a[184:187],v241,63488
R16 a[188:191],v241,64512
MF v[12:15],v[112:115],a[132:135],v[12:15]
MF v[12:15],v[120:123],a[136:139],v[12:15]
MF v[12:15],a[64:67],a[140:143],v[12:15]
s_waitcnt lgkmcnt(8)
MF v[8:11],a[68:71],a[144:147],v[8:11]
MF v[8:11],a[76:79],a[148:151],v[8:11]
MF v[8:11],a[84:87],a[152:155],v[8:11]
MF v[8:11],a[92:95],a[156:159],v[8:11]
MF v[12:15],a[72:75],a[144:147],v[12:15]
MF v[12:15],a[80:83],a[148:151],v[12:15]
MF v[12:15],a[88:91],a[152:155],v[12:15]
MF v[12:15],a[96:99],a[156:159],v[12:15]
s_waitcnt lgkmcnt(4)
MF v[8:11],a[100:103],a[160:163],v[8:11]
MF v[8:11],a[108:111],a[164:167],v[8:11]
MF v[8:11],a[116:119],a[168:171],v[8:11]
MF v[8:11],a[124:127],a[172:175],v[8:11]
MF v[12:15],a[104:107],a[160:163],v[12:15]
MF v[12:15],a[112:115],a[164:167],v[12:15]
MF v[12:15],a[120:123],a[168:171],v[12:15]
MF v[12:15],a[32:35],a[172:175],v[12:15]
s_waitcnt lgkmcnt(0)
MF v[8:11],a[36:39],a[176:179],v[8:11]
s_waitcnt vmcnt(1)
v_cmp_ne_u32 vcc, s37, v206
s_nop 0
s_cbranch_vccnz L_SP_B_%=
L_RT_B_%=:
s_cmp_lt_u32 s28, 2
s_cselect_b32 s48, 0x8000, 0
s_xor_b32 s48, s48, s54
s_xor_b32 m0, s48, 0x1c000
s_nop 0
buffer_load_dwordx4 v242, s[20:23], s64 offen offset:0 sc1 lds
buffer_load_dwordx4 v242, s[20:23], s64 offen offset:1024 sc1 lds
buffer_load_dwordx4 v242, s[20:23], s64 offen offset:2048 sc1 lds
buffer_load_dwordx4 v242, s[20:23], s64 offen offset:3072 sc1 lds
s_add_u32 m0, m0, 0x1000
s_add_u32 s48, s64, 0x1000
buffer_load_dwordx4 v242, s[20:23], s48 offen offset:0 sc1 lds
buffer_load_dwordx4 v242, s[20:23], s48 offen offset:1024 sc1 lds
buffer_load_dwordx4 v242, s[20:23], s48 offen offset:2048 sc1 lds
buffer_load_dwordx4 v242, s[20:23], s48 offen offset:3072 sc1 lds
s_lshl_b32 s48, s28, 13
s_add_u32 m0, s48, 0x20000
s_nop 0
buffer_load_dwordx4 v242, s[20:23], s65 offen offset:0 sc1 lds
buffer_load_dwordx4 v242, s[20:23], s65 offen offset:1024 sc1 lds
buffer_load_dwordx4 v242, s[20:23], s65 offen offset:2048 sc1 lds
buffer_load_dwordx4 v242, s[20:23], s65 offen offset:3072 sc1 lds
s_add_u32 m0, m0, 0x1000
s_add_u32 s48, s65, 0x1000
buffer_load_dwordx4 v242, s[20:23], s48 offen offset:0 sc1 lds
buffer_load_dwordx4 v242, s[20:23], s48 offen offset:1024 sc1 lds
buffer_load_dwordx4 v242, s[20:23], s48 offen offset:2048 sc1 lds
buffer_load_dwordx4 v242, s[20:23], s48 offen offset:3072 sc1 lds
s_waitcnt vmcnt(8)
s_barrier
MF v[8:11],a[44:47],a[180:183],v[8:11]
R16 a[128:131],v243,16384
R16 a[132:135],v243,17408
MF v[8:11],a[52:55],a[184:187],v[8:11]
R16 a[136:139],v243,18432
R16 a[140:143],v243,19456
MF v[8:11],a[60:63],a[188:191],v[8:11]
R16 a[144:147],v243,20480
R16 a[148:151],v243,21504
MF v[12:15],a[40:43],a[176:179],v[12:15]
R16 a[152:155],v243,22528
R16 a[156:159],v243,23552
MF v[12:15],a[48:51],a[180:183],v[12:15]
R16 a[160:163],v243,24576
R16 a[164:167],v243,25600
MF v[12:15],a[56:59],a[184:187],v[12:15]
R16 a[168:171],v243,26624
R16 a[172:175],v243,27648
MF v[12:15],v[224:227],a[188:191],v[12:15]
s_waitcnt lgkmcnt(8)
MF v[0:3],a[196:199],a[128:131],0
R16 a[176:179],v243,28672
R16 a[180:183],v243,29696
MF v[0:3],a[204:207],a[132:135],v[0:3]
s_cmp_lt_u32 s28, 2
s_cselect_b32 s48, 0x8000, 0
s_xor_b32 s48, s48, s54
MF v[0:3],a[212:215],a[136:139],v[0:3]
s_xor_b32 m0, s48, 0x8000
MF v[0:3],a[220:223],a[140:143],v[0:3]
buffer_load_dwordx4 v242, s[20:23], s66 offen offset:0 sc1 lds
buffer_load_dwordx4 v242, s[20:23], s66 offen offset:1024 sc1 lds
MF v[4:7],a[200:203],a[128:131],0
R16 a[184:187],v243,30720
R16 a[188:191],v243,31744
MF v[4:7],a[208:211],a[132:135],v[4:7]
buffer_load_dwordx4 v242, s[20:23], s66 offen offset:2048 sc1 lds
PK v28,v8,v9
MF v[4:7],a[216:219],a[136:139],v[4:7]
PK v29,v10,v11
PK v30,v12,v13
MF v[4:7],a[224:227],a[140:143],v[4:7]
buffer_load_dwordx4 v242, s[20:23], s66 offen offset:3072 sc1 lds
s_waitcnt lgkmcnt(8)
MF v[0:3],a[228:231],a[144:147],v[0:3]
R16 a[128:131],v243,49152
R16 a[132:135],v243,50176
MF v[0:3],a[236:239],a[148:151],v[0:3]
s_add_u32 m0, m0, 0x1000
s_add_u32 s48, s66, 0x1000
MF v[0:3],a[244:247],a[152:155],v[0:3]
buffer_load_dwordx4 v242, s[20:23], s48 offen offset:0 sc1 lds
PK v31,v14,v15
MF v[0:3],a[252:255],a[156:159],v[0:3]
s_add_u32 s52, s8, 0x60000
MF v[4:7],a[232:235],a[144:147],v[4:7]
R16 a[136:139],v243,51200
R16 a[140:143],v243,52224
MF v[4:7],a[240:243],a[148:151],v[4:7]
buffer_load_dwordx4 v242, s[20:23], s48 offen offset:1024 sc1 lds
buffer_store_dwordx4 v[28:31], v242, s[20:23], s52 offen offset:0 sc1
MF v[4:7],a[248:251],a[152:155],v[4:7]
buffer_load_dwordx4 v242, s[20:23], s48 offen offset:2048 sc1 lds
MF v[4:7],a[0:3],a[156:159],v[4:7]
s_waitcnt lgkmcnt(8)
MF v[0:3],a[4:7],a[160:163],v[0:3]
R16 a[144:147],v243,53248
R16 a[148:151],v243,54272
MF v[0:3],a[12:15],a[164:167],v[0:3]
buffer_load_dwordx4 v242, s[20:23], s48 offen offset:3072 sc1 lds
v_mov_b32 v149, s37
MF v[0:3],a[20:23],a[168:171],v[0:3]
v_mov_b32 v151, s37
v_mov_b32 v153, s37
MF v[0:3],a[28:31],a[172:175],v[0:3]
s_cmp_lt_u32 s28, 2
s_cselect_b32 s48, 0x14000, 0
s_xor_b32 s48, s48, s54
MF v[4:7],a[8:11],a[160:163],v[4:7]
R16 a[152:155],v243,55296
R16 a[156:159],v243,56320
MF v[4:7],a[16:19],a[164:167],v[4:7]
s_xor_b32 m0, s48, 0x4000
v_mov_b32 v155, s37
MF v[4:7],a[24:27],a[168:171],v[4:7]
buffer_load_dwordx4 v242, s[20:23], s67 offen offset:0 sc1 lds
buffer_load_dwordx4 v242, s[20:23], s67 offen offset:1024 sc1 lds
MF v[4:7],v[64:67],a[172:175],v[4:7]
buffer_load_dwordx4 v242, s[20:23], s67 offen offset:2048 sc1 lds
s_waitcnt lgkmcnt(8)
MF v[0:3],v[68:71],a[176:179],v[0:3]
R16 a[160:163],v243,57344
R16 a[164:167],v243,58368
MF v[0:3],v[76:79],a[180:183],v[0:3]
v_mov_b32 v17, s37
v_mov_b32 v19, s37
MF v[0:3],v[84:87],a[184:187],v[0:3]
buffer_load_dwordx4 v242, s[20:23], s67 offen offset:3072 sc1 lds
v_mov_b32 v21, s37
MF v[0:3],v[92:95],a[188:191],v[0:3]
v_mov_b32 v23, s37
MF v[4:7],v[72:75],a[176:179],v[4:7]
R16 a[168:171],v243,59392
R16 a[172:175],v243,60416
MF v[4:7],v[80:83],a[180:183],v[4:7]
s_add_u32 m0, m0, 0x1000
s_add_u32 s48, s67, 0x1000
MF v[4:7],v[88:91],a[184:187],v[4:7]
buffer_load_dwordx4 v242, s[20:23], s48 offen offset:0 sc1 lds
MF v[4:7],v[96:99],a[188:191],v[4:7]
buffer_load_dwordx4 v242, s[20:23], s48 offen offset:1024 sc1 lds
s_waitcnt lgkmcnt(8)
MF v[0:3],v[100:103],a[128:131],v[0:3]
R16 a[176:179],v243,61440
R16 a[180:183],v243,62464
MF v[0:3],v[108:111],a[132:135],v[0:3]
buffer_load_dwordx4 v242, s[20:23], s48 offen offset:2048 sc1 lds
MF v[0:3],v[116:119],a[136:139],v[0:3]
MF v[0:3],v[124:127],a[140:143],v[0:3]
buffer_load_dwordx4 v242, s[20:23], s48 offen offset:3072 sc1 lds
MF v[4:7],v[104:107],a[128:131],v[4:7]
R16 a[184:187],v243,63488
R16 a[188:191],v243,64512
MF v[4:7],v[112:115],a[132:135],v[4:7]
MF v[4:7],v[120:123],a[136:139],v[4:7]
s_waitcnt vmcnt(17)
s_barrier
MF v[4:7],a[64:67],a[140:143],v[4:7]
s_waitcnt lgkmcnt(8)
MF v[0:3],a[68:71],a[144:147],v[0:3]
R16 a[128:131],v240,0
R16 a[132:135],v240,1024
MF v[0:3],a[76:79],a[148:151],v[0:3]
MF v[0:3],a[84:87],a[152:155],v[0:3]
MF v[0:3],a[92:95],a[156:159],v[0:3]
MF v[4:7],a[72:75],a[144:147],v[4:7]
R16 a[136:139],v240,2048
R16 a[140:143],v240,3072
MF v[4:7],a[80:83],a[148:151],v[4:7]
MF v[4:7],a[88:91],a[152:155],v[4:7]
MF v[4:7],a[96:99],a[156:159],v[4:7]
s_waitcnt lgkmcnt(8)
MF v[0:3],a[100:103],a[160:163],v[0:3]
R16 a[144:147],v240,4096
R16 a[148:151],v240,5120
MF v[0:3],a[108:111],a[164:167],v[0:3]
MF v[0:3],a[116:119],a[168:171],v[0:3]
MF v[0:3],a[124:127],a[172:175],v[0:3]
MF v[4:7],a[104:107],a[160:163],v[4:7]
R16 a[152:155],v240,6144
R16 a[156:159],v240,7168
MF v[4:7],a[112:115],a[164:167],v[4:7]
MF v[4:7],a[120:123],a[168:171],v[4:7]
MF v[4:7],a[32:35],a[172:175],v[4:7]
s_waitcnt lgkmcnt(8)
MF v[0:3],a[36:39],a[176:179],v[0:3]
R16 a[160:163],v240,8192
R16 a[164:167],v240,9216
MF v[0:3],a[44:47],a[180:183],v[0:3]
MF v[0:3],a[52:55],a[184:187],v[0:3]
MF v[0:3],a[60:63],a[188:191],v[0:3]
MF v[4:7],a[40:43],a[176:179],v[4:7]
R16 a[168:171],v240,10240
R16 a[172:175],v240,11264
MF v[4:7],a[48:51],a[180:183],v[4:7]
MF v[4:7],a[56:59],a[184:187],v[4:7]
MF v[4:7],v[224:227],a[188:191],v[4:7]
s_waitcnt lgkmcnt(8)
s_branch L_OJ_%=
L_OE_%=:
MF v[8:11],a[196:199],a[128:131],0
R16 a[176:179],v243,45056
R16 a[180:183],v243,46080
MF v[8:11],a[204:207],a[132:135],v[8:11]
s_cmp_lt_u32 s28, 2
s_cselect_b32 s48, 0x8000, 0
s_xor_b32 s48, s48, s54
MF v[8:11],a[212:215],a[136:139],v[8:11]
s_xor_b32 m0, s48, 0x1c000
MF v[8:11],a[220:223],a[140:143],v[8:11]
buffer_load_dwordx4 v242, s[20:23], s64 offen offset:0 sc1 lds
buffer_load_dwordx4 v242, s[20:23], s64 offen offset:1024 sc1 lds
MF v[12:15],a[200:203],a[128:131],0
R16 a[184:187],v243,47104
R16 a[188:191],v243,48128
MF v[12:15],a[208:211],a[132:135],v[12:15]
buffer_load_dwordx4 v242, s[20:23], s64 offen offset:2048 sc1 lds
PK v24,v0,v1
MF v[12:15],a[216:219],a[136:139],v[12:15]
buffer_load_dwordx4 v242, s[20:23], s64 offen offset:3072 sc1 lds
PK v25,v2,v3
MF v[12:15],a[224:227],a[140:143],v[12:15]
s_waitcnt lgkmcnt(8)
PK v26,v4,v5
MF v[8:11],a[228:231],a[144:147],v[8:11]
R16 a[128:131],v241,49152
R16 a[132:135],v241,50176
MF v[8:11],a[236:239],a[148:151],v[8:11]
)A"
R"A(s_add_u32 m0, m0, 0x1000
s_add_u32 s48, s64, 0x1000
MF v[8:11],a[244:247],a[152:155],v[8:11]
buffer_load_dwordx4 v242, s[20:23], s48 offen offset:0 sc1 lds
buffer_load_dwordx4 v242, s[20:23], s48 offen offset:1024 sc1 lds
MF v[8:11],a[252:255],a[156:159],v[8:11]
PK v27,v6,v7
s_add_u32 s52, s8, 0x40000
MF v[12:15],a[232:235],a[144:147],v[12:15]
R16 a[136:139],v241,51200
R16 a[140:143],v241,52224
MF v[12:15],a[240:243],a[148:151],v[12:15]
buffer_load_dwordx4 v242, s[20:23], s48 offen offset:2048 sc1 lds
buffer_store_dwordx4 v[24:27], v242, s[20:23], s52 offen offset:0 sc1
MF v[12:15],a[248:251],a[152:155],v[12:15]
buffer_load_dwordx4 v242, s[20:23], s48 offen offset:3072 sc1 lds
MF v[12:15],a[0:3],a[156:159],v[12:15]
s_waitcnt lgkmcnt(8)
MF v[8:11],a[4:7],a[160:163],v[8:11]
R16 a[144:147],v241,53248
R16 a[148:151],v241,54272
MF v[8:11],a[12:15],a[164:167],v[8:11]
MF v[8:11],a[20:23],a[168:171],v[8:11]
s_lshl_b32 s48, s28, 13
s_add_u32 m0, s48, 0x20000
MF v[8:11],a[28:31],a[172:175],v[8:11]
buffer_load_dwordx4 v242, s[20:23], s65 offen offset:0 sc1 lds
MF v[12:15],a[8:11],a[160:163],v[12:15]
R16 a[152:155],v241,55296
R16 a[156:159],v241,56320
MF v[12:15],a[16:19],a[164:167],v[12:15]
buffer_load_dwordx4 v242, s[20:23], s65 offen offset:1024 sc1 lds
MF v[12:15],a[24:27],a[168:171],v[12:15]
MF v[12:15],v[64:67],a[172:175],v[12:15]
s_waitcnt lgkmcnt(8)
MF v[8:11],v[68:71],a[176:179],v[8:11]
R16 a[160:163],v241,57344
R16 a[164:167],v241,58368
MF v[8:11],v[76:79],a[180:183],v[8:11]
buffer_load_dwordx4 v242, s[20:23], s65 offen offset:2048 sc1 lds
MF v[8:11],v[84:87],a[184:187],v[8:11]
MF v[8:11],v[92:95],a[188:191],v[8:11]
MF v[12:15],v[72:75],a[176:179],v[12:15]
R16 a[168:171],v241,59392
R16 a[172:175],v241,60416
MF v[12:15],v[80:83],a[180:183],v[12:15]
buffer_load_dwordx4 v242, s[20:23], s65 offen offset:3072 sc1 lds
MF v[12:15],v[88:91],a[184:187],v[12:15]
MF v[12:15],v[96:99],a[188:191],v[12:15]
s_waitcnt lgkmcnt(8)
MF v[8:11],v[100:103],a[128:131],v[8:11]
R16 a[176:179],v241,61440
R16 a[180:183],v241,62464
MF v[8:11],v[108:111],a[132:135],v[8:11]
s_add_u32 m0, m0, 0x1000
s_add_u32 s48, s65, 0x1000
MF v[8:11],v[116:119],a[136:139],v[8:11]
buffer_load_dwordx4 v242, s[20:23], s48 offen offset:0 sc1 lds
MF v[8:11],v[124:127],a[140:143],v[8:11]
buffer_load_dwordx4 v242, s[20:23], s48 offen offset:1024 sc1 lds
MF v[12:15],v[104:107],a[128:131],v[12:15]
R16 a[184:187],v241,63488
R16 a[188:191],v241,64512
MF v[12:15],v[112:115],a[132:135],v[12:15]
MF v[12:15],v[120:123],a[136:139],v[12:15]
MF v[12:15],a[64:67],a[140:143],v[12:15]
buffer_load_dwordx4 v242, s[20:23], s48 offen offset:2048 sc1 lds
s_waitcnt lgkmcnt(8)
MF v[8:11],a[68:71],a[144:147],v[8:11]
MF v[8:11],a[76:79],a[148:151],v[8:11]
MF v[8:11],a[84:87],a[152:155],v[8:11]
buffer_load_dwordx4 v242, s[20:23], s48 offen offset:3072 sc1 lds
MF v[8:11],a[92:95],a[156:159],v[8:11]
MF v[12:15],a[72:75],a[144:147],v[12:15]
MF v[12:15],a[80:83],a[148:151],v[12:15]
MF v[12:15],a[88:91],a[152:155],v[12:15]
s_cmp_lt_u32 s28, 2
s_cselect_b32 s48, 0x8000, 0
s_xor_b32 s48, s48, s54
MF v[12:15],a[96:99],a[156:159],v[12:15]
s_waitcnt lgkmcnt(4)
s_xor_b32 m0, s48, 0x8000
MF v[8:11],a[100:103],a[160:163],v[8:11]
buffer_load_dwordx4 v242, s[20:23], s66 offen offset:0 sc1 lds
MF v[8:11],a[108:111],a[164:167],v[8:11]
buffer_load_dwordx4 v242, s[20:23], s66 offen offset:1024 sc1 lds
MF v[8:11],a[116:119],a[168:171],v[8:11]
MF v[8:11],a[124:127],a[172:175],v[8:11]
MF v[12:15],a[104:107],a[160:163],v[12:15]
MF v[12:15],a[112:115],a[164:167],v[12:15]
buffer_load_dwordx4 v242, s[20:23], s66 offen offset:2048 sc1 lds
MF v[12:15],a[120:123],a[168:171],v[12:15]
MF v[12:15],a[32:35],a[172:175],v[12:15]
s_waitcnt lgkmcnt(0)
MF v[8:11],a[36:39],a[176:179],v[8:11]
s_waitcnt vmcnt(11)
s_barrier
MF v[8:11],a[44:47],a[180:183],v[8:11]
R16 a[128:131],v243,16384
R16 a[132:135],v243,17408
MF v[8:11],a[52:55],a[184:187],v[8:11]
R16 a[136:139],v243,18432
R16 a[140:143],v243,19456
MF v[8:11],a[60:63],a[188:191],v[8:11]
R16 a[144:147],v243,20480
R16 a[148:151],v243,21504
MF v[12:15],a[40:43],a[176:179],v[12:15]
R16 a[152:155],v243,22528
R16 a[156:159],v243,23552
MF v[12:15],a[48:51],a[180:183],v[12:15]
R16 a[160:163],v243,24576
R16 a[164:167],v243,25600
MF v[12:15],a[56:59],a[184:187],v[12:15]
R16 a[168:171],v243,26624
R16 a[172:175],v243,27648
MF v[12:15],v[224:227],a[188:191],v[12:15]
buffer_load_dwordx4 v242, s[20:23], s66 offen offset:3072 sc1 lds
s_waitcnt lgkmcnt(8)
MF v[0:3],a[196:199],a[128:131],0
R16 a[176:179],v243,28672
R16 a[180:183],v243,29696
MF v[0:3],a[204:207],a[132:135],v[0:3]
s_add_u32 m0, m0, 0x1000
s_add_u32 s48, s66, 0x1000
MF v[0:3],a[212:215],a[136:139],v[0:3]
buffer_load_dwordx4 v242, s[20:23], s48 offen offset:0 sc1 lds
MF v[0:3],a[220:223],a[140:143],v[0:3]
buffer_load_dwordx4 v242, s[20:23], s48 offen offset:1024 sc1 lds
MF v[4:7],a[200:203],a[128:131],0
R16 a[184:187],v243,30720
R16 a[188:191],v243,31744
MF v[4:7],a[208:211],a[132:135],v[4:7]
PK v28,v8,v9
PK v29,v10,v11
MF v[4:7],a[216:219],a[136:139],v[4:7]
buffer_load_dwordx4 v242, s[20:23], s48 offen offset:2048 sc1 lds
PK v30,v12,v13
MF v[4:7],a[224:227],a[140:143],v[4:7]
s_waitcnt lgkmcnt(8)
PK v31,v14,v15
MF v[0:3],a[228:231],a[144:147],v[0:3]
R16 a[128:131],v243,49152
R16 a[132:135],v243,50176
MF v[0:3],a[236:239],a[148:151],v[0:3]
buffer_load_dwordx4 v242, s[20:23], s48 offen offset:3072 sc1 lds
s_add_u32 s52, s8, 0x60000
MF v[0:3],a[244:247],a[152:155],v[0:3]
buffer_store_dwordx4 v[28:31], v242, s[20:23], s52 offen offset:0 sc1
MF v[0:3],a[252:255],a[156:159],v[0:3]
MF v[4:7],a[232:235],a[144:147],v[4:7]
R16 a[136:139],v243,51200
R16 a[140:143],v243,52224
MF v[4:7],a[240:243],a[148:151],v[4:7]
s_cmp_lt_u32 s28, 2
s_cselect_b32 s48, 0x14000, 0
s_xor_b32 s48, s48, s54
MF v[4:7],a[248:251],a[152:155],v[4:7]
s_xor_b32 m0, s48, 0x4000
MF v[4:7],a[0:3],a[156:159],v[4:7]
buffer_load_dwordx4 v242, s[20:23], s67 offen offset:0 sc1 lds
s_waitcnt lgkmcnt(8)
MF v[0:3],a[4:7],a[160:163],v[0:3]
R16 a[144:147],v243,53248
R16 a[148:151],v243,54272
MF v[0:3],a[12:15],a[164:167],v[0:3]
buffer_load_dwordx4 v242, s[20:23], s67 offen offset:1024 sc1 lds
v_mov_b32 v149, s37
MF v[0:3],a[20:23],a[168:171],v[0:3]
buffer_load_dwordx4 v242, s[20:23], s67 offen offset:2048 sc1 lds
v_mov_b32 v151, s37
MF v[0:3],a[28:31],a[172:175],v[0:3]
v_mov_b32 v153, s37
v_mov_b32 v155, s37
MF v[4:7],a[8:11],a[160:163],v[4:7]
R16 a[152:155],v243,55296
R16 a[156:159],v243,56320
MF v[4:7],a[16:19],a[164:167],v[4:7]
buffer_load_dwordx4 v242, s[20:23], s67 offen offset:3072 sc1 lds
v_mov_b32 v17, s37
MF v[4:7],a[24:27],a[168:171],v[4:7]
v_mov_b32 v19, s37
v_mov_b32 v21, s37
MF v[4:7],v[64:67],a[172:175],v[4:7]
s_waitcnt lgkmcnt(8)
v_mov_b32 v23, s37
MF v[0:3],v[68:71],a[176:179],v[0:3]
R16 a[160:163],v243,57344
R16 a[164:167],v243,58368
MF v[0:3],v[76:79],a[180:183],v[0:3]
s_add_u32 m0, m0, 0x1000
s_add_u32 s48, s67, 0x1000
MF v[0:3],v[84:87],a[184:187],v[0:3]
buffer_load_dwordx4 v242, s[20:23], s48 offen offset:0 sc1 lds
MF v[0:3],v[92:95],a[188:191],v[0:3]
buffer_load_dwordx4 v242, s[20:23], s48 offen offset:1024 sc1 lds
MF v[4:7],v[72:75],a[176:179],v[4:7]
R16 a[168:171],v243,59392
R16 a[172:175],v243,60416
MF v[4:7],v[80:83],a[180:183],v[4:7]
MF v[4:7],v[88:91],a[184:187],v[4:7]
buffer_load_dwordx4 v242, s[20:23], s48 offen offset:2048 sc1 lds
MF v[4:7],v[96:99],a[188:191],v[4:7]
s_waitcnt lgkmcnt(8)
MF v[0:3],v[100:103],a[128:131],v[0:3]
R16 a[176:179],v243,61440
R16 a[180:183],v243,62464
MF v[0:3],v[108:111],a[132:135],v[0:3]
buffer_load_dwordx4 v242, s[20:23], s48 offen offset:3072 sc1 lds
MF v[0:3],v[116:119],a[136:139],v[0:3]
MF v[0:3],v[124:127],a[140:143],v[0:3]
MF v[4:7],v[104:107],a[128:131],v[4:7]
R16 a[184:187],v243,63488
R16 a[188:191],v243,64512
MF v[4:7],v[112:115],a[132:135],v[4:7]
MF v[4:7],v[120:123],a[136:139],v[4:7]
s_waitcnt vmcnt(17)
s_barrier
MF v[4:7],a[64:67],a[140:143],v[4:7]
s_waitcnt lgkmcnt(8)
MF v[0:3],a[68:71],a[144:147],v[0:3]
R16 a[128:131],v240,0
R16 a[132:135],v240,1024
MF v[0:3],a[76:79],a[148:151],v[0:3]
MF v[0:3],a[84:87],a[152:155],v[0:3]
MF v[0:3],a[92:95],a[156:159],v[0:3]
MF v[4:7],a[72:75],a[144:147],v[4:7]
R16 a[136:139],v240,2048
R16 a[140:143],v240,3072
MF v[4:7],a[80:83],a[148:151],v[4:7]
MF v[4:7],a[88:91],a[152:155],v[4:7]
MF v[4:7],a[96:99],a[156:159],v[4:7]
s_waitcnt lgkmcnt(8)
MF v[0:3],a[100:103],a[160:163],v[0:3]
R16 a[144:147],v240,4096
R16 a[148:151],v240,5120
MF v[0:3],a[108:111],a[164:167],v[0:3]
MF v[0:3],a[116:119],a[168:171],v[0:3]
MF v[0:3],a[124:127],a[172:175],v[0:3]
MF v[4:7],a[104:107],a[160:163],v[4:7]
R16 a[152:155],v240,6144
R16 a[156:159],v240,7168
MF v[4:7],a[112:115],a[164:167],v[4:7]
MF v[4:7],a[120:123],a[168:171],v[4:7]
MF v[4:7],a[32:35],a[172:175],v[4:7]
s_waitcnt lgkmcnt(8)
MF v[0:3],a[36:39],a[176:179],v[0:3]
R16 a[160:163],v240,8192
R16 a[164:167],v240,9216
MF v[0:3],a[44:47],a[180:183],v[0:3]
MF v[0:3],a[52:55],a[184:187],v[0:3]
MF v[0:3],a[60:63],a[188:191],v[0:3]
MF v[4:7],a[40:43],a[176:179],v[4:7]
R16 a[168:171],v240,10240
R16 a[172:175],v240,11264
MF v[4:7],a[48:51],a[180:183],v[4:7]
MF v[4:7],a[56:59],a[184:187],v[4:7]
MF v[4:7],v[224:227],a[188:191],v[4:7]
s_waitcnt lgkmcnt(8)
L_OJ_%=:
MF v[8:11],a[196:199],a[128:131],0
R16 a[176:179],v240,12288
R16 a[180:183],v240,13312
MF v[8:11],a[204:207],a[132:135],v[8:11]
v_mov_b32 v218, s37
v_mov_b32 v219, 0
MF v[8:11],a[212:215],a[136:139],v[8:11]
s_waitcnt vmcnt(8)
MF v[8:11],a[220:223],a[140:143],v[8:11]
buffer_store_dword v218, v219, s[20:23], s10 offen offset:0 sc1
MF v[12:15],a[200:203],a[128:131],0
R16 a[184:187],v240,14336
R16 a[188:191],v240,15360
MF v[12:15],a[208:211],a[132:135],v[12:15]
PK v24,v0,v1
PK v25,v2,v3
MF v[12:15],a[216:219],a[136:139],v[12:15]
PK v26,v4,v5
PK v27,v6,v7
MF v[12:15],a[224:227],a[140:143],v[12:15]
s_waitcnt lgkmcnt(8)
s_add_u32 s52, s80, 0x1
MF v[8:11],a[228:231],a[144:147],v[8:11]
R16 a[128:131],v240,16384
R16 a[132:135],v240,17408
MF v[8:11],a[236:239],a[148:151],v[8:11]
s_and_b32 s52, s52, 3
s_lshl_b32 s52, s52, 17
MF v[8:11],a[244:247],a[152:155],v[8:11]
s_add_u32 s52, s52, s9
MF v[8:11],a[252:255],a[156:159],v[8:11]
buffer_store_dwordx4 v[24:27], v242, s[20:23], s52 offen offset:0 sc1
MF v[12:15],a[232:235],a[144:147],v[12:15]
R16 a[136:139],v240,18432
R16 a[140:143],v240,19456
MF v[12:15],a[240:243],a[148:151],v[12:15]
MF v[12:15],a[248:251],a[152:155],v[12:15]
s_waitcnt vmcnt(48)
v_lshlrev_b32 v208, 16, v44
MF v[12:15],a[0:3],a[156:159],v[12:15]
s_waitcnt lgkmcnt(8)
v_and_b32 v209, 0xffff0000, v44
MF v[8:11],a[4:7],a[160:163],v[8:11]
R16 a[144:147],v240,20480
R16 a[148:151],v240,21504
MF v[8:11],a[12:15],a[164:167],v[8:11]
v_lshlrev_b32 v210, 16, v45
v_and_b32 v211, 0xffff0000, v45
MF v[8:11],a[20:23],a[168:171],v[8:11]
v_lshlrev_b32 v212, 16, v46
v_and_b32 v213, 0xffff0000, v46
MF v[8:11],a[28:31],a[172:175],v[8:11]
v_lshlrev_b32 v214, 16, v47
v_and_b32 v215, 0xffff0000, v47
MF v[12:15],a[8:11],a[160:163],v[12:15]
R16 a[152:155],v240,22528
R16 a[156:159],v240,23552
MF v[12:15],a[16:19],a[164:167],v[12:15]
s_cmp_eq_u32 s71, 0
s_cbranch_scc1 L_Y1_%=
L_RT_Y1_%=:
MF v[12:15],a[24:27],a[168:171],v[12:15]
v_mul_f32 v184, 0xbfb8aa3b, v208
v_mul_f32 v185, 0xbfb8aa3b, v209
MF v[12:15],v[64:67],a[172:175],v[12:15]
s_waitcnt lgkmcnt(8)
v_mul_f32 v186, 0xbfb8aa3b, v210
MF v[8:11],v[68:71],a[176:179],v[8:11]
R16 a[160:163],v240,24576
R16 a[164:167],v240,25600
MF v[8:11],v[76:79],a[180:183],v[8:11]
v_mul_f32 v187, 0xbfb8aa3b, v211
v_mul_f32 v188, 0xbfb8aa3b, v212
MF v[8:11],v[84:87],a[184:187],v[8:11]
v_mul_f32 v189, 0xbfb8aa3b, v213
v_mul_f32 v190, 0xbfb8aa3b, v214
MF v[8:11],v[92:95],a[188:191],v[8:11]
v_mul_f32 v191, 0xbfb8aa3b, v215
MF v[12:15],v[72:75],a[176:179],v[12:15]
R16 a[168:171],v240,26624
R16 a[172:175],v240,27648
MF v[12:15],v[80:83],a[180:183],v[12:15]
v_exp_f32 v184, v184
MF v[12:15],v[88:91],a[184:187],v[12:15]
v_exp_f32 v185, v185
MF v[12:15],v[96:99],a[188:191],v[12:15]
s_waitcnt lgkmcnt(8)
MF v[8:11],v[100:103],a[128:131],v[8:11]
R16 a[176:179],v240,28672
R16 a[180:183],v240,29696
MF v[8:11],v[108:111],a[132:135],v[8:11]
v_exp_f32 v186, v186
MF v[8:11],v[116:119],a[136:139],v[8:11]
v_exp_f32 v187, v187
MF v[8:11],v[124:127],a[140:143],v[8:11]
v_exp_f32 v188, v188
MF v[12:15],v[104:107],a[128:131],v[12:15]
R16 a[184:187],v240,30720
R16 a[188:191],v240,31744
MF v[12:15],v[112:115],a[132:135],v[12:15]
v_exp_f32 v189, v189
MF v[12:15],v[120:123],a[136:139],v[12:15]
s_waitcnt vmcnt(10)
s_barrier
MF v[12:15],a[64:67],a[140:143],v[12:15]
s_waitcnt lgkmcnt(8)
MF v[8:11],a[68:71],a[144:147],v[8:11]
R16 a[128:131],v241,0
R16 a[132:135],v241,1024
MF v[8:11],a[76:79],a[148:151],v[8:11]
v_and_b32 v217, 48, v242
v_lshlrev_b32 v217, 6, v217
MF v[8:11],a[84:87],a[152:155],v[8:11]
buffer_load_dword v220, v217, s[20:23], s11 offen offset:0 sc1
MF v[8:11],a[92:95],a[156:159],v[8:11]
v_exp_f32 v190, v190
MF v[12:15],a[72:75],a[144:147],v[12:15]
R16 a[136:139],v241,2048
R16 a[140:143],v241,3072
MF v[12:15],a[80:83],a[148:151],v[12:15]
v_exp_f32 v191, v191
MF v[12:15],a[88:91],a[152:155],v[12:15]
v_add_f32 v184, 1.0, v184
v_add_f32 v185, 1.0, v185
MF v[12:15],a[96:99],a[156:159],v[12:15]
s_waitcnt lgkmcnt(8)
v_add_f32 v186, 1.0, v186
MF v[8:11],a[100:103],a[160:163],v[8:11]
R16 a[144:147],v241,4096
R16 a[148:151],v241,5120
MF v[8:11],a[108:111],a[164:167],v[8:11]
v_add_f32 v187, 1.0, v187
v_add_f32 v188, 1.0, v188
MF v[8:11],a[116:119],a[168:171],v[8:11]
v_add_f32 v189, 1.0, v189
v_add_f32 v190, 1.0, v190
MF v[8:11],a[124:127],a[172:175],v[8:11]
v_add_f32 v191, 1.0, v191
MF v[12:15],a[104:107],a[160:163],v[12:15]
R16 a[152:155],v241,6144
R16 a[156:159],v241,7168
)A"
R"A(MF v[12:15],a[112:115],a[164:167],v[12:15]
v_rcp_f32 v184, v184
MF v[12:15],a[120:123],a[168:171],v[12:15]
v_rcp_f32 v185, v185
MF v[12:15],a[32:35],a[172:175],v[12:15]
s_waitcnt lgkmcnt(8)
MF v[8:11],a[36:39],a[176:179],v[8:11]
R16 a[160:163],v241,8192
R16 a[164:167],v241,9216
MF v[8:11],a[44:47],a[180:183],v[8:11]
v_rcp_f32 v186, v186
MF v[8:11],a[52:55],a[184:187],v[8:11]
v_rcp_f32 v187, v187
MF v[8:11],a[60:63],a[188:191],v[8:11]
v_rcp_f32 v188, v188
MF v[12:15],a[40:43],a[176:179],v[12:15]
R16 a[168:171],v241,10240
R16 a[172:175],v241,11264
MF v[12:15],a[48:51],a[180:183],v[12:15]
v_rcp_f32 v189, v189
MF v[12:15],a[56:59],a[184:187],v[12:15]
MF v[12:15],v[224:227],a[188:191],v[12:15]
s_waitcnt lgkmcnt(8)
MF v[0:3],a[196:199],a[128:131],0
R16 a[176:179],v241,12288
R16 a[180:183],v241,13312
MF v[0:3],a[204:207],a[132:135],v[0:3]
v_rcp_f32 v190, v190
MF v[0:3],a[212:215],a[136:139],v[0:3]
v_rcp_f32 v191, v191
MF v[0:3],a[220:223],a[140:143],v[0:3]
v_mul_f32 v208, v208, v184
v_mul_f32 v209, v209, v185
MF v[4:7],a[200:203],a[128:131],0
R16 a[184:187],v241,14336
R16 a[188:191],v241,15360
MF v[4:7],a[208:211],a[132:135],v[4:7]
PK v16,v8,v9
PK v18,v10,v11
MF v[4:7],a[216:219],a[136:139],v[4:7]
PK v20,v12,v13
PK v22,v14,v15
MF v[4:7],a[224:227],a[140:143],v[4:7]
s_waitcnt lgkmcnt(8)
s_add_u32 s52, s80, 0x2
MF v[0:3],a[228:231],a[144:147],v[0:3]
R16 a[128:131],v241,32768
R16 a[132:135],v241,33792
MF v[0:3],a[236:239],a[148:151],v[0:3]
s_and_b32 s52, s52, 3
s_lshl_b32 s52, s52, 17
MF v[0:3],a[244:247],a[152:155],v[0:3]
s_add_u32 s52, s52, s9
v_mul_f32 v210, v210, v186
MF v[0:3],a[252:255],a[156:159],v[0:3]
buffer_store_dwordx4 v[16:19], v242, s[20:23], s52 offen offset:0 sc1
buffer_store_dwordx4 v[20:23], v242, s[20:23], s52 offen offset:1024 sc1
MF v[4:7],a[232:235],a[144:147],v[4:7]
R16 a[136:139],v241,34816
R16 a[140:143],v241,35840
MF v[4:7],a[240:243],a[148:151],v[4:7]
v_mul_f32 v211, v211, v187
v_mul_f32 v212, v212, v188
MF v[4:7],a[248:251],a[152:155],v[4:7]
v_mov_b32 v218, s37
v_mov_b32 v219, 0
MF v[4:7],a[0:3],a[156:159],v[4:7]
s_waitcnt lgkmcnt(8)
s_waitcnt vmcnt(3)
MF v[0:3],a[4:7],a[160:163],v[0:3]
R16 a[144:147],v241,36864
R16 a[148:151],v241,37888
MF v[0:3],a[12:15],a[164:167],v[0:3]
buffer_store_dword v218, v219, s[20:23], s10 offen offset:64 sc1
v_mul_f32 v213, v213, v189
MF v[0:3],a[20:23],a[168:171],v[0:3]
v_mul_f32 v214, v214, v190
v_mul_f32 v215, v215, v191
MF v[0:3],a[28:31],a[172:175],v[0:3]
PK v208,v208,v209
v_and_b32 v209, 0xffff0000, v208
MF v[4:7],a[8:11],a[160:163],v[4:7]
R16 a[152:155],v241,38912
R16 a[156:159],v241,39936
MF v[4:7],a[16:19],a[164:167],v[4:7]
v_lshlrev_b32 v208, 16, v208
PK v210,v210,v211
MF v[4:7],a[24:27],a[168:171],v[4:7]
v_and_b32 v211, 0xffff0000, v210
v_lshlrev_b32 v210, 16, v210
MF v[4:7],v[64:67],a[172:175],v[4:7]
s_waitcnt lgkmcnt(8)
PK v212,v212,v213
MF v[0:3],v[68:71],a[176:179],v[0:3]
R16 a[160:163],v241,40960
R16 a[164:167],v241,41984
MF v[0:3],v[76:79],a[180:183],v[0:3]
v_and_b32 v213, 0xffff0000, v212
v_lshlrev_b32 v212, 16, v212
MF v[0:3],v[84:87],a[184:187],v[0:3]
PK v214,v214,v215
v_and_b32 v215, 0xffff0000, v214
MF v[0:3],v[92:95],a[188:191],v[0:3]
v_lshlrev_b32 v214, 16, v214
MF v[4:7],v[72:75],a[176:179],v[4:7]
R16 a[168:171],v241,43008
R16 a[172:175],v241,44032
MF v[4:7],v[80:83],a[180:183],v[4:7]
MF v[4:7],v[88:91],a[184:187],v[4:7]
MF v[4:7],v[96:99],a[188:191],v[4:7]
s_waitcnt lgkmcnt(8)
MF v[0:3],v[100:103],a[128:131],v[0:3]
R16 a[176:179],v241,45056
R16 a[180:183],v241,46080
MF v[0:3],v[108:111],a[132:135],v[0:3]
s_waitcnt vmcnt(3)
v_cmp_ne_u32 vcc, s37, v220
MF v[0:3],v[116:119],a[136:139],v[0:3]
s_nop 0
s_cbranch_vccnz L_SP_PA_%=
L_RT_PA_%=:
MF v[0:3],v[124:127],a[140:143],v[0:3]
s_add_u32 s49, s12, 0x0
MF v[4:7],v[104:107],a[128:131],v[4:7]
R16 a[184:187],v241,47104
R16 a[188:191],v241,48128
MF v[4:7],v[112:115],a[132:135],v[4:7]
buffer_load_dwordx4 v[128:131], v242, s[20:23], s49 offen offset:0 sc1
s_add_u32 s49, s12, 0x2000
MF v[4:7],v[120:123],a[136:139],v[4:7]
s_waitcnt vmcnt(7)
s_barrier
MF v[4:7],a[64:67],a[140:143],v[4:7]
buffer_load_dwordx4 v[132:135], v242, s[20:23], s49 offen offset:0 sc1
s_waitcnt lgkmcnt(8)
MF v[0:3],a[68:71],a[144:147],v[0:3]
R16 a[128:131],v243,0
R16 a[132:135],v243,1024
MF v[0:3],a[76:79],a[148:151],v[0:3]
s_add_u32 s49, s12, 0x4000
MF v[0:3],a[84:87],a[152:155],v[0:3]
buffer_load_dwordx4 v[136:139], v242, s[20:23], s49 offen offset:0 sc1
s_add_u32 s49, s12, 0x6000
MF v[0:3],a[92:95],a[156:159],v[0:3]
buffer_load_dwordx4 v[140:143], v242, s[20:23], s49 offen offset:0 sc1
MF v[4:7],a[72:75],a[144:147],v[4:7]
R16 a[136:139],v243,2048
R16 a[140:143],v243,3072
MF v[4:7],a[80:83],a[148:151],v[4:7]
MF v[4:7],a[88:91],a[152:155],v[4:7]
MF v[4:7],a[96:99],a[156:159],v[4:7]
s_waitcnt lgkmcnt(8)
MF v[0:3],a[100:103],a[160:163],v[0:3]
R16 a[144:147],v243,4096
R16 a[148:151],v243,5120
MF v[0:3],a[108:111],a[164:167],v[0:3]
MF v[0:3],a[116:119],a[168:171],v[0:3]
MF v[0:3],a[124:127],a[172:175],v[0:3]
MF v[4:7],a[104:107],a[160:163],v[4:7]
R16 a[152:155],v243,6144
R16 a[156:159],v243,7168
MF v[4:7],a[112:115],a[164:167],v[4:7]
s_add_u32 s51, s80, 0x3
s_and_b32 s51, s51, 3
MF v[4:7],a[120:123],a[168:171],v[4:7]
s_lshl_b32 s13, s51, 10
s_add_u32 s13, s13, s11
MF v[4:7],a[32:35],a[172:175],v[4:7]
s_waitcnt lgkmcnt(8)
s_add_u32 s13, s13, 0x40
MF v[0:3],a[36:39],a[176:179],v[0:3]
R16 a[160:163],v243,8192
R16 a[164:167],v243,9216
MF v[0:3],a[44:47],a[180:183],v[0:3]
s_lshl_b32 s14, s51, 13
s_add_u32 s14, s14, s12
MF v[0:3],a[52:55],a[184:187],v[0:3]
s_add_u32 s14, s14, 0x8000
v_mov_b32 v221, 0
MF v[0:3],a[60:63],a[188:191],v[0:3]
buffer_load_dword v222, v221, s[20:23], s13 offen offset:0 sc1
MF v[4:7],a[40:43],a[176:179],v[4:7]
R16 a[168:171],v243,10240
R16 a[172:175],v243,11264
MF v[4:7],a[48:51],a[180:183],v[4:7]
MF v[4:7],a[56:59],a[184:187],v[4:7]
MF v[4:7],v[224:227],a[188:191],v[4:7]
s_waitcnt lgkmcnt(8)
MF v[8:11],a[196:199],a[128:131],0
R16 a[176:179],v243,12288
R16 a[180:183],v243,13312
MF v[8:11],a[204:207],a[132:135],v[8:11]
MF v[8:11],a[212:215],a[136:139],v[8:11]
MF v[8:11],a[220:223],a[140:143],v[8:11]
MF v[12:15],a[200:203],a[128:131],0
R16 a[184:187],v243,14336
R16 a[188:191],v243,15360
MF v[12:15],a[208:211],a[132:135],v[12:15]
PK v148,v0,v1
PK v150,v2,v3
MF v[12:15],a[216:219],a[136:139],v[12:15]
PK v152,v4,v5
PK v154,v6,v7
MF v[12:15],a[224:227],a[140:143],v[12:15]
s_waitcnt lgkmcnt(8)
s_add_u32 s52, s80, 0x3
MF v[8:11],a[228:231],a[144:147],v[8:11]
R16 a[128:131],v241,16384
R16 a[132:135],v241,17408
MF v[8:11],a[236:239],a[148:151],v[8:11]
s_and_b32 s52, s52, 3
s_lshl_b32 s52, s52, 17
MF v[8:11],a[244:247],a[152:155],v[8:11]
s_add_u32 s52, s52, s9
MF v[8:11],a[252:255],a[156:159],v[8:11]
buffer_store_dwordx4 v[148:151], v242, s[20:23], s52 offen offset:0 sc1
buffer_store_dwordx4 v[152:155], v242, s[20:23], s52 offen offset:1024 sc1
MF v[12:15],a[232:235],a[144:147],v[12:15]
R16 a[136:139],v241,18432
R16 a[140:143],v241,19456
MF v[12:15],a[240:243],a[148:151],v[12:15]
MF v[12:15],a[248:251],a[152:155],v[12:15]
s_add_u32 s49, s80, 0x2
s_and_b32 s49, s49, 3
MF v[12:15],a[0:3],a[156:159],v[12:15]
s_waitcnt lgkmcnt(8)
s_lshl_b32 s49, s49, 13
MF v[8:11],a[4:7],a[160:163],v[8:11]
R16 a[144:147],v241,20480
R16 a[148:151],v241,21504
MF v[8:11],a[12:15],a[164:167],v[8:11]
s_add_u32 s49, s49, s12
s_add_u32 s49, s49, 0x8000
MF v[8:11],a[20:23],a[168:171],v[8:11]
buffer_load_dwordx4 v[160:163], v242, s[20:23], s49 offen offset:0 sc1
buffer_load_dwordx4 v[164:167], v242, s[20:23], s49 offen offset:1024 sc1
MF v[8:11],a[28:31],a[172:175],v[8:11]
s_add_u32 s49, s80, 0x1
s_and_b32 s49, s49, 3
MF v[12:15],a[8:11],a[160:163],v[12:15]
R16 a[152:155],v241,22528
R16 a[156:159],v241,23552
MF v[12:15],a[16:19],a[164:167],v[12:15]
s_lshl_b32 s49, s49, 13
s_add_u32 s49, s49, s12
MF v[12:15],a[24:27],a[168:171],v[12:15]
s_add_u32 s49, s49, 0x8000
s_waitcnt vmcnt(5)
MF v[12:15],v[64:67],a[172:175],v[12:15]
buffer_load_dwordx4 v[168:171], v242, s[20:23], s49 offen offset:0 sc1
s_waitcnt lgkmcnt(8)
MF v[8:11],v[68:71],a[176:179],v[8:11]
R16 a[160:163],v241,24576
R16 a[164:167],v241,25600
MF v[8:11],v[76:79],a[180:183],v[8:11]
buffer_load_dwordx4 v[172:175], v242, s[20:23], s49 offen offset:1024 sc1
v_lshlrev_b32 v48, 16, v128
MF v[8:11],v[84:87],a[184:187],v[8:11]
v_and_b32 v49, 0xffff0000, v128
v_lshlrev_b32 v50, 16, v129
MF v[8:11],v[92:95],a[188:191],v[8:11]
v_and_b32 v51, 0xffff0000, v129
v_lshlrev_b32 v52, 16, v130
MF v[12:15],v[72:75],a[176:179],v[12:15]
R16 a[168:171],v241,26624
R16 a[172:175],v241,27648
MF v[12:15],v[80:83],a[180:183],v[12:15]
v_and_b32 v53, 0xffff0000, v130
v_lshlrev_b32 v54, 16, v131
MF v[12:15],v[88:91],a[184:187],v[12:15]
v_and_b32 v55, 0xffff0000, v131
v_lshlrev_b32 v184, 16, v132
MF v[12:15],v[96:99],a[188:191],v[12:15]
s_waitcnt lgkmcnt(8)
v_and_b32 v185, 0xffff0000, v132
MF v[8:11],v[100:103],a[128:131],v[8:11]
R16 a[176:179],v241,28672
R16 a[180:183],v241,29696
MF v[8:11],v[108:111],a[132:135],v[8:11]
v_add_f32 v48, v48, v184
v_add_f32 v49, v49, v185
MF v[8:11],v[116:119],a[136:139],v[8:11]
v_lshlrev_b32 v186, 16, v133
v_and_b32 v187, 0xffff0000, v133
MF v[8:11],v[124:127],a[140:143],v[8:11]
v_add_f32 v50, v50, v186
v_add_f32 v51, v51, v187
MF v[12:15],v[104:107],a[128:131],v[12:15]
R16 a[184:187],v241,30720
R16 a[188:191],v241,31744
MF v[12:15],v[112:115],a[132:135],v[12:15]
v_lshlrev_b32 v188, 16, v134
v_and_b32 v189, 0xffff0000, v134
MF v[12:15],v[120:123],a[136:139],v[12:15]
v_add_f32 v52, v52, v188
v_add_f32 v53, v53, v189
MF v[12:15],a[64:67],a[140:143],v[12:15]
s_waitcnt lgkmcnt(8)
v_lshlrev_b32 v190, 16, v135
MF v[8:11],a[68:71],a[144:147],v[8:11]
v_and_b32 v191, 0xffff0000, v135
v_add_f32 v54, v54, v190
MF v[8:11],a[76:79],a[148:151],v[8:11]
v_add_f32 v55, v55, v191
v_lshlrev_b32 v184, 16, v136
MF v[8:11],a[84:87],a[152:155],v[8:11]
v_and_b32 v185, 0xffff0000, v136
v_add_f32 v48, v48, v184
MF v[8:11],a[92:95],a[156:159],v[8:11]
v_add_f32 v49, v49, v185
v_lshlrev_b32 v186, 16, v137
MF v[12:15],a[72:75],a[144:147],v[12:15]
s_waitcnt vmcnt(6)
v_cmp_ne_u32 vcc, s37, v222
MF v[12:15],a[80:83],a[148:151],v[12:15]
s_nop 0
s_cbranch_vccnz L_SP_PB_%=
L_RT_PB_%=:
MF v[12:15],a[88:91],a[152:155],v[12:15]
buffer_load_dwordx4 v[144:147], v242, s[20:23], s14 offen offset:0 sc1
v_and_b32 v187, 0xffff0000, v137
MF v[12:15],a[96:99],a[156:159],v[12:15]
s_waitcnt lgkmcnt(4)
v_add_f32 v50, v50, v186
MF v[8:11],a[100:103],a[160:163],v[8:11]
v_add_f32 v51, v51, v187
v_lshlrev_b32 v188, 16, v138
MF v[8:11],a[108:111],a[164:167],v[8:11]
v_and_b32 v189, 0xffff0000, v138
v_add_f32 v52, v52, v188
MF v[8:11],a[116:119],a[168:171],v[8:11]
v_add_f32 v53, v53, v189
v_lshlrev_b32 v190, 16, v139
MF v[8:11],a[124:127],a[172:175],v[8:11]
v_and_b32 v191, 0xffff0000, v139
v_add_f32 v54, v54, v190
MF v[12:15],a[104:107],a[160:163],v[12:15]
v_add_f32 v55, v55, v191
v_lshlrev_b32 v184, 16, v140
MF v[12:15],a[112:115],a[164:167],v[12:15]
v_and_b32 v185, 0xffff0000, v140
v_add_f32 v48, v48, v184
MF v[12:15],a[120:123],a[168:171],v[12:15]
v_add_f32 v49, v49, v185
v_lshlrev_b32 v186, 16, v141
MF v[12:15],a[32:35],a[172:175],v[12:15]
s_waitcnt lgkmcnt(0)
v_and_b32 v187, 0xffff0000, v141
MF v[8:11],a[36:39],a[176:179],v[8:11]
v_add_f32 v50, v50, v186
v_add_f32 v51, v51, v187
MF v[8:11],a[44:47],a[180:183],v[8:11]
v_lshlrev_b32 v188, 16, v142
v_and_b32 v189, 0xffff0000, v142
MF v[8:11],a[52:55],a[184:187],v[8:11]
v_add_f32 v52, v52, v188
v_add_f32 v53, v53, v189
MF v[8:11],a[60:63],a[188:191],v[8:11]
v_lshlrev_b32 v190, 16, v143
v_and_b32 v191, 0xffff0000, v143
MF v[12:15],a[40:43],a[176:179],v[12:15]
v_add_f32 v54, v54, v190
v_add_f32 v55, v55, v191
MF v[12:15],a[48:51],a[180:183],v[12:15]
PK v48,v48,v49
v_and_b32 v49, 0xffff0000, v48
MF v[12:15],a[56:59],a[184:187],v[12:15]
MF v[12:15],v[224:227],a[188:191],v[12:15]
s_nop 3
v_lshlrev_b32 v48, 16, v48
PK v50,v50,v51
v_and_b32 v51, 0xffff0000, v50
v_lshlrev_b32 v50, 16, v50
PK v52,v52,v53
v_and_b32 v53, 0xffff0000, v52
v_lshlrev_b32 v52, 16, v52
PK v54,v54,v55
v_and_b32 v55, 0xffff0000, v54
v_lshlrev_b32 v54, 16, v54
v_mul_f32 v48, v48, v56
v_mul_f32 v49, v49, v57
v_mul_f32 v50, v50, v58
v_mul_f32 v51, v51, v59
v_mul_f32 v52, v52, v60
v_mul_f32 v53, v53, v61
v_mul_f32 v54, v54, v62
v_mul_f32 v55, v55, v63
PK v48,v48,v49
v_and_b32 v49, 0xffff0000, v48
v_lshlrev_b32 v48, 16, v48
PK v50,v50,v51
v_and_b32 v51, 0xffff0000, v50
v_lshlrev_b32 v50, 16, v50
PK v52,v52,v53
v_and_b32 v53, 0xffff0000, v52
v_lshlrev_b32 v52, 16, v52
PK v54,v54,v55
v_and_b32 v55, 0xffff0000, v54
v_lshlrev_b32 v54, 16, v54
v_lshlrev_b32 v184, 16, v32
)A"
R"A(v_and_b32 v185, 0xffff0000, v32
v_add_f32 v184, v184, v48
v_add_f32 v185, v185, v49
PK v56,v184,v185
v_lshlrev_b32 v186, 16, v33
v_and_b32 v187, 0xffff0000, v33
v_add_f32 v186, v186, v50
v_add_f32 v187, v187, v51
PK v57,v186,v187
v_lshlrev_b32 v188, 16, v34
v_and_b32 v189, 0xffff0000, v34
v_add_f32 v188, v188, v52
v_add_f32 v189, v189, v53
PK v58,v188,v189
v_lshlrev_b32 v190, 16, v35
v_and_b32 v191, 0xffff0000, v35
v_add_f32 v190, v190, v54
v_add_f32 v191, v191, v55
PK v59,v190,v191
buffer_store_dwordx4 v[56:59], v244, s[24:27], s84 offen offset:0
s_waitcnt vmcnt(1)
v_lshlrev_b32 v184, 16, v144
v_and_b32 v185, 0xffff0000, v144
v_add_f32 v48, v8, v184
v_add_f32 v49, v9, v185
v_lshlrev_b32 v186, 16, v145
v_and_b32 v187, 0xffff0000, v145
v_add_f32 v50, v10, v186
v_add_f32 v51, v11, v187
v_lshlrev_b32 v188, 16, v146
v_and_b32 v189, 0xffff0000, v146
v_add_f32 v52, v12, v188
v_add_f32 v53, v13, v189
v_lshlrev_b32 v190, 16, v147
v_and_b32 v191, 0xffff0000, v147
v_add_f32 v54, v14, v190
v_add_f32 v55, v15, v191
s_waitcnt vmcnt(2)
v_xor_b32 v161, s37, v161
v_xor_b32 v163, s37, v163
v_xor_b32 v165, s37, v165
v_xor_b32 v167, s37, v167
v_or3_b32 v200, v161, v163, v165
v_or_b32 v200, v200, v167
v_xor_b32 v169, s37, v169
v_xor_b32 v171, s37, v171
v_xor_b32 v173, s37, v173
v_xor_b32 v175, s37, v175
v_or3_b32 v200, v200, v169, v171
v_or3_b32 v200, v200, v173, v175
v_cmp_ne_u32 vcc, 0, v200
s_nop 0
s_cbranch_vccnz L_SP_RC1_%=
L_RT_RC1_%=:
v_lshlrev_b32 v184, 16, v160
v_and_b32 v185, 0xffff0000, v160
v_add_f32 v48, v48, v184
v_add_f32 v49, v49, v185
v_lshlrev_b32 v186, 16, v162
v_and_b32 v187, 0xffff0000, v162
v_add_f32 v50, v50, v186
v_add_f32 v51, v51, v187
v_lshlrev_b32 v188, 16, v164
v_and_b32 v189, 0xffff0000, v164
v_add_f32 v52, v52, v188
v_add_f32 v53, v53, v189
v_lshlrev_b32 v190, 16, v166
v_and_b32 v191, 0xffff0000, v166
v_add_f32 v54, v54, v190
v_add_f32 v55, v55, v191
v_lshlrev_b32 v184, 16, v168
v_and_b32 v185, 0xffff0000, v168
v_add_f32 v48, v48, v184
v_add_f32 v49, v49, v185
v_lshlrev_b32 v186, 16, v170
v_and_b32 v187, 0xffff0000, v170
v_add_f32 v50, v50, v186
v_add_f32 v51, v51, v187
v_lshlrev_b32 v188, 16, v172
v_and_b32 v189, 0xffff0000, v172
v_add_f32 v52, v52, v188
v_add_f32 v53, v53, v189
v_lshlrev_b32 v190, 16, v174
v_and_b32 v191, 0xffff0000, v174
v_add_f32 v54, v54, v190
v_add_f32 v55, v55, v191
PK v48,v48,v49
v_and_b32 v49, 0xffff0000, v48
v_lshlrev_b32 v48, 16, v48
PK v50,v50,v51
v_and_b32 v51, 0xffff0000, v50
v_lshlrev_b32 v50, 16, v50
PK v52,v52,v53
v_and_b32 v53, 0xffff0000, v52
v_lshlrev_b32 v52, 16, v52
PK v54,v54,v55
v_and_b32 v55, 0xffff0000, v54
v_lshlrev_b32 v54, 16, v54
v_mul_f32 v48, v48, v208
v_mul_f32 v49, v49, v209
v_mul_f32 v50, v50, v210
v_mul_f32 v51, v51, v211
v_mul_f32 v52, v52, v212
v_mul_f32 v53, v53, v213
v_mul_f32 v54, v54, v214
v_mul_f32 v55, v55, v215
PK v48,v48,v49
v_and_b32 v49, 0xffff0000, v48
v_lshlrev_b32 v48, 16, v48
PK v50,v50,v51
v_and_b32 v51, 0xffff0000, v50
v_lshlrev_b32 v50, 16, v50
PK v52,v52,v53
v_and_b32 v53, 0xffff0000, v52
v_lshlrev_b32 v52, 16, v52
PK v54,v54,v55
v_and_b32 v55, 0xffff0000, v54
v_lshlrev_b32 v54, 16, v54
v_lshlrev_b32 v184, 16, v40
v_and_b32 v185, 0xffff0000, v40
v_add_f32 v184, v184, v48
v_add_f32 v185, v185, v49
PK v208,v184,v185
v_lshlrev_b32 v186, 16, v41
v_and_b32 v187, 0xffff0000, v41
v_add_f32 v186, v186, v50
v_add_f32 v187, v187, v51
PK v209,v186,v187
v_lshlrev_b32 v188, 16, v42
v_and_b32 v189, 0xffff0000, v42
v_add_f32 v188, v188, v52
v_add_f32 v189, v189, v53
PK v210,v188,v189
v_lshlrev_b32 v190, 16, v43
v_and_b32 v191, 0xffff0000, v43
v_add_f32 v190, v190, v54
v_add_f32 v191, v191, v55
PK v211,v190,v191
buffer_store_dwordx4 v[208:211], v244, s[24:27], s85 offen offset:0
s_mov_b32 %[code], 0
s_cmp_lg_u32 %[bw], 0
s_cbranch_scc1 L_END_%=
s_mov_b32 s48, 0x4901000
L_FN_%=:
buffer_load_dwordx4 v[0:3], v242, s[20:23], s48 offen offset:0 sc1
buffer_load_dwordx4 v[4:7], v242, s[20:23], s48 offen offset:1024 sc1
buffer_load_dwordx4 v[8:11], v242, s[20:23], s48 offen offset:2048 sc1
buffer_load_dwordx4 v[12:15], v242, s[20:23], s48 offen offset:3072 sc1
s_waitcnt vmcnt(0)
s_mov_b64 s[40:41], 0
v_cmp_ne_u32 vcc, s37, v0
s_or_b64 s[40:41], s[40:41], vcc
v_cmp_ne_u32 vcc, s37, v1
s_or_b64 s[40:41], s[40:41], vcc
v_cmp_ne_u32 vcc, s37, v2
s_or_b64 s[40:41], s[40:41], vcc
v_cmp_ne_u32 vcc, s37, v3
s_or_b64 s[40:41], s[40:41], vcc
v_cmp_ne_u32 vcc, s37, v4
s_or_b64 s[40:41], s[40:41], vcc
v_cmp_ne_u32 vcc, s37, v5
s_or_b64 s[40:41], s[40:41], vcc
v_cmp_ne_u32 vcc, s37, v6
s_or_b64 s[40:41], s[40:41], vcc
v_cmp_ne_u32 vcc, s37, v7
s_or_b64 s[40:41], s[40:41], vcc
v_cmp_ne_u32 vcc, s37, v8
s_or_b64 s[40:41], s[40:41], vcc
v_cmp_ne_u32 vcc, s37, v9
s_or_b64 s[40:41], s[40:41], vcc
v_cmp_ne_u32 vcc, s37, v10
s_or_b64 s[40:41], s[40:41], vcc
v_cmp_ne_u32 vcc, s37, v11
s_or_b64 s[40:41], s[40:41], vcc
v_cmp_ne_u32 vcc, s37, v12
s_or_b64 s[40:41], s[40:41], vcc
v_cmp_ne_u32 vcc, s37, v13
s_or_b64 s[40:41], s[40:41], vcc
v_cmp_ne_u32 vcc, s37, v14
s_or_b64 s[40:41], s[40:41], vcc
v_cmp_ne_u32 vcc, s37, v15
s_or_b64 s[40:41], s[40:41], vcc
s_cmp_eq_u64 s[40:41], 0
s_cbranch_scc1 L_FOK_%=
s_sleep 4
s_sub_u32 s87, s87, 1
s_cbranch_scc0 L_FN_%=
L_FOK_%=:
v_mov_b32 v0, s37
v_mov_b32 v1, 0
s_mov_b32 s48, 0x4900000
buffer_store_dword v0, v1, s[20:23], s48 offen sc1
s_branch L_END_%=
L_SP_K2_%=:
s_sleep 4
buffer_load_dword v248, v249, s[20:23], s51 offen offset:0 sc1
s_waitcnt vmcnt(0)
v_cmp_ne_u32 vcc, s37, v248
s_sub_u32 s87, s87, 1
s_cbranch_scc1 L_TO_K2_%=
s_cbranch_vccnz L_SP_K2_%=
s_branch L_RT_K2_%=
L_TO_K2_%=:
s_mov_b32 s87, 0
s_branch L_RT_K2_%=
L_STUB0_%=:
s_mov_b32 s90, 0x0
v_lshrrev_b32 v252, 6, v242
v_lshlrev_b32 v252, 9, v252
v_and_b32 v253, 48, v242
v_add_u32 v252, v252, v253
s_lshl_b32 s79, s28, 7
s_add_u32 s79, s79, 0x20000
v_add_u32 v252, s79, v252
RT a[196:197],v252,0
RT a[198:199],v252,8192
RT a[200:201],v252,8
RT a[202:203],v252,8200
RT a[204:205],v252,64
RT a[206:207],v252,8256
RT a[208:209],v252,72
RT a[210:211],v252,8264
v_lshrrev_b32 v249, 2, v242
v_and_b32 v249, 0x7c, v249
s_lshl_b32 s51, s30, 7
s_add_u32 s51, s51, 0x4906000
buffer_load_dword v248, v249, s[20:23], s51 offen offset:0 sc1
s_and_b32 s79, s30, 6
s_lshl_b32 s79, s79, 19
s_and_b32 s49, s29, 1
s_lshl_b32 s49, s49, 19
s_add_u32 s79, s79, s49
s_lshl_b32 s49, s28, 17
s_add_u32 s79, s79, s49
s_and_b32 s49, s30, 1
s_lshl_b32 s49, s49, 13
s_add_u32 s79, s79, s49
s_lshr_b32 s49, s30, 3
s_lshl_b32 s49, s49, 14
s_add_u32 s79, s79, s49
s_lshr_b32 s49, s29, 1
s_lshl_b32 s49, s49, 11
s_add_u32 s79, s79, s49
s_add_u32 s79, s79, 0x3400000
s_waitcnt lgkmcnt(0)
buffer_store_dwordx4 a[196:199], v242, s[20:23], s79 offen offset:0 sc1
buffer_store_dwordx4 a[200:203], v242, s[20:23], s79 offen offset:1024 sc1
s_add_u32 s79, s79, 0x10000
buffer_store_dwordx4 a[204:207], v242, s[20:23], s79 offen offset:0 sc1
buffer_store_dwordx4 a[208:211], v242, s[20:23], s79 offen offset:1024 sc1
s_swappc_b64 s[16:17], s[18:19]
s_mov_b32 s90, 0x7fffffff
s_lshl_b32 s49, s30, 3
s_add_u32 s49, s49, s29
s_lshl_b32 s49, s49, 2
s_add_u32 s49, s49, s28
s_lshl_b32 s49, s49, 2
s_add_u32 s49, s49, 0x4905000
v_mov_b32 v252, s37
v_mov_b32 v253, 0
s_waitcnt vmcnt(8)
buffer_store_dword v252, v253, s[20:23], s49 offen offset:0 sc1
s_waitcnt vmcnt(13)
v_cmp_ne_u32 vcc, s37, v248
s_nop 0
s_cbranch_vccnz L_SP_K2_%=
L_RT_K2_%=:
s_swappc_b64 s[16:17], s[18:19]
L_SP_KEY_%=:
s_sleep 4
s_add_u32 s51, 0, 0x4902000
buffer_load_dwordx4 v[192:195], v242, s[20:23], s51 offen offset:0 sc1
buffer_load_dwordx4 v[196:199], v242, s[20:23], s51 offen offset:1024 sc1
buffer_load_dwordx4 v[200:203], v242, s[20:23], s51 offen offset:2048 sc1
buffer_load_dwordx4 v[204:207], v242, s[20:23], s51 offen offset:3072 sc1
s_add_u32 s51, s51, 0x1000
buffer_load_dwordx4 v[208:211], v242, s[20:23], s51 offen offset:0 sc1
buffer_load_dwordx4 v[212:215], v242, s[20:23], s51 offen offset:1024 sc1
buffer_load_dwordx4 v[216:219], v242, s[20:23], s51 offen offset:2048 sc1
buffer_load_dwordx4 v[220:223], v242, s[20:23], s51 offen offset:3072 sc1
s_waitcnt vmcnt(0)
v_max_u32 v236, v192, v194
v_xor_b32 v193, s37, v193
v_xor_b32 v195, s37, v195
v_or_b32 v237, v193, v195
v_max3_u32 v236, v236, v196, v198
v_xor_b32 v197, s37, v197
v_xor_b32 v199, s37, v199
v_or3_b32 v237, v237, v197, v199
v_max3_u32 v236, v236, v200, v202
v_xor_b32 v201, s37, v201
v_xor_b32 v203, s37, v203
v_or3_b32 v237, v237, v201, v203
v_max3_u32 v236, v236, v204, v206
v_xor_b32 v205, s37, v205
v_xor_b32 v207, s37, v207
v_or3_b32 v237, v237, v205, v207
v_max3_u32 v236, v236, v208, v210
v_xor_b32 v209, s37, v209
v_xor_b32 v211, s37, v211
v_or3_b32 v237, v237, v209, v211
v_max3_u32 v236, v236, v212, v214
v_xor_b32 v213, s37, v213
v_xor_b32 v215, s37, v215
v_or3_b32 v237, v237, v213, v215
v_max3_u32 v236, v236, v216, v218
v_xor_b32 v217, s37, v217
v_xor_b32 v219, s37, v219
v_or3_b32 v237, v237, v217, v219
v_max3_u32 v236, v236, v220, v222
v_xor_b32 v221, s37, v221
v_xor_b32 v223, s37, v223
v_or3_b32 v237, v237, v221, v223
v_cmp_ne_u32 vcc, 0, v237
s_sub_u32 s87, s87, 1
s_cbranch_scc1 L_TO_KEY_%=
s_cbranch_vccnz L_SP_KEY_%=
s_branch L_RT_KEY_%=
L_TO_KEY_%=:
s_mov_b32 s87, 0
s_branch L_RT_KEY_%=
L_SP_GP_%=:
s_sleep 4
buffer_load_dwordx4 v[56:59], v236, s[20:23], s51 offen offset:0 sc1
buffer_load_dwordx4 v[32:35], v236, s[20:23], s79 offen offset:0 sc1
s_waitcnt vmcnt(0)
v_xor_b32 v57, s37, v57
v_xor_b32 v59, s37, v59
v_xor_b32 v33, s37, v33
v_xor_b32 v35, s37, v35
v_or3_b32 v237, v57, v59, v33
v_or_b32 v237, v237, v35
v_cmp_ne_u32 vcc, 0, v237
s_sub_u32 s87, s87, 1
s_cbranch_scc1 L_TO_GP_%=
s_cbranch_vccnz L_SP_GP_%=
s_branch L_RT_GP_%=
L_TO_GP_%=:
s_mov_b32 s87, 0
s_branch L_RT_GP_%=
L_SP_K1_%=:
s_sleep 4
buffer_load_dword v243, v244, s[20:23], s51 offen offset:0 sc1
s_waitcnt vmcnt(0)
v_cmp_ne_u32 vcc, s37, v243
s_sub_u32 s87, s87, 1
s_cbranch_scc1 L_TO_K1_%=
s_cbranch_vccnz L_SP_K1_%=
s_branch L_RT_K1_%=
L_TO_K1_%=:
s_mov_b32 s87, 0
s_branch L_RT_K1_%=
L_SP_YB_%=:
s_sleep 4
buffer_load_dwordx4 v[224:227], v242, s[20:23], s79 offen offset:0 sc1
buffer_load_dwordx4 v[228:231], v242, s[20:23], s79 offen offset:1024 sc1
buffer_load_dwordx4 v[232:235], v242, s[20:23], s79 offen offset:2048 sc1
buffer_load_dwordx4 v[236:239], v242, s[20:23], s79 offen offset:3072 sc1
s_waitcnt vmcnt(0)
v_xor_b32 v225, s37, v225
v_xor_b32 v227, s37, v227
v_xor_b32 v229, s37, v229
v_xor_b32 v231, s37, v231
v_xor_b32 v233, s37, v233
v_xor_b32 v235, s37, v235
v_xor_b32 v237, s37, v237
v_xor_b32 v239, s37, v239
v_or3_b32 v243, v225, v227, v229
v_or3_b32 v243, v243, v231, v233
v_or3_b32 v243, v243, v235, v237
v_or_b32 v243, v243, v239
v_cmp_ne_u32 vcc, 0, v243
s_sub_u32 s87, s87, 1
s_cbranch_scc1 L_TO_YB_%=
s_cbranch_vccnz L_SP_YB_%=
s_branch L_RT_YB_%=
L_TO_YB_%=:
s_mov_b32 s87, 0
s_branch L_RT_YB_%=
L_SP_WF6_%=:
s_sleep 4
buffer_load_dword v251, v250, s[20:23], s52 offen offset:0 sc1
s_waitcnt vmcnt(0)
v_cmp_ne_u32 vcc, s37, v251
s_sub_u32 s87, s87, 1
s_cbranch_scc1 L_TO_WF6_%=
s_cbranch_vccnz L_SP_WF6_%=
s_branch L_RT_WF6_%=
L_TO_WF6_%=:
s_mov_b32 s87, 0
s_branch L_RT_WF6_%=
L_SP_WF_%=:
s_sleep 4
buffer_load_dword v251, v250, s[20:23], s52 offen offset:0 sc1
s_waitcnt vmcnt(0)
v_cmp_ne_u32 vcc, s37, v251
s_sub_u32 s87, s87, 1
s_cbranch_scc1 L_TO_WF_%=
s_cbranch_vccnz L_SP_WF_%=
s_branch L_RT_WF_%=
)A"
R"A(L_TO_WF_%=:
s_mov_b32 s87, 0
s_branch L_RT_WF_%=
L_SP_A3_%=:
s_sleep 4
buffer_load_dword v206, v207, s[20:23], s95 offen offset:0 sc1
s_waitcnt vmcnt(0)
v_cmp_ne_u32 vcc, s37, v206
s_sub_u32 s87, s87, 1
s_cbranch_scc1 L_TO_A3_%=
s_cbranch_vccnz L_SP_A3_%=
s_branch L_RT_A3_%=
L_TO_A3_%=:
s_mov_b32 s87, 0
s_branch L_RT_A3_%=
L_SP_B_%=:
buffer_load_dword v206, v207, s[20:23], s95 offen offset:0 sc1
s_waitcnt vmcnt(0)
v_cmp_ne_u32 vcc, s37, v206
s_sub_u32 s87, s87, 1
s_cbranch_scc1 L_TO_B_%=
s_cbranch_vccnz L_SP_B_%=
s_branch L_RT_B_%=
L_TO_B_%=:
s_mov_b32 s87, 0
s_branch L_RT_B_%=
L_Y0_%=:
v_xor_b32 v184, s37, v193
v_xor_b32 v185, s37, v195
v_xor_b32 v186, s37, v197
v_xor_b32 v187, s37, v199
v_or3_b32 v184, v184, v185, v186
v_or_b32 v184, v184, v187
v_cmp_ne_u32 vcc, 0, v184
s_nop 1
s_cbranch_vccz L_OK_Y0_%=
s_sub_u32 s87, s87, 1
s_cbranch_scc1 L_TO_Y0_%=
s_sleep 4
buffer_load_dwordx4 v[192:195], v205, s[20:23], s94 offen sc1
buffer_load_dwordx4 v[196:199], v205, s[20:23], s94 offen offset:16 sc1
s_waitcnt vmcnt(0)
s_branch L_Y0_%=
L_TO_Y0_%=:
s_mov_b32 s87, 0
L_OK_Y0_%=:
v_add_f32_dpp v192, v192, v192 quad_perm:[1,0,3,2] row_mask:0xf bank_mask:0xf
v_add_f32_dpp v194, v194, v194 quad_perm:[1,0,3,2] row_mask:0xf bank_mask:0xf
v_add_f32_dpp v196, v196, v196 quad_perm:[1,0,3,2] row_mask:0xf bank_mask:0xf
v_add_f32_dpp v198, v198, v198 quad_perm:[1,0,3,2] row_mask:0xf bank_mask:0xf
v_add_f32_dpp v192, v192, v192 quad_perm:[2,3,0,1] row_mask:0xf bank_mask:0xf
v_add_f32_dpp v194, v194, v194 quad_perm:[2,3,0,1] row_mask:0xf bank_mask:0xf
v_add_f32_dpp v196, v196, v196 quad_perm:[2,3,0,1] row_mask:0xf bank_mask:0xf
v_add_f32_dpp v198, v198, v198 quad_perm:[2,3,0,1] row_mask:0xf bank_mask:0xf
v_add_f32_dpp v192, v192, v192 row_half_mirror row_mask:0xf bank_mask:0xf
v_add_f32_dpp v194, v194, v194 row_half_mirror row_mask:0xf bank_mask:0xf
v_add_f32_dpp v196, v196, v196 row_half_mirror row_mask:0xf bank_mask:0xf
v_add_f32_dpp v198, v198, v198 row_half_mirror row_mask:0xf bank_mask:0xf
s_and_b32 s91, s78, 0xffff0000
s_ashr_i32 s92, s91, 31
s_not_b32 s92, s92
s_or_b32 s92, s92, 0x80000000
s_xor_b32 s91, s91, s92
s_and_b32 s91, s91, 0xffff0000
v_mov_b32 v193, 0xbfb8aa3b
v_mul_f32 v193, s91, v193
v_exp_f32 v193, v193
s_nop 0
v_add_f32 v193, 1.0, v193
v_rcp_f32 v193, v193
s_nop 0
PK v193,v193,v193
v_lshlrev_b32 v193, 16, v193
PK v192,v192,v192
v_lshlrev_b32 v192, 16, v192
v_mul_f32 v192, v192, v193
PK v192,v192,v192
v_lshlrev_b32 v192, 16, v192
PK v194,v194,v194
v_lshlrev_b32 v194, 16, v194
v_mul_f32 v194, v194, v193
PK v194,v194,v194
v_lshlrev_b32 v194, 16, v194
PK v196,v196,v196
v_lshlrev_b32 v196, 16, v196
v_mul_f32 v196, v196, v193
PK v196,v196,v196
v_lshlrev_b32 v196, 16, v196
PK v198,v198,v198
v_lshlrev_b32 v198, 16, v198
v_mul_f32 v198, v198, v193
PK v198,v198,v198
v_lshlrev_b32 v198, 16, v198
v_bfe_u32 v195, v242, 7, 1
v_cmp_eq_u32 vcc, 0, v195
v_mov_b32_dpp v197, v192 row_ror:8 row_mask:0xf bank_mask:0xf
s_nop 0
v_cndmask_b32 v184, v197, v192, vcc
v_cndmask_b32 v188, v192, v197, vcc
v_mov_b32_dpp v197, v194 row_ror:8 row_mask:0xf bank_mask:0xf
v_cndmask_b32 v185, v197, v194, vcc
v_cndmask_b32 v189, v194, v197, vcc
v_mov_b32_dpp v197, v196 row_ror:8 row_mask:0xf bank_mask:0xf
v_cndmask_b32 v186, v197, v196, vcc
v_cndmask_b32 v190, v196, v197, vcc
v_mov_b32_dpp v197, v198 row_ror:8 row_mask:0xf bank_mask:0xf
v_cndmask_b32 v187, v197, v198, vcc
v_cndmask_b32 v191, v198, v197, vcc
v_add_f32 v184, v184, v56
v_add_f32 v185, v185, v57
v_add_f32 v186, v186, v58
v_add_f32 v187, v187, v59
v_add_f32 v188, v188, v60
v_add_f32 v189, v189, v61
v_add_f32 v190, v190, v62
v_add_f32 v191, v191, v63
s_and_b32 s91, s77, 15
v_bfe_u32 v195, v242, 4, 4
v_cmp_eq_u32 vcc, s91, v195
PK v184,v184,v185
v_and_b32 v185, 0xffff0000, v184
v_lshlrev_b32 v184, 16, v184
PK v186,v186,v187
v_and_b32 v187, 0xffff0000, v186
v_lshlrev_b32 v186, 16, v186
PK v188,v188,v189
v_and_b32 v189, 0xffff0000, v188
v_lshlrev_b32 v188, 16, v188
PK v190,v190,v191
v_and_b32 v191, 0xffff0000, v190
v_lshlrev_b32 v190, 16, v190
v_cndmask_b32 v56, v56, v184, vcc
v_cndmask_b32 v57, v57, v185, vcc
v_cndmask_b32 v58, v58, v186, vcc
v_cndmask_b32 v59, v59, v187, vcc
v_cndmask_b32 v60, v60, v188, vcc
v_cndmask_b32 v61, v61, v189, vcc
v_cndmask_b32 v62, v62, v190, vcc
v_cndmask_b32 v63, v63, v191, vcc
s_branch L_RT_Y0_%=
L_SP_RC1_%=:
s_add_u32 s49, s80, 0x2
s_and_b32 s49, s49, 3
s_lshl_b32 s49, s49, 13
s_add_u32 s49, s49, s12
s_add_u32 s49, s49, 0x8000
buffer_load_dwordx4 v[160:163], v242, s[20:23], s49 offen offset:0 sc1
buffer_load_dwordx4 v[164:167], v242, s[20:23], s49 offen offset:1024 sc1
s_add_u32 s49, s80, 0x1
s_and_b32 s49, s49, 3
s_lshl_b32 s49, s49, 13
s_add_u32 s49, s49, s12
s_add_u32 s49, s49, 0x8000
buffer_load_dwordx4 v[168:171], v242, s[20:23], s49 offen offset:0 sc1
buffer_load_dwordx4 v[172:175], v242, s[20:23], s49 offen offset:1024 sc1
s_waitcnt vmcnt(0)
v_xor_b32 v161, s37, v161
v_xor_b32 v163, s37, v163
v_xor_b32 v165, s37, v165
v_xor_b32 v167, s37, v167
v_or3_b32 v200, v161, v163, v165
v_or_b32 v200, v200, v167
v_xor_b32 v169, s37, v169
v_xor_b32 v171, s37, v171
v_xor_b32 v173, s37, v173
v_xor_b32 v175, s37, v175
v_or3_b32 v200, v200, v169, v171
v_or3_b32 v200, v200, v173, v175
v_cmp_ne_u32 vcc, 0, v200
s_sub_u32 s87, s87, 1
s_cbranch_scc1 L_TO_RC1_%=
s_cbranch_vccnz L_SP_RC1_%=
s_branch L_RT_RC1_%=
L_TO_RC1_%=:
s_mov_b32 s87, 0
s_branch L_RT_RC1_%=
L_Y1_%=:
v_xor_b32 v184, s37, v193
v_xor_b32 v185, s37, v195
v_xor_b32 v186, s37, v197
v_xor_b32 v187, s37, v199
v_or3_b32 v184, v184, v185, v186
v_or_b32 v184, v184, v187
v_cmp_ne_u32 vcc, 0, v184
s_nop 1
s_cbranch_vccz L_OK_Y1_%=
s_sub_u32 s87, s87, 1
s_cbranch_scc1 L_TO_Y1_%=
s_sleep 4
buffer_load_dwordx4 v[192:195], v205, s[20:23], s94 offen sc1
buffer_load_dwordx4 v[196:199], v205, s[20:23], s94 offen offset:16 sc1
s_waitcnt vmcnt(0)
s_branch L_Y1_%=
L_TO_Y1_%=:
s_mov_b32 s87, 0
L_OK_Y1_%=:
v_add_f32_dpp v192, v192, v192 quad_perm:[1,0,3,2] row_mask:0xf bank_mask:0xf
v_add_f32_dpp v194, v194, v194 quad_perm:[1,0,3,2] row_mask:0xf bank_mask:0xf
v_add_f32_dpp v196, v196, v196 quad_perm:[1,0,3,2] row_mask:0xf bank_mask:0xf
v_add_f32_dpp v198, v198, v198 quad_perm:[1,0,3,2] row_mask:0xf bank_mask:0xf
v_add_f32_dpp v192, v192, v192 quad_perm:[2,3,0,1] row_mask:0xf bank_mask:0xf
v_add_f32_dpp v194, v194, v194 quad_perm:[2,3,0,1] row_mask:0xf bank_mask:0xf
v_add_f32_dpp v196, v196, v196 quad_perm:[2,3,0,1] row_mask:0xf bank_mask:0xf
v_add_f32_dpp v198, v198, v198 quad_perm:[2,3,0,1] row_mask:0xf bank_mask:0xf
v_add_f32_dpp v192, v192, v192 row_half_mirror row_mask:0xf bank_mask:0xf
v_add_f32_dpp v194, v194, v194 row_half_mirror row_mask:0xf bank_mask:0xf
v_add_f32_dpp v196, v196, v196 row_half_mirror row_mask:0xf bank_mask:0xf
v_add_f32_dpp v198, v198, v198 row_half_mirror row_mask:0xf bank_mask:0xf
s_and_b32 s91, s78, 0xffff0000
s_ashr_i32 s92, s91, 31
s_not_b32 s92, s92
s_or_b32 s92, s92, 0x80000000
s_xor_b32 s91, s91, s92
s_and_b32 s91, s91, 0xffff0000
v_mov_b32 v193, 0xbfb8aa3b
v_mul_f32 v193, s91, v193
v_exp_f32 v193, v193
s_nop 0
v_add_f32 v193, 1.0, v193
v_rcp_f32 v193, v193
s_nop 0
PK v193,v193,v193
v_lshlrev_b32 v193, 16, v193
PK v192,v192,v192
v_lshlrev_b32 v192, 16, v192
v_mul_f32 v192, v192, v193
PK v192,v192,v192
v_lshlrev_b32 v192, 16, v192
PK v194,v194,v194
v_lshlrev_b32 v194, 16, v194
v_mul_f32 v194, v194, v193
PK v194,v194,v194
v_lshlrev_b32 v194, 16, v194
PK v196,v196,v196
v_lshlrev_b32 v196, 16, v196
v_mul_f32 v196, v196, v193
PK v196,v196,v196
v_lshlrev_b32 v196, 16, v196
PK v198,v198,v198
v_lshlrev_b32 v198, 16, v198
v_mul_f32 v198, v198, v193
PK v198,v198,v198
v_lshlrev_b32 v198, 16, v198
v_bfe_u32 v195, v242, 7, 1
v_cmp_eq_u32 vcc, 0, v195
v_mov_b32_dpp v197, v192 row_ror:8 row_mask:0xf bank_mask:0xf
s_nop 0
v_cndmask_b32 v184, v197, v192, vcc
v_cndmask_b32 v188, v192, v197, vcc
v_mov_b32_dpp v197, v194 row_ror:8 row_mask:0xf bank_mask:0xf
v_cndmask_b32 v185, v197, v194, vcc
v_cndmask_b32 v189, v194, v197, vcc
v_mov_b32_dpp v197, v196 row_ror:8 row_mask:0xf bank_mask:0xf
v_cndmask_b32 v186, v197, v196, vcc
v_cndmask_b32 v190, v196, v197, vcc
v_mov_b32_dpp v197, v198 row_ror:8 row_mask:0xf bank_mask:0xf
v_cndmask_b32 v187, v197, v198, vcc
v_cndmask_b32 v191, v198, v197, vcc
v_add_f32 v184, v184, v208
v_add_f32 v185, v185, v209
v_add_f32 v186, v186, v210
v_add_f32 v187, v187, v211
v_add_f32 v188, v188, v212
v_add_f32 v189, v189, v213
v_add_f32 v190, v190, v214
v_add_f32 v191, v191, v215
s_and_b32 s91, s77, 15
v_bfe_u32 v195, v242, 4, 4
v_cmp_eq_u32 vcc, s91, v195
PK v184,v184,v185
v_and_b32 v185, 0xffff0000, v184
v_lshlrev_b32 v184, 16, v184
PK v186,v186,v187
v_and_b32 v187, 0xffff0000, v186
v_lshlrev_b32 v186, 16, v186
PK v188,v188,v189
v_and_b32 v189, 0xffff0000, v188
v_lshlrev_b32 v188, 16, v188
PK v190,v190,v191
v_and_b32 v191, 0xffff0000, v190
v_lshlrev_b32 v190, 16, v190
v_cndmask_b32 v208, v208, v184, vcc
v_cndmask_b32 v209, v209, v185, vcc
v_cndmask_b32 v210, v210, v186, vcc
v_cndmask_b32 v211, v211, v187, vcc
v_cndmask_b32 v212, v212, v188, vcc
v_cndmask_b32 v213, v213, v189, vcc
v_cndmask_b32 v214, v214, v190, vcc
v_cndmask_b32 v215, v215, v191, vcc
s_branch L_RT_Y1_%=
L_SP_PA_%=:
buffer_load_dword v220, v217, s[20:23], s11 offen offset:0 sc1
s_waitcnt vmcnt(0)
v_cmp_ne_u32 vcc, s37, v220
s_sub_u32 s87, s87, 1
s_cbranch_scc1 L_TO_PA_%=
s_cbranch_vccnz L_SP_PA_%=
s_branch L_RT_PA_%=
L_TO_PA_%=:
s_mov_b32 s87, 0
s_branch L_RT_PA_%=
L_SP_PB_%=:
buffer_load_dword v222, v221, s[20:23], s13 offen offset:0 sc1
s_waitcnt vmcnt(0)
v_cmp_ne_u32 vcc, s37, v222
s_sub_u32 s87, s87, 1
s_cbranch_scc1 L_TO_PB_%=
s_cbranch_vccnz L_SP_PB_%=
s_branch L_RT_PB_%=
L_TO_PB_%=:
s_mov_b32 s87, 0
s_branch L_RT_PB_%=
L_EXIT1_%=:
v_mul_f32 v128, v128, v218
v_mul_f32 v129, v129, v218
v_mul_f32 v130, v130, v218
v_mul_f32 v131, v131, v218
v_mul_f32 v132, v132, v218
v_mul_f32 v133, v133, v218
v_mul_f32 v134, v134, v218
v_mul_f32 v135, v135, v218
PK v128,v128,v129
PK v129,v130,v131
PK v130,v132,v133
PK v131,v134,v135
buffer_store_dwordx4 v[128:131], v242, s[20:23], s36 offen offset:0 sc1
v_mul_f32 v136, v136, v218
v_mul_f32 v137, v137, v218
v_mul_f32 v138, v138, v218
v_mul_f32 v139, v139, v218
v_mul_f32 v140, v140, v218
v_mul_f32 v141, v141, v218
v_mul_f32 v142, v142, v218
v_mul_f32 v143, v143, v218
PK v136,v136,v137
PK v137,v138,v139
PK v138,v140,v141
PK v139,v142,v143
buffer_store_dwordx4 v[136:139], v242, s[20:23], s36 offen offset:1024 sc1
v_mul_f32 v144, v144, v218
v_mul_f32 v145, v145, v218
v_mul_f32 v146, v146, v218
v_mul_f32 v147, v147, v218
v_mul_f32 v148, v148, v218
v_mul_f32 v149, v149, v218
v_mul_f32 v150, v150, v218
v_mul_f32 v151, v151, v218
PK v144,v144,v145
PK v145,v146,v147
PK v146,v148,v149
PK v147,v150,v151
buffer_store_dwordx4 v[144:147], v242, s[20:23], s36 offen offset:2048 sc1
v_mul_f32 v152, v152, v218
v_mul_f32 v153, v153, v218
v_mul_f32 v154, v154, v218
v_mul_f32 v155, v155, v218
v_mul_f32 v156, v156, v218
v_mul_f32 v157, v157, v218
v_mul_f32 v158, v158, v218
v_mul_f32 v159, v159, v218
PK v152,v152,v153
PK v153,v154,v155
PK v154,v156,v157
PK v155,v158,v159
buffer_store_dwordx4 v[152:155], v242, s[20:23], s36 offen offset:3072 sc1
v_mul_f32 v160, v160, v222
v_mul_f32 v161, v161, v222
v_mul_f32 v162, v162, v222
v_mul_f32 v163, v163, v222
v_mul_f32 v164, v164, v222
v_mul_f32 v165, v165, v222
v_mul_f32 v166, v166, v222
v_mul_f32 v167, v167, v222
PK v160,v160,v161
PK v161,v162,v163
PK v162,v164,v165
PK v163,v166,v167
s_add_u32 s36, s36, 0x20000
buffer_store_dwordx4 v[160:163], v242, s[20:23], s36 offen offset:0 sc1
v_mul_f32 v168, v168, v222
v_mul_f32 v169, v169, v222
v_mul_f32 v170, v170, v222
v_mul_f32 v171, v171, v222
v_mul_f32 v172, v172, v222
v_mul_f32 v173, v173, v222
v_mul_f32 v174, v174, v222
v_mul_f32 v175, v175, v222
PK v168,v168,v169
PK v169,v170,v171
PK v170,v172,v173
PK v171,v174,v175
buffer_store_dwordx4 v[168:171], v242, s[20:23], s36 offen offset:1024 sc1
v_mul_f32 v176, v176, v222
v_mul_f32 v177, v177, v222
v_mul_f32 v178, v178, v222
v_mul_f32 v179, v179, v222
v_mul_f32 v180, v180, v222
v_mul_f32 v181, v181, v222
v_mul_f32 v182, v182, v222
v_mul_f32 v183, v183, v222
)A"
R"A(PK v176,v176,v177
PK v177,v178,v179
PK v178,v180,v181
PK v179,v182,v183
buffer_store_dwordx4 v[176:179], v242, s[20:23], s36 offen offset:2048 sc1
v_mul_f32 v184, v184, v222
v_mul_f32 v185, v185, v222
v_mul_f32 v186, v186, v222
v_mul_f32 v187, v187, v222
v_mul_f32 v188, v188, v222
v_mul_f32 v189, v189, v222
v_mul_f32 v190, v190, v222
v_mul_f32 v191, v191, v222
PK v184,v184,v185
PK v185,v186,v187
PK v186,v188,v189
PK v187,v190,v191
buffer_store_dwordx4 v[184:187], v242, s[20:23], s36 offen offset:3072 sc1
s_waitcnt vmcnt(0) lgkmcnt(0)
s_cmp_lg_u32 s89, s37
s_cselect_b32 s49, 4, 0
s_or_b32 %[code], s48, s49
s_mov_b32 %[tag], s37
s_mov_b32 %[key], s78
L_END_%=:
)A"
      : [code] "=&s"(code), [tag] "=&s"(tag), [key] "=&s"(key)
      : [ka] "s"(__builtin_amdgcn_kernarg_segment_ptr()), [bw] "s"(bw)
      : CLOBBERS);
  if (code != 0) {
    const char* ka = (const char*)__builtin_amdgcn_kernarg_segment_ptr();
    unsigned b2 = bw;
    asm volatile("" : "+s"(ka), "+s"(b2));
    if (code & 1) slow_rows(*(const u16* const*)(ka + 16), *(char* const*)(ka + 88), b2 >> 2, b2 & 3, (code & 4) ? 0 : 2);
    asm volatile(
R"A(s_load_dwordx2 s[8:9], %[ka], 24
s_load_dwordx2 s[12:13], %[ka], 32
s_load_dwordx2 s[16:17], %[ka], 8
s_load_dwordx2 s[56:57], %[ka], 0
s_load_dwordx2 s[60:61], %[ka], 48
s_load_dwordx2 s[20:21], %[ka], 88
s_load_dwordx2 s[24:25], %[ka], 16
s_load_dwordx2 s[72:73], %[ka], 40
s_load_dwordx2 s[68:69], %[ka], 56
s_load_dwordx2 s[64:65], %[ka], 64
s_load_dwordx2 s[70:71], %[ka], 72
s_load_dwordx2 s[74:75], %[ka], 80
s_lshr_b32 s76, %[bw], 2
s_and_b32 s28, %[bw], 3
s_lshr_b32 s29, s76, 3
s_and_b32 s30, s76, 7
s_lshr_b32 s31, s29, 3
s_lshl_b32 s31, s31, 3
s_add_u32 s30, s30, s31
s_and_b32 s29, s29, 7
v_mbcnt_lo_u32_b32 v0, -1, 0
v_mbcnt_hi_u32_b32 v0, -1, v0
v_lshlrev_b32 v242, 4, v0
v_mov_b32 v245, 0
v_lshlrev_b32 v243, 5, v0
v_and_b32 v244, 0x3ff, v243
v_lshrrev_b32 v1, 4, v0
v_and_b32 v2, 15, v0
v_lshlrev_b32 v3, 1, v1
v_xor_b32 v3, v3, v2
v_lshlrev_b32 v3, 4, v3
v_lshl_add_u32 v232, v1, 13, v3
v_add_u32 v233, 31744, v232
v_xor_b32 v234, 0x80, v232
v_add_u32 v235, 95232, v234
v_add_u32 v234, 63488, v234
v_lshrrev_b32 v3, 3, v0
v_and_b32 v4, 7, v0
v_lshrrev_b32 v5, 1, v3
v_xor_b32 v4, v4, v5
v_lshlrev_b32 v4, 4, v4
v_lshl_add_u32 v228, v3, 10, v4
v_xor_b32 v229, 0x40, v228
v_add_u32 v230, 14336, v228
v_add_u32 v231, 21504, v229
v_add_u32 v229, 7168, v229
s_lshl_b32 s34, s28, 12
s_lshl_b32 s85, s28, 13
s_lshl_b32 s80, s28, 17
s_mov_b32 s86, s80
s_lshl_b32 s81, s28, 18
s_lshl_b32 s48, s29, 15
s_add_u32 s81, s81, s48
s_lshr_b32 s48, s30, 3
s_add_u32 s48, s48, s29
s_and_b32 s48, s48, 1
s_lshl_b32 s49, s48, 19
s_add_u32 s80, s80, s49
s_lshl_b32 s51, s48, 7
s_add_u32 s81, s81, s51
s_mov_b32 s78, s80
s_mov_b32 s84, s81
s_lshl_b32 s79, s76, 13
s_lshl_b32 s48, s28, 11
s_add_u32 s79, s79, s48
s_mov_b32 s11, 0x20000
s_mov_b32 s15, 0x20000
s_mov_b32 s19, 0x20000
s_mov_b32 s18, 0x200000
s_mov_b32 s58, 0x200000
s_mov_b32 s59, 0x20000
s_mov_b32 s62, 0x400
s_mov_b32 s63, 0x20000
s_lshl_b32 s31, s30, 8
s_sub_u32 s10, 0x400000, s31
s_mov_b32 s14, s10
s_waitcnt lgkmcnt(0)
s_and_b32 s57, s57, 0xffff
s_and_b32 s61, s61, 0xffff
s_and_b32 s17, s17, 0xffff
s_mov_b32 s37, %[tag]
s_mov_b32 s78, %[key]
s_and_b32 s77, s78, 0xffff
s_sub_u32 s77, 0xffff, s77
s_min_u32 s77, s77, 2047
s_and_b32 s21, s21, 0xffff
s_mov_b32 s22, 0x4c00000
s_mov_b32 s23, 0x20000
s_mov_b32 s87, 0x40000
s_mov_b64 s[38:39], 0
s_mov_b32 s36, 0x4b00000
s_and_b32 s80, s76, 7
s_and_b32 s81, s80, 1
s_lshr_b32 s80, s80, 1
s_lshl_b32 s81, s81, 3
s_lshr_b32 s82, s76, 5
s_add_u32 s81, s81, s82
s_lshr_b32 s82, s76, 3
s_and_b32 s82, s82, 3
s_lshl_b32 s53, s80, 2
s_add_u32 s53, s53, s82
s_lshl_b32 s53, s53, 2
s_add_u32 s53, s53, s28
s_lshl_b32 s53, s53, 16
s_add_u32 s53, s53, 0x3400000
s_lshl_b32 s47, s80, 15
s_lshl_b32 s55, s28, 13
s_add_u32 s47, s47, s55
s_add_u32 s47, s47, 0x2400000
s_mul_i32 s55, s81, 3
s_add_u32 s84, s55, s80
s_lshl_b32 s55, s55, 17
s_add_u32 s60, s55, s47
s_add_u32 s61, s60, 0x20000
s_add_u32 s62, s60, 0x40000
s_add_u32 s55, s81, 0x30
s_cmp_eq_u32 s80, 3
s_cselect_b32 s84, s55, s84
s_lshl_b32 s55, s55, 17
s_add_u32 s63, s55, s47
s_lshl_b32 s85, s81, 2
s_add_u32 s85, s85, 0x40
s_add_u32 s55, s80, 0x1
s_and_b32 s55, s55, 3
s_add_u32 s55, s55, s85
s_lshl_b32 s55, s55, 17
s_add_u32 s64, s55, s47
s_add_u32 s55, s80, 0x2
s_and_b32 s55, s55, 3
s_add_u32 s55, s55, s85
s_lshl_b32 s55, s55, 17
s_add_u32 s65, s55, s47
s_add_u32 s55, s80, 0x3
s_and_b32 s55, s55, 3
s_add_u32 s55, s55, s85
s_lshl_b32 s55, s55, 17
s_add_u32 s66, s55, s47
s_add_u32 s55, s80, 0x4
s_and_b32 s55, s55, 3
s_add_u32 s55, s55, s85
s_lshl_b32 s55, s55, 17
s_add_u32 s67, s55, s47
s_add_u32 s85, s85, s80
s_and_b32 s54, s29, 1
s_lshl_b32 s54, s54, 3
s_and_b32 s55, s28, 1
s_add_u32 s54, s54, s55
s_lshl_b32 s54, s54, 13
s_lshl_b32 s86, s30, 6
s_lshl_b32 s55, s29, 2
s_add_u32 s88, s86, s28
s_add_u32 s86, s88, s55
s_sub_u32 s88, s88, s55
s_add_u32 s88, s88, 0x3c
s_lshl_b32 s86, s86, 7
s_lshl_b32 s88, s88, 7
s_add_u32 s86, s86, 0x4990000
s_add_u32 s88, s88, 0x4990000
s_lshl_b32 s47, s82, 2
s_add_u32 s47, s47, s28
s_lshl_b32 s94, s47, 11
s_add_u32 s94, s94, 0x4908000
s_lshr_b32 s55, s77, 4
s_sub_u32 s70, s55, s84
s_lshl_b32 s84, s84, 14
s_lshl_b32 s55, s47, 10
s_add_u32 s68, s55, s84
s_lshl_b32 s55, s47, 6
s_add_u32 s84, s84, s55
s_lshr_b32 s55, s77, 4
s_sub_u32 s71, s55, s85
s_lshl_b32 s85, s85, 14
s_lshl_b32 s55, s47, 10
s_add_u32 s69, s55, s85
s_lshl_b32 s55, s47, 6
s_add_u32 s85, s85, s55
v_mov_b32 v0, s37
v_mov_b32 v1, 0
buffer_store_dword v0, v1, s[20:23], s86 offen offset:0 sc1
s_add_u32 s79, s53, 0x0
buffer_load_dwordx4 a[196:199], v242, s[20:23], s79 offen offset:0 sc1
buffer_load_dwordx4 a[200:203], v242, s[20:23], s79 offen offset:1024 sc1
buffer_load_dwordx4 a[204:207], v242, s[20:23], s79 offen offset:2048 sc1
buffer_load_dwordx4 a[208:211], v242, s[20:23], s79 offen offset:3072 sc1
s_add_u32 s79, s53, 0x1000
buffer_load_dwordx4 a[212:215], v242, s[20:23], s79 offen offset:0 sc1
buffer_load_dwordx4 a[216:219], v242, s[20:23], s79 offen offset:1024 sc1
buffer_load_dwordx4 a[220:223], v242, s[20:23], s79 offen offset:2048 sc1
buffer_load_dwordx4 a[224:227], v242, s[20:23], s79 offen offset:3072 sc1
s_add_u32 s79, s53, 0x2000
buffer_load_dwordx4 a[228:231], v242, s[20:23], s79 offen offset:0 sc1
buffer_load_dwordx4 a[232:235], v242, s[20:23], s79 offen offset:1024 sc1
buffer_load_dwordx4 a[236:239], v242, s[20:23], s79 offen offset:2048 sc1
buffer_load_dwordx4 a[240:243], v242, s[20:23], s79 offen offset:3072 sc1
s_add_u32 s79, s53, 0x3000
buffer_load_dwordx4 a[244:247], v242, s[20:23], s79 offen offset:0 sc1
buffer_load_dwordx4 a[248:251], v242, s[20:23], s79 offen offset:1024 sc1
buffer_load_dwordx4 a[252:255], v242, s[20:23], s79 offen offset:2048 sc1
buffer_load_dwordx4 a[0:3], v242, s[20:23], s79 offen offset:3072 sc1
s_add_u32 s79, s53, 0x4000
buffer_load_dwordx4 a[4:7], v242, s[20:23], s79 offen offset:0 sc1
buffer_load_dwordx4 a[8:11], v242, s[20:23], s79 offen offset:1024 sc1
buffer_load_dwordx4 a[12:15], v242, s[20:23], s79 offen offset:2048 sc1
buffer_load_dwordx4 a[16:19], v242, s[20:23], s79 offen offset:3072 sc1
s_add_u32 s79, s53, 0x5000
buffer_load_dwordx4 a[20:23], v242, s[20:23], s79 offen offset:0 sc1
buffer_load_dwordx4 a[24:27], v242, s[20:23], s79 offen offset:1024 sc1
buffer_load_dwordx4 a[28:31], v242, s[20:23], s79 offen offset:2048 sc1
buffer_load_dwordx4 v[64:67], v242, s[20:23], s79 offen offset:3072 sc1
s_add_u32 s79, s53, 0x6000
buffer_load_dwordx4 v[68:71], v242, s[20:23], s79 offen offset:0 sc1
buffer_load_dwordx4 v[72:75], v242, s[20:23], s79 offen offset:1024 sc1
buffer_load_dwordx4 v[76:79], v242, s[20:23], s79 offen offset:2048 sc1
buffer_load_dwordx4 v[80:83], v242, s[20:23], s79 offen offset:3072 sc1
s_add_u32 s79, s53, 0x7000
buffer_load_dwordx4 v[84:87], v242, s[20:23], s79 offen offset:0 sc1
buffer_load_dwordx4 v[88:91], v242, s[20:23], s79 offen offset:1024 sc1
buffer_load_dwordx4 v[92:95], v242, s[20:23], s79 offen offset:2048 sc1
buffer_load_dwordx4 v[96:99], v242, s[20:23], s79 offen offset:3072 sc1
s_waitcnt vmcnt(0)
s_add_u32 s79, s53, 0x8000
buffer_load_dwordx4 v[100:103], v242, s[20:23], s79 offen offset:0 sc1
buffer_load_dwordx4 v[104:107], v242, s[20:23], s79 offen offset:1024 sc1
buffer_load_dwordx4 v[108:111], v242, s[20:23], s79 offen offset:2048 sc1
buffer_load_dwordx4 v[112:115], v242, s[20:23], s79 offen offset:3072 sc1
s_add_u32 s79, s53, 0x9000
buffer_load_dwordx4 v[116:119], v242, s[20:23], s79 offen offset:0 sc1
buffer_load_dwordx4 v[120:123], v242, s[20:23], s79 offen offset:1024 sc1
buffer_load_dwordx4 v[124:127], v242, s[20:23], s79 offen offset:2048 sc1
buffer_load_dwordx4 a[64:67], v242, s[20:23], s79 offen offset:3072 sc1
s_add_u32 s79, s53, 0xa000
buffer_load_dwordx4 a[68:71], v242, s[20:23], s79 offen offset:0 sc1
buffer_load_dwordx4 a[72:75], v242, s[20:23], s79 offen offset:1024 sc1
buffer_load_dwordx4 a[76:79], v242, s[20:23], s79 offen offset:2048 sc1
buffer_load_dwordx4 a[80:83], v242, s[20:23], s79 offen offset:3072 sc1
s_add_u32 s79, s53, 0xb000
buffer_load_dwordx4 a[84:87], v242, s[20:23], s79 offen offset:0 sc1
buffer_load_dwordx4 a[88:91], v242, s[20:23], s79 offen offset:1024 sc1
buffer_load_dwordx4 a[92:95], v242, s[20:23], s79 offen offset:2048 sc1
buffer_load_dwordx4 a[96:99], v242, s[20:23], s79 offen offset:3072 sc1
s_add_u32 s79, s53, 0xc000
buffer_load_dwordx4 a[100:103], v242, s[20:23], s79 offen offset:0 sc1
buffer_load_dwordx4 a[104:107], v242, s[20:23], s79 offen offset:1024 sc1
buffer_load_dwordx4 a[108:111], v242, s[20:23], s79 offen offset:2048 sc1
buffer_load_dwordx4 a[112:115], v242, s[20:23], s79 offen offset:3072 sc1
s_add_u32 s79, s53, 0xd000
buffer_load_dwordx4 a[116:119], v242, s[20:23], s79 offen offset:0 sc1
buffer_load_dwordx4 a[120:123], v242, s[20:23], s79 offen offset:1024 sc1
buffer_load_dwordx4 a[124:127], v242, s[20:23], s79 offen offset:2048 sc1
buffer_load_dwordx4 a[32:35], v242, s[20:23], s79 offen offset:3072 sc1
s_add_u32 s79, s53, 0xe000
buffer_load_dwordx4 a[36:39], v242, s[20:23], s79 offen offset:0 sc1
buffer_load_dwordx4 a[40:43], v242, s[20:23], s79 offen offset:1024 sc1
buffer_load_dwordx4 a[44:47], v242, s[20:23], s79 offen offset:2048 sc1
buffer_load_dwordx4 a[48:51], v242, s[20:23], s79 offen offset:3072 sc1
s_add_u32 s79, s53, 0xf000
buffer_load_dwordx4 a[52:55], v242, s[20:23], s79 offen offset:0 sc1
buffer_load_dwordx4 a[56:59], v242, s[20:23], s79 offen offset:1024 sc1
buffer_load_dwordx4 a[60:63], v242, s[20:23], s79 offen offset:2048 sc1
buffer_load_dwordx4 v[224:227], v242, s[20:23], s79 offen offset:3072 sc1
v_add_u32 v240, 0x20000, v242
s_and_b32 s48, s54, 0x10000
v_add_u32 v241, s48, v242
v_xor_b32 v243, 0x10000, v241
s_and_b32 s8, s76, 0xf9
s_add_u32 s8, s8, 0x0
s_lshl_b32 s8, s8, 3
s_add_u32 s8, s8, s80
s_lshl_b32 s8, s8, 13
s_lshl_b32 s15, s28, 11
s_add_u32 s8, s8, s15
s_add_u32 s8, s8, 0x3800000
s_add_u32 s9, s8, 0x8000
s_and_b32 s12, s76, 0xf9
s_lshl_b32 s15, s80, 1
s_add_u32 s12, s12, s15
s_lshl_b32 s12, s12, 3
s_add_u32 s12, s12, 0x0
s_lshl_b32 s12, s12, 13
s_lshl_b32 s15, s28, 11
s_add_u32 s12, s12, s15
s_add_u32 s12, s12, 0x3800000
s_lshl_b32 s10, s76, 2
s_add_u32 s10, s10, s28
s_lshl_b32 s10, s10, 7
s_add_u32 s10, s10, 0x49d0000
s_lshl_b32 s11, s80, 10
s_sub_u32 s11, s10, s11
s_bitcmp1_b32 %[code], 1
s_cbranch_scc0 L_NR
v_lshrrev_b32 v251, 7, v242
v_min_u32 v251, 2, v251
s_mul_i32 s52, s81, 3
v_add_u32 v251, s52, v251
v_lshrrev_b32 v251, 1, v251
v_lshlrev_b32 v251, 7, v251
v_bfe_u32 v250, v242, 4, 1
v_lshl_add_u32 v250, v250, 13, v251
s_lshl_b32 s52, s28, 2
s_add_u32 s52, s52, s80
s_lshl_b32 s52, s52, 14
s_add_u32 s52, s52, 0x4990000
buffer_load_dword v251, v250, s[20:23], s52 offen offset:0 sc1
s_waitcnt vmcnt(0)
v_cmp_ne_u32 vcc, s37, v251
s_nop 0
s_cbranch_vccnz L_SP_AR
L_RT_AR:
s_lshl_b32 s48, s28, 13
s_add_u32 m0, s48, 0x20000
s_nop 0
buffer_load_dwordx4 v242, s[20:23], s60 offen offset:0 sc1 lds
buffer_load_dwordx4 v242, s[20:23], s60 offen offset:1024 sc1 lds
buffer_load_dwordx4 v242, s[20:23], s60 offen offset:2048 sc1 lds
buffer_load_dwordx4 v242, s[20:23], s60 offen offset:3072 sc1 lds
s_add_u32 m0, m0, 0x1000
s_add_u32 s48, s60, 0x1000
buffer_load_dwordx4 v242, s[20:23], s48 offen offset:0 sc1 lds
buffer_load_dwordx4 v242, s[20:23], s48 offen offset:1024 sc1 lds
buffer_load_dwordx4 v242, s[20:23], s48 offen offset:2048 sc1 lds
buffer_load_dwordx4 v242, s[20:23], s48 offen offset:3072 sc1 lds
s_cmp_lt_u32 s28, 2
s_cselect_b32 s48, 0x8000, 0
s_xor_b32 s48, s48, s54
s_xor_b32 m0, s48, 0x8000
s_nop 0
buffer_load_dwordx4 v242, s[20:23], s61 offen offset:0 sc1 lds
buffer_load_dwordx4 v242, s[20:23], s61 offen offset:1024 sc1 lds
buffer_load_dwordx4 v242, s[20:23], s61 offen offset:2048 sc1 lds
buffer_load_dwordx4 v242, s[20:23], s61 offen offset:3072 sc1 lds
s_add_u32 m0, m0, 0x1000
s_add_u32 s48, s61, 0x1000
buffer_load_dwordx4 v242, s[20:23], s48 offen offset:0 sc1 lds
buffer_load_dwordx4 v242, s[20:23], s48 offen offset:1024 sc1 lds
buffer_load_dwordx4 v242, s[20:23], s48 offen offset:2048 sc1 lds
buffer_load_dwordx4 v242, s[20:23], s48 offen offset:3072 sc1 lds
s_cmp_lt_u32 s28, 2
s_cselect_b32 s48, 0x14000, 0
s_xor_b32 s48, s48, s54
s_xor_b32 m0, s48, 0x4000
s_nop 0
buffer_load_dwordx4 v242, s[20:23], s62 offen offset:0 sc1 lds
buffer_load_dwordx4 v242, s[20:23], s62 offen offset:1024 sc1 lds
buffer_load_dwordx4 v242, s[20:23], s62 offen offset:2048 sc1 lds
buffer_load_dwordx4 v242, s[20:23], s62 offen offset:3072 sc1 lds
s_add_u32 m0, m0, 0x1000
s_add_u32 s48, s62, 0x1000
buffer_load_dwordx4 v242, s[20:23], s48 offen offset:0 sc1 lds
buffer_load_dwordx4 v242, s[20:23], s48 offen offset:1024 sc1 lds
buffer_load_dwordx4 v242, s[20:23], s48 offen offset:2048 sc1 lds
buffer_load_dwordx4 v242, s[20:23], s48 offen offset:3072 sc1 lds
s_waitcnt vmcnt(0)
s_barrier
L_NR:
R16 a[128:131],v240,0
R16 a[132:135],v240,1024
R16 a[136:139],v240,2048
R16 a[140:143],v240,3072
R16 a[144:147],v240,4096
R16 a[148:151],v240,5120
R16 a[152:155],v240,6144
R16 a[156:159],v240,7168
R16 a[160:163],v240,8192
R16 a[164:167],v240,9216
R16 a[168:171],v240,10240
R16 a[172:175],v240,11264
s_waitcnt vmcnt(0) lgkmcnt(0)
L_OP0_%=:
s_waitcnt vmcnt(63) lgkmcnt(8)
MF v[0:3],a[196:199],a[128:131],0
R16 a[176:179],v240,12288
R16 a[180:183],v240,13312
v_mul_f32 v128, v128, v218
MF v[0:3],a[204:207],a[132:135],v[0:3]
v_mul_f32 v129, v129, v218
v_mul_f32 v130, v130, v218
v_mul_f32 v131, v131, v218
MF v[0:3],a[212:215],a[136:139],v[0:3]
v_mul_f32 v132, v132, v218
v_mul_f32 v133, v133, v218
v_mul_f32 v134, v134, v218
MF v[0:3],a[220:223],a[140:143],v[0:3]
v_mul_f32 v135, v135, v218
PK v128,v128,v129
PK v129,v130,v131
MF v[4:7],a[200:203],a[128:131],0
R16 a[184:187],v240,14336
R16 a[188:191],v240,15360
PK v130,v132,v133
MF v[4:7],a[208:211],a[132:135],v[4:7]
PK v131,v134,v135
MF v[4:7],a[216:219],a[136:139],v[4:7]
buffer_store_dwordx4 v[128:131], v242, s[20:23], s36 offen offset:0 sc1
v_mul_f32 v136, v136, v218
v_mul_f32 v137, v137, v218
MF v[4:7],a[224:227],a[140:143],v[4:7]
s_waitcnt vmcnt(63) lgkmcnt(8)
v_mul_f32 v138, v138, v218
v_mul_f32 v139, v139, v218
MF v[0:3],a[228:231],a[144:147],v[0:3]
)A"
R"A(R16 a[128:131],v240,16384
R16 a[132:135],v240,17408
v_mul_f32 v140, v140, v218
MF v[0:3],a[236:239],a[148:151],v[0:3]
v_mul_f32 v141, v141, v218
v_mul_f32 v142, v142, v218
v_mul_f32 v143, v143, v218
MF v[0:3],a[244:247],a[152:155],v[0:3]
PK v136,v136,v137
PK v137,v138,v139
PK v138,v140,v141
MF v[0:3],a[252:255],a[156:159],v[0:3]
PK v139,v142,v143
MF v[4:7],a[232:235],a[144:147],v[4:7]
R16 a[136:139],v240,18432
R16 a[140:143],v240,19456
MF v[4:7],a[240:243],a[148:151],v[4:7]
buffer_store_dwordx4 v[136:139], v242, s[20:23], s36 offen offset:1024 sc1
v_mul_f32 v144, v144, v218
v_mul_f32 v145, v145, v218
MF v[4:7],a[248:251],a[152:155],v[4:7]
v_mul_f32 v146, v146, v218
v_mul_f32 v147, v147, v218
v_mul_f32 v148, v148, v218
MF v[4:7],a[0:3],a[156:159],v[4:7]
s_waitcnt vmcnt(58) lgkmcnt(8)
v_mul_f32 v149, v149, v218
v_mul_f32 v150, v150, v218
MF v[0:3],a[4:7],a[160:163],v[0:3]
R16 a[144:147],v240,20480
R16 a[148:151],v240,21504
v_mul_f32 v151, v151, v218
MF v[0:3],a[12:15],a[164:167],v[0:3]
PK v144,v144,v145
PK v145,v146,v147
PK v146,v148,v149
MF v[0:3],a[20:23],a[168:171],v[0:3]
PK v147,v150,v151
MF v[0:3],a[28:31],a[172:175],v[0:3]
buffer_store_dwordx4 v[144:147], v242, s[20:23], s36 offen offset:2048 sc1
v_mul_f32 v152, v152, v218
v_mul_f32 v153, v153, v218
MF v[4:7],a[8:11],a[160:163],v[4:7]
R16 a[152:155],v240,22528
R16 a[156:159],v240,23552
s_add_u32 s95, s81, 0x30
MF v[4:7],a[16:19],a[164:167],v[4:7]
v_mov_b32 v206, s95
v_lshrrev_b32 v206, 1, v206
v_lshlrev_b32 v206, 7, v206
MF v[4:7],a[24:27],a[168:171],v[4:7]
v_bfe_u32 v207, v242, 4, 1
v_lshl_add_u32 v207, v207, 13, v206
s_lshl_b32 s95, s28, 2
MF v[4:7],v[64:67],a[172:175],v[4:7]
s_waitcnt vmcnt(47) lgkmcnt(8)
s_add_u32 s95, s95, s80
s_lshl_b32 s95, s95, 14
MF v[0:3],v[68:71],a[176:179],v[0:3]
R16 a[160:163],v240,24576
R16 a[164:167],v240,25600
s_add_u32 s95, s95, 0x4990000
MF v[0:3],v[76:79],a[180:183],v[0:3]
buffer_load_dword v206, v207, s[20:23], s95 offen offset:0 sc1
v_mul_f32 v154, v154, v218
v_mul_f32 v155, v155, v218
MF v[0:3],v[84:87],a[184:187],v[0:3]
v_mul_f32 v156, v156, v218
v_mul_f32 v157, v157, v218
v_mul_f32 v158, v158, v218
MF v[0:3],v[92:95],a[188:191],v[0:3]
v_mul_f32 v159, v159, v218
PK v152,v152,v153
PK v153,v154,v155
MF v[4:7],v[72:75],a[176:179],v[4:7]
R16 a[168:171],v240,26624
R16 a[172:175],v240,27648
PK v154,v156,v157
MF v[4:7],v[80:83],a[180:183],v[4:7]
PK v155,v158,v159
MF v[4:7],v[88:91],a[184:187],v[4:7]
buffer_store_dwordx4 v[152:155], v242, s[20:23], s36 offen offset:3072 sc1
v_mul_f32 v160, v160, v222
v_mul_f32 v161, v161, v222
MF v[4:7],v[96:99],a[188:191],v[4:7]
s_waitcnt vmcnt(30) lgkmcnt(8)
v_mul_f32 v162, v162, v222
v_mul_f32 v163, v163, v222
MF v[0:3],v[100:103],a[128:131],v[0:3]
R16 a[176:179],v240,28672
R16 a[180:183],v240,29696
v_mul_f32 v164, v164, v222
MF v[0:3],v[108:111],a[132:135],v[0:3]
v_mul_f32 v165, v165, v222
v_mul_f32 v166, v166, v222
v_mul_f32 v167, v167, v222
MF v[0:3],v[116:119],a[136:139],v[0:3]
PK v160,v160,v161
PK v161,v162,v163
PK v162,v164,v165
MF v[0:3],v[124:127],a[140:143],v[0:3]
PK v163,v166,v167
s_add_u32 s36, s36, 0x20000
MF v[4:7],v[104:107],a[128:131],v[4:7]
R16 a[184:187],v240,30720
R16 a[188:191],v240,31744
MF v[4:7],v[112:115],a[132:135],v[4:7]
buffer_store_dwordx4 v[160:163], v242, s[20:23], s36 offen offset:0 sc1
v_mul_f32 v168, v168, v222
v_mul_f32 v169, v169, v222
MF v[4:7],v[120:123],a[136:139],v[4:7]
s_waitcnt vmcnt(40)
s_barrier
v_mul_f32 v170, v170, v222
MF v[4:7],a[64:67],a[140:143],v[4:7]
s_waitcnt vmcnt(22) lgkmcnt(8)
v_mul_f32 v171, v171, v222
v_mul_f32 v172, v172, v222
MF v[0:3],a[68:71],a[144:147],v[0:3]
R16 a[128:131],v241,0
R16 a[132:135],v241,1024
v_mul_f32 v173, v173, v222
MF v[0:3],a[76:79],a[148:151],v[0:3]
v_mul_f32 v174, v174, v222
v_mul_f32 v175, v175, v222
PK v168,v168,v169
MF v[0:3],a[84:87],a[152:155],v[0:3]
PK v169,v170,v171
PK v170,v172,v173
PK v171,v174,v175
MF v[0:3],a[92:95],a[156:159],v[0:3]
buffer_store_dwordx4 v[168:171], v242, s[20:23], s36 offen offset:1024 sc1
v_mul_f32 v176, v176, v222
v_mul_f32 v177, v177, v222
MF v[4:7],a[72:75],a[144:147],v[4:7]
R16 a[136:139],v241,2048
R16 a[140:143],v241,3072
v_mul_f32 v178, v178, v222
MF v[4:7],a[80:83],a[148:151],v[4:7]
v_mul_f32 v179, v179, v222
v_mul_f32 v180, v180, v222
v_mul_f32 v181, v181, v222
MF v[4:7],a[88:91],a[152:155],v[4:7]
v_mul_f32 v182, v182, v222
v_mul_f32 v183, v183, v222
PK v176,v176,v177
MF v[4:7],a[96:99],a[156:159],v[4:7]
s_waitcnt vmcnt(15) lgkmcnt(8)
PK v177,v178,v179
PK v178,v180,v181
MF v[0:3],a[100:103],a[160:163],v[0:3]
R16 a[144:147],v241,4096
R16 a[148:151],v241,5120
PK v179,v182,v183
MF v[0:3],a[108:111],a[164:167],v[0:3]
buffer_store_dwordx4 v[176:179], v242, s[20:23], s36 offen offset:2048 sc1
v_mul_f32 v184, v184, v222
v_mul_f32 v185, v185, v222
MF v[0:3],a[116:119],a[168:171],v[0:3]
v_mul_f32 v186, v186, v222
v_mul_f32 v187, v187, v222
v_mul_f32 v188, v188, v222
MF v[0:3],a[124:127],a[172:175],v[0:3]
v_mul_f32 v189, v189, v222
v_mul_f32 v190, v190, v222
v_mul_f32 v191, v191, v222
MF v[4:7],a[104:107],a[160:163],v[4:7]
R16 a[152:155],v241,6144
R16 a[156:159],v241,7168
PK v184,v184,v185
MF v[4:7],a[112:115],a[164:167],v[4:7]
PK v185,v186,v187
PK v186,v188,v189
PK v187,v190,v191
MF v[4:7],a[120:123],a[168:171],v[4:7]
buffer_store_dwordx4 v[184:187], v242, s[20:23], s36 offen offset:3072 sc1
s_mov_b32 s24, s74
s_and_b32 s25, s75, 0xffff
MF v[4:7],a[32:35],a[172:175],v[4:7]
s_waitcnt vmcnt(9) lgkmcnt(8)
s_mov_b32 s26, 0x200000
s_mov_b32 s27, 0x20000
MF v[0:3],a[36:39],a[176:179],v[0:3]
R16 a[160:163],v241,8192
R16 a[164:167],v241,9216
v_bfe_u32 v244, v242, 4, 4
MF v[0:3],a[44:47],a[180:183],v[0:3]
v_lshlrev_b32 v244, 10, v244
v_lshrrev_b32 v200, 8, v242
v_lshl_add_u32 v244, v200, 4, v244
MF v[0:3],a[52:55],a[184:187],v[0:3]
buffer_load_dwordx4 v[36:39], v242, s[20:23], s68 offen offset:0 sc1
buffer_load_dwordx4 v[32:35], v244, s[56:59], s84 offen offset:0
MF v[0:3],a[60:63],a[188:191],v[0:3]
buffer_load_dwordx4 v[44:47], v242, s[20:23], s69 offen offset:0 sc1
buffer_load_dwordx4 v[40:43], v244, s[56:59], s85 offen offset:0
v_and_b32 v205, 0x70, v242
MF v[4:7],a[40:43],a[176:179],v[4:7]
R16 a[168:171],v241,10240
R16 a[172:175],v241,11264
v_lshlrev_b32 v205, 3, v205
MF v[4:7],a[48:51],a[180:183],v[4:7]
v_bfe_u32 v200, v242, 7, 1
v_lshl_add_u32 v205, v200, 5, v205
v_bfe_u32 v200, v242, 8, 1
MF v[4:7],a[56:59],a[184:187],v[4:7]
MF v[4:7],v[224:227],a[188:191],v[4:7]
s_waitcnt lgkmcnt(8)
MF v[8:11],a[196:199],a[128:131],0
R16 a[176:179],v241,12288
R16 a[180:183],v241,13312
MF v[8:11],a[204:207],a[132:135],v[8:11]
v_lshl_add_u32 v205, v200, 6, v205
v_bfe_u32 v200, v242, 9, 1
MF v[8:11],a[212:215],a[136:139],v[8:11]
v_lshl_add_u32 v205, v200, 10, v205
MF v[8:11],a[220:223],a[140:143],v[8:11]
buffer_load_dwordx4 v[192:195], v205, s[20:23], s94 offen offset:0 sc1
buffer_load_dwordx4 v[196:199], v205, s[20:23], s94 offen offset:16 sc1
MF v[12:15],a[200:203],a[128:131],0
R16 a[184:187],v241,14336
R16 a[188:191],v241,15360
MF v[12:15],a[208:211],a[132:135],v[12:15]
PK v24,v0,v1
PK v25,v2,v3
MF v[12:15],a[216:219],a[136:139],v[12:15]
PK v26,v4,v5
PK v27,v6,v7
MF v[12:15],a[224:227],a[140:143],v[12:15]
s_waitcnt lgkmcnt(8)
s_add_u32 s52, s8, 0x0
MF v[8:11],a[228:231],a[144:147],v[8:11]
R16 a[128:131],v241,32768
R16 a[132:135],v241,33792
MF v[8:11],a[236:239],a[148:151],v[8:11]
buffer_store_dwordx4 v[24:27], v242, s[20:23], s52 offen offset:0 sc1
MF v[8:11],a[244:247],a[152:155],v[8:11]
MF v[8:11],a[252:255],a[156:159],v[8:11]
MF v[12:15],a[232:235],a[144:147],v[12:15]
R16 a[136:139],v241,34816
R16 a[140:143],v241,35840
MF v[12:15],a[240:243],a[148:151],v[12:15]
MF v[12:15],a[248:251],a[152:155],v[12:15]
MF v[12:15],a[0:3],a[156:159],v[12:15]
s_waitcnt lgkmcnt(8)
MF v[8:11],a[4:7],a[160:163],v[8:11]
R16 a[144:147],v241,36864
R16 a[148:151],v241,37888
MF v[8:11],a[12:15],a[164:167],v[8:11]
MF v[8:11],a[20:23],a[168:171],v[8:11]
MF v[8:11],a[28:31],a[172:175],v[8:11]
MF v[12:15],a[8:11],a[160:163],v[12:15]
R16 a[152:155],v241,38912
R16 a[156:159],v241,39936
MF v[12:15],a[16:19],a[164:167],v[12:15]
MF v[12:15],a[24:27],a[168:171],v[12:15]
MF v[12:15],v[64:67],a[172:175],v[12:15]
s_waitcnt lgkmcnt(8)
MF v[8:11],v[68:71],a[176:179],v[8:11]
R16 a[160:163],v241,40960
R16 a[164:167],v241,41984
MF v[8:11],v[76:79],a[180:183],v[8:11]
s_waitcnt vmcnt(12)
v_cmp_ne_u32 vcc, s37, v206
MF v[8:11],v[84:87],a[184:187],v[8:11]
s_nop 0
s_cbranch_vccnz L_SP_A3_%=
L_RT_A3_%=:
MF v[8:11],v[92:95],a[188:191],v[8:11]
v_bfe_u32 v206, v242, 7, 1
v_lshlrev_b32 v206, 1, v206
MF v[12:15],v[72:75],a[176:179],v[12:15]
R16 a[168:171],v241,43008
R16 a[172:175],v241,44032
MF v[12:15],v[80:83],a[180:183],v[12:15]
s_lshl_b32 s48, s81, 2
s_add_u32 s48, s48, 0x40
MF v[12:15],v[88:91],a[184:187],v[12:15]
v_add_u32 v206, s48, v206
v_lshrrev_b32 v206, 1, v206
MF v[12:15],v[96:99],a[188:191],v[12:15]
s_waitcnt lgkmcnt(8)
v_lshlrev_b32 v206, 7, v206
MF v[8:11],v[100:103],a[128:131],v[8:11]
R16 a[176:179],v241,45056
R16 a[180:183],v241,46080
MF v[8:11],v[108:111],a[132:135],v[8:11]
v_bfe_u32 v207, v242, 4, 1
v_lshl_add_u32 v207, v207, 13, v206
MF v[8:11],v[116:119],a[136:139],v[8:11]
buffer_load_dword v206, v207, s[20:23], s95 offen offset:0 sc1
MF v[8:11],v[124:127],a[140:143],v[8:11]
s_cmp_lt_u32 s28, 2
s_cselect_b32 s48, 0x14000, 0
s_xor_b32 s48, s48, s54
MF v[12:15],v[104:107],a[128:131],v[12:15]
R16 a[184:187],v241,47104
R16 a[188:191],v241,48128
MF v[12:15],v[112:115],a[132:135],v[12:15]
s_xor_b32 m0, s48, 0xc000
MF v[12:15],v[120:123],a[136:139],v[12:15]
s_waitcnt vmcnt(43)
s_barrier
MF v[12:15],a[64:67],a[140:143],v[12:15]
buffer_load_dwordx4 v242, s[20:23], s63 offen offset:0 sc1 lds
s_waitcnt lgkmcnt(8)
MF v[8:11],a[68:71],a[144:147],v[8:11]
R16 a[128:131],v243,0
R16 a[132:135],v243,1024
MF v[8:11],a[76:79],a[148:151],v[8:11]
buffer_load_dwordx4 v242, s[20:23], s63 offen offset:1024 sc1 lds
buffer_load_dwordx4 v242, s[20:23], s63 offen offset:2048 sc1 lds
MF v[8:11],a[84:87],a[152:155],v[8:11]
buffer_load_dwordx4 v242, s[20:23], s63 offen offset:3072 sc1 lds
s_add_u32 m0, m0, 0x1000
MF v[8:11],a[92:95],a[156:159],v[8:11]
s_add_u32 s48, s63, 0x1000
MF v[12:15],a[72:75],a[144:147],v[12:15]
R16 a[136:139],v243,2048
R16 a[140:143],v243,3072
MF v[12:15],a[80:83],a[148:151],v[12:15]
buffer_load_dwordx4 v242, s[20:23], s48 offen offset:0 sc1 lds
buffer_load_dwordx4 v242, s[20:23], s48 offen offset:1024 sc1 lds
MF v[12:15],a[88:91],a[152:155],v[12:15]
buffer_load_dwordx4 v242, s[20:23], s48 offen offset:2048 sc1 lds
buffer_load_dwordx4 v242, s[20:23], s48 offen offset:3072 sc1 lds
MF v[12:15],a[96:99],a[156:159],v[12:15]
s_waitcnt lgkmcnt(8)
v_mov_b32 v203, s37
MF v[8:11],a[100:103],a[160:163],v[8:11]
R16 a[144:147],v243,4096
R16 a[148:151],v243,5120
MF v[8:11],a[108:111],a[164:167],v[8:11]
v_mov_b32 v204, 0
s_waitcnt vmcnt(16)
MF v[8:11],a[116:119],a[168:171],v[8:11]
buffer_store_dword v203, v204, s[20:23], s88 offen offset:0 sc1
s_waitcnt vmcnt(11)
MF v[8:11],a[124:127],a[172:175],v[8:11]
v_lshlrev_b32 v56, 16, v36
v_and_b32 v57, 0xffff0000, v36
MF v[12:15],a[104:107],a[160:163],v[12:15]
R16 a[152:155],v243,6144
R16 a[156:159],v243,7168
MF v[12:15],a[112:115],a[164:167],v[12:15]
v_lshlrev_b32 v58, 16, v37
v_and_b32 v59, 0xffff0000, v37
MF v[12:15],a[120:123],a[168:171],v[12:15]
v_lshlrev_b32 v60, 16, v38
v_and_b32 v61, 0xffff0000, v38
MF v[12:15],a[32:35],a[172:175],v[12:15]
s_waitcnt lgkmcnt(8)
v_lshlrev_b32 v62, 16, v39
MF v[8:11],a[36:39],a[176:179],v[8:11]
R16 a[160:163],v243,8192
R16 a[164:167],v243,9216
MF v[8:11],a[44:47],a[180:183],v[8:11]
v_and_b32 v63, 0xffff0000, v39
MF v[8:11],a[52:55],a[184:187],v[8:11]
s_cmp_eq_u32 s70, 0
s_cbranch_scc1 L_Y0_%=
L_RT_Y0_%=:
MF v[8:11],a[60:63],a[188:191],v[8:11]
v_mul_f32 v184, 0xbfb8aa3b, v56
v_mul_f32 v185, 0xbfb8aa3b, v57
MF v[12:15],a[40:43],a[176:179],v[12:15]
R16 a[168:171],v243,10240
R16 a[172:175],v243,11264
MF v[12:15],a[48:51],a[180:183],v[12:15]
v_mul_f32 v186, 0xbfb8aa3b, v58
v_mul_f32 v187, 0xbfb8aa3b, v59
MF v[12:15],a[56:59],a[184:187],v[12:15]
MF v[12:15],v[224:227],a[188:191],v[12:15]
s_waitcnt lgkmcnt(8)
MF v[0:3],a[196:199],a[128:131],0
R16 a[176:179],v243,12288
R16 a[180:183],v243,13312
MF v[0:3],a[204:207],a[132:135],v[0:3]
MF v[0:3],a[212:215],a[136:139],v[0:3]
MF v[0:3],a[220:223],a[140:143],v[0:3]
MF v[4:7],a[200:203],a[128:131],0
R16 a[184:187],v243,14336
R16 a[188:191],v243,15360
MF v[4:7],a[208:211],a[132:135],v[4:7]
PK v28,v8,v9
PK v29,v10,v11
MF v[4:7],a[216:219],a[136:139],v[4:7]
PK v30,v12,v13
PK v31,v14,v15
MF v[4:7],a[224:227],a[140:143],v[4:7]
s_waitcnt lgkmcnt(8)
s_add_u32 s52, s8, 0x20000
MF v[0:3],a[228:231],a[144:147],v[0:3]
R16 a[128:131],v241,16384
R16 a[132:135],v241,17408
MF v[0:3],a[236:239],a[148:151],v[0:3]
buffer_store_dwordx4 v[28:31], v242, s[20:23], s52 offen offset:0 sc1
v_mul_f32 v188, 0xbfb8aa3b, v60
MF v[0:3],a[244:247],a[152:155],v[0:3]
v_mul_f32 v189, 0xbfb8aa3b, v61
)A"
R"A(v_mul_f32 v190, 0xbfb8aa3b, v62
MF v[0:3],a[252:255],a[156:159],v[0:3]
v_mul_f32 v191, 0xbfb8aa3b, v63
MF v[4:7],a[232:235],a[144:147],v[4:7]
R16 a[136:139],v241,18432
R16 a[140:143],v241,19456
MF v[4:7],a[240:243],a[148:151],v[4:7]
v_exp_f32 v184, v184
MF v[4:7],a[248:251],a[152:155],v[4:7]
v_exp_f32 v185, v185
MF v[4:7],a[0:3],a[156:159],v[4:7]
s_waitcnt lgkmcnt(8)
MF v[0:3],a[4:7],a[160:163],v[0:3]
R16 a[144:147],v241,20480
R16 a[148:151],v241,21504
MF v[0:3],a[12:15],a[164:167],v[0:3]
v_exp_f32 v186, v186
MF v[0:3],a[20:23],a[168:171],v[0:3]
v_exp_f32 v187, v187
MF v[0:3],a[28:31],a[172:175],v[0:3]
v_exp_f32 v188, v188
MF v[4:7],a[8:11],a[160:163],v[4:7]
R16 a[152:155],v241,22528
R16 a[156:159],v241,23552
MF v[4:7],a[16:19],a[164:167],v[4:7]
v_exp_f32 v189, v189
MF v[4:7],a[24:27],a[168:171],v[4:7]
v_exp_f32 v190, v190
MF v[4:7],v[64:67],a[172:175],v[4:7]
s_waitcnt lgkmcnt(8)
MF v[0:3],v[68:71],a[176:179],v[0:3]
R16 a[160:163],v241,24576
R16 a[164:167],v241,25600
MF v[0:3],v[76:79],a[180:183],v[0:3]
v_exp_f32 v191, v191
MF v[0:3],v[84:87],a[184:187],v[0:3]
v_add_f32 v184, 1.0, v184
v_add_f32 v185, 1.0, v185
MF v[0:3],v[92:95],a[188:191],v[0:3]
v_add_f32 v186, 1.0, v186
v_add_f32 v187, 1.0, v187
MF v[4:7],v[72:75],a[176:179],v[4:7]
R16 a[168:171],v241,26624
R16 a[172:175],v241,27648
MF v[4:7],v[80:83],a[180:183],v[4:7]
v_add_f32 v188, 1.0, v188
v_add_f32 v189, 1.0, v189
MF v[4:7],v[88:91],a[184:187],v[4:7]
v_add_f32 v190, 1.0, v190
v_add_f32 v191, 1.0, v191
MF v[4:7],v[96:99],a[188:191],v[4:7]
s_waitcnt lgkmcnt(8)
MF v[0:3],v[100:103],a[128:131],v[0:3]
R16 a[176:179],v241,28672
R16 a[180:183],v241,29696
MF v[0:3],v[108:111],a[132:135],v[0:3]
v_rcp_f32 v184, v184
MF v[0:3],v[116:119],a[136:139],v[0:3]
v_rcp_f32 v185, v185
MF v[0:3],v[124:127],a[140:143],v[0:3]
v_rcp_f32 v186, v186
MF v[4:7],v[104:107],a[128:131],v[4:7]
R16 a[184:187],v241,30720
R16 a[188:191],v241,31744
MF v[4:7],v[112:115],a[132:135],v[4:7]
v_rcp_f32 v187, v187
MF v[4:7],v[120:123],a[136:139],v[4:7]
v_rcp_f32 v188, v188
MF v[4:7],a[64:67],a[140:143],v[4:7]
s_waitcnt lgkmcnt(8)
MF v[0:3],a[68:71],a[144:147],v[0:3]
v_rcp_f32 v189, v189
MF v[0:3],a[76:79],a[148:151],v[0:3]
v_rcp_f32 v190, v190
MF v[0:3],a[84:87],a[152:155],v[0:3]
v_rcp_f32 v191, v191
MF v[0:3],a[92:95],a[156:159],v[0:3]
v_mul_f32 v56, v56, v184
v_mul_f32 v57, v57, v185
MF v[4:7],a[72:75],a[144:147],v[4:7]
v_mul_f32 v58, v58, v186
v_mul_f32 v59, v59, v187
MF v[4:7],a[80:83],a[148:151],v[4:7]
v_mul_f32 v60, v60, v188
v_mul_f32 v61, v61, v189
MF v[4:7],a[88:91],a[152:155],v[4:7]
s_waitcnt vmcnt(2)
s_barrier
MF v[4:7],a[96:99],a[156:159],v[4:7]
R16 a[128:131],v243,32768
s_waitcnt lgkmcnt(5)
MF v[0:3],a[100:103],a[160:163],v[0:3]
R16 a[132:135],v243,33792
R16 a[136:139],v243,34816
MF v[0:3],a[108:111],a[164:167],v[0:3]
R16 a[140:143],v243,35840
R16 a[144:147],v243,36864
MF v[0:3],a[116:119],a[168:171],v[0:3]
R16 a[148:151],v243,37888
v_mul_f32 v62, v62, v190
MF v[0:3],a[124:127],a[172:175],v[0:3]
v_mul_f32 v63, v63, v191
PK v56,v56,v57
MF v[4:7],a[104:107],a[160:163],v[4:7]
R16 a[152:155],v243,38912
R16 a[156:159],v243,39936
MF v[4:7],a[112:115],a[164:167],v[4:7]
v_and_b32 v57, 0xffff0000, v56
v_lshlrev_b32 v56, 16, v56
MF v[4:7],a[120:123],a[168:171],v[4:7]
PK v58,v58,v59
v_and_b32 v59, 0xffff0000, v58
MF v[4:7],a[32:35],a[172:175],v[4:7]
s_waitcnt lgkmcnt(8)
v_lshlrev_b32 v58, 16, v58
MF v[0:3],a[36:39],a[176:179],v[0:3]
R16 a[160:163],v243,40960
R16 a[164:167],v243,41984
MF v[0:3],a[44:47],a[180:183],v[0:3]
PK v60,v60,v61
v_and_b32 v61, 0xffff0000, v60
MF v[0:3],a[52:55],a[184:187],v[0:3]
v_lshlrev_b32 v60, 16, v60
PK v62,v62,v63
MF v[0:3],a[60:63],a[188:191],v[0:3]
v_and_b32 v63, 0xffff0000, v62
v_lshlrev_b32 v62, 16, v62
MF v[4:7],a[40:43],a[176:179],v[4:7]
R16 a[168:171],v243,43008
R16 a[172:175],v243,44032
MF v[4:7],a[48:51],a[180:183],v[4:7]
MF v[4:7],a[56:59],a[184:187],v[4:7]
MF v[4:7],v[224:227],a[188:191],v[4:7]
s_waitcnt lgkmcnt(8)
s_waitcnt vmcnt(10)
v_cmp_ne_u32 vcc, s37, v206
s_nop 0
s_cbranch_vccz L_OE_%=
MF v[8:11],a[196:199],a[128:131],0
R16 a[176:179],v243,45056
R16 a[180:183],v243,46080
MF v[8:11],a[204:207],a[132:135],v[8:11]
buffer_load_dword v206, v207, s[20:23], s95 offen offset:0 sc1
MF v[8:11],a[212:215],a[136:139],v[8:11]
MF v[8:11],a[220:223],a[140:143],v[8:11]
MF v[12:15],a[200:203],a[128:131],0
R16 a[184:187],v243,47104
R16 a[188:191],v243,48128
MF v[12:15],a[208:211],a[132:135],v[12:15]
PK v24,v0,v1
PK v25,v2,v3
MF v[12:15],a[216:219],a[136:139],v[12:15]
PK v26,v4,v5
PK v27,v6,v7
MF v[12:15],a[224:227],a[140:143],v[12:15]
s_waitcnt lgkmcnt(8)
s_add_u32 s52, s8, 0x40000
MF v[8:11],a[228:231],a[144:147],v[8:11]
R16 a[128:131],v241,49152
R16 a[132:135],v241,50176
MF v[8:11],a[236:239],a[148:151],v[8:11]
buffer_store_dwordx4 v[24:27], v242, s[20:23], s52 offen offset:0 sc1
MF v[8:11],a[244:247],a[152:155],v[8:11]
MF v[8:11],a[252:255],a[156:159],v[8:11]
MF v[12:15],a[232:235],a[144:147],v[12:15]
R16 a[136:139],v241,51200
R16 a[140:143],v241,52224
MF v[12:15],a[240:243],a[148:151],v[12:15]
MF v[12:15],a[248:251],a[152:155],v[12:15]
MF v[12:15],a[0:3],a[156:159],v[12:15]
s_waitcnt lgkmcnt(8)
MF v[8:11],a[4:7],a[160:163],v[8:11]
R16 a[144:147],v241,53248
R16 a[148:151],v241,54272
MF v[8:11],a[12:15],a[164:167],v[8:11]
MF v[8:11],a[20:23],a[168:171],v[8:11]
MF v[8:11],a[28:31],a[172:175],v[8:11]
MF v[12:15],a[8:11],a[160:163],v[12:15]
R16 a[152:155],v241,55296
R16 a[156:159],v241,56320
MF v[12:15],a[16:19],a[164:167],v[12:15]
MF v[12:15],a[24:27],a[168:171],v[12:15]
MF v[12:15],v[64:67],a[172:175],v[12:15]
s_waitcnt lgkmcnt(8)
MF v[8:11],v[68:71],a[176:179],v[8:11]
R16 a[160:163],v241,57344
R16 a[164:167],v241,58368
MF v[8:11],v[76:79],a[180:183],v[8:11]
MF v[8:11],v[84:87],a[184:187],v[8:11]
MF v[8:11],v[92:95],a[188:191],v[8:11]
MF v[12:15],v[72:75],a[176:179],v[12:15]
R16 a[168:171],v241,59392
R16 a[172:175],v241,60416
MF v[12:15],v[80:83],a[180:183],v[12:15]
MF v[12:15],v[88:91],a[184:187],v[12:15]
MF v[12:15],v[96:99],a[188:191],v[12:15]
s_waitcnt lgkmcnt(8)
MF v[8:11],v[100:103],a[128:131],v[8:11]
R16 a[176:179],v241,61440
R16 a[180:183],v241,62464
MF v[8:11],v[108:111],a[132:135],v[8:11]
MF v[8:11],v[116:119],a[136:139],v[8:11]
MF v[8:11],v[124:127],a[140:143],v[8:11]
MF v[12:15],v[104:107],a[128:131],v[12:15]
R16 a[184:187],v241,63488
R16 a[188:191],v241,64512
MF v[12:15],v[112:115],a[132:135],v[12:15]
MF v[12:15],v[120:123],a[136:139],v[12:15]
MF v[12:15],a[64:67],a[140:143],v[12:15]
s_waitcnt lgkmcnt(8)
MF v[8:11],a[68:71],a[144:147],v[8:11]
MF v[8:11],a[76:79],a[148:151],v[8:11]
MF v[8:11],a[84:87],a[152:155],v[8:11]
MF v[8:11],a[92:95],a[156:159],v[8:11]
MF v[12:15],a[72:75],a[144:147],v[12:15]
MF v[12:15],a[80:83],a[148:151],v[12:15]
MF v[12:15],a[88:91],a[152:155],v[12:15]
MF v[12:15],a[96:99],a[156:159],v[12:15]
s_waitcnt lgkmcnt(4)
MF v[8:11],a[100:103],a[160:163],v[8:11]
MF v[8:11],a[108:111],a[164:167],v[8:11]
MF v[8:11],a[116:119],a[168:171],v[8:11]
MF v[8:11],a[124:127],a[172:175],v[8:11]
MF v[12:15],a[104:107],a[160:163],v[12:15]
MF v[12:15],a[112:115],a[164:167],v[12:15]
MF v[12:15],a[120:123],a[168:171],v[12:15]
MF v[12:15],a[32:35],a[172:175],v[12:15]
s_waitcnt lgkmcnt(0)
MF v[8:11],a[36:39],a[176:179],v[8:11]
s_waitcnt vmcnt(1)
v_cmp_ne_u32 vcc, s37, v206
s_nop 0
s_cbranch_vccnz L_SP_B_%=
L_RT_B_%=:
s_cmp_lt_u32 s28, 2
s_cselect_b32 s48, 0x8000, 0
s_xor_b32 s48, s48, s54
s_xor_b32 m0, s48, 0x1c000
s_nop 0
buffer_load_dwordx4 v242, s[20:23], s64 offen offset:0 sc1 lds
buffer_load_dwordx4 v242, s[20:23], s64 offen offset:1024 sc1 lds
buffer_load_dwordx4 v242, s[20:23], s64 offen offset:2048 sc1 lds
buffer_load_dwordx4 v242, s[20:23], s64 offen offset:3072 sc1 lds
s_add_u32 m0, m0, 0x1000
s_add_u32 s48, s64, 0x1000
buffer_load_dwordx4 v242, s[20:23], s48 offen offset:0 sc1 lds
buffer_load_dwordx4 v242, s[20:23], s48 offen offset:1024 sc1 lds
buffer_load_dwordx4 v242, s[20:23], s48 offen offset:2048 sc1 lds
buffer_load_dwordx4 v242, s[20:23], s48 offen offset:3072 sc1 lds
s_lshl_b32 s48, s28, 13
s_add_u32 m0, s48, 0x20000
s_nop 0
buffer_load_dwordx4 v242, s[20:23], s65 offen offset:0 sc1 lds
buffer_load_dwordx4 v242, s[20:23], s65 offen offset:1024 sc1 lds
buffer_load_dwordx4 v242, s[20:23], s65 offen offset:2048 sc1 lds
buffer_load_dwordx4 v242, s[20:23], s65 offen offset:3072 sc1 lds
s_add_u32 m0, m0, 0x1000
s_add_u32 s48, s65, 0x1000
buffer_load_dwordx4 v242, s[20:23], s48 offen offset:0 sc1 lds
buffer_load_dwordx4 v242, s[20:23], s48 offen offset:1024 sc1 lds
buffer_load_dwordx4 v242, s[20:23], s48 offen offset:2048 sc1 lds
buffer_load_dwordx4 v242, s[20:23], s48 offen offset:3072 sc1 lds
s_waitcnt vmcnt(8)
s_barrier
MF v[8:11],a[44:47],a[180:183],v[8:11]
R16 a[128:131],v243,16384
R16 a[132:135],v243,17408
MF v[8:11],a[52:55],a[184:187],v[8:11]
R16 a[136:139],v243,18432
R16 a[140:143],v243,19456
MF v[8:11],a[60:63],a[188:191],v[8:11]
R16 a[144:147],v243,20480
R16 a[148:151],v243,21504
MF v[12:15],a[40:43],a[176:179],v[12:15]
R16 a[152:155],v243,22528
R16 a[156:159],v243,23552
MF v[12:15],a[48:51],a[180:183],v[12:15]
R16 a[160:163],v243,24576
R16 a[164:167],v243,25600
MF v[12:15],a[56:59],a[184:187],v[12:15]
R16 a[168:171],v243,26624
R16 a[172:175],v243,27648
MF v[12:15],v[224:227],a[188:191],v[12:15]
s_waitcnt lgkmcnt(8)
MF v[0:3],a[196:199],a[128:131],0
R16 a[176:179],v243,28672
R16 a[180:183],v243,29696
MF v[0:3],a[204:207],a[132:135],v[0:3]
s_cmp_lt_u32 s28, 2
s_cselect_b32 s48, 0x8000, 0
s_xor_b32 s48, s48, s54
MF v[0:3],a[212:215],a[136:139],v[0:3]
s_xor_b32 m0, s48, 0x8000
MF v[0:3],a[220:223],a[140:143],v[0:3]
buffer_load_dwordx4 v242, s[20:23], s66 offen offset:0 sc1 lds
buffer_load_dwordx4 v242, s[20:23], s66 offen offset:1024 sc1 lds
MF v[4:7],a[200:203],a[128:131],0
R16 a[184:187],v243,30720
R16 a[188:191],v243,31744
MF v[4:7],a[208:211],a[132:135],v[4:7]
buffer_load_dwordx4 v242, s[20:23], s66 offen offset:2048 sc1 lds
PK v28,v8,v9
MF v[4:7],a[216:219],a[136:139],v[4:7]
PK v29,v10,v11
PK v30,v12,v13
MF v[4:7],a[224:227],a[140:143],v[4:7]
buffer_load_dwordx4 v242, s[20:23], s66 offen offset:3072 sc1 lds
s_waitcnt lgkmcnt(8)
MF v[0:3],a[228:231],a[144:147],v[0:3]
R16 a[128:131],v243,49152
R16 a[132:135],v243,50176
MF v[0:3],a[236:239],a[148:151],v[0:3]
s_add_u32 m0, m0, 0x1000
s_add_u32 s48, s66, 0x1000
MF v[0:3],a[244:247],a[152:155],v[0:3]
buffer_load_dwordx4 v242, s[20:23], s48 offen offset:0 sc1 lds
PK v31,v14,v15
MF v[0:3],a[252:255],a[156:159],v[0:3]
s_add_u32 s52, s8, 0x60000
MF v[4:7],a[232:235],a[144:147],v[4:7]
R16 a[136:139],v243,51200
R16 a[140:143],v243,52224
MF v[4:7],a[240:243],a[148:151],v[4:7]
buffer_load_dwordx4 v242, s[20:23], s48 offen offset:1024 sc1 lds
buffer_store_dwordx4 v[28:31], v242, s[20:23], s52 offen offset:0 sc1
MF v[4:7],a[248:251],a[152:155],v[4:7]
buffer_load_dwordx4 v242, s[20:23], s48 offen offset:2048 sc1 lds
MF v[4:7],a[0:3],a[156:159],v[4:7]
s_waitcnt lgkmcnt(8)
MF v[0:3],a[4:7],a[160:163],v[0:3]
R16 a[144:147],v243,53248
R16 a[148:151],v243,54272
MF v[0:3],a[12:15],a[164:167],v[0:3]
buffer_load_dwordx4 v242, s[20:23], s48 offen offset:3072 sc1 lds
v_mov_b32 v149, s37
MF v[0:3],a[20:23],a[168:171],v[0:3]
v_mov_b32 v151, s37
v_mov_b32 v153, s37
MF v[0:3],a[28:31],a[172:175],v[0:3]
s_cmp_lt_u32 s28, 2
s_cselect_b32 s48, 0x14000, 0
s_xor_b32 s48, s48, s54
MF v[4:7],a[8:11],a[160:163],v[4:7]
R16 a[152:155],v243,55296
R16 a[156:159],v243,56320
MF v[4:7],a[16:19],a[164:167],v[4:7]
s_xor_b32 m0, s48, 0x4000
v_mov_b32 v155, s37
MF v[4:7],a[24:27],a[168:171],v[4:7]
buffer_load_dwordx4 v242, s[20:23], s67 offen offset:0 sc1 lds
buffer_load_dwordx4 v242, s[20:23], s67 offen offset:1024 sc1 lds
MF v[4:7],v[64:67],a[172:175],v[4:7]
buffer_load_dwordx4 v242, s[20:23], s67 offen offset:2048 sc1 lds
s_waitcnt lgkmcnt(8)
MF v[0:3],v[68:71],a[176:179],v[0:3]
R16 a[160:163],v243,57344
R16 a[164:167],v243,58368
MF v[0:3],v[76:79],a[180:183],v[0:3]
v_mov_b32 v17, s37
v_mov_b32 v19, s37
MF v[0:3],v[84:87],a[184:187],v[0:3]
buffer_load_dwordx4 v242, s[20:23], s67 offen offset:3072 sc1 lds
v_mov_b32 v21, s37
MF v[0:3],v[92:95],a[188:191],v[0:3]
v_mov_b32 v23, s37
MF v[4:7],v[72:75],a[176:179],v[4:7]
R16 a[168:171],v243,59392
R16 a[172:175],v243,60416
MF v[4:7],v[80:83],a[180:183],v[4:7]
s_add_u32 m0, m0, 0x1000
s_add_u32 s48, s67, 0x1000
MF v[4:7],v[88:91],a[184:187],v[4:7]
buffer_load_dwordx4 v242, s[20:23], s48 offen offset:0 sc1 lds
MF v[4:7],v[96:99],a[188:191],v[4:7]
buffer_load_dwordx4 v242, s[20:23], s48 offen offset:1024 sc1 lds
s_waitcnt lgkmcnt(8)
MF v[0:3],v[100:103],a[128:131],v[0:3]
R16 a[176:179],v243,61440
R16 a[180:183],v243,62464
MF v[0:3],v[108:111],a[132:135],v[0:3]
buffer_load_dwordx4 v242, s[20:23], s48 offen offset:2048 sc1 lds
MF v[0:3],v[116:119],a[136:139],v[0:3]
MF v[0:3],v[124:127],a[140:143],v[0:3]
buffer_load_dwordx4 v242, s[20:23], s48 offen offset:3072 sc1 lds
MF v[4:7],v[104:107],a[128:131],v[4:7]
R16 a[184:187],v243,63488
R16 a[188:191],v243,64512
MF v[4:7],v[112:115],a[132:135],v[4:7]
MF v[4:7],v[120:123],a[136:139],v[4:7]
s_waitcnt vmcnt(17)
s_barrier
MF v[4:7],a[64:67],a[140:143],v[4:7]
s_waitcnt lgkmcnt(8)
MF v[0:3],a[68:71],a[144:147],v[0:3]
R16 a[128:131],v240,0
R16 a[132:135],v240,1024
MF v[0:3],a[76:79],a[148:151],v[0:3]
MF v[0:3],a[84:87],a[152:155],v[0:3]
MF v[0:3],a[92:95],a[156:159],v[0:3]
MF v[4:7],a[72:75],a[144:147],v[4:7]
)A"
R"A(R16 a[136:139],v240,2048
R16 a[140:143],v240,3072
MF v[4:7],a[80:83],a[148:151],v[4:7]
MF v[4:7],a[88:91],a[152:155],v[4:7]
MF v[4:7],a[96:99],a[156:159],v[4:7]
s_waitcnt lgkmcnt(8)
MF v[0:3],a[100:103],a[160:163],v[0:3]
R16 a[144:147],v240,4096
R16 a[148:151],v240,5120
MF v[0:3],a[108:111],a[164:167],v[0:3]
MF v[0:3],a[116:119],a[168:171],v[0:3]
MF v[0:3],a[124:127],a[172:175],v[0:3]
MF v[4:7],a[104:107],a[160:163],v[4:7]
R16 a[152:155],v240,6144
R16 a[156:159],v240,7168
MF v[4:7],a[112:115],a[164:167],v[4:7]
MF v[4:7],a[120:123],a[168:171],v[4:7]
MF v[4:7],a[32:35],a[172:175],v[4:7]
s_waitcnt lgkmcnt(8)
MF v[0:3],a[36:39],a[176:179],v[0:3]
R16 a[160:163],v240,8192
R16 a[164:167],v240,9216
MF v[0:3],a[44:47],a[180:183],v[0:3]
MF v[0:3],a[52:55],a[184:187],v[0:3]
MF v[0:3],a[60:63],a[188:191],v[0:3]
MF v[4:7],a[40:43],a[176:179],v[4:7]
R16 a[168:171],v240,10240
R16 a[172:175],v240,11264
MF v[4:7],a[48:51],a[180:183],v[4:7]
MF v[4:7],a[56:59],a[184:187],v[4:7]
MF v[4:7],v[224:227],a[188:191],v[4:7]
s_waitcnt lgkmcnt(8)
s_branch L_OJ_%=
L_OE_%=:
MF v[8:11],a[196:199],a[128:131],0
R16 a[176:179],v243,45056
R16 a[180:183],v243,46080
MF v[8:11],a[204:207],a[132:135],v[8:11]
s_cmp_lt_u32 s28, 2
s_cselect_b32 s48, 0x8000, 0
s_xor_b32 s48, s48, s54
MF v[8:11],a[212:215],a[136:139],v[8:11]
s_xor_b32 m0, s48, 0x1c000
MF v[8:11],a[220:223],a[140:143],v[8:11]
buffer_load_dwordx4 v242, s[20:23], s64 offen offset:0 sc1 lds
buffer_load_dwordx4 v242, s[20:23], s64 offen offset:1024 sc1 lds
MF v[12:15],a[200:203],a[128:131],0
R16 a[184:187],v243,47104
R16 a[188:191],v243,48128
MF v[12:15],a[208:211],a[132:135],v[12:15]
buffer_load_dwordx4 v242, s[20:23], s64 offen offset:2048 sc1 lds
PK v24,v0,v1
MF v[12:15],a[216:219],a[136:139],v[12:15]
buffer_load_dwordx4 v242, s[20:23], s64 offen offset:3072 sc1 lds
PK v25,v2,v3
MF v[12:15],a[224:227],a[140:143],v[12:15]
s_waitcnt lgkmcnt(8)
PK v26,v4,v5
MF v[8:11],a[228:231],a[144:147],v[8:11]
R16 a[128:131],v241,49152
R16 a[132:135],v241,50176
MF v[8:11],a[236:239],a[148:151],v[8:11]
s_add_u32 m0, m0, 0x1000
s_add_u32 s48, s64, 0x1000
MF v[8:11],a[244:247],a[152:155],v[8:11]
buffer_load_dwordx4 v242, s[20:23], s48 offen offset:0 sc1 lds
buffer_load_dwordx4 v242, s[20:23], s48 offen offset:1024 sc1 lds
MF v[8:11],a[252:255],a[156:159],v[8:11]
PK v27,v6,v7
s_add_u32 s52, s8, 0x40000
MF v[12:15],a[232:235],a[144:147],v[12:15]
R16 a[136:139],v241,51200
R16 a[140:143],v241,52224
MF v[12:15],a[240:243],a[148:151],v[12:15]
buffer_load_dwordx4 v242, s[20:23], s48 offen offset:2048 sc1 lds
buffer_store_dwordx4 v[24:27], v242, s[20:23], s52 offen offset:0 sc1
MF v[12:15],a[248:251],a[152:155],v[12:15]
buffer_load_dwordx4 v242, s[20:23], s48 offen offset:3072 sc1 lds
MF v[12:15],a[0:3],a[156:159],v[12:15]
s_waitcnt lgkmcnt(8)
MF v[8:11],a[4:7],a[160:163],v[8:11]
R16 a[144:147],v241,53248
R16 a[148:151],v241,54272
MF v[8:11],a[12:15],a[164:167],v[8:11]
MF v[8:11],a[20:23],a[168:171],v[8:11]
s_lshl_b32 s48, s28, 13
s_add_u32 m0, s48, 0x20000
MF v[8:11],a[28:31],a[172:175],v[8:11]
buffer_load_dwordx4 v242, s[20:23], s65 offen offset:0 sc1 lds
MF v[12:15],a[8:11],a[160:163],v[12:15]
R16 a[152:155],v241,55296
R16 a[156:159],v241,56320
MF v[12:15],a[16:19],a[164:167],v[12:15]
buffer_load_dwordx4 v242, s[20:23], s65 offen offset:1024 sc1 lds
MF v[12:15],a[24:27],a[168:171],v[12:15]
MF v[12:15],v[64:67],a[172:175],v[12:15]
s_waitcnt lgkmcnt(8)
MF v[8:11],v[68:71],a[176:179],v[8:11]
R16 a[160:163],v241,57344
R16 a[164:167],v241,58368
MF v[8:11],v[76:79],a[180:183],v[8:11]
buffer_load_dwordx4 v242, s[20:23], s65 offen offset:2048 sc1 lds
MF v[8:11],v[84:87],a[184:187],v[8:11]
MF v[8:11],v[92:95],a[188:191],v[8:11]
MF v[12:15],v[72:75],a[176:179],v[12:15]
R16 a[168:171],v241,59392
R16 a[172:175],v241,60416
MF v[12:15],v[80:83],a[180:183],v[12:15]
buffer_load_dwordx4 v242, s[20:23], s65 offen offset:3072 sc1 lds
MF v[12:15],v[88:91],a[184:187],v[12:15]
MF v[12:15],v[96:99],a[188:191],v[12:15]
s_waitcnt lgkmcnt(8)
MF v[8:11],v[100:103],a[128:131],v[8:11]
R16 a[176:179],v241,61440
R16 a[180:183],v241,62464
MF v[8:11],v[108:111],a[132:135],v[8:11]
s_add_u32 m0, m0, 0x1000
s_add_u32 s48, s65, 0x1000
MF v[8:11],v[116:119],a[136:139],v[8:11]
buffer_load_dwordx4 v242, s[20:23], s48 offen offset:0 sc1 lds
MF v[8:11],v[124:127],a[140:143],v[8:11]
buffer_load_dwordx4 v242, s[20:23], s48 offen offset:1024 sc1 lds
MF v[12:15],v[104:107],a[128:131],v[12:15]
R16 a[184:187],v241,63488
R16 a[188:191],v241,64512
MF v[12:15],v[112:115],a[132:135],v[12:15]
MF v[12:15],v[120:123],a[136:139],v[12:15]
MF v[12:15],a[64:67],a[140:143],v[12:15]
buffer_load_dwordx4 v242, s[20:23], s48 offen offset:2048 sc1 lds
s_waitcnt lgkmcnt(8)
MF v[8:11],a[68:71],a[144:147],v[8:11]
MF v[8:11],a[76:79],a[148:151],v[8:11]
MF v[8:11],a[84:87],a[152:155],v[8:11]
buffer_load_dwordx4 v242, s[20:23], s48 offen offset:3072 sc1 lds
MF v[8:11],a[92:95],a[156:159],v[8:11]
MF v[12:15],a[72:75],a[144:147],v[12:15]
MF v[12:15],a[80:83],a[148:151],v[12:15]
MF v[12:15],a[88:91],a[152:155],v[12:15]
s_cmp_lt_u32 s28, 2
s_cselect_b32 s48, 0x8000, 0
s_xor_b32 s48, s48, s54
MF v[12:15],a[96:99],a[156:159],v[12:15]
s_waitcnt lgkmcnt(4)
s_xor_b32 m0, s48, 0x8000
MF v[8:11],a[100:103],a[160:163],v[8:11]
buffer_load_dwordx4 v242, s[20:23], s66 offen offset:0 sc1 lds
MF v[8:11],a[108:111],a[164:167],v[8:11]
buffer_load_dwordx4 v242, s[20:23], s66 offen offset:1024 sc1 lds
MF v[8:11],a[116:119],a[168:171],v[8:11]
MF v[8:11],a[124:127],a[172:175],v[8:11]
MF v[12:15],a[104:107],a[160:163],v[12:15]
MF v[12:15],a[112:115],a[164:167],v[12:15]
buffer_load_dwordx4 v242, s[20:23], s66 offen offset:2048 sc1 lds
MF v[12:15],a[120:123],a[168:171],v[12:15]
MF v[12:15],a[32:35],a[172:175],v[12:15]
s_waitcnt lgkmcnt(0)
MF v[8:11],a[36:39],a[176:179],v[8:11]
s_waitcnt vmcnt(11)
s_barrier
MF v[8:11],a[44:47],a[180:183],v[8:11]
R16 a[128:131],v243,16384
R16 a[132:135],v243,17408
MF v[8:11],a[52:55],a[184:187],v[8:11]
R16 a[136:139],v243,18432
R16 a[140:143],v243,19456
MF v[8:11],a[60:63],a[188:191],v[8:11]
R16 a[144:147],v243,20480
R16 a[148:151],v243,21504
MF v[12:15],a[40:43],a[176:179],v[12:15]
R16 a[152:155],v243,22528
R16 a[156:159],v243,23552
MF v[12:15],a[48:51],a[180:183],v[12:15]
R16 a[160:163],v243,24576
R16 a[164:167],v243,25600
MF v[12:15],a[56:59],a[184:187],v[12:15]
R16 a[168:171],v243,26624
R16 a[172:175],v243,27648
MF v[12:15],v[224:227],a[188:191],v[12:15]
buffer_load_dwordx4 v242, s[20:23], s66 offen offset:3072 sc1 lds
s_waitcnt lgkmcnt(8)
MF v[0:3],a[196:199],a[128:131],0
R16 a[176:179],v243,28672
R16 a[180:183],v243,29696
MF v[0:3],a[204:207],a[132:135],v[0:3]
s_add_u32 m0, m0, 0x1000
s_add_u32 s48, s66, 0x1000
MF v[0:3],a[212:215],a[136:139],v[0:3]
buffer_load_dwordx4 v242, s[20:23], s48 offen offset:0 sc1 lds
MF v[0:3],a[220:223],a[140:143],v[0:3]
buffer_load_dwordx4 v242, s[20:23], s48 offen offset:1024 sc1 lds
MF v[4:7],a[200:203],a[128:131],0
R16 a[184:187],v243,30720
R16 a[188:191],v243,31744
MF v[4:7],a[208:211],a[132:135],v[4:7]
PK v28,v8,v9
PK v29,v10,v11
MF v[4:7],a[216:219],a[136:139],v[4:7]
buffer_load_dwordx4 v242, s[20:23], s48 offen offset:2048 sc1 lds
PK v30,v12,v13
MF v[4:7],a[224:227],a[140:143],v[4:7]
s_waitcnt lgkmcnt(8)
PK v31,v14,v15
MF v[0:3],a[228:231],a[144:147],v[0:3]
R16 a[128:131],v243,49152
R16 a[132:135],v243,50176
MF v[0:3],a[236:239],a[148:151],v[0:3]
buffer_load_dwordx4 v242, s[20:23], s48 offen offset:3072 sc1 lds
s_add_u32 s52, s8, 0x60000
MF v[0:3],a[244:247],a[152:155],v[0:3]
buffer_store_dwordx4 v[28:31], v242, s[20:23], s52 offen offset:0 sc1
MF v[0:3],a[252:255],a[156:159],v[0:3]
MF v[4:7],a[232:235],a[144:147],v[4:7]
R16 a[136:139],v243,51200
R16 a[140:143],v243,52224
MF v[4:7],a[240:243],a[148:151],v[4:7]
s_cmp_lt_u32 s28, 2
s_cselect_b32 s48, 0x14000, 0
s_xor_b32 s48, s48, s54
MF v[4:7],a[248:251],a[152:155],v[4:7]
s_xor_b32 m0, s48, 0x4000
MF v[4:7],a[0:3],a[156:159],v[4:7]
buffer_load_dwordx4 v242, s[20:23], s67 offen offset:0 sc1 lds
s_waitcnt lgkmcnt(8)
MF v[0:3],a[4:7],a[160:163],v[0:3]
R16 a[144:147],v243,53248
R16 a[148:151],v243,54272
MF v[0:3],a[12:15],a[164:167],v[0:3]
buffer_load_dwordx4 v242, s[20:23], s67 offen offset:1024 sc1 lds
v_mov_b32 v149, s37
MF v[0:3],a[20:23],a[168:171],v[0:3]
buffer_load_dwordx4 v242, s[20:23], s67 offen offset:2048 sc1 lds
v_mov_b32 v151, s37
MF v[0:3],a[28:31],a[172:175],v[0:3]
v_mov_b32 v153, s37
v_mov_b32 v155, s37
MF v[4:7],a[8:11],a[160:163],v[4:7]
R16 a[152:155],v243,55296
R16 a[156:159],v243,56320
MF v[4:7],a[16:19],a[164:167],v[4:7]
buffer_load_dwordx4 v242, s[20:23], s67 offen offset:3072 sc1 lds
v_mov_b32 v17, s37
MF v[4:7],a[24:27],a[168:171],v[4:7]
v_mov_b32 v19, s37
v_mov_b32 v21, s37
MF v[4:7],v[64:67],a[172:175],v[4:7]
s_waitcnt lgkmcnt(8)
v_mov_b32 v23, s37
MF v[0:3],v[68:71],a[176:179],v[0:3]
R16 a[160:163],v243,57344
R16 a[164:167],v243,58368
MF v[0:3],v[76:79],a[180:183],v[0:3]
s_add_u32 m0, m0, 0x1000
s_add_u32 s48, s67, 0x1000
MF v[0:3],v[84:87],a[184:187],v[0:3]
buffer_load_dwordx4 v242, s[20:23], s48 offen offset:0 sc1 lds
MF v[0:3],v[92:95],a[188:191],v[0:3]
buffer_load_dwordx4 v242, s[20:23], s48 offen offset:1024 sc1 lds
MF v[4:7],v[72:75],a[176:179],v[4:7]
R16 a[168:171],v243,59392
R16 a[172:175],v243,60416
MF v[4:7],v[80:83],a[180:183],v[4:7]
MF v[4:7],v[88:91],a[184:187],v[4:7]
buffer_load_dwordx4 v242, s[20:23], s48 offen offset:2048 sc1 lds
MF v[4:7],v[96:99],a[188:191],v[4:7]
s_waitcnt lgkmcnt(8)
MF v[0:3],v[100:103],a[128:131],v[0:3]
R16 a[176:179],v243,61440
R16 a[180:183],v243,62464
MF v[0:3],v[108:111],a[132:135],v[0:3]
buffer_load_dwordx4 v242, s[20:23], s48 offen offset:3072 sc1 lds
MF v[0:3],v[116:119],a[136:139],v[0:3]
MF v[0:3],v[124:127],a[140:143],v[0:3]
MF v[4:7],v[104:107],a[128:131],v[4:7]
R16 a[184:187],v243,63488
R16 a[188:191],v243,64512
MF v[4:7],v[112:115],a[132:135],v[4:7]
MF v[4:7],v[120:123],a[136:139],v[4:7]
s_waitcnt vmcnt(17)
s_barrier
MF v[4:7],a[64:67],a[140:143],v[4:7]
s_waitcnt lgkmcnt(8)
MF v[0:3],a[68:71],a[144:147],v[0:3]
R16 a[128:131],v240,0
R16 a[132:135],v240,1024
MF v[0:3],a[76:79],a[148:151],v[0:3]
MF v[0:3],a[84:87],a[152:155],v[0:3]
MF v[0:3],a[92:95],a[156:159],v[0:3]
MF v[4:7],a[72:75],a[144:147],v[4:7]
R16 a[136:139],v240,2048
R16 a[140:143],v240,3072
MF v[4:7],a[80:83],a[148:151],v[4:7]
MF v[4:7],a[88:91],a[152:155],v[4:7]
MF v[4:7],a[96:99],a[156:159],v[4:7]
s_waitcnt lgkmcnt(8)
MF v[0:3],a[100:103],a[160:163],v[0:3]
R16 a[144:147],v240,4096
R16 a[148:151],v240,5120
MF v[0:3],a[108:111],a[164:167],v[0:3]
MF v[0:3],a[116:119],a[168:171],v[0:3]
MF v[0:3],a[124:127],a[172:175],v[0:3]
MF v[4:7],a[104:107],a[160:163],v[4:7]
R16 a[152:155],v240,6144
R16 a[156:159],v240,7168
MF v[4:7],a[112:115],a[164:167],v[4:7]
MF v[4:7],a[120:123],a[168:171],v[4:7]
MF v[4:7],a[32:35],a[172:175],v[4:7]
s_waitcnt lgkmcnt(8)
MF v[0:3],a[36:39],a[176:179],v[0:3]
R16 a[160:163],v240,8192
R16 a[164:167],v240,9216
MF v[0:3],a[44:47],a[180:183],v[0:3]
MF v[0:3],a[52:55],a[184:187],v[0:3]
MF v[0:3],a[60:63],a[188:191],v[0:3]
MF v[4:7],a[40:43],a[176:179],v[4:7]
R16 a[168:171],v240,10240
R16 a[172:175],v240,11264
MF v[4:7],a[48:51],a[180:183],v[4:7]
MF v[4:7],a[56:59],a[184:187],v[4:7]
MF v[4:7],v[224:227],a[188:191],v[4:7]
s_waitcnt lgkmcnt(8)
L_OJ_%=:
MF v[8:11],a[196:199],a[128:131],0
R16 a[176:179],v240,12288
R16 a[180:183],v240,13312
MF v[8:11],a[204:207],a[132:135],v[8:11]
v_mov_b32 v218, s37
v_mov_b32 v219, 0
MF v[8:11],a[212:215],a[136:139],v[8:11]
s_waitcnt vmcnt(8)
MF v[8:11],a[220:223],a[140:143],v[8:11]
buffer_store_dword v218, v219, s[20:23], s10 offen offset:0 sc1
MF v[12:15],a[200:203],a[128:131],0
R16 a[184:187],v240,14336
R16 a[188:191],v240,15360
MF v[12:15],a[208:211],a[132:135],v[12:15]
PK v24,v0,v1
PK v25,v2,v3
MF v[12:15],a[216:219],a[136:139],v[12:15]
PK v26,v4,v5
PK v27,v6,v7
MF v[12:15],a[224:227],a[140:143],v[12:15]
s_waitcnt lgkmcnt(8)
s_add_u32 s52, s80, 0x1
MF v[8:11],a[228:231],a[144:147],v[8:11]
R16 a[128:131],v240,16384
R16 a[132:135],v240,17408
MF v[8:11],a[236:239],a[148:151],v[8:11]
s_and_b32 s52, s52, 3
s_lshl_b32 s52, s52, 17
MF v[8:11],a[244:247],a[152:155],v[8:11]
s_add_u32 s52, s52, s9
MF v[8:11],a[252:255],a[156:159],v[8:11]
buffer_store_dwordx4 v[24:27], v242, s[20:23], s52 offen offset:0 sc1
MF v[12:15],a[232:235],a[144:147],v[12:15]
R16 a[136:139],v240,18432
R16 a[140:143],v240,19456
MF v[12:15],a[240:243],a[148:151],v[12:15]
MF v[12:15],a[248:251],a[152:155],v[12:15]
s_waitcnt vmcnt(48)
v_lshlrev_b32 v208, 16, v44
MF v[12:15],a[0:3],a[156:159],v[12:15]
s_waitcnt lgkmcnt(8)
v_and_b32 v209, 0xffff0000, v44
MF v[8:11],a[4:7],a[160:163],v[8:11]
R16 a[144:147],v240,20480
R16 a[148:151],v240,21504
MF v[8:11],a[12:15],a[164:167],v[8:11]
v_lshlrev_b32 v210, 16, v45
v_and_b32 v211, 0xffff0000, v45
MF v[8:11],a[20:23],a[168:171],v[8:11]
v_lshlrev_b32 v212, 16, v46
v_and_b32 v213, 0xffff0000, v46
MF v[8:11],a[28:31],a[172:175],v[8:11]
v_lshlrev_b32 v214, 16, v47
v_and_b32 v215, 0xffff0000, v47
MF v[12:15],a[8:11],a[160:163],v[12:15]
R16 a[152:155],v240,22528
R16 a[156:159],v240,23552
MF v[12:15],a[16:19],a[164:167],v[12:15]
s_cmp_eq_u32 s71, 0
s_cbranch_scc1 L_Y1_%=
L_RT_Y1_%=:
MF v[12:15],a[24:27],a[168:171],v[12:15]
v_mul_f32 v184, 0xbfb8aa3b, v208
v_mul_f32 v185, 0xbfb8aa3b, v209
MF v[12:15],v[64:67],a[172:175],v[12:15]
s_waitcnt lgkmcnt(8)
v_mul_f32 v186, 0xbfb8aa3b, v210
MF v[8:11],v[68:71],a[176:179],v[8:11]
R16 a[160:163],v240,24576
R16 a[164:167],v240,25600
MF v[8:11],v[76:79],a[180:183],v[8:11]
v_mul_f32 v187, 0xbfb8aa3b, v211
v_mul_f32 v188, 0xbfb8aa3b, v212
MF v[8:11],v[84:87],a[184:187],v[8:11]
v_mul_f32 v189, 0xbfb8aa3b, v213
v_mul_f32 v190, 0xbfb8aa3b, v214
MF v[8:11],v[92:95],a[188:191],v[8:11]
v_mul_f32 v191, 0xbfb8aa3b, v215
MF v[12:15],v[72:75],a[176:179],v[12:15]
)A"
R"A(R16 a[168:171],v240,26624
R16 a[172:175],v240,27648
MF v[12:15],v[80:83],a[180:183],v[12:15]
v_exp_f32 v184, v184
MF v[12:15],v[88:91],a[184:187],v[12:15]
v_exp_f32 v185, v185
MF v[12:15],v[96:99],a[188:191],v[12:15]
s_waitcnt lgkmcnt(8)
MF v[8:11],v[100:103],a[128:131],v[8:11]
R16 a[176:179],v240,28672
R16 a[180:183],v240,29696
MF v[8:11],v[108:111],a[132:135],v[8:11]
v_exp_f32 v186, v186
MF v[8:11],v[116:119],a[136:139],v[8:11]
v_exp_f32 v187, v187
MF v[8:11],v[124:127],a[140:143],v[8:11]
v_exp_f32 v188, v188
MF v[12:15],v[104:107],a[128:131],v[12:15]
R16 a[184:187],v240,30720
R16 a[188:191],v240,31744
MF v[12:15],v[112:115],a[132:135],v[12:15]
v_exp_f32 v189, v189
MF v[12:15],v[120:123],a[136:139],v[12:15]
s_waitcnt vmcnt(10)
s_barrier
MF v[12:15],a[64:67],a[140:143],v[12:15]
s_waitcnt lgkmcnt(8)
MF v[8:11],a[68:71],a[144:147],v[8:11]
R16 a[128:131],v241,0
R16 a[132:135],v241,1024
MF v[8:11],a[76:79],a[148:151],v[8:11]
v_and_b32 v217, 48, v242
v_lshlrev_b32 v217, 6, v217
MF v[8:11],a[84:87],a[152:155],v[8:11]
buffer_load_dword v220, v217, s[20:23], s11 offen offset:0 sc1
MF v[8:11],a[92:95],a[156:159],v[8:11]
v_exp_f32 v190, v190
MF v[12:15],a[72:75],a[144:147],v[12:15]
R16 a[136:139],v241,2048
R16 a[140:143],v241,3072
MF v[12:15],a[80:83],a[148:151],v[12:15]
v_exp_f32 v191, v191
MF v[12:15],a[88:91],a[152:155],v[12:15]
v_add_f32 v184, 1.0, v184
v_add_f32 v185, 1.0, v185
MF v[12:15],a[96:99],a[156:159],v[12:15]
s_waitcnt lgkmcnt(8)
v_add_f32 v186, 1.0, v186
MF v[8:11],a[100:103],a[160:163],v[8:11]
R16 a[144:147],v241,4096
R16 a[148:151],v241,5120
MF v[8:11],a[108:111],a[164:167],v[8:11]
v_add_f32 v187, 1.0, v187
v_add_f32 v188, 1.0, v188
MF v[8:11],a[116:119],a[168:171],v[8:11]
v_add_f32 v189, 1.0, v189
v_add_f32 v190, 1.0, v190
MF v[8:11],a[124:127],a[172:175],v[8:11]
v_add_f32 v191, 1.0, v191
MF v[12:15],a[104:107],a[160:163],v[12:15]
R16 a[152:155],v241,6144
R16 a[156:159],v241,7168
MF v[12:15],a[112:115],a[164:167],v[12:15]
v_rcp_f32 v184, v184
MF v[12:15],a[120:123],a[168:171],v[12:15]
v_rcp_f32 v185, v185
MF v[12:15],a[32:35],a[172:175],v[12:15]
s_waitcnt lgkmcnt(8)
MF v[8:11],a[36:39],a[176:179],v[8:11]
R16 a[160:163],v241,8192
R16 a[164:167],v241,9216
MF v[8:11],a[44:47],a[180:183],v[8:11]
v_rcp_f32 v186, v186
MF v[8:11],a[52:55],a[184:187],v[8:11]
v_rcp_f32 v187, v187
MF v[8:11],a[60:63],a[188:191],v[8:11]
v_rcp_f32 v188, v188
MF v[12:15],a[40:43],a[176:179],v[12:15]
R16 a[168:171],v241,10240
R16 a[172:175],v241,11264
MF v[12:15],a[48:51],a[180:183],v[12:15]
v_rcp_f32 v189, v189
MF v[12:15],a[56:59],a[184:187],v[12:15]
MF v[12:15],v[224:227],a[188:191],v[12:15]
s_waitcnt lgkmcnt(8)
MF v[0:3],a[196:199],a[128:131],0
R16 a[176:179],v241,12288
R16 a[180:183],v241,13312
MF v[0:3],a[204:207],a[132:135],v[0:3]
v_rcp_f32 v190, v190
MF v[0:3],a[212:215],a[136:139],v[0:3]
v_rcp_f32 v191, v191
MF v[0:3],a[220:223],a[140:143],v[0:3]
v_mul_f32 v208, v208, v184
v_mul_f32 v209, v209, v185
MF v[4:7],a[200:203],a[128:131],0
R16 a[184:187],v241,14336
R16 a[188:191],v241,15360
MF v[4:7],a[208:211],a[132:135],v[4:7]
PK v16,v8,v9
PK v18,v10,v11
MF v[4:7],a[216:219],a[136:139],v[4:7]
PK v20,v12,v13
PK v22,v14,v15
MF v[4:7],a[224:227],a[140:143],v[4:7]
s_waitcnt lgkmcnt(8)
s_add_u32 s52, s80, 0x2
MF v[0:3],a[228:231],a[144:147],v[0:3]
R16 a[128:131],v241,32768
R16 a[132:135],v241,33792
MF v[0:3],a[236:239],a[148:151],v[0:3]
s_and_b32 s52, s52, 3
s_lshl_b32 s52, s52, 17
MF v[0:3],a[244:247],a[152:155],v[0:3]
s_add_u32 s52, s52, s9
v_mul_f32 v210, v210, v186
MF v[0:3],a[252:255],a[156:159],v[0:3]
buffer_store_dwordx4 v[16:19], v242, s[20:23], s52 offen offset:0 sc1
buffer_store_dwordx4 v[20:23], v242, s[20:23], s52 offen offset:1024 sc1
MF v[4:7],a[232:235],a[144:147],v[4:7]
R16 a[136:139],v241,34816
R16 a[140:143],v241,35840
MF v[4:7],a[240:243],a[148:151],v[4:7]
v_mul_f32 v211, v211, v187
v_mul_f32 v212, v212, v188
MF v[4:7],a[248:251],a[152:155],v[4:7]
v_mov_b32 v218, s37
v_mov_b32 v219, 0
MF v[4:7],a[0:3],a[156:159],v[4:7]
s_waitcnt lgkmcnt(8)
s_waitcnt vmcnt(3)
MF v[0:3],a[4:7],a[160:163],v[0:3]
R16 a[144:147],v241,36864
R16 a[148:151],v241,37888
MF v[0:3],a[12:15],a[164:167],v[0:3]
buffer_store_dword v218, v219, s[20:23], s10 offen offset:64 sc1
v_mul_f32 v213, v213, v189
MF v[0:3],a[20:23],a[168:171],v[0:3]
v_mul_f32 v214, v214, v190
v_mul_f32 v215, v215, v191
MF v[0:3],a[28:31],a[172:175],v[0:3]
PK v208,v208,v209
v_and_b32 v209, 0xffff0000, v208
MF v[4:7],a[8:11],a[160:163],v[4:7]
R16 a[152:155],v241,38912
R16 a[156:159],v241,39936
MF v[4:7],a[16:19],a[164:167],v[4:7]
v_lshlrev_b32 v208, 16, v208
PK v210,v210,v211
MF v[4:7],a[24:27],a[168:171],v[4:7]
v_and_b32 v211, 0xffff0000, v210
v_lshlrev_b32 v210, 16, v210
MF v[4:7],v[64:67],a[172:175],v[4:7]
s_waitcnt lgkmcnt(8)
PK v212,v212,v213
MF v[0:3],v[68:71],a[176:179],v[0:3]
R16 a[160:163],v241,40960
R16 a[164:167],v241,41984
MF v[0:3],v[76:79],a[180:183],v[0:3]
v_and_b32 v213, 0xffff0000, v212
v_lshlrev_b32 v212, 16, v212
MF v[0:3],v[84:87],a[184:187],v[0:3]
PK v214,v214,v215
v_and_b32 v215, 0xffff0000, v214
MF v[0:3],v[92:95],a[188:191],v[0:3]
v_lshlrev_b32 v214, 16, v214
MF v[4:7],v[72:75],a[176:179],v[4:7]
R16 a[168:171],v241,43008
R16 a[172:175],v241,44032
MF v[4:7],v[80:83],a[180:183],v[4:7]
MF v[4:7],v[88:91],a[184:187],v[4:7]
MF v[4:7],v[96:99],a[188:191],v[4:7]
s_waitcnt lgkmcnt(8)
MF v[0:3],v[100:103],a[128:131],v[0:3]
R16 a[176:179],v241,45056
R16 a[180:183],v241,46080
MF v[0:3],v[108:111],a[132:135],v[0:3]
s_waitcnt vmcnt(3)
v_cmp_ne_u32 vcc, s37, v220
MF v[0:3],v[116:119],a[136:139],v[0:3]
s_nop 0
s_cbranch_vccnz L_SP_PA_%=
L_RT_PA_%=:
MF v[0:3],v[124:127],a[140:143],v[0:3]
s_add_u32 s49, s12, 0x0
MF v[4:7],v[104:107],a[128:131],v[4:7]
R16 a[184:187],v241,47104
R16 a[188:191],v241,48128
MF v[4:7],v[112:115],a[132:135],v[4:7]
buffer_load_dwordx4 v[128:131], v242, s[20:23], s49 offen offset:0 sc1
s_add_u32 s49, s12, 0x2000
MF v[4:7],v[120:123],a[136:139],v[4:7]
s_waitcnt vmcnt(7)
s_barrier
MF v[4:7],a[64:67],a[140:143],v[4:7]
buffer_load_dwordx4 v[132:135], v242, s[20:23], s49 offen offset:0 sc1
s_waitcnt lgkmcnt(8)
MF v[0:3],a[68:71],a[144:147],v[0:3]
R16 a[128:131],v243,0
R16 a[132:135],v243,1024
MF v[0:3],a[76:79],a[148:151],v[0:3]
s_add_u32 s49, s12, 0x4000
MF v[0:3],a[84:87],a[152:155],v[0:3]
buffer_load_dwordx4 v[136:139], v242, s[20:23], s49 offen offset:0 sc1
s_add_u32 s49, s12, 0x6000
MF v[0:3],a[92:95],a[156:159],v[0:3]
buffer_load_dwordx4 v[140:143], v242, s[20:23], s49 offen offset:0 sc1
MF v[4:7],a[72:75],a[144:147],v[4:7]
R16 a[136:139],v243,2048
R16 a[140:143],v243,3072
MF v[4:7],a[80:83],a[148:151],v[4:7]
MF v[4:7],a[88:91],a[152:155],v[4:7]
MF v[4:7],a[96:99],a[156:159],v[4:7]
s_waitcnt lgkmcnt(8)
MF v[0:3],a[100:103],a[160:163],v[0:3]
R16 a[144:147],v243,4096
R16 a[148:151],v243,5120
MF v[0:3],a[108:111],a[164:167],v[0:3]
MF v[0:3],a[116:119],a[168:171],v[0:3]
MF v[0:3],a[124:127],a[172:175],v[0:3]
MF v[4:7],a[104:107],a[160:163],v[4:7]
R16 a[152:155],v243,6144
R16 a[156:159],v243,7168
MF v[4:7],a[112:115],a[164:167],v[4:7]
s_add_u32 s51, s80, 0x3
s_and_b32 s51, s51, 3
MF v[4:7],a[120:123],a[168:171],v[4:7]
s_lshl_b32 s13, s51, 10
s_add_u32 s13, s13, s11
MF v[4:7],a[32:35],a[172:175],v[4:7]
s_waitcnt lgkmcnt(8)
s_add_u32 s13, s13, 0x40
MF v[0:3],a[36:39],a[176:179],v[0:3]
R16 a[160:163],v243,8192
R16 a[164:167],v243,9216
MF v[0:3],a[44:47],a[180:183],v[0:3]
s_lshl_b32 s14, s51, 13
s_add_u32 s14, s14, s12
MF v[0:3],a[52:55],a[184:187],v[0:3]
s_add_u32 s14, s14, 0x8000
v_mov_b32 v221, 0
MF v[0:3],a[60:63],a[188:191],v[0:3]
buffer_load_dword v222, v221, s[20:23], s13 offen offset:0 sc1
MF v[4:7],a[40:43],a[176:179],v[4:7]
R16 a[168:171],v243,10240
R16 a[172:175],v243,11264
MF v[4:7],a[48:51],a[180:183],v[4:7]
MF v[4:7],a[56:59],a[184:187],v[4:7]
MF v[4:7],v[224:227],a[188:191],v[4:7]
s_waitcnt lgkmcnt(8)
MF v[8:11],a[196:199],a[128:131],0
R16 a[176:179],v243,12288
R16 a[180:183],v243,13312
MF v[8:11],a[204:207],a[132:135],v[8:11]
MF v[8:11],a[212:215],a[136:139],v[8:11]
MF v[8:11],a[220:223],a[140:143],v[8:11]
MF v[12:15],a[200:203],a[128:131],0
R16 a[184:187],v243,14336
R16 a[188:191],v243,15360
MF v[12:15],a[208:211],a[132:135],v[12:15]
PK v148,v0,v1
PK v150,v2,v3
MF v[12:15],a[216:219],a[136:139],v[12:15]
PK v152,v4,v5
PK v154,v6,v7
MF v[12:15],a[224:227],a[140:143],v[12:15]
s_waitcnt lgkmcnt(8)
s_add_u32 s52, s80, 0x3
MF v[8:11],a[228:231],a[144:147],v[8:11]
R16 a[128:131],v241,16384
R16 a[132:135],v241,17408
MF v[8:11],a[236:239],a[148:151],v[8:11]
s_and_b32 s52, s52, 3
s_lshl_b32 s52, s52, 17
MF v[8:11],a[244:247],a[152:155],v[8:11]
s_add_u32 s52, s52, s9
MF v[8:11],a[252:255],a[156:159],v[8:11]
buffer_store_dwordx4 v[148:151], v242, s[20:23], s52 offen offset:0 sc1
buffer_store_dwordx4 v[152:155], v242, s[20:23], s52 offen offset:1024 sc1
MF v[12:15],a[232:235],a[144:147],v[12:15]
R16 a[136:139],v241,18432
R16 a[140:143],v241,19456
MF v[12:15],a[240:243],a[148:151],v[12:15]
MF v[12:15],a[248:251],a[152:155],v[12:15]
s_add_u32 s49, s80, 0x2
s_and_b32 s49, s49, 3
MF v[12:15],a[0:3],a[156:159],v[12:15]
s_waitcnt lgkmcnt(8)
s_lshl_b32 s49, s49, 13
MF v[8:11],a[4:7],a[160:163],v[8:11]
R16 a[144:147],v241,20480
R16 a[148:151],v241,21504
MF v[8:11],a[12:15],a[164:167],v[8:11]
s_add_u32 s49, s49, s12
s_add_u32 s49, s49, 0x8000
MF v[8:11],a[20:23],a[168:171],v[8:11]
buffer_load_dwordx4 v[160:163], v242, s[20:23], s49 offen offset:0 sc1
buffer_load_dwordx4 v[164:167], v242, s[20:23], s49 offen offset:1024 sc1
MF v[8:11],a[28:31],a[172:175],v[8:11]
s_add_u32 s49, s80, 0x1
s_and_b32 s49, s49, 3
MF v[12:15],a[8:11],a[160:163],v[12:15]
R16 a[152:155],v241,22528
R16 a[156:159],v241,23552
MF v[12:15],a[16:19],a[164:167],v[12:15]
s_lshl_b32 s49, s49, 13
s_add_u32 s49, s49, s12
MF v[12:15],a[24:27],a[168:171],v[12:15]
s_add_u32 s49, s49, 0x8000
s_waitcnt vmcnt(5)
MF v[12:15],v[64:67],a[172:175],v[12:15]
buffer_load_dwordx4 v[168:171], v242, s[20:23], s49 offen offset:0 sc1
s_waitcnt lgkmcnt(8)
MF v[8:11],v[68:71],a[176:179],v[8:11]
R16 a[160:163],v241,24576
R16 a[164:167],v241,25600
MF v[8:11],v[76:79],a[180:183],v[8:11]
buffer_load_dwordx4 v[172:175], v242, s[20:23], s49 offen offset:1024 sc1
v_lshlrev_b32 v48, 16, v128
MF v[8:11],v[84:87],a[184:187],v[8:11]
v_and_b32 v49, 0xffff0000, v128
v_lshlrev_b32 v50, 16, v129
MF v[8:11],v[92:95],a[188:191],v[8:11]
v_and_b32 v51, 0xffff0000, v129
v_lshlrev_b32 v52, 16, v130
MF v[12:15],v[72:75],a[176:179],v[12:15]
R16 a[168:171],v241,26624
R16 a[172:175],v241,27648
MF v[12:15],v[80:83],a[180:183],v[12:15]
v_and_b32 v53, 0xffff0000, v130
v_lshlrev_b32 v54, 16, v131
MF v[12:15],v[88:91],a[184:187],v[12:15]
v_and_b32 v55, 0xffff0000, v131
v_lshlrev_b32 v184, 16, v132
MF v[12:15],v[96:99],a[188:191],v[12:15]
s_waitcnt lgkmcnt(8)
v_and_b32 v185, 0xffff0000, v132
MF v[8:11],v[100:103],a[128:131],v[8:11]
R16 a[176:179],v241,28672
R16 a[180:183],v241,29696
MF v[8:11],v[108:111],a[132:135],v[8:11]
v_add_f32 v48, v48, v184
v_add_f32 v49, v49, v185
MF v[8:11],v[116:119],a[136:139],v[8:11]
v_lshlrev_b32 v186, 16, v133
v_and_b32 v187, 0xffff0000, v133
MF v[8:11],v[124:127],a[140:143],v[8:11]
v_add_f32 v50, v50, v186
v_add_f32 v51, v51, v187
MF v[12:15],v[104:107],a[128:131],v[12:15]
R16 a[184:187],v241,30720
R16 a[188:191],v241,31744
MF v[12:15],v[112:115],a[132:135],v[12:15]
v_lshlrev_b32 v188, 16, v134
v_and_b32 v189, 0xffff0000, v134
MF v[12:15],v[120:123],a[136:139],v[12:15]
v_add_f32 v52, v52, v188
v_add_f32 v53, v53, v189
MF v[12:15],a[64:67],a[140:143],v[12:15]
s_waitcnt lgkmcnt(8)
v_lshlrev_b32 v190, 16, v135
MF v[8:11],a[68:71],a[144:147],v[8:11]
v_and_b32 v191, 0xffff0000, v135
v_add_f32 v54, v54, v190
MF v[8:11],a[76:79],a[148:151],v[8:11]
v_add_f32 v55, v55, v191
v_lshlrev_b32 v184, 16, v136
MF v[8:11],a[84:87],a[152:155],v[8:11]
v_and_b32 v185, 0xffff0000, v136
v_add_f32 v48, v48, v184
MF v[8:11],a[92:95],a[156:159],v[8:11]
v_add_f32 v49, v49, v185
v_lshlrev_b32 v186, 16, v137
MF v[12:15],a[72:75],a[144:147],v[12:15]
s_waitcnt vmcnt(6)
v_cmp_ne_u32 vcc, s37, v222
MF v[12:15],a[80:83],a[148:151],v[12:15]
s_nop 0
s_cbranch_vccnz L_SP_PB_%=
L_RT_PB_%=:
MF v[12:15],a[88:91],a[152:155],v[12:15]
buffer_load_dwordx4 v[144:147], v242, s[20:23], s14 offen offset:0 sc1
v_and_b32 v187, 0xffff0000, v137
MF v[12:15],a[96:99],a[156:159],v[12:15]
s_waitcnt lgkmcnt(4)
v_add_f32 v50, v50, v186
MF v[8:11],a[100:103],a[160:163],v[8:11]
v_add_f32 v51, v51, v187
v_lshlrev_b32 v188, 16, v138
MF v[8:11],a[108:111],a[164:167],v[8:11]
v_and_b32 v189, 0xffff0000, v138
v_add_f32 v52, v52, v188
MF v[8:11],a[116:119],a[168:171],v[8:11]
v_add_f32 v53, v53, v189
v_lshlrev_b32 v190, 16, v139
MF v[8:11],a[124:127],a[172:175],v[8:11]
v_and_b32 v191, 0xffff0000, v139
v_add_f32 v54, v54, v190
MF v[12:15],a[104:107],a[160:163],v[12:15]
v_add_f32 v55, v55, v191
)A"
R"A(v_lshlrev_b32 v184, 16, v140
MF v[12:15],a[112:115],a[164:167],v[12:15]
v_and_b32 v185, 0xffff0000, v140
v_add_f32 v48, v48, v184
MF v[12:15],a[120:123],a[168:171],v[12:15]
v_add_f32 v49, v49, v185
v_lshlrev_b32 v186, 16, v141
MF v[12:15],a[32:35],a[172:175],v[12:15]
s_waitcnt lgkmcnt(0)
v_and_b32 v187, 0xffff0000, v141
MF v[8:11],a[36:39],a[176:179],v[8:11]
v_add_f32 v50, v50, v186
v_add_f32 v51, v51, v187
MF v[8:11],a[44:47],a[180:183],v[8:11]
v_lshlrev_b32 v188, 16, v142
v_and_b32 v189, 0xffff0000, v142
MF v[8:11],a[52:55],a[184:187],v[8:11]
v_add_f32 v52, v52, v188
v_add_f32 v53, v53, v189
MF v[8:11],a[60:63],a[188:191],v[8:11]
v_lshlrev_b32 v190, 16, v143
v_and_b32 v191, 0xffff0000, v143
MF v[12:15],a[40:43],a[176:179],v[12:15]
v_add_f32 v54, v54, v190
v_add_f32 v55, v55, v191
MF v[12:15],a[48:51],a[180:183],v[12:15]
PK v48,v48,v49
v_and_b32 v49, 0xffff0000, v48
MF v[12:15],a[56:59],a[184:187],v[12:15]
MF v[12:15],v[224:227],a[188:191],v[12:15]
s_nop 3
v_lshlrev_b32 v48, 16, v48
PK v50,v50,v51
v_and_b32 v51, 0xffff0000, v50
v_lshlrev_b32 v50, 16, v50
PK v52,v52,v53
v_and_b32 v53, 0xffff0000, v52
v_lshlrev_b32 v52, 16, v52
PK v54,v54,v55
v_and_b32 v55, 0xffff0000, v54
v_lshlrev_b32 v54, 16, v54
v_mul_f32 v48, v48, v56
v_mul_f32 v49, v49, v57
v_mul_f32 v50, v50, v58
v_mul_f32 v51, v51, v59
v_mul_f32 v52, v52, v60
v_mul_f32 v53, v53, v61
v_mul_f32 v54, v54, v62
v_mul_f32 v55, v55, v63
PK v48,v48,v49
v_and_b32 v49, 0xffff0000, v48
v_lshlrev_b32 v48, 16, v48
PK v50,v50,v51
v_and_b32 v51, 0xffff0000, v50
v_lshlrev_b32 v50, 16, v50
PK v52,v52,v53
v_and_b32 v53, 0xffff0000, v52
v_lshlrev_b32 v52, 16, v52
PK v54,v54,v55
v_and_b32 v55, 0xffff0000, v54
v_lshlrev_b32 v54, 16, v54
v_lshlrev_b32 v184, 16, v32
v_and_b32 v185, 0xffff0000, v32
v_add_f32 v184, v184, v48
v_add_f32 v185, v185, v49
PK v56,v184,v185
v_lshlrev_b32 v186, 16, v33
v_and_b32 v187, 0xffff0000, v33
v_add_f32 v186, v186, v50
v_add_f32 v187, v187, v51
PK v57,v186,v187
v_lshlrev_b32 v188, 16, v34
v_and_b32 v189, 0xffff0000, v34
v_add_f32 v188, v188, v52
v_add_f32 v189, v189, v53
PK v58,v188,v189
v_lshlrev_b32 v190, 16, v35
v_and_b32 v191, 0xffff0000, v35
v_add_f32 v190, v190, v54
v_add_f32 v191, v191, v55
PK v59,v190,v191
buffer_store_dwordx4 v[56:59], v244, s[24:27], s84 offen offset:0
s_waitcnt vmcnt(1)
v_lshlrev_b32 v184, 16, v144
v_and_b32 v185, 0xffff0000, v144
v_add_f32 v48, v8, v184
v_add_f32 v49, v9, v185
v_lshlrev_b32 v186, 16, v145
v_and_b32 v187, 0xffff0000, v145
v_add_f32 v50, v10, v186
v_add_f32 v51, v11, v187
v_lshlrev_b32 v188, 16, v146
v_and_b32 v189, 0xffff0000, v146
v_add_f32 v52, v12, v188
v_add_f32 v53, v13, v189
v_lshlrev_b32 v190, 16, v147
v_and_b32 v191, 0xffff0000, v147
v_add_f32 v54, v14, v190
v_add_f32 v55, v15, v191
s_waitcnt vmcnt(2)
v_xor_b32 v161, s37, v161
v_xor_b32 v163, s37, v163
v_xor_b32 v165, s37, v165
v_xor_b32 v167, s37, v167
v_or3_b32 v200, v161, v163, v165
v_or_b32 v200, v200, v167
v_xor_b32 v169, s37, v169
v_xor_b32 v171, s37, v171
v_xor_b32 v173, s37, v173
v_xor_b32 v175, s37, v175
v_or3_b32 v200, v200, v169, v171
v_or3_b32 v200, v200, v173, v175
v_cmp_ne_u32 vcc, 0, v200
s_nop 0
s_cbranch_vccnz L_SP_RC1_%=
L_RT_RC1_%=:
v_lshlrev_b32 v184, 16, v160
v_and_b32 v185, 0xffff0000, v160
v_add_f32 v48, v48, v184
v_add_f32 v49, v49, v185
v_lshlrev_b32 v186, 16, v162
v_and_b32 v187, 0xffff0000, v162
v_add_f32 v50, v50, v186
v_add_f32 v51, v51, v187
v_lshlrev_b32 v188, 16, v164
v_and_b32 v189, 0xffff0000, v164
v_add_f32 v52, v52, v188
v_add_f32 v53, v53, v189
v_lshlrev_b32 v190, 16, v166
v_and_b32 v191, 0xffff0000, v166
v_add_f32 v54, v54, v190
v_add_f32 v55, v55, v191
v_lshlrev_b32 v184, 16, v168
v_and_b32 v185, 0xffff0000, v168
v_add_f32 v48, v48, v184
v_add_f32 v49, v49, v185
v_lshlrev_b32 v186, 16, v170
v_and_b32 v187, 0xffff0000, v170
v_add_f32 v50, v50, v186
v_add_f32 v51, v51, v187
v_lshlrev_b32 v188, 16, v172
v_and_b32 v189, 0xffff0000, v172
v_add_f32 v52, v52, v188
v_add_f32 v53, v53, v189
v_lshlrev_b32 v190, 16, v174
v_and_b32 v191, 0xffff0000, v174
v_add_f32 v54, v54, v190
v_add_f32 v55, v55, v191
PK v48,v48,v49
v_and_b32 v49, 0xffff0000, v48
v_lshlrev_b32 v48, 16, v48
PK v50,v50,v51
v_and_b32 v51, 0xffff0000, v50
v_lshlrev_b32 v50, 16, v50
PK v52,v52,v53
v_and_b32 v53, 0xffff0000, v52
v_lshlrev_b32 v52, 16, v52
PK v54,v54,v55
v_and_b32 v55, 0xffff0000, v54
v_lshlrev_b32 v54, 16, v54
v_mul_f32 v48, v48, v208
v_mul_f32 v49, v49, v209
v_mul_f32 v50, v50, v210
v_mul_f32 v51, v51, v211
v_mul_f32 v52, v52, v212
v_mul_f32 v53, v53, v213
v_mul_f32 v54, v54, v214
v_mul_f32 v55, v55, v215
PK v48,v48,v49
v_and_b32 v49, 0xffff0000, v48
v_lshlrev_b32 v48, 16, v48
PK v50,v50,v51
v_and_b32 v51, 0xffff0000, v50
v_lshlrev_b32 v50, 16, v50
PK v52,v52,v53
v_and_b32 v53, 0xffff0000, v52
v_lshlrev_b32 v52, 16, v52
PK v54,v54,v55
v_and_b32 v55, 0xffff0000, v54
v_lshlrev_b32 v54, 16, v54
v_lshlrev_b32 v184, 16, v40
v_and_b32 v185, 0xffff0000, v40
v_add_f32 v184, v184, v48
v_add_f32 v185, v185, v49
PK v208,v184,v185
v_lshlrev_b32 v186, 16, v41
v_and_b32 v187, 0xffff0000, v41
v_add_f32 v186, v186, v50
v_add_f32 v187, v187, v51
PK v209,v186,v187
v_lshlrev_b32 v188, 16, v42
v_and_b32 v189, 0xffff0000, v42
v_add_f32 v188, v188, v52
v_add_f32 v189, v189, v53
PK v210,v188,v189
v_lshlrev_b32 v190, 16, v43
v_and_b32 v191, 0xffff0000, v43
v_add_f32 v190, v190, v54
v_add_f32 v191, v191, v55
PK v211,v190,v191
buffer_store_dwordx4 v[208:211], v244, s[24:27], s85 offen offset:0
s_cmp_lg_u32 %[bw], 0
s_cbranch_scc1 L_END_%=
s_mov_b32 s48, 0x4901000
L_FN_%=:
buffer_load_dwordx4 v[0:3], v242, s[20:23], s48 offen offset:0 sc1
buffer_load_dwordx4 v[4:7], v242, s[20:23], s48 offen offset:1024 sc1
buffer_load_dwordx4 v[8:11], v242, s[20:23], s48 offen offset:2048 sc1
buffer_load_dwordx4 v[12:15], v242, s[20:23], s48 offen offset:3072 sc1
s_waitcnt vmcnt(0)
s_mov_b64 s[40:41], 0
v_cmp_ne_u32 vcc, s37, v0
s_or_b64 s[40:41], s[40:41], vcc
v_cmp_ne_u32 vcc, s37, v1
s_or_b64 s[40:41], s[40:41], vcc
v_cmp_ne_u32 vcc, s37, v2
s_or_b64 s[40:41], s[40:41], vcc
v_cmp_ne_u32 vcc, s37, v3
s_or_b64 s[40:41], s[40:41], vcc
v_cmp_ne_u32 vcc, s37, v4
s_or_b64 s[40:41], s[40:41], vcc
v_cmp_ne_u32 vcc, s37, v5
s_or_b64 s[40:41], s[40:41], vcc
v_cmp_ne_u32 vcc, s37, v6
s_or_b64 s[40:41], s[40:41], vcc
v_cmp_ne_u32 vcc, s37, v7
s_or_b64 s[40:41], s[40:41], vcc
v_cmp_ne_u32 vcc, s37, v8
s_or_b64 s[40:41], s[40:41], vcc
v_cmp_ne_u32 vcc, s37, v9
s_or_b64 s[40:41], s[40:41], vcc
v_cmp_ne_u32 vcc, s37, v10
s_or_b64 s[40:41], s[40:41], vcc
v_cmp_ne_u32 vcc, s37, v11
s_or_b64 s[40:41], s[40:41], vcc
v_cmp_ne_u32 vcc, s37, v12
s_or_b64 s[40:41], s[40:41], vcc
v_cmp_ne_u32 vcc, s37, v13
s_or_b64 s[40:41], s[40:41], vcc
v_cmp_ne_u32 vcc, s37, v14
s_or_b64 s[40:41], s[40:41], vcc
v_cmp_ne_u32 vcc, s37, v15
s_or_b64 s[40:41], s[40:41], vcc
s_cmp_eq_u64 s[40:41], 0
s_cbranch_scc1 L_FOK_%=
s_sleep 4
s_sub_u32 s87, s87, 1
s_cbranch_scc0 L_FN_%=
L_FOK_%=:
v_mov_b32 v0, s37
v_mov_b32 v1, 0
s_mov_b32 s48, 0x4900000
buffer_store_dword v0, v1, s[20:23], s48 offen sc1
s_branch L_END_%=
L_SP_A3_%=:
s_sleep 4
buffer_load_dword v206, v207, s[20:23], s95 offen offset:0 sc1
s_waitcnt vmcnt(0)
v_cmp_ne_u32 vcc, s37, v206
s_sub_u32 s87, s87, 1
s_cbranch_scc1 L_TO_A3_%=
s_cbranch_vccnz L_SP_A3_%=
s_branch L_RT_A3_%=
L_TO_A3_%=:
s_mov_b32 s87, 0
s_branch L_RT_A3_%=
L_SP_B_%=:
buffer_load_dword v206, v207, s[20:23], s95 offen offset:0 sc1
s_waitcnt vmcnt(0)
v_cmp_ne_u32 vcc, s37, v206
s_sub_u32 s87, s87, 1
s_cbranch_scc1 L_TO_B_%=
s_cbranch_vccnz L_SP_B_%=
s_branch L_RT_B_%=
L_TO_B_%=:
s_mov_b32 s87, 0
s_branch L_RT_B_%=
L_Y0_%=:
v_xor_b32 v184, s37, v193
v_xor_b32 v185, s37, v195
v_xor_b32 v186, s37, v197
v_xor_b32 v187, s37, v199
v_or3_b32 v184, v184, v185, v186
v_or_b32 v184, v184, v187
v_cmp_ne_u32 vcc, 0, v184
s_nop 1
s_cbranch_vccz L_OK_Y0_%=
s_sub_u32 s87, s87, 1
s_cbranch_scc1 L_TO_Y0_%=
s_sleep 4
buffer_load_dwordx4 v[192:195], v205, s[20:23], s94 offen sc1
buffer_load_dwordx4 v[196:199], v205, s[20:23], s94 offen offset:16 sc1
s_waitcnt vmcnt(0)
s_branch L_Y0_%=
L_TO_Y0_%=:
s_mov_b32 s87, 0
L_OK_Y0_%=:
v_add_f32_dpp v192, v192, v192 quad_perm:[1,0,3,2] row_mask:0xf bank_mask:0xf
v_add_f32_dpp v194, v194, v194 quad_perm:[1,0,3,2] row_mask:0xf bank_mask:0xf
v_add_f32_dpp v196, v196, v196 quad_perm:[1,0,3,2] row_mask:0xf bank_mask:0xf
v_add_f32_dpp v198, v198, v198 quad_perm:[1,0,3,2] row_mask:0xf bank_mask:0xf
v_add_f32_dpp v192, v192, v192 quad_perm:[2,3,0,1] row_mask:0xf bank_mask:0xf
v_add_f32_dpp v194, v194, v194 quad_perm:[2,3,0,1] row_mask:0xf bank_mask:0xf
v_add_f32_dpp v196, v196, v196 quad_perm:[2,3,0,1] row_mask:0xf bank_mask:0xf
v_add_f32_dpp v198, v198, v198 quad_perm:[2,3,0,1] row_mask:0xf bank_mask:0xf
v_add_f32_dpp v192, v192, v192 row_half_mirror row_mask:0xf bank_mask:0xf
v_add_f32_dpp v194, v194, v194 row_half_mirror row_mask:0xf bank_mask:0xf
v_add_f32_dpp v196, v196, v196 row_half_mirror row_mask:0xf bank_mask:0xf
v_add_f32_dpp v198, v198, v198 row_half_mirror row_mask:0xf bank_mask:0xf
s_and_b32 s91, s78, 0xffff0000
s_ashr_i32 s92, s91, 31
s_not_b32 s92, s92
s_or_b32 s92, s92, 0x80000000
s_xor_b32 s91, s91, s92
s_and_b32 s91, s91, 0xffff0000
v_mov_b32 v193, 0xbfb8aa3b
v_mul_f32 v193, s91, v193
v_exp_f32 v193, v193
s_nop 0
v_add_f32 v193, 1.0, v193
v_rcp_f32 v193, v193
s_nop 0
PK v193,v193,v193
v_lshlrev_b32 v193, 16, v193
PK v192,v192,v192
v_lshlrev_b32 v192, 16, v192
v_mul_f32 v192, v192, v193
PK v192,v192,v192
v_lshlrev_b32 v192, 16, v192
PK v194,v194,v194
v_lshlrev_b32 v194, 16, v194
v_mul_f32 v194, v194, v193
PK v194,v194,v194
v_lshlrev_b32 v194, 16, v194
PK v196,v196,v196
v_lshlrev_b32 v196, 16, v196
v_mul_f32 v196, v196, v193
PK v196,v196,v196
v_lshlrev_b32 v196, 16, v196
PK v198,v198,v198
v_lshlrev_b32 v198, 16, v198
v_mul_f32 v198, v198, v193
PK v198,v198,v198
v_lshlrev_b32 v198, 16, v198
v_bfe_u32 v195, v242, 7, 1
v_cmp_eq_u32 vcc, 0, v195
v_mov_b32_dpp v197, v192 row_ror:8 row_mask:0xf bank_mask:0xf
s_nop 0
v_cndmask_b32 v184, v197, v192, vcc
v_cndmask_b32 v188, v192, v197, vcc
v_mov_b32_dpp v197, v194 row_ror:8 row_mask:0xf bank_mask:0xf
v_cndmask_b32 v185, v197, v194, vcc
v_cndmask_b32 v189, v194, v197, vcc
v_mov_b32_dpp v197, v196 row_ror:8 row_mask:0xf bank_mask:0xf
v_cndmask_b32 v186, v197, v196, vcc
v_cndmask_b32 v190, v196, v197, vcc
v_mov_b32_dpp v197, v198 row_ror:8 row_mask:0xf bank_mask:0xf
v_cndmask_b32 v187, v197, v198, vcc
v_cndmask_b32 v191, v198, v197, vcc
v_add_f32 v184, v184, v56
v_add_f32 v185, v185, v57
v_add_f32 v186, v186, v58
v_add_f32 v187, v187, v59
v_add_f32 v188, v188, v60
v_add_f32 v189, v189, v61
v_add_f32 v190, v190, v62
v_add_f32 v191, v191, v63
s_and_b32 s91, s77, 15
v_bfe_u32 v195, v242, 4, 4
v_cmp_eq_u32 vcc, s91, v195
PK v184,v184,v185
v_and_b32 v185, 0xffff0000, v184
v_lshlrev_b32 v184, 16, v184
PK v186,v186,v187
v_and_b32 v187, 0xffff0000, v186
v_lshlrev_b32 v186, 16, v186
PK v188,v188,v189
v_and_b32 v189, 0xffff0000, v188
v_lshlrev_b32 v188, 16, v188
PK v190,v190,v191
v_and_b32 v191, 0xffff0000, v190
v_lshlrev_b32 v190, 16, v190
v_cndmask_b32 v56, v56, v184, vcc
v_cndmask_b32 v57, v57, v185, vcc
v_cndmask_b32 v58, v58, v186, vcc
v_cndmask_b32 v59, v59, v187, vcc
v_cndmask_b32 v60, v60, v188, vcc
v_cndmask_b32 v61, v61, v189, vcc
v_cndmask_b32 v62, v62, v190, vcc
v_cndmask_b32 v63, v63, v191, vcc
s_branch L_RT_Y0_%=
L_SP_RC1_%=:
s_add_u32 s49, s80, 0x2
s_and_b32 s49, s49, 3
s_lshl_b32 s49, s49, 13
s_add_u32 s49, s49, s12
s_add_u32 s49, s49, 0x8000
buffer_load_dwordx4 v[160:163], v242, s[20:23], s49 offen offset:0 sc1
buffer_load_dwordx4 v[164:167], v242, s[20:23], s49 offen offset:1024 sc1
s_add_u32 s49, s80, 0x1
)A"
R"A(s_and_b32 s49, s49, 3
s_lshl_b32 s49, s49, 13
s_add_u32 s49, s49, s12
s_add_u32 s49, s49, 0x8000
buffer_load_dwordx4 v[168:171], v242, s[20:23], s49 offen offset:0 sc1
buffer_load_dwordx4 v[172:175], v242, s[20:23], s49 offen offset:1024 sc1
s_waitcnt vmcnt(0)
v_xor_b32 v161, s37, v161
v_xor_b32 v163, s37, v163
v_xor_b32 v165, s37, v165
v_xor_b32 v167, s37, v167
v_or3_b32 v200, v161, v163, v165
v_or_b32 v200, v200, v167
v_xor_b32 v169, s37, v169
v_xor_b32 v171, s37, v171
v_xor_b32 v173, s37, v173
v_xor_b32 v175, s37, v175
v_or3_b32 v200, v200, v169, v171
v_or3_b32 v200, v200, v173, v175
v_cmp_ne_u32 vcc, 0, v200
s_sub_u32 s87, s87, 1
s_cbranch_scc1 L_TO_RC1_%=
s_cbranch_vccnz L_SP_RC1_%=
s_branch L_RT_RC1_%=
L_TO_RC1_%=:
s_mov_b32 s87, 0
s_branch L_RT_RC1_%=
L_Y1_%=:
v_xor_b32 v184, s37, v193
v_xor_b32 v185, s37, v195
v_xor_b32 v186, s37, v197
v_xor_b32 v187, s37, v199
v_or3_b32 v184, v184, v185, v186
v_or_b32 v184, v184, v187
v_cmp_ne_u32 vcc, 0, v184
s_nop 1
s_cbranch_vccz L_OK_Y1_%=
s_sub_u32 s87, s87, 1
s_cbranch_scc1 L_TO_Y1_%=
s_sleep 4
buffer_load_dwordx4 v[192:195], v205, s[20:23], s94 offen sc1
buffer_load_dwordx4 v[196:199], v205, s[20:23], s94 offen offset:16 sc1
s_waitcnt vmcnt(0)
s_branch L_Y1_%=
L_TO_Y1_%=:
s_mov_b32 s87, 0
L_OK_Y1_%=:
v_add_f32_dpp v192, v192, v192 quad_perm:[1,0,3,2] row_mask:0xf bank_mask:0xf
v_add_f32_dpp v194, v194, v194 quad_perm:[1,0,3,2] row_mask:0xf bank_mask:0xf
v_add_f32_dpp v196, v196, v196 quad_perm:[1,0,3,2] row_mask:0xf bank_mask:0xf
v_add_f32_dpp v198, v198, v198 quad_perm:[1,0,3,2] row_mask:0xf bank_mask:0xf
v_add_f32_dpp v192, v192, v192 quad_perm:[2,3,0,1] row_mask:0xf bank_mask:0xf
v_add_f32_dpp v194, v194, v194 quad_perm:[2,3,0,1] row_mask:0xf bank_mask:0xf
v_add_f32_dpp v196, v196, v196 quad_perm:[2,3,0,1] row_mask:0xf bank_mask:0xf
v_add_f32_dpp v198, v198, v198 quad_perm:[2,3,0,1] row_mask:0xf bank_mask:0xf
v_add_f32_dpp v192, v192, v192 row_half_mirror row_mask:0xf bank_mask:0xf
v_add_f32_dpp v194, v194, v194 row_half_mirror row_mask:0xf bank_mask:0xf
v_add_f32_dpp v196, v196, v196 row_half_mirror row_mask:0xf bank_mask:0xf
v_add_f32_dpp v198, v198, v198 row_half_mirror row_mask:0xf bank_mask:0xf
s_and_b32 s91, s78, 0xffff0000
s_ashr_i32 s92, s91, 31
s_not_b32 s92, s92
s_or_b32 s92, s92, 0x80000000
s_xor_b32 s91, s91, s92
s_and_b32 s91, s91, 0xffff0000
v_mov_b32 v193, 0xbfb8aa3b
v_mul_f32 v193, s91, v193
v_exp_f32 v193, v193
s_nop 0
v_add_f32 v193, 1.0, v193
v_rcp_f32 v193, v193
s_nop 0
PK v193,v193,v193
v_lshlrev_b32 v193, 16, v193
PK v192,v192,v192
v_lshlrev_b32 v192, 16, v192
v_mul_f32 v192, v192, v193
PK v192,v192,v192
v_lshlrev_b32 v192, 16, v192
PK v194,v194,v194
v_lshlrev_b32 v194, 16, v194
v_mul_f32 v194, v194, v193
PK v194,v194,v194
v_lshlrev_b32 v194, 16, v194
PK v196,v196,v196
v_lshlrev_b32 v196, 16, v196
v_mul_f32 v196, v196, v193
PK v196,v196,v196
v_lshlrev_b32 v196, 16, v196
PK v198,v198,v198
v_lshlrev_b32 v198, 16, v198
v_mul_f32 v198, v198, v193
PK v198,v198,v198
v_lshlrev_b32 v198, 16, v198
v_bfe_u32 v195, v242, 7, 1
v_cmp_eq_u32 vcc, 0, v195
v_mov_b32_dpp v197, v192 row_ror:8 row_mask:0xf bank_mask:0xf
s_nop 0
v_cndmask_b32 v184, v197, v192, vcc
v_cndmask_b32 v188, v192, v197, vcc
v_mov_b32_dpp v197, v194 row_ror:8 row_mask:0xf bank_mask:0xf
v_cndmask_b32 v185, v197, v194, vcc
v_cndmask_b32 v189, v194, v197, vcc
v_mov_b32_dpp v197, v196 row_ror:8 row_mask:0xf bank_mask:0xf
v_cndmask_b32 v186, v197, v196, vcc
v_cndmask_b32 v190, v196, v197, vcc
v_mov_b32_dpp v197, v198 row_ror:8 row_mask:0xf bank_mask:0xf
v_cndmask_b32 v187, v197, v198, vcc
v_cndmask_b32 v191, v198, v197, vcc
v_add_f32 v184, v184, v208
v_add_f32 v185, v185, v209
v_add_f32 v186, v186, v210
v_add_f32 v187, v187, v211
v_add_f32 v188, v188, v212
v_add_f32 v189, v189, v213
v_add_f32 v190, v190, v214
v_add_f32 v191, v191, v215
s_and_b32 s91, s77, 15
v_bfe_u32 v195, v242, 4, 4
v_cmp_eq_u32 vcc, s91, v195
PK v184,v184,v185
v_and_b32 v185, 0xffff0000, v184
v_lshlrev_b32 v184, 16, v184
PK v186,v186,v187
v_and_b32 v187, 0xffff0000, v186
v_lshlrev_b32 v186, 16, v186
PK v188,v188,v189
v_and_b32 v189, 0xffff0000, v188
v_lshlrev_b32 v188, 16, v188
PK v190,v190,v191
v_and_b32 v191, 0xffff0000, v190
v_lshlrev_b32 v190, 16, v190
v_cndmask_b32 v208, v208, v184, vcc
v_cndmask_b32 v209, v209, v185, vcc
v_cndmask_b32 v210, v210, v186, vcc
v_cndmask_b32 v211, v211, v187, vcc
v_cndmask_b32 v212, v212, v188, vcc
v_cndmask_b32 v213, v213, v189, vcc
v_cndmask_b32 v214, v214, v190, vcc
v_cndmask_b32 v215, v215, v191, vcc
s_branch L_RT_Y1_%=
L_SP_PA_%=:
buffer_load_dword v220, v217, s[20:23], s11 offen offset:0 sc1
s_waitcnt vmcnt(0)
v_cmp_ne_u32 vcc, s37, v220
s_sub_u32 s87, s87, 1
s_cbranch_scc1 L_TO_PA_%=
s_cbranch_vccnz L_SP_PA_%=
s_branch L_RT_PA_%=
L_TO_PA_%=:
s_mov_b32 s87, 0
s_branch L_RT_PA_%=
L_SP_PB_%=:
buffer_load_dword v222, v221, s[20:23], s13 offen offset:0 sc1
s_waitcnt vmcnt(0)
v_cmp_ne_u32 vcc, s37, v222
s_sub_u32 s87, s87, 1
s_cbranch_scc1 L_TO_PB_%=
s_cbranch_vccnz L_SP_PB_%=
s_branch L_RT_PB_%=
L_TO_PB_%=:
s_mov_b32 s87, 0
s_branch L_RT_PB_%=
L_SP_AR:
s_sleep 4
buffer_load_dword v251, v250, s[20:23], s52 offen offset:0 sc1
s_waitcnt vmcnt(0)
v_cmp_ne_u32 vcc, s37, v251
s_sub_u32 s87, s87, 1
s_cbranch_scc1 L_TO_AR
s_cbranch_vccnz L_SP_AR
s_branch L_RT_AR
L_TO_AR:
s_mov_b32 s87, 0
s_branch L_RT_AR
L_END_%=:
)A"
        :
        : [ka] "s"(__builtin_amdgcn_kernarg_segment_ptr()), [bw] "s"(bw), [tag] "s"(tag), [key] "s"(key), [code] "s"(code)
        : CLOBBERS);
  }
}

extern "C" void kernel_launch(void* const* d_in, const int* in_sizes, int n_in, void* d_out, int out_size, void* d_ws, size_t ws_size,
                              hipStream_t stream) {
  (void)in_sizes; (void)n_in; (void)out_size; (void)ws_size;
  const u16* x = (const u16*)d_in[0]; const u16* ckv = (const u16*)d_in[1]; const u16* wq = (const u16*)d_in[2];
  const u16* wuk = (const u16*)d_in[3]; const u16* wuv = (const u16*)d_in[4]; const u16* wo = (const u16*)d_in[5];
  const u16* wr = (const u16*)d_in[7]; const u16* wg = (const u16*)d_in[8]; const u16* wu = (const u16*)d_in[9]; const u16* wd = (const u16*)d_in[10];
  char* ws = (char*)d_ws;
  (void)hipFuncSetAttribute((const void*)fused_kernel, hipFuncAttributeMaxDynamicSharedMemorySize, 163840);
  fused_kernel<<<256, 256, 163840, stream>>>(x, ckv, wq, wuk, wuv, wo, wr, wg, wu, wd, (u16*)d_out, ws);
}
```
